```python
import torch

def _softmax(x, axis=-1):
    return torch.softmax(x, dim=axis)
def _gelu(x):
    return torch.nn.functional.gelu(x, approximate="tanh")
def _silu(x):
    return torch.nn.functional.silu(x)
def _sigmoid(x):
    return torch.sigmoid(x)
def _layernorm(x, axis=-1, eps=1e-5):
    m = x.mean(dim=axis, keepdim=True)
    v = x.var(dim=axis, keepdim=True, unbiased=False)
    return (x - m) / torch.sqrt(v + eps)
def _scatter_add(tgt, idx, src, axis=0):
    return tgt.clone().index_add_(axis, idx, src)
def _segment_sum(src, seg, n):
    out = torch.zeros((n,) + src.shape[1:], dtype=src.dtype)
    return out.index_add_(0, seg, src)
def _pad(x, pw):
    flat = tuple(v for pair in reversed(pw) for v in pair)
    return torch.nn.functional.pad(x, flat)

kernel_name = 'parallel_sum__sliding_block_cached__glu_gated_geglu__decode'

def setup_inputs():

    _w = (lambda a: (lambda t: t.to(torch.bfloat16) if t.is_floating_point() else t)(torch.as_tensor(a)))
    def _A_setup():
        B, Sq, D = 2, 1, 640
        Hq, Hkv, Sk, Dh = 32, 32, 8192, 192
        assert Hq % Hkv == 0, (Hq, Hkv)
        return {
            "x": _w((lambda *s: torch.randn(*s, dtype=torch.bfloat16) * 0.05)(B, Sq, D)),
            "k_cache": _w((lambda *s: torch.randn(*s, dtype=torch.bfloat16) * 1.6)(B, Hkv, Sk, Dh)),
            "v_cache": _w((lambda *s: torch.randn(*s, dtype=torch.bfloat16) * 0.8)(B, Hkv, Sk, Dh)),
            "wq": _w((lambda *s: torch.randn(*s, dtype=torch.bfloat16) * 1.6)(D, Hq * Dh) * (D ** -0.5)),
            "wo": _w((lambda *s: torch.randn(*s, dtype=torch.bfloat16) * 0.8)(Hq * Dh, D) * ((Hq * Dh) ** -0.5)),
        }
    _A_in = _A_setup()

    def _B_setup():
        B, S, Dm, Df = 2, 1, 640, 1536
        return {
            "x": _w((lambda *s: torch.randn(*s, dtype=torch.bfloat16) * 0.05)(B, S, Dm)),
            "w_gate": _w((lambda *s: torch.randn(*s, dtype=torch.bfloat16) * 0.8)(Dm, Df) * (Dm ** -0.5)),
            "w_up": _w((lambda *s: torch.randn(*s, dtype=torch.bfloat16) * 0.8)(Dm, Df) * (Dm ** -0.5)),
            "w_down": _w((lambda *s: torch.randn(*s, dtype=torch.bfloat16) * 0.8)(Df, Dm) * (Df ** -0.5)),
        }
    _B_in = _B_setup()

    return {
        "A_x": _A_in["x"],
        "A_k_cache": _A_in["k_cache"],
        "A_v_cache": _A_in["v_cache"],
        "A_wq": _A_in["wq"],
        "A_wo": _A_in["wo"],
        "B_x": _B_in["x"],
        "B_w_gate": _B_in["w_gate"],
        "B_w_up": _B_in["w_up"],
        "B_w_down": _B_in["w_down"],
    }

def reference(A_x, A_k_cache, A_v_cache, A_wq, A_wo, B_x, B_w_gate, B_w_up, B_w_down):
    def _A_ref(x, k_cache, v_cache, wq, wo):
        B, Sq, D = x.shape
        Hq, Dh = 32, 192
        Hkv, Sk = k_cache.shape[1], k_cache.shape[2]
        W = min(4096, max(256, Sk // 4))
        rep = Hq // Hkv
        q = (lambda x, *d: x.permute(*d))((x @ wq).reshape(B, Sq, Hq, Dh), 0, 2, 1, 3)
        k = (lambda x, n, axis: torch.repeat_interleave(x, n, dim=axis))(k_cache[:, :, Sk - W:, :], rep, axis=1)
        v = (lambda x, n, axis: torch.repeat_interleave(x, n, dim=axis))(v_cache[:, :, Sk - W:, :], rep, axis=1)
        scores = torch.einsum("bhqd,bhkd->bhqk", q, k) * (Dh ** -0.5)
        p = _softmax(scores, axis=-1)
        o = torch.einsum("bhqk,bhkd->bhqd", p, v)
        return (lambda x, *d: x.permute(*d))(o, 0, 2, 1, 3).reshape(B, Sq, Hq * Dh) @ wo

    def _B_ref(x, w_gate, w_up, w_down):
        g = torch.einsum("bsd,df->bsf", x, w_gate)
        u = torch.einsum("bsd,df->bsf", x, w_up)
        h = _gelu(g) * u
        return torch.einsum("bsf,fd->bsd", h, w_down)

    x = A_x
    xn = _layernorm(x, axis=-1)
    return x + _A_ref(xn, A_k_cache, A_v_cache, A_wq, A_wo) + _B_ref(xn, B_w_gate, B_w_up, B_w_down)
```

```cpp
#include <hip/hip_runtime.h>
#include <hip/hip_bf16.h>
#include <stdint.h>

typedef unsigned short u16;
typedef unsigned u32x4 __attribute__((ext_vector_type(4)));
constexpr int B=2, D=640, H=32, SK=8192, DH=192, W=2048, DF=1536;
__device__ __forceinline__ float bf2f(u16 v){ return __uint_as_float(((unsigned)v)<<16); }
__device__ __forceinline__ float rbf(float f){ return __bfloat162float(__float2bfloat16(f)); }

constexpr int WS_Q=0;
constexpr int WS_G=WS_Q+2*6144;
constexpr int WS_U=WS_G+2*1536;
constexpr int WS_P=WS_U+2*1536;
constexpr int WS_Y=WS_P+64*4*196;

__global__ void __launch_bounds__(256) k_front(const u16* x, const u16* wq, const u16* wg, const u16* wu, float* ws){
  __shared__ float xn[2][D];
  __shared__ float red[4][2][64];
  int tid=threadIdx.x, lane=tid&63, w=tid>>6;
  if (w<2){
    float v[10]; float s=0;
    for(int i=0;i<10;i++){ v[i]=bf2f(x[w*D+lane+64*i]); s+=v[i]; }
    for(int o=32;o;o>>=1) s+=__shfl_xor(s,o);
    float m=s/D;
    float q=0; for(int i=0;i<10;i++){ float d=v[i]-m; q+=d*d; }
    for(int o=32;o;o>>=1) q+=__shfl_xor(q,o);
    float var=rbf(q/D); float mb=rbf(m);
    float den=rbf(sqrtf(rbf(var+1e-5f)));
    for(int i=0;i<10;i++) xn[w][lane+64*i]=rbf(rbf(v[i]-mb)/den);
  }
  __syncthreads();
  int u=blockIdx.x; const u16* wp; int ld; float* o; int ostr;
  if(u<96){ wp=wq+u*64; ld=6144; o=ws+WS_Q+u*64; ostr=6144; }
  else if(u<120){ wp=wg+(u-96)*64; ld=DF; o=ws+WS_G+(u-96)*64; ostr=DF; }
  else { wp=wu+(u-120)*64; ld=DF; o=ws+WS_U+(u-120)*64; ostr=DF; }
  float a0=0,a1=0;
  for(int k=w*160;k<w*160+160;k++){ float wv=bf2f(wp[(size_t)k*ld+lane]); a0+=xn[0][k]*wv; a1+=xn[1][k]*wv; }
  red[w][0][lane]=a0; red[w][1][lane]=a1;
  __syncthreads();
  if(tid<128){ int b=tid>>6; float s=red[0][b][lane]+red[1][b][lane]+red[2][b][lane]+red[3][b][lane]; o[b*ostr+lane]=rbf(s); }
}

__global__ void __launch_bounds__(512) k_attn(const u16* kc, const u16* vc, float* ws){
  __shared__ float sacc[8][DH]; __shared__ float sm[8], sl[8];
  int bh=blockIdx.x>>2, s=blockIdx.x&3; int b=bh>>5, h=bh&31;
  int tid=threadIdx.x, lane=tid&63, w=tid>>6, g=lane>>3, j=lane&7;
  size_t base=((size_t)bh*SK+(SK-W)+s*512+w*64)*DH;
  const u32x4* kp=(const u32x4*)(kc+base); const u32x4* vp=(const u32x4*)(vc+base);
  float q[24];
  for(int i=0;i<3;i++) for(int e=0;e<8;e++) q[i*8+e]=ws[WS_Q+b*6144+h*DH+i*64+j*8+e];
  float sc[8];
  #pragma unroll
  for(int t=0;t<8;t++){
    float a=0;
    #pragma unroll
    for(int i=0;i<3;i++){ u32x4 kv=__builtin_nontemporal_load(kp+(t*8+g)*24+i*8+j);
      #pragma unroll
      for(int e=0;e<4;e++){ a+=q[i*8+2*e]*__uint_as_float(kv[e]<<16)+q[i*8+2*e+1]*__uint_as_float(kv[e]&0xffff0000u);} }
    a+=__shfl_xor(a,1); a+=__shfl_xor(a,2); a+=__shfl_xor(a,4);
    sc[t]=a*0.07216878364870322f;
  }
  float m=sc[0];
  for(int t=1;t<8;t++) m=fmaxf(m,sc[t]);
  m=fmaxf(m,__shfl_xor(m,8)); m=fmaxf(m,__shfl_xor(m,16)); m=fmaxf(m,__shfl_xor(m,32));
  float l=0; float acc[24]; for(int i=0;i<24;i++) acc[i]=0;
  #pragma unroll
  for(int t=0;t<8;t++){
    float p=__expf(sc[t]-m); l+=p;
    #pragma unroll
    for(int i=0;i<3;i++){ u32x4 kv=__builtin_nontemporal_load(vp+(t*8+g)*24+i*8+j);
      #pragma unroll
      for(int e=0;e<4;e++){ acc[i*8+2*e]+=p*__uint_as_float(kv[e]<<16); acc[i*8+2*e+1]+=p*__uint_as_float(kv[e]&0xffff0000u);} }
  }
  l+=__shfl_xor(l,8); l+=__shfl_xor(l,16); l+=__shfl_xor(l,32);
  for(int i=0;i<24;i++){ float a=acc[i]; a+=__shfl_xor(a,8); a+=__shfl_xor(a,16); a+=__shfl_xor(a,32); acc[i]=a; }
  if(g==0){ for(int i=0;i<3;i++) for(int e=0;e<8;e++) sacc[w][i*64+j*8+e]=acc[i*8+e]; if(j==0){sm[w]=m; sl[w]=l;} }
  __syncthreads();
  if(tid<DH){
    float M=sm[0]; for(int i=1;i<8;i++) M=fmaxf(M,sm[i]);
    float L=0,o=0; for(int i=0;i<8;i++){ float f=__expf(sm[i]-M); L+=f*sl[i]; o+=f*sacc[i][tid]; }
    float* pp=ws+WS_P+blockIdx.x*196; pp[tid]=o; if(tid==0){pp[192]=M; pp[193]=L;}
  }
}

__global__ void __launch_bounds__(640) k_back(const u16* wo, const u16* wd, float* ws){
  __shared__ float ov[2][DH];
  int u=blockIdx.x, tid=threadIdx.x;
  if(tid<2*DH){ int b=tid/DH, d=tid%DH;
    if(u<32){ const float* pp=ws+WS_P+(b*32+u)*4*196;
      float M=fmaxf(fmaxf(pp[192],pp[196+192]),fmaxf(pp[392+192],pp[588+192]));
      float L=0,o=0; for(int s=0;s<4;s++){ float f=__expf(pp[s*196+192]-M); L+=f*pp[s*196+193]; o+=f*pp[s*196+d]; }
      ov[b][d]=rbf(o/L);
    } else { int f=(u-32)*192+d; float g=ws[WS_G+b*DF+f], uu=ws[WS_U+b*DF+f];
      float t=tanhf(0.7978845608028654f*(g+0.044715f*g*g*g)); float ge=rbf(0.5f*g*(1.f+t)); ov[b][d]=rbf(ge*uu); }
  }
  __syncthreads();
  const u16* wp = u<32 ? wo+(size_t)u*192*D : wd+(size_t)(u-32)*192*D;
  float a0=0,a1=0;
  for(int k=0;k<192;k++){ float wv=bf2f(wp[k*D+tid]); a0+=ov[0][k]*wv; a1+=ov[1][k]*wv; }
  ws[WS_Y+(u*2+0)*D+tid]=a0; ws[WS_Y+(u*2+1)*D+tid]=a1;
}
__global__ void __launch_bounds__(640) k_fin(const u16* x, const float* ws, __hip_bfloat16* out){
  int b=blockIdx.x, tid=threadIdx.x; float a=0,f=0;
  for(int u=0;u<32;u++) a+=ws[WS_Y+(u*2+b)*D+tid];
  for(int u=32;u<40;u++) f+=ws[WS_Y+(u*2+b)*D+tid];
  out[b*D+tid]=__float2bfloat16(rbf(bf2f(x[b*D+tid])+rbf(a))+rbf(f));
}

extern "C" void kernel_launch(void* const* d_in, const int* in_sizes, int n_in, void* d_out, int out_size, void* d_ws, size_t ws_size, hipStream_t stream){
  const u16* x=(const u16*)d_in[0]; const u16* kc=(const u16*)d_in[1]; const u16* vc=(const u16*)d_in[2];
  const u16* wq=(const u16*)d_in[3]; const u16* wo=(const u16*)d_in[4];
  const u16* wg=(const u16*)d_in[6]; const u16* wu=(const u16*)d_in[7]; const u16* wd=(const u16*)d_in[8];
  float* ws=(float*)d_ws;
  k_front<<<144,256,0,stream>>>(x,wq,wg,wu,ws);
  k_attn<<<256,512,0,stream>>>(kc,vc,ws);
  k_back<<<40,640,0,stream>>>(wo,wd,ws);
  k_fin<<<2,640,0,stream>>>(x,ws,(__hip_bfloat16*)d_out);
}
```

```cpp
#include <hip/hip_runtime.h>
#include <hip/hip_bf16.h>
#include <stdint.h>
#include <type_traits>

typedef unsigned short u16;
typedef unsigned u32x4 __attribute__((ext_vector_type(4)));
typedef unsigned u32x2 __attribute__((ext_vector_type(2)));
typedef float f32x4 __attribute__((ext_vector_type(4)));
typedef float f32x2 __attribute__((ext_vector_type(2)));
typedef short s16x4 __attribute__((ext_vector_type(4)));
typedef __bf16 bf16x8 __attribute__((ext_vector_type(8)));
typedef __bf16 bf16x2 __attribute__((ext_vector_type(2)));
typedef __amdgpu_buffer_rsrc_t rsrc_t;

#define WIN 8
#define B1_AT 30

constexpr int AUX_NT = 2, AUX_SC1 = 16;
constexpr int RSRC_W3 = 0x00020000;
constexpr unsigned SPIN_CAP = 1u << 18;

constexpr int WS_EPOCH = 0;
constexpr int WS_QP = 4096;
constexpr int WS_GU = WS_QP + 32 * 8 * 2 * 192 * 8;
constexpr int WS_ML = WS_GU + 8 * 5 * 2 * 2 * 192 * 8;
constexpr int WS_OP = WS_ML + 32 * 2 * 4 * 4 * 16;
constexpr int WS_YP = WS_OP + 32 * 2 * 4 * 192 * 8;
constexpr int WS_END = WS_YP + 10 * 2 * 96 * 64 * 8;

template <int I, int N, class F> __device__ __forceinline__ void static_for(F&& f) {
  if constexpr (I < N) { f(std::integral_constant<int, I>{}); static_for<I + 1, N>(f); }
}
__device__ __forceinline__ rsrc_t mk_rsrc(const void* p, int bytes) {
  return __builtin_amdgcn_make_buffer_rsrc(const_cast<void*>(p), 0, bytes, RSRC_W3);
}
__device__ __forceinline__ u32x4 ld_nt(rsrc_t r, int voff, int soff) { return __builtin_amdgcn_raw_buffer_load_b128(r, voff, soff, AUX_NT); }
__device__ __forceinline__ u32x4 ld_sc1(rsrc_t r, int voff, int soff) { return __builtin_amdgcn_raw_buffer_load_b128(r, voff, soff, AUX_SC1); }
__device__ __forceinline__ void st_pair(rsrc_t r, int voff, int soff, float a, float b, unsigned tag) {
  u32x4 v = {__float_as_uint(a), tag, __float_as_uint(b), tag};
  __builtin_amdgcn_raw_buffer_store_b128(v, r, voff, soff, AUX_SC1);
}
__device__ __forceinline__ float bflo(unsigned v) { return __uint_as_float(v << 16); }
__device__ __forceinline__ float bfhi(unsigned v) { return __uint_as_float(v & 0xffff0000u); }
__device__ __forceinline__ unsigned cvtpk(float lo, float hi) {
  bf16x2 r = {(__bf16)lo, (__bf16)hi};
  return __builtin_bit_cast(unsigned, r);
}
__device__ __forceinline__ float rbf(float x) { return (float)(__bf16)x; }
template <int CTRL> __device__ __forceinline__ float dpp(float x) {
  return __builtin_bit_cast(float, __builtin_amdgcn_mov_dpp(__builtin_bit_cast(int, x), CTRL, 0xf, 0xf, true));
}
constexpr int XOR1 = 0xB1, XOR2 = 0x4E, HMIR = 0x141, ROR8 = 0x128, ROR4 = 0x124;
__device__ __forceinline__ float x16_sum(float x) { auto s = __builtin_amdgcn_permlane16_swap(__float_as_uint(x), __float_as_uint(x), false, false); return __uint_as_float(s[0]) + __uint_as_float(s[1]); }
__device__ __forceinline__ float x32_sum(float x) { auto s = __builtin_amdgcn_permlane32_swap(__float_as_uint(x), __float_as_uint(x), false, false); return __uint_as_float(s[0]) + __uint_as_float(s[1]); }
__device__ __forceinline__ float x16_max(float x) { auto s = __builtin_amdgcn_permlane16_swap(__float_as_uint(x), __float_as_uint(x), false, false); return fmaxf(__uint_as_float(s[0]), __uint_as_float(s[1])); }
__device__ __forceinline__ float x32_max(float x) { auto s = __builtin_amdgcn_permlane32_swap(__float_as_uint(x), __float_as_uint(x), false, false); return fmaxf(__uint_as_float(s[0]), __uint_as_float(s[1])); }
__device__ __forceinline__ float row_sum(float x) { x += dpp<XOR1>(x); x += dpp<XOR2>(x); x += dpp<HMIR>(x); x += dpp<ROR8>(x); return x; }
__device__ __forceinline__ float row_max(float x) { x = fmaxf(x, dpp<XOR1>(x)); x = fmaxf(x, dpp<XOR2>(x)); x = fmaxf(x, dpp<HMIR>(x)); x = fmaxf(x, dpp<ROR8>(x)); return x; }
__device__ __forceinline__ float quad_sum(float x) { x += dpp<XOR1>(x); x += dpp<XOR2>(x); return x; }
__device__ __forceinline__ float dot2(unsigned a, unsigned b, float c) {
  return __builtin_amdgcn_fdot2_f32_bf16(__builtin_bit_cast(bf16x2, a), __builtin_bit_cast(bf16x2, b), c, false);
}
__device__ __forceinline__ f32x4 mfma16(unsigned a0, unsigned a1, unsigned b0, unsigned b1, f32x4 c) {
  u32x2 a = {a0, a1}, b = {b0, b1};
  return __builtin_amdgcn_mfma_f32_16x16x16bf16_1k(__builtin_bit_cast(s16x4, a), __builtin_bit_cast(s16x4, b), c, 0, 0, 0);
}
__device__ __forceinline__ f32x4 mfma32(u32x4 a, u32x4 b, f32x4 c) {
  return __builtin_amdgcn_mfma_f32_16x16x32_bf16(__builtin_bit_cast(bf16x8, a), __builtin_bit_cast(bf16x8, b), c, 0, 0, 0);
}
#define SB() __builtin_amdgcn_sched_barrier(0)
#define WG_BARRIER() do { asm volatile("s_waitcnt lgkmcnt(0)\n\ts_barrier" ::: "memory"); } while (0)
#define MEMFENCE() asm volatile("" ::: "memory")

#define STAMP(i)

__device__ __forceinline__ void proj_epilogue(f32x4 acc0, f32x4 acc1, bool z, float& v0, float& v1) {
  float n0[4], n1[4];
#pragma unroll
  for (int r = 0; r < 4; ++r) {
    auto s = __builtin_amdgcn_permlane32_swap(__float_as_uint(acc0[r]), __float_as_uint(acc1[r]), false, false);
    n0[r] = __uint_as_float(s[0]); n1[r] = __uint_as_float(s[1]);
  }
  const float r0 = n0[0] + dpp<ROR8>(n1[0]), r1 = n0[1] + dpp<ROR8>(n1[1]);
  const float r2 = n1[2] + dpp<ROR8>(n0[2]), r3 = n1[3] + dpp<ROR8>(n0[3]);
  v0 = z ? r2 : r0; v1 = z ? r3 : r1;
}

__global__ void __launch_bounds__(512, 2)
fused_decode(const u16* __restrict__ x, const u16* __restrict__ kc, const u16* __restrict__ vc, const u16* __restrict__ wq,
             const u16* __restrict__ wo, const u16* __restrict__ wg, const u16* __restrict__ wu, const u16* __restrict__ wd,
             u16* __restrict__ out, unsigned char* __restrict__ ws) {
  __shared__ __attribute__((aligned(16))) float lds[96 + 8 + 4 + 4 + 8 * 192];
  unsigned* l_q = (unsigned*)lds;
  float* l_ml = lds + 96;
  float* l_c = lds + 104;
  unsigned* l_tag = (unsigned*)(lds + 108);
  float* l_acc = lds + 112;

  const int bid = blockIdx.x;
  const int unit = (bid & 7) * 32 + (bid >> 3);
  const int h = unit >> 3, j8 = unit & 7, b = j8 >> 2, s = j8 & 3;
  const int tid = threadIdx.x, lane = tid & 63, wave = __builtin_amdgcn_readfirstlane(tid >> 6);
  const int g = lane >> 3, j = lane & 7;
  const rsrc_t rws = mk_rsrc(ws, WS_END);
  STAMP(0);

  if (wave >= 4) {
    const int sw = wave - 4;
    const size_t kvoff = ((size_t)(b * 32 + h) * 8192 + 6144 + 512 * s) * 192;
    const rsrc_t rk = mk_rsrc(kc + kvoff, 512 * 384), rv = mk_rsrc(vc + kvoff, 512 * 384);
    const int voff = (sw * 8 + g) * 384 + 16 * j;
    u32x4 buf[96];
    u32x4 qv[3];
    float sc[16];
    unsigned pd[16];
    f32x4 acc[3] = {{0.f, 0.f, 0.f, 0.f}, {0.f, 0.f, 0.f, 0.f}, {0.f, 0.f, 0.f, 0.f}};
    unsigned msk[4];
#pragma unroll
    for (int d = 0; d < 4; ++d) msk[d] = (j >> 1) == d ? ((j & 1) ? 0xffff0000u : 0x0000ffffu) : 0u;
    constexpr float C2 = 0.07216878364870322f * 1.4426950408889634f;

    auto consume = [&](auto G) {
      constexpr int t = decltype(G)::value;
      if constexpr (t < 16) {
        float a0 = 0.f, a1 = 0.f, a2 = 0.f;
#pragma unroll
        for (int e = 0; e < 4; ++e) { a0 = dot2(buf[3 * t][e], qv[0][e], a0); a1 = dot2(buf[3 * t + 1][e], qv[1][e], a1); a2 = dot2(buf[3 * t + 2][e], qv[2][e], a2); }
        float a = (a0 + a1) + a2;
        a += dpp<XOR1>(a); a += dpp<XOR2>(a); a += dpp<HMIR>(a);
        sc[t] = a;
        if constexpr (t == 15) {
          float m = sc[0];
#pragma unroll
          for (int u = 1; u < 16; ++u) m = fmaxf(m, sc[u]);
          m = fmaxf(m, dpp<ROR8>(m)); m = x16_max(m); m = x32_max(m);
          const float mc = m * C2;
          float l = 0.f;
#pragma unroll
          for (int u = 0; u < 16; ++u) { const float p = __builtin_amdgcn_exp2f(fmaf(sc[u], C2, -mc)); pd[u] = cvtpk(p, p); l += bfhi(pd[u]); }
          l += dpp<ROR8>(l); l = x16_sum(l); l = x32_sum(l);
          if (lane == 0) *(f32x2*)(l_ml + 2 * sw) = f32x2{mc, l};
          STAMP(3);
          WG_BARRIER();
        }
      } else {
        constexpr int u = t - 16;
        const u32x4 a = {pd[u] & msk[0], pd[u] & msk[1], pd[u] & msk[2], pd[u] & msk[3]};
#pragma unroll
        for (int i = 0; i < 3; ++i) acc[i] = mfma32(a, buf[3 * t + i], acc[i]);
      }
    };
    constexpr auto done_after = [](int n) constexpr {
      if (n < B1_AT) return 0;
      const int landed = (n - WIN + 1) / 3;
      const int cu = 1 + (2 * (n - B1_AT)) / 3;
      return landed < cu ? landed : cu;
    };
    static_for<0, 96>([&](auto N) {
      constexpr int n = decltype(N)::value;
      if constexpr (n == B1_AT) {
        STAMP(1);
        WG_BARRIER();
        STAMP(2);
#pragma unroll
        for (int i = 0; i < 3; ++i) qv[i] = *(const u32x4*)(l_q + 32 * i + 4 * j);
      }
      if constexpr (n >= WIN) asm volatile("s_waitcnt vmcnt(%0)" ::"i"(WIN - 1) : "memory");
      constexpr int m = n < 48 ? n : n - 48;
      buf[n] = ld_nt(n < 48 ? rk : rv, voff + (m % 3) * 128, (m / 3) * 12288);
      SB();
      static_for<(n > 0 ? done_after(n - 1) : 0), done_after(n)>(consume);
      SB();
    });
    STAMP(4);
    WG_BARRIER();
    static_for<done_after(95), 32>([&](auto G) { consume(G); SB(); });
    if ((lane >> 5) == (g & 1)) {
      float* dst = l_acc + (sw * 2 + (g & 1)) * 192 + 8 * j + 4 * ((lane >> 4) & 1);
#pragma unroll
      for (int i = 0; i < 3; ++i) *(f32x4*)(dst + 64 * i) = acc[i];
    }
    STAMP(5);
    WG_BARRIER();
    if (sw < 3 && lane < 32) {
      const unsigned tag = *l_tag;
      const f32x4 c = *(const f32x4*)l_c;
      const float* src = l_acc + 64 * sw + 8 * j + 2 * g;
      float o0 = 0.f, o1 = 0.f;
#pragma unroll
      for (int w = 0; w < 4; ++w) {
        const f32x2 u = *(const f32x2*)(src + (2 * w) * 192), v = *(const f32x2*)(src + (2 * w + 1) * 192);
        o0 = fmaf(c[w], u[0] + v[0], o0); o1 = fmaf(c[w], u[1] + v[1], o1);
      }
      st_pair(rws, lane * 16, WS_OP + ((h * 2 + b) * 4 + s) * 1536 + sw * 512, o0, o1, tag);
    }
    STAMP(6);
    return;
  }

  __builtin_amdgcn_s_setprio(2);
  const int w = wave;
  const int bl = (lane >> 2) & 1, e4 = lane & 3;
  const unsigned m0 = e4 == 0 ? 0x0000ffffu : e4 == 1 ? 0xffff0000u : 0u;
  const unsigned m1 = e4 == 2 ? 0x0000ffffu : e4 == 3 ? 0xffff0000u : 0u;
  const int st_voff = ((lane >> 4) & 1) * 1536 + (8 * j + 4 * (lane >> 5) + 2 * (g & 1)) * 8;
  const rsrc_t rx = mk_rsrc(x, 2560);
  const int uq = unit & 31;
  unsigned tag;
  {
    const unsigned ep = __builtin_amdgcn_raw_buffer_load_b32(rws, 0, WS_EPOCH, AUX_SC1);
    const u32x4 xa = __builtin_amdgcn_raw_buffer_load_b128(rx, lane * 40, 0, 0);
    const u32x4 xb = __builtin_amdgcn_raw_buffer_load_b128(rx, lane * 40 + 16, 0, 0);
    const u32x2 xc = __builtin_amdgcn_raw_buffer_load_b64(rx, lane * 40 + 32, 0, 0);
    auto front = [&](auto NN, rsrc_t rw, int row_bytes, int xoff, int rec) {
      constexpr int N = decltype(NN)::value;
      unsigned xr[N / 2];
      {
        const u32x4 t0 = __builtin_amdgcn_raw_buffer_load_b128(rx, bl * 1280 + g * (2 * N), xoff, 0);
        xr[0] = t0[0]; xr[1] = t0[1]; xr[2] = t0[2]; xr[3] = t0[3];
        if constexpr (N == 10) xr[4] = __builtin_amdgcn_raw_buffer_load_b32(rx, bl * 1280 + g * (2 * N) + 16, xoff, 0);
        else { const u32x4 t1 = __builtin_amdgcn_raw_buffer_load_b128(rx, bl * 1280 + g * (2 * N) + 16, xoff, 0); xr[4] = t1[0]; xr[5] = t1[1]; xr[6] = t1[2]; xr[7] = t1[3]; }
      }
      u32x4 wv[N];
      const int wvoff = g * N * row_bytes + 16 * j;
#pragma unroll
      for (int n = 0; n < N; ++n) wv[n] = ld_nt(rw, wvoff, n * row_bytes);
      SB();
      float v[20];
#pragma unroll
      for (int e = 0; e < 4; ++e) { v[2 * e] = bflo(xa[e]); v[2 * e + 1] = bfhi(xa[e]); v[8 + 2 * e] = bflo(xb[e]); v[9 + 2 * e] = bfhi(xb[e]); }
      v[16] = bflo(xc[0]); v[17] = bfhi(xc[0]); v[18] = bflo(xc[1]); v[19] = bfhi(xc[1]);
      float sum = 0.f;
#pragma unroll
      for (int e = 0; e < 20; ++e) sum += v[e];
      sum = x16_sum(row_sum(sum));
      const float mean = sum * (1.f / 640.f);
      float sq = 0.f;
#pragma unroll
      for (int e = 0; e < 20; ++e) { const float d = v[e] - mean; sq = fmaf(d, d, sq); }
      sq = x16_sum(row_sum(sq));
      const float mb = rbf(mean), den = rbf(sqrtf(rbf(rbf(sq * (1.f / 640.f)) + 1e-5f)));
      const float inv = 1.f / den;
      const float mb0 = __builtin_bit_cast(float, __builtin_amdgcn_readlane(__builtin_bit_cast(int, mb), 0));
      const float mb1 = __builtin_bit_cast(float, __builtin_amdgcn_readlane(__builtin_bit_cast(int, mb), 32));
      const float iv0 = __builtin_bit_cast(float, __builtin_amdgcn_readlane(__builtin_bit_cast(int, inv), 0));
      const float iv1 = __builtin_bit_cast(float, __builtin_amdgcn_readlane(__builtin_bit_cast(int, inv), 32));
      const float mm = bl ? mb1 : mb0, ii = bl ? iv1 : iv0;
      f32x4 acc0 = {0.f, 0.f, 0.f, 0.f}, acc1 = {0.f, 0.f, 0.f, 0.f};
#pragma unroll
      for (int n = 0; n < N; ++n) {
        const float xv = (n & 1) ? bfhi(xr[n >> 1]) : bflo(xr[n >> 1]);
        const float xn = rbf(xv - mm) * ii;
        const unsigned d = cvtpk(xn, xn), a0 = d & m0, a1 = d & m1;
        acc0 = mfma16(a0, a1, wv[n][0], wv[n][1], acc0);
        acc1 = mfma16(a0, a1, wv[n][2], wv[n][3], acc1);
      }
      float v0, v1;
      proj_epilogue(acc0, acc1, g & 1, v0, v1);
      st_pair(rws, st_voff, rec, v0, v1, __builtin_amdgcn_readfirstlane(ep) + 1u);
    };
    if (w < 3) {
      front(std::integral_constant<int, 10>{}, mk_rsrc(wq + (size_t)(80 * j8) * 6144 + 192 * h + 64 * w, 79 * 12288 + 128), 12288, 160 * j8,
            WS_QP + (h * 8 + j8) * 3072 + w * 512);
    } else if (uq < 30) {
      const int r = uq / 6, lc = uq - 6 * r, mat = lc >= 3 ? 1 : 0, c3 = lc - 3 * mat;
      front(std::integral_constant<int, 16>{}, mk_rsrc((mat ? wu : wg) + (size_t)(128 * r) * 1536 + 192 * (h >> 2) + 64 * c3, 127 * 3072 + 128), 3072, 256 * r,
            WS_GU + (((h >> 2) * 5 + r) * 2 + mat) * 3072 + c3 * 512);
    }
    tag = __builtin_amdgcn_readfirstlane(ep) + 1u;
  }
  STAMP(1);
  {
    u32x4 t[3];
    for (unsigned spin = 0;; ++spin) {
      MEMFENCE();
#pragma unroll
      for (int k = 0; k < 3; ++k) t[k] = ld_sc1(rws, g * 3072 + 16 * j + 128 * k, WS_QP + h * 8 * 3072 + b * 1536 + w * 384);
      bool ok = true;
#pragma unroll
      for (int k = 0; k < 3; ++k) ok &= (t[k][1] == tag) & (t[k][3] == tag);
      if (__all(ok) || spin > SPIN_CAP) break;
    }
#pragma unroll
    for (int k = 0; k < 3; ++k) {
      float a = __uint_as_float(t[k][0]), c = __uint_as_float(t[k][2]);
      a += dpp<ROR8>(a); c += dpp<ROR8>(c);
      a = x32_sum(x16_sum(a)); c = x32_sum(x16_sum(c));
      if (lane < 8) l_q[24 * w + 8 * k + j] = cvtpk(a, c);
    }
    if (tid == 0) *l_tag = tag;
  }
  STAMP(2);
  WG_BARRIER();
  const int slot = 4 * j8 + w;
  const bool has_back = slot < 30;
  const int t3 = slot / 10, cc = slot - 10 * t3;
  unsigned ha[2][2] = {{0u, 0u}, {0u, 0u}};
  if (has_back) {
    u32x4 t[3];
    const int so = WS_GU + (h >> 2) * 5 * 6144 + (h & 3) * 384 + t3 * 128;
    const int vo = bl * 1536 + 16 * g;
    for (unsigned spin = 0;; ++spin) {
      MEMFENCE();
      t[0] = ld_sc1(rws, vo + e4 * 6144, so);
      t[1] = ld_sc1(rws, vo + e4 * 6144 + 3072, so);
      t[2] = ld_sc1(rws, vo + 4 * 6144 + (e4 & 1) * 3072, so);
      bool ok = true;
#pragma unroll
      for (int k = 0; k < 3; ++k) ok &= (t[k][1] == tag) & (t[k][3] == tag);
      if (__all(ok) || spin > SPIN_CAP) break;
    }
#pragma unroll
    for (int z = 0; z < 2; ++z) {
      const float ex = __uint_as_float(t[2][2 * z]);
      const float gg = rbf(quad_sum(__uint_as_float(t[0][2 * z]) + (e4 == 0 ? ex : 0.f)));
      const float uu = rbf(quad_sum(__uint_as_float(t[1][2 * z]) + (e4 == 1 ? ex : 0.f)));
      const float zz = 0.7978845608028654f * (gg + 0.044715f * gg * gg * gg);
      const float ge = rbf(gg / (1.f + __builtin_amdgcn_exp2f(-2.8853900817779268f * zz)));
      const float hv = ge * uu;
      const unsigned d = cvtpk(hv, hv);
      ha[z][0] = d & m0; ha[z][1] = d & m1;
    }
  }
  const bool reducer = (w == 0) && (bid < 160);
  const int rb = bid / 80, rc = (bid - 80 * rb) >> 3, ro = bid & 7;
  unsigned xres = 0;
  if (reducer) xres = __builtin_amdgcn_raw_buffer_load_b32(rx, (lane & 3) * 4, rb * 1280 + rc * 128 + ro * 16, 0);
  STAMP(3);
  WG_BARRIER();
  if (w == 0) {
    const int so = WS_ML + (h * 2 + b) * 256;
    if (lane < 4) { const f32x2 ml = *(const f32x2*)(l_ml + 2 * lane); st_pair(rws, lane * 16, so + s * 64, ml[0], ml[1], tag); }
    u32x4 t;
    for (unsigned spin = 0;; ++spin) {
      MEMFENCE();
      t = ld_sc1(rws, (lane & 15) * 16, so);
      if (__all((t[1] == tag) & (t[3] == tag)) || spin > SPIN_CAP) break;
    }
    const float mv = __uint_as_float(t[0]);
    const float M = row_max(mv);
    const float f = __builtin_amdgcn_exp2f(mv - M);
    const float L = row_sum(f * __uint_as_float(t[2]));
    if ((lane >> 2) == s) l_c[lane & 3] = f / L;
  }
  STAMP(4);
  WG_BARRIER();
  u32x4 wb[10];
  if (has_back) {
    const rsrc_t rd = mk_rsrc(wd + (size_t)(48 * h + 16 * t3) * 640 + 64 * cc, 15 * 1280 + 128);
    const rsrc_t ro_ = mk_rsrc(wo + (size_t)(192 * h + 64 * t3) * 640 + 64 * cc, 63 * 1280 + 128);
#pragma unroll
    for (int n = 0; n < 2; ++n) wb[8 + n] = ld_nt(rd, g * 2560 + 16 * j + n * 1280, 0);
#pragma unroll
    for (int n = 0; n < 8; ++n) wb[n] = ld_nt(ro_, g * 10240 + 16 * j, n * 1280);
    SB();
  }
  STAMP(5);
  WG_BARRIER();
  STAMP(6);
  if (has_back) {
    u32x4 t[4];
    const int so = WS_OP + h * 8 * 1536 + t3 * 512;
    const int vo = (bl * 4 + e4) * 1536 + 16 * g;
    for (unsigned spin = 0;; ++spin) {
      MEMFENCE();
#pragma unroll
      for (int k = 0; k < 4; ++k) t[k] = ld_sc1(rws, vo + 128 * k, so);
      bool ok = true;
#pragma unroll
      for (int k = 0; k < 4; ++k) ok &= (t[k][1] == tag) & (t[k][3] == tag);
      if (__all(ok) || spin > SPIN_CAP) break;
    }
    STAMP(7);
    f32x4 acc0 = {0.f, 0.f, 0.f, 0.f}, acc1 = {0.f, 0.f, 0.f, 0.f};
#pragma unroll
    for (int z = 0; z < 2; ++z) {
      acc0 = mfma16(ha[z][0], ha[z][1], wb[8 + z][0], wb[8 + z][1], acc0);
      acc1 = mfma16(ha[z][0], ha[z][1], wb[8 + z][2], wb[8 + z][3], acc1);
    }
#pragma unroll
    for (int n = 0; n < 8; ++n) {
      const float ov = quad_sum(__uint_as_float(t[n >> 1][2 * (n & 1)]));
      const unsigned d = cvtpk(ov, ov), a0 = d & m0, a1 = d & m1;
      acc0 = mfma16(a0, a1, wb[n][0], wb[n][1], acc0);
      acc1 = mfma16(a0, a1, wb[n][2], wb[n][3], acc1);
    }
    float v0, v1;
    proj_epilogue(acc0, acc1, g & 1, v0, v1);
    st_pair(rws, ((lane >> 4) & 1) * (96 * 512) + (8 * j + 4 * (lane >> 5) + 2 * (g & 1)) * 8, WS_YP + cc * 2 * 96 * 512 + (h * 3 + t3) * 512, v0, v1, tag);
  }
  STAMP(8);
  if (reducer) {
    u32x4 t[6];
    const int so = WS_YP + (rc * 2 + rb) * 96 * 512 + ro * 64;
    const int vo = (lane >> 2) * 512 + (lane & 3) * 16;
    for (unsigned spin = 0;; ++spin) {
      MEMFENCE();
#pragma unroll
      for (int k = 0; k < 6; ++k) t[k] = ld_sc1(rws, vo, so + k * 16 * 512);
      bool ok = true;
#pragma unroll
      for (int k = 0; k < 6; ++k) ok &= (t[k][1] == tag) & (t[k][3] == tag);
      if (__all(ok) || spin > SPIN_CAP) break;
    }
    STAMP(9);
    float y0 = 0.f, y1 = 0.f;
#pragma unroll
    for (int k = 0; k < 6; ++k) { y0 += __uint_as_float(t[k][0]); y1 += __uint_as_float(t[k][2]); }
    y0 += dpp<ROR4>(y0); y1 += dpp<ROR4>(y1);
    y0 += dpp<ROR8>(y0); y1 += dpp<ROR8>(y1);
    y0 = x32_sum(x16_sum(y0)); y1 = x32_sum(x16_sum(y1));
    if (lane < 4) ((unsigned*)out)[rb * 320 + rc * 32 + ro * 4 + lane] = cvtpk(bflo(xres) + y0, bfhi(xres) + y1);
    if (bid == 0 && lane == 0) __builtin_amdgcn_raw_buffer_store_b32(tag, rws, 0, WS_EPOCH, AUX_SC1);
    STAMP(10);
  }
}

extern "C" void kernel_launch(void* const* d_in, const int* in_sizes, int n_in, void* d_out, int out_size, void* d_ws, size_t ws_size,
                              hipStream_t stream) {
  fused_decode<<<256, 512, 0, stream>>>((const u16*)d_in[0], (const u16*)d_in[1], (const u16*)d_in[2], (const u16*)d_in[3], (const u16*)d_in[4],
                                        (const u16*)d_in[6], (const u16*)d_in[7], (const u16*)d_in[8], (u16*)d_out, (unsigned char*)d_ws);
}
```

```cpp
#define B1_AT 48
#define WS_BASE (ws_size - (4u<<20))
#include <hip/hip_runtime.h>
#include <hip/hip_bf16.h>
#include <stdint.h>
#include <type_traits>

typedef unsigned short u16;
typedef unsigned u32x4 __attribute__((ext_vector_type(4)));
typedef unsigned u32x2 __attribute__((ext_vector_type(2)));
typedef float f32x4 __attribute__((ext_vector_type(4)));
typedef float f32x2 __attribute__((ext_vector_type(2)));
typedef short s16x4 __attribute__((ext_vector_type(4)));
typedef __bf16 bf16x8 __attribute__((ext_vector_type(8)));
typedef __bf16 bf16x2 __attribute__((ext_vector_type(2)));
typedef __amdgpu_buffer_rsrc_t rsrc_t;

#define WIN 8

#define CATCH 3
#define AUX_X 2
#define QSLEEP 16
#define MLSLEEP 32
#define PACE 3
#define PREWRITE 0
#define CODEPF 0
#define H1SLEEP 0
constexpr int AUX_NT = 2, AUX_SC1 = 16;
constexpr int RSRC_W3 = 0x00020000;
constexpr unsigned SPIN_CAP = 1u << 18;

constexpr int WS_EPOCH = 0;
constexpr int WS_QP = 4096;
constexpr int WS_GU = WS_QP + 32 * 8 * 2 * 192 * 8;
constexpr int WS_ML = WS_GU + 8 * 5 * 2 * 2 * 192 * 8;
constexpr int WS_OP = WS_ML + 32 * 2 * 4 * 4 * 16;
constexpr int WS_YP = WS_OP + 32 * 2 * 4 * 192 * 8;
constexpr int WS_END = WS_YP + 10 * 2 * 96 * 64 * 8;

template <int I, int N, class F> __device__ __forceinline__ void static_for(F&& f) {
  if constexpr (I < N) { f(std::integral_constant<int, I>{}); static_for<I + 1, N>(f); }
}
__device__ __forceinline__ rsrc_t mk_rsrc(const void* p, int bytes) {
  return __builtin_amdgcn_make_buffer_rsrc(const_cast<void*>(p), 0, bytes, RSRC_W3);
}
__device__ __forceinline__ u32x4 ld_nt(rsrc_t r, int voff, int soff) { return __builtin_amdgcn_raw_buffer_load_b128(r, voff, soff, AUX_NT); }
__device__ __forceinline__ u32x4 ld_sc1(rsrc_t r, int voff, int soff) { return __builtin_amdgcn_raw_buffer_load_b128(r, voff, soff, AUX_SC1); }
__device__ __forceinline__ void st_pair(rsrc_t r, int voff, int soff, float a, float b, unsigned tag) {
  u32x4 v = {__float_as_uint(a), tag, __float_as_uint(b), tag};
  __builtin_amdgcn_raw_buffer_store_b128(v, r, voff, soff, AUX_SC1);
}
__device__ __forceinline__ float bflo(unsigned v) { return __uint_as_float(v << 16); }
__device__ __forceinline__ float bfhi(unsigned v) { return __uint_as_float(v & 0xffff0000u); }
__device__ __forceinline__ unsigned cvtpk(float lo, float hi) {
  bf16x2 r = {(__bf16)lo, (__bf16)hi};
  return __builtin_bit_cast(unsigned, r);
}
__device__ __forceinline__ float rbf(float x) { return (float)(__bf16)x; }
template <int CTRL> __device__ __forceinline__ float dpp(float x) {
  return __builtin_bit_cast(float, __builtin_amdgcn_mov_dpp(__builtin_bit_cast(int, x), CTRL, 0xf, 0xf, true));
}
constexpr int XOR1 = 0xB1, XOR2 = 0x4E, HMIR = 0x141, ROR8 = 0x128, ROR4 = 0x124;
__device__ __forceinline__ float x16_sum(float x) { auto s = __builtin_amdgcn_permlane16_swap(__float_as_uint(x), __float_as_uint(x), false, false); return __uint_as_float(s[0]) + __uint_as_float(s[1]); }
__device__ __forceinline__ float x32_sum(float x) { auto s = __builtin_amdgcn_permlane32_swap(__float_as_uint(x), __float_as_uint(x), false, false); return __uint_as_float(s[0]) + __uint_as_float(s[1]); }
__device__ __forceinline__ float x16_max(float x) { auto s = __builtin_amdgcn_permlane16_swap(__float_as_uint(x), __float_as_uint(x), false, false); return fmaxf(__uint_as_float(s[0]), __uint_as_float(s[1])); }
__device__ __forceinline__ float x32_max(float x) { auto s = __builtin_amdgcn_permlane32_swap(__float_as_uint(x), __float_as_uint(x), false, false); return fmaxf(__uint_as_float(s[0]), __uint_as_float(s[1])); }
__device__ __forceinline__ float row_sum(float x) { x += dpp<XOR1>(x); x += dpp<XOR2>(x); x += dpp<HMIR>(x); x += dpp<ROR8>(x); return x; }
__device__ __forceinline__ float row_max(float x) { x = fmaxf(x, dpp<XOR1>(x)); x = fmaxf(x, dpp<XOR2>(x)); x = fmaxf(x, dpp<HMIR>(x)); x = fmaxf(x, dpp<ROR8>(x)); return x; }
__device__ __forceinline__ float quad_sum(float x) { x += dpp<XOR1>(x); x += dpp<XOR2>(x); return x; }
__device__ __forceinline__ float dot2(unsigned a, unsigned b, float c) {
  return __builtin_amdgcn_fdot2_f32_bf16(__builtin_bit_cast(bf16x2, a), __builtin_bit_cast(bf16x2, b), c, false);
}
__device__ __forceinline__ f32x4 mfma16(unsigned a0, unsigned a1, unsigned b0, unsigned b1, f32x4 c) {
  u32x2 a = {a0, a1}, b = {b0, b1};
  return __builtin_amdgcn_mfma_f32_16x16x16bf16_1k(__builtin_bit_cast(s16x4, a), __builtin_bit_cast(s16x4, b), c, 0, 0, 0);
}
__device__ __forceinline__ f32x4 mfma32(u32x4 a, u32x4 b, f32x4 c) {
  return __builtin_amdgcn_mfma_f32_16x16x32_bf16(__builtin_bit_cast(bf16x8, a), __builtin_bit_cast(bf16x8, b), c, 0, 0, 0);
}
#define SB() __builtin_amdgcn_sched_barrier(0)
#define WG_BARRIER() do { asm volatile("s_waitcnt lgkmcnt(0)\n\ts_barrier" ::: "memory"); } while (0)
#define MEMFENCE() asm volatile("" ::: "memory")

#define FS(i)
#define STAMP(i)
#define STAMPV(i, v)
#define STAMP_DECL
#define STAMP_FLUSH()

__device__ __forceinline__ void proj_epilogue(f32x4 acc0, f32x4 acc1, bool z, float& v0, float& v1) {
  float n0[4], n1[4];
#pragma unroll
  for (int r = 0; r < 4; ++r) {
    auto s = __builtin_amdgcn_permlane32_swap(__float_as_uint(acc0[r]), __float_as_uint(acc1[r]), false, false);
    n0[r] = __uint_as_float(s[0]); n1[r] = __uint_as_float(s[1]);
  }
  const float r0 = n0[0] + dpp<ROR8>(n1[0]), r1 = n0[1] + dpp<ROR8>(n1[1]);
  const float r2 = n1[2] + dpp<ROR8>(n0[2]), r3 = n1[3] + dpp<ROR8>(n0[3]);
  v0 = z ? r2 : r0; v1 = z ? r3 : r1;
}

__global__ void __launch_bounds__(512, 2)
fused_decode(const u16* __restrict__ x, const u16* __restrict__ kc, const u16* __restrict__ vc, const u16* __restrict__ wq,
             const u16* __restrict__ wo, const u16* __restrict__ wg, const u16* __restrict__ wu, const u16* __restrict__ wd,
             u16* __restrict__ out, unsigned char* __restrict__ ws) {
  __shared__ __attribute__((aligned(16))) float lds[96 + 8 + 4 + 4 + 8 * 192];
  unsigned* l_q = (unsigned*)lds;
  float* l_ml = lds + 96;
  float* l_c = lds + 104;
  unsigned* l_tag = (unsigned*)(lds + 108);
  float* l_acc = lds + 112;

  STAMP_DECL;
  STAMP(0);
  asm volatile("" ::"s"(x), "s"(kc), "s"(vc), "s"(wq), "s"(wo), "s"(wg), "s"(wu), "s"(wd), "s"(out), "s"(ws));
  const int bid = blockIdx.x;
  const int unit = (bid & 7) * 32 + (bid >> 3);
  const int h = unit >> 3, j8 = unit & 7, b = j8 >> 2, s = j8 & 3;
  const int tid = threadIdx.x, lane = tid & 63, wave = __builtin_amdgcn_readfirstlane(tid >> 6);
  const int g = lane >> 3, j = lane & 7;
  const rsrc_t rws = mk_rsrc(ws, WS_END);
  const rsrc_t rwg = rws;
#define GOK(x) (x)

  if (CODEPF) {
    if (wave == 3) {
      unsigned long long pc; asm volatile("s_getpc_b64 %0" : "=s"(pc));
      const rsrc_t rc_ = mk_rsrc((const void*)(pc & ~255ull), 14848);
      unsigned d0 = __builtin_amdgcn_raw_buffer_load_b32(rc_, lane * 128, 0, CODEPF == 2 ? AUX_NT : 0), d1 = __builtin_amdgcn_raw_buffer_load_b32(rc_, lane * 128, 8192, CODEPF == 2 ? AUX_NT : 0);
      asm volatile("" ::"v"(d0), "v"(d1));
    }
  }
  if (wave >= 4) {
    const int sw = wave - 4;
    const size_t kvoff = ((size_t)(b * 32 + h) * 8192 + 6144 + 512 * s) * 192;
    const rsrc_t rk = mk_rsrc(kc + kvoff, 512 * 384), rv = mk_rsrc(vc + kvoff, 512 * 384);
    const int voff = (sw * 8 + g) * 384 + 16 * j;
    u32x4 buf[96];
    u32x4 qv[3];
    float sc[16];
    unsigned pd[16];
    f32x4 acc[3] = {{0.f, 0.f, 0.f, 0.f}, {0.f, 0.f, 0.f, 0.f}, {0.f, 0.f, 0.f, 0.f}};
    unsigned msk[4];
#pragma unroll
    for (int d = 0; d < 4; ++d) msk[d] = (j >> 1) == d ? ((j & 1) ? 0xffff0000u : 0x0000ffffu) : 0u;
    constexpr float C2 = 0.07216878364870322f * 1.4426950408889634f;

    auto consume = [&](auto G) {
      constexpr int t = decltype(G)::value;
      if constexpr (t < 16) {
        float a0 = 0.f, a1 = 0.f, a2 = 0.f;
#pragma unroll
        for (int e = 0; e < 4; ++e) { a0 = dot2(buf[3 * t][e], qv[0][e], a0); a1 = dot2(buf[3 * t + 1][e], qv[1][e], a1); a2 = dot2(buf[3 * t + 2][e], qv[2][e], a2); }
        float a = (a0 + a1) + a2;
        a += dpp<XOR1>(a); a += dpp<XOR2>(a); a += dpp<HMIR>(a);
        sc[t] = a;
        if constexpr (t == 15) {
          float m = sc[0];
#pragma unroll
          for (int u = 1; u < 16; ++u) m = fmaxf(m, sc[u]);
          m = fmaxf(m, dpp<ROR8>(m)); m = x16_max(m); m = x32_max(m);
          const float mc = m * C2;
          float l = 0.f;
#pragma unroll
          for (int u = 0; u < 16; ++u) { const float p = __builtin_amdgcn_exp2f(fmaf(sc[u], C2, -mc)); pd[u] = cvtpk(p, p); l += bfhi(pd[u]); }
          l += dpp<ROR8>(l); l = x16_sum(l); l = x32_sum(l);
          if (lane == 0) *(f32x2*)(l_ml + 2 * sw) = f32x2{mc, l};
          STAMP(3);
          WG_BARRIER();
        }
      } else {
        constexpr int u = t - 16;
        const u32x4 a = {pd[u] & msk[0], pd[u] & msk[1], pd[u] & msk[2], pd[u] & msk[3]};
#pragma unroll
        for (int i = 0; i < 3; ++i) acc[i] = mfma32(a, buf[3 * t + i], acc[i]);
      }
    };
    constexpr auto done_after = [](int n) constexpr {
      if (n < B1_AT) return 0;
      const int landed = (n - WIN + 1) / 3;
      const int cu = 1 + (CATCH * (n - B1_AT)) / 3;
      return landed < cu ? landed : cu;
    };
    static_for<0, 96>([&](auto N) {
      constexpr int n = decltype(N)::value;
      if constexpr (n == B1_AT) {
        STAMP(1);
        WG_BARRIER();
        STAMP(2);
#pragma unroll
        for (int i = 0; i < 3; ++i) qv[i] = *(const u32x4*)(l_q + 32 * i + 4 * j);
      }
      if constexpr (n == 0) FS(1);
      if constexpr (n == WIN) FS(2);
      if constexpr (n >= WIN) asm volatile("s_waitcnt vmcnt(%0)" ::"i"(WIN - 1) : "memory");
      if constexpr (n >= WIN && n < WIN + 45 && (n - WIN) % 5 == 0) FS(3 + (n - WIN) / 5);
      constexpr int m = n < 48 ? n : n - 48;
      buf[n] = ld_nt(n < 48 ? rk : rv, voff + (m % 3) * 128, (m / 3) * 12288);
      SB();
      static_for<(n > 0 ? done_after(n - 1) : 0), done_after(n)>(consume);
      SB();
    });
    STAMP(4);
    WG_BARRIER();
    static_for<done_after(95), 32>([&](auto G) { consume(G); SB(); });
    if ((lane >> 5) == (g & 1)) {
      float* dst = l_acc + (sw * 2 + (g & 1)) * 192 + 8 * j + 4 * ((lane >> 4) & 1);
#pragma unroll
      for (int i = 0; i < 3; ++i) *(f32x4*)(dst + 64 * i) = acc[i];
    }
    STAMP(5);
    WG_BARRIER();
    if (sw < 3 && lane < 32) {
      const unsigned tag = *l_tag;
      const f32x4 c = *(const f32x4*)l_c;
      const float* src = l_acc + 64 * sw + 8 * j + 2 * g;
      float o0 = 0.f, o1 = 0.f;
#pragma unroll
      for (int w = 0; w < 4; ++w) {
        const f32x2 u = *(const f32x2*)(src + (2 * w) * 192), v = *(const f32x2*)(src + (2 * w + 1) * 192);
        o0 = fmaf(c[w], u[0] + v[0], o0); o1 = fmaf(c[w], u[1] + v[1], o1);
      }
      st_pair(rws, lane * 16, WS_OP + ((h * 2 + b) * 4 + s) * 1536 + sw * 512, o0, o1, tag);
    }
    STAMP(6);
    asm volatile("s_waitcnt vmcnt(0)" ::: "memory");
    STAMP(7);
    WG_BARRIER();
    STAMP_FLUSH();
    return;
  }

  __builtin_amdgcn_s_setprio(2);
  const int w = wave;
  const int bl = (lane >> 2) & 1, e4 = lane & 3;
  const unsigned m0 = e4 == 0 ? 0x0000ffffu : e4 == 1 ? 0xffff0000u : 0u;
  const unsigned m1 = e4 == 2 ? 0x0000ffffu : e4 == 3 ? 0xffff0000u : 0u;
  const int st_voff = ((lane >> 4) & 1) * 1536 + (8 * j + 4 * (lane >> 5) + 2 * (g & 1)) * 8;
  const rsrc_t rx = mk_rsrc(x, 2560);
  const int uq = unit & 31;
  unsigned tag;
  {
    const unsigned ep = *(const __attribute__((address_space(4))) unsigned*)(ws + WS_EPOCH);
    const u32x4 xa = __builtin_amdgcn_raw_buffer_load_b128(rx, lane * 40, 0, AUX_X);
    const u32x4 xb = __builtin_amdgcn_raw_buffer_load_b128(rx, lane * 40 + 16, 0, AUX_X);
    const u32x2 xc = __builtin_amdgcn_raw_buffer_load_b64(rx, lane * 40 + 32, 0, AUX_X);
    auto front = [&](auto NN, rsrc_t rw, int row_bytes, int xoff, int rec) {
      constexpr int N = decltype(NN)::value;
      unsigned xr[N / 2];
      {
        const u32x4 t0 = __builtin_amdgcn_raw_buffer_load_b128(rx, bl * 1280 + g * (2 * N), xoff, AUX_X);
        xr[0] = t0[0]; xr[1] = t0[1]; xr[2] = t0[2]; xr[3] = t0[3];
        if constexpr (N == 10) xr[4] = __builtin_amdgcn_raw_buffer_load_b32(rx, bl * 1280 + g * (2 * N) + 16, xoff, AUX_X);
        else { const u32x4 t1 = __builtin_amdgcn_raw_buffer_load_b128(rx, bl * 1280 + g * (2 * N) + 16, xoff, AUX_X); xr[4] = t1[0]; xr[5] = t1[1]; xr[6] = t1[2]; xr[7] = t1[3]; }
      }
      FS(1);
      u32x4 wv[N];
      const int wvoff = g * N * row_bytes + 16 * j;
#pragma unroll
      for (int n = 0; n < N; ++n) wv[n] = ld_nt(rw, wvoff, n * row_bytes);
      SB();
      FS(2);
      float v[20];
#pragma unroll
      for (int e = 0; e < 4; ++e) { v[2 * e] = bflo(xa[e]); v[2 * e + 1] = bfhi(xa[e]); v[8 + 2 * e] = bflo(xb[e]); v[9 + 2 * e] = bfhi(xb[e]); }
      v[16] = bflo(xc[0]); v[17] = bfhi(xc[0]); v[18] = bflo(xc[1]); v[19] = bfhi(xc[1]);
      float sum = 0.f;
#pragma unroll
      for (int e = 0; e < 20; ++e) sum += v[e];
      sum = x16_sum(row_sum(sum));
      const float mean = sum * (1.f / 640.f);
      float sq = 0.f;
#pragma unroll
      for (int e = 0; e < 20; ++e) { const float d = v[e] - mean; sq = fmaf(d, d, sq); }
      sq = x16_sum(row_sum(sq));
      const float mb = rbf(mean), den = rbf(sqrtf(rbf(rbf(sq * (1.f / 640.f)) + 1e-5f)));
      const float inv = 1.f / den;
      const float mb0 = __builtin_bit_cast(float, __builtin_amdgcn_readlane(__builtin_bit_cast(int, mb), 0));
      const float mb1 = __builtin_bit_cast(float, __builtin_amdgcn_readlane(__builtin_bit_cast(int, mb), 32));
      const float iv0 = __builtin_bit_cast(float, __builtin_amdgcn_readlane(__builtin_bit_cast(int, inv), 0));
      const float iv1 = __builtin_bit_cast(float, __builtin_amdgcn_readlane(__builtin_bit_cast(int, inv), 32));
      const float mm = bl ? mb1 : mb0, ii = bl ? iv1 : iv0;
      FS(7);
      f32x4 acc0 = {0.f, 0.f, 0.f, 0.f}, acc1 = {0.f, 0.f, 0.f, 0.f};
#pragma unroll
      for (int n = 0; n < N; ++n) {
        const float xv = (n & 1) ? bfhi(xr[n >> 1]) : bflo(xr[n >> 1]);
        const float xn = rbf(xv - mm) * ii;
        const unsigned d = cvtpk(xn, xn), a0 = d & m0, a1 = d & m1;
        acc0 = mfma16(a0, a1, wv[n][0], wv[n][1], acc0);
        acc1 = mfma16(a0, a1, wv[n][2], wv[n][3], acc1);
      }
      float v0, v1;
      proj_epilogue(acc0, acc1, g & 1, v0, v1);
      st_pair(rws, st_voff, rec, v0, v1, ep + 1u);
    };
    if (w < 3) {
      front(std::integral_constant<int, 10>{}, mk_rsrc(wq + (size_t)(80 * j8) * 6144 + 192 * h + 64 * w, 79 * 12288 + 128), 12288, 160 * j8,
            WS_QP + (h * 8 + j8) * 3072 + w * 512);
    } else if (uq < 30) {
      const int r = uq / 6, lc = uq - 6 * r, mat = lc >= 3 ? 1 : 0, c3 = lc - 3 * mat;
      front(std::integral_constant<int, 16>{}, mk_rsrc((mat ? wu : wg) + (size_t)(128 * r) * 1536 + 192 * (h >> 2) + 64 * c3, 127 * 3072 + 128), 3072, 256 * r,
            WS_GU + (((h >> 2) * 5 + r) * 2 + mat) * 3072 + c3 * 512);
    }
    tag = ep + 1u;
  }
  STAMP(1); FS(8);
  asm volatile("s_waitcnt vmcnt(0)" ::: "memory");
  STAMP(11); FS(9);
  __builtin_amdgcn_s_sleep(QSLEEP);
  {
    u32x4 t[3];
    for (unsigned spin = 0;; ++spin) {
      MEMFENCE();
#pragma unroll
      for (int k = 0; k < 3; ++k) t[k] = ld_sc1(rwg, g * 3072 + 16 * j + 128 * k, WS_QP + h * 8 * 3072 + b * 1536 + w * 384);
      bool ok = true;
#pragma unroll
      for (int k = 0; k < 3; ++k) ok &= (t[k][1] == tag) & (t[k][3] == tag);
      if (GOK(__all(ok)) || spin > SPIN_CAP) { STAMPV(12, spin + 1); break; }
    }
#pragma unroll
    for (int k = 0; k < 3; ++k) {
      float a = __uint_as_float(t[k][0]), c = __uint_as_float(t[k][2]);
      a += dpp<ROR8>(a); c += dpp<ROR8>(c);
      a = x32_sum(x16_sum(a)); c = x32_sum(x16_sum(c));
      if (lane < 8) l_q[24 * w + 8 * k + j] = cvtpk(a, c);
    }
    if (tid == 0) *l_tag = tag;
  }
  STAMP(2); FS(10);
  WG_BARRIER();
  const int slot = 4 * j8 + w;
  const bool has_back = slot < 30;
  const int t3 = slot / 10, cc = slot - 10 * t3;
  if (PREWRITE) {
    if (w < 3 && lane < 32) st_pair(rws, lane * 16, WS_OP + ((h * 2 + b) * 4 + s) * 1536 + w * 512, 0.f, 0.f, tag - 1u);
    if (has_back) st_pair(rws, ((lane >> 4) & 1) * (96 * 512) + (8 * j + 4 * (lane >> 5) + 2 * (g & 1)) * 8, WS_YP + cc * 2 * 96 * 512 + (h * 3 + t3) * 512, 0.f, 0.f, tag - 1u);
    asm volatile("s_waitcnt vmcnt(0)" ::: "memory");
  }
  unsigned ha[2][2] = {{0u, 0u}, {0u, 0u}};
  if (has_back) {
    u32x4 t[3];
    const int so = WS_GU + (h >> 2) * 5 * 6144 + (h & 3) * 384 + t3 * 128;
    const int vo = bl * 1536 + 16 * g;
    for (unsigned spin = 0;; ++spin) {
      MEMFENCE();
      t[0] = ld_sc1(rwg, vo + e4 * 6144, so);
      t[1] = ld_sc1(rwg, vo + e4 * 6144 + 3072, so);
      t[2] = ld_sc1(rwg, vo + 4 * 6144 + (e4 & 1) * 3072, so);
      bool ok = true;
#pragma unroll
      for (int k = 0; k < 3; ++k) ok &= (t[k][1] == tag) & (t[k][3] == tag);
      if (GOK(__all(ok)) || spin > SPIN_CAP) { STAMPV(13, spin + 1); break; }
    }
#pragma unroll
    for (int z = 0; z < 2; ++z) {
      const float ex = __uint_as_float(t[2][2 * z]);
      const float gg = rbf(quad_sum(__uint_as_float(t[0][2 * z]) + (e4 == 0 ? ex : 0.f)));
      const float uu = rbf(quad_sum(__uint_as_float(t[1][2 * z]) + (e4 == 1 ? ex : 0.f)));
      const float zz = 0.7978845608028654f * (gg + 0.044715f * gg * gg * gg);
      const float ge = rbf(gg / (1.f + __builtin_amdgcn_exp2f(-2.8853900817779268f * zz)));
      const float hv = ge * uu;
      const unsigned d = cvtpk(hv, hv);
      ha[z][0] = d & m0; ha[z][1] = d & m1;
    }
  }
  const bool reducer = (w == 0) && (bid < 160);
  const int rb = bid / 80, rc = (bid - 80 * rb) >> 3, ro = bid & 7;
  unsigned xres = 0;
  if (reducer) xres = __builtin_amdgcn_raw_buffer_load_b32(rx, (lane & 3) * 4, rb * 1280 + rc * 128 + ro * 16, AUX_X);
  STAMP(3);
  WG_BARRIER();
  if (w == 0) {
    const int so = WS_ML + (h * 2 + b) * 256;
    if (lane < 4) { const f32x2 ml = *(const f32x2*)(l_ml + 2 * lane); st_pair(rws, lane * 16, so + s * 64, ml[0], ml[1], tag); }
    asm volatile("s_waitcnt vmcnt(0)" ::: "memory");
    __builtin_amdgcn_s_sleep(MLSLEEP);
    u32x4 t;
    for (unsigned spin = 0;; ++spin) {
      MEMFENCE();
      t = ld_sc1(rws, (lane & 15) * 16, so);
      if (__all((t[1] == tag) & (t[3] == tag)) || spin > SPIN_CAP) break;
    }
    const float mv = __uint_as_float(t[0]);
    const float M = row_max(mv);
    const float f = __builtin_amdgcn_exp2f(mv - M);
    const float L = row_sum(f * __uint_as_float(t[2]));
    if ((lane >> 2) == s) l_c[lane & 3] = f / L;
  }
  STAMP(4);
  WG_BARRIER();
  u32x4 wb[10];
  if (has_back) {
    const rsrc_t rd = mk_rsrc(wd + (size_t)(48 * h + 16 * t3) * 640 + 64 * cc, 15 * 1280 + 128);
    const rsrc_t ro_ = mk_rsrc(wo + (size_t)(192 * h + 64 * t3) * 640 + 64 * cc, 63 * 1280 + 128);
#pragma unroll
    for (int n = 0; n < 2; ++n) { wb[8 + n] = ld_nt(rd, g * 2560 + 16 * j + n * 1280, 0); SB(); if (PACE) __builtin_amdgcn_s_sleep(PACE); SB(); }
#pragma unroll
    for (int n = 0; n < 8; ++n) { wb[n] = ld_nt(ro_, g * 10240 + 16 * j, n * 1280); SB(); if (PACE && n < 7) __builtin_amdgcn_s_sleep(PACE); SB(); }
    SB();
  }
  STAMP(5);
  WG_BARRIER();
  WG_BARRIER();
  if (H1SLEEP) __builtin_amdgcn_s_sleep(H1SLEEP);
  STAMP(6);
  if (has_back) {
    u32x4 t[4];
    const int so = WS_OP + h * 8 * 1536 + t3 * 512;
    const int vo = (bl * 4 + e4) * 1536 + 16 * g;
    for (unsigned spin = 0;; ++spin) {
      MEMFENCE();
#pragma unroll
      for (int k = 0; k < 4; ++k) t[k] = ld_sc1(rws, vo + 128 * k, so);
      bool ok = true;
#pragma unroll
      for (int k = 0; k < 4; ++k) ok &= (t[k][1] == tag) & (t[k][3] == tag);
      if (__all(ok) || spin > SPIN_CAP) { STAMPV(14, spin + 1); break; }
    }
    STAMP(7);
    f32x4 acc0 = {0.f, 0.f, 0.f, 0.f}, acc1 = {0.f, 0.f, 0.f, 0.f};
#pragma unroll
    for (int z = 0; z < 2; ++z) {
      acc0 = mfma16(ha[z][0], ha[z][1], wb[8 + z][0], wb[8 + z][1], acc0);
      acc1 = mfma16(ha[z][0], ha[z][1], wb[8 + z][2], wb[8 + z][3], acc1);
    }
#pragma unroll
    for (int n = 0; n < 8; ++n) {
      const float ov = quad_sum(__uint_as_float(t[n >> 1][2 * (n & 1)]));
      const unsigned d = cvtpk(ov, ov), a0 = d & m0, a1 = d & m1;
      acc0 = mfma16(a0, a1, wb[n][0], wb[n][1], acc0);
      acc1 = mfma16(a0, a1, wb[n][2], wb[n][3], acc1);
    }
    float v0, v1;
    proj_epilogue(acc0, acc1, g & 1, v0, v1);
    st_pair(rws, ((lane >> 4) & 1) * (96 * 512) + (8 * j + 4 * (lane >> 5) + 2 * (g & 1)) * 8, WS_YP + cc * 2 * 96 * 512 + (h * 3 + t3) * 512, v0, v1, tag);
  }
  STAMP(8);
  if (reducer) {
    u32x4 t[6];
    const int so = WS_YP + (rc * 2 + rb) * 96 * 512 + ro * 64;
    const int vo = (lane >> 2) * 512 + (lane & 3) * 16;
    for (unsigned spin = 0;; ++spin) {
      MEMFENCE();
#pragma unroll
      for (int k = 0; k < 6; ++k) t[k] = ld_sc1(rws, vo, so + k * 16 * 512);
      bool ok = true;
#pragma unroll
      for (int k = 0; k < 6; ++k) ok &= (t[k][1] == tag) & (t[k][3] == tag);
      if (__all(ok) || spin > SPIN_CAP) { STAMPV(15, spin + 1); break; }
    }
    STAMP(9);
    float y0 = 0.f, y1 = 0.f;
#pragma unroll
    for (int k = 0; k < 6; ++k) { y0 += __uint_as_float(t[k][0]); y1 += __uint_as_float(t[k][2]); }
    y0 += dpp<ROR4>(y0); y1 += dpp<ROR4>(y1);
    y0 += dpp<ROR8>(y0); y1 += dpp<ROR8>(y1);
    y0 = x32_sum(x16_sum(y0)); y1 = x32_sum(x16_sum(y1));
    if (lane < 4) ((unsigned*)out)[rb * 320 + rc * 32 + ro * 4 + lane] = cvtpk(bflo(xres) + y0, bfhi(xres) + y1);
    if (bid == 0 && lane == 0) __builtin_amdgcn_raw_buffer_store_b32(tag, rws, 0, WS_EPOCH, AUX_SC1);
    STAMP(10);
  }
  STAMP_FLUSH();
}

extern "C" void kernel_launch(void* const* d_in, const int* in_sizes, int n_in, void* d_out, int out_size, void* d_ws, size_t ws_size,
                              hipStream_t stream) {
  fused_decode<<<256, 512, 0, stream>>>((const u16*)d_in[0], (const u16*)d_in[1], (const u16*)d_in[2], (const u16*)d_in[3], (const u16*)d_in[4],
                                        (const u16*)d_in[6], (const u16*)d_in[7], (const u16*)d_in[8], (u16*)d_out, (unsigned char*)d_ws + WS_BASE);
}
```

```cpp
#define B1_AT 48
#define AUX_W 0
#include <hip/hip_runtime.h>
#include <hip/hip_bf16.h>
#include <stdint.h>
#include <type_traits>

typedef unsigned short u16;
typedef unsigned u32x4 __attribute__((ext_vector_type(4)));
typedef unsigned u32x2 __attribute__((ext_vector_type(2)));
typedef float f32x4 __attribute__((ext_vector_type(4)));
typedef float f32x2 __attribute__((ext_vector_type(2)));
typedef short s16x4 __attribute__((ext_vector_type(4)));
typedef __bf16 bf16x8 __attribute__((ext_vector_type(8)));
typedef __bf16 bf16x2 __attribute__((ext_vector_type(2)));
typedef __amdgpu_buffer_rsrc_t rsrc_t;

#define WIN 8

#define CATCH 3
#define AUX_X 2
#define QSLEEP 16
#define MLSLEEP 32
#define PACE 3
#define PREWRITE 0
#define CODEPF 0
#define H1SLEEP 0
#define WS_BASE 0
#define AUX_K 2
#define AUX_V 2
constexpr int AUX_NT = 2, AUX_SC1 = 16;
constexpr int RSRC_W3 = 0x00020000;
constexpr unsigned SPIN_CAP = 1u << 18;

constexpr int WS_EPOCH = 0;
constexpr int WS_QP = 4096;
constexpr int WS_GU = WS_QP + 32 * 8 * 2 * 192 * 8;
constexpr int WS_ML = WS_GU + 8 * 5 * 2 * 2 * 192 * 8;
constexpr int WS_OP = WS_ML + 32 * 2 * 4 * 4 * 16;
constexpr int WS_YP = WS_OP + 32 * 2 * 4 * 192 * 8;
constexpr int WS_END = WS_YP + 10 * 2 * 96 * 64 * 8;

template <int I, int N, class F> __device__ __forceinline__ void static_for(F&& f) {
  if constexpr (I < N) { f(std::integral_constant<int, I>{}); static_for<I + 1, N>(f); }
}
__device__ __forceinline__ rsrc_t mk_rsrc(const void* p, int bytes) {
  return __builtin_amdgcn_make_buffer_rsrc(const_cast<void*>(p), 0, bytes, RSRC_W3);
}
__device__ __forceinline__ u32x4 ld_nt(rsrc_t r, int voff, int soff) { return __builtin_amdgcn_raw_buffer_load_b128(r, voff, soff, AUX_W); }
__device__ __forceinline__ u32x4 ld_sc1(rsrc_t r, int voff, int soff) { return __builtin_amdgcn_raw_buffer_load_b128(r, voff, soff, AUX_SC1); }
__device__ __forceinline__ void st_pair(rsrc_t r, int voff, int soff, float a, float b, unsigned tag) {
  u32x4 v = {__float_as_uint(a), tag, __float_as_uint(b), tag};
  __builtin_amdgcn_raw_buffer_store_b128(v, r, voff, soff, AUX_SC1);
}
__device__ __forceinline__ float bflo(unsigned v) { return __uint_as_float(v << 16); }
__device__ __forceinline__ float bfhi(unsigned v) { return __uint_as_float(v & 0xffff0000u); }
__device__ __forceinline__ unsigned cvtpk(float lo, float hi) {
  bf16x2 r = {(__bf16)lo, (__bf16)hi};
  return __builtin_bit_cast(unsigned, r);
}
__device__ __forceinline__ float rbf(float x) { return (float)(__bf16)x; }
template <int CTRL> __device__ __forceinline__ float dpp(float x) {
  return __builtin_bit_cast(float, __builtin_amdgcn_mov_dpp(__builtin_bit_cast(int, x), CTRL, 0xf, 0xf, true));
}
constexpr int XOR1 = 0xB1, XOR2 = 0x4E, HMIR = 0x141, ROR8 = 0x128, ROR4 = 0x124;
__device__ __forceinline__ float x16_sum(float x) { auto s = __builtin_amdgcn_permlane16_swap(__float_as_uint(x), __float_as_uint(x), false, false); return __uint_as_float(s[0]) + __uint_as_float(s[1]); }
__device__ __forceinline__ float x32_sum(float x) { auto s = __builtin_amdgcn_permlane32_swap(__float_as_uint(x), __float_as_uint(x), false, false); return __uint_as_float(s[0]) + __uint_as_float(s[1]); }
__device__ __forceinline__ float x16_max(float x) { auto s = __builtin_amdgcn_permlane16_swap(__float_as_uint(x), __float_as_uint(x), false, false); return fmaxf(__uint_as_float(s[0]), __uint_as_float(s[1])); }
__device__ __forceinline__ float x32_max(float x) { auto s = __builtin_amdgcn_permlane32_swap(__float_as_uint(x), __float_as_uint(x), false, false); return fmaxf(__uint_as_float(s[0]), __uint_as_float(s[1])); }
__device__ __forceinline__ float row_sum(float x) { x += dpp<XOR1>(x); x += dpp<XOR2>(x); x += dpp<HMIR>(x); x += dpp<ROR8>(x); return x; }
__device__ __forceinline__ float row_max(float x) { x = fmaxf(x, dpp<XOR1>(x)); x = fmaxf(x, dpp<XOR2>(x)); x = fmaxf(x, dpp<HMIR>(x)); x = fmaxf(x, dpp<ROR8>(x)); return x; }
__device__ __forceinline__ float quad_sum(float x) { x += dpp<XOR1>(x); x += dpp<XOR2>(x); return x; }
__device__ __forceinline__ float dot2(unsigned a, unsigned b, float c) {
  return __builtin_amdgcn_fdot2_f32_bf16(__builtin_bit_cast(bf16x2, a), __builtin_bit_cast(bf16x2, b), c, false);
}
__device__ __forceinline__ f32x4 mfma16(unsigned a0, unsigned a1, unsigned b0, unsigned b1, f32x4 c) {
  u32x2 a = {a0, a1}, b = {b0, b1};
  return __builtin_amdgcn_mfma_f32_16x16x16bf16_1k(__builtin_bit_cast(s16x4, a), __builtin_bit_cast(s16x4, b), c, 0, 0, 0);
}
__device__ __forceinline__ f32x4 mfma32(u32x4 a, u32x4 b, f32x4 c) {
  return __builtin_amdgcn_mfma_f32_16x16x32_bf16(__builtin_bit_cast(bf16x8, a), __builtin_bit_cast(bf16x8, b), c, 0, 0, 0);
}
#define SB() __builtin_amdgcn_sched_barrier(0)
#define WG_BARRIER() do { asm volatile("s_waitcnt lgkmcnt(0)\n\ts_barrier" ::: "memory"); } while (0)
#define MEMFENCE() asm volatile("" ::: "memory")

#define FS(i)
#define STAMP(i)
#define STAMPV(i, v)
#define STAMP_DECL
#define STAMP_FLUSH()

__device__ __forceinline__ void proj_epilogue(f32x4 acc0, f32x4 acc1, bool z, float& v0, float& v1) {
  float n0[4], n1[4];
#pragma unroll
  for (int r = 0; r < 4; ++r) {
    auto s = __builtin_amdgcn_permlane32_swap(__float_as_uint(acc0[r]), __float_as_uint(acc1[r]), false, false);
    n0[r] = __uint_as_float(s[0]); n1[r] = __uint_as_float(s[1]);
  }
  const float r0 = n0[0] + dpp<ROR8>(n1[0]), r1 = n0[1] + dpp<ROR8>(n1[1]);
  const float r2 = n1[2] + dpp<ROR8>(n0[2]), r3 = n1[3] + dpp<ROR8>(n0[3]);
  v0 = z ? r2 : r0; v1 = z ? r3 : r1;
}

__global__ void __launch_bounds__(512, 2)
fused_decode(const u16* __restrict__ x, const u16* __restrict__ kc, const u16* __restrict__ vc, const u16* __restrict__ wq,
             const u16* __restrict__ wo, const u16* __restrict__ wg, const u16* __restrict__ wu, const u16* __restrict__ wd,
             u16* __restrict__ out, unsigned char* __restrict__ ws) {
  __shared__ __attribute__((aligned(16))) float lds[96 + 8 + 4 + 4 + 8 * 192];
  unsigned* l_q = (unsigned*)lds;
  float* l_ml = lds + 96;
  float* l_c = lds + 104;
  unsigned* l_tag = (unsigned*)(lds + 108);
  float* l_acc = lds + 112;

  STAMP_DECL;
  STAMP(0);
  asm volatile("" ::"s"(x), "s"(kc), "s"(vc), "s"(wq), "s"(wo), "s"(wg), "s"(wu), "s"(wd), "s"(out), "s"(ws));
  const int bid = blockIdx.x;
  const int unit = (bid & 7) * 32 + (bid >> 3);
  const int h = unit >> 3, j8 = unit & 7, b = j8 >> 2, s = j8 & 3;
  const int tid = threadIdx.x, lane = tid & 63, wave = __builtin_amdgcn_readfirstlane(tid >> 6);
  const int g = lane >> 3, j = lane & 7;
  const rsrc_t rws = mk_rsrc(ws, WS_END);
  const rsrc_t rwg = rws;
#define GOK(x) (x)

  if (CODEPF) {
    if (wave == 3) {
      unsigned long long pc; asm volatile("s_getpc_b64 %0" : "=s"(pc));
      const rsrc_t rc_ = mk_rsrc((const void*)(pc & ~255ull), 14848);
      unsigned d0 = __builtin_amdgcn_raw_buffer_load_b32(rc_, lane * 128, 0, CODEPF == 2 ? AUX_NT : 0), d1 = __builtin_amdgcn_raw_buffer_load_b32(rc_, lane * 128, 8192, CODEPF == 2 ? AUX_NT : 0);
      asm volatile("" ::"v"(d0), "v"(d1));
    }
  }
  if (wave >= 4) {
    const int sw = wave - 4;
    const size_t kvoff = ((size_t)(b * 32 + h) * 8192 + 6144 + 512 * s) * 192;
    const rsrc_t rk = mk_rsrc(kc + kvoff, 512 * 384), rv = mk_rsrc(vc + kvoff, 512 * 384);
    const int voff = (sw * 8 + g) * 384 + 16 * j;
    u32x4 buf[96];
    u32x4 qv[3];
    float sc[16];
    unsigned pd[16];
    f32x4 acc[3] = {{0.f, 0.f, 0.f, 0.f}, {0.f, 0.f, 0.f, 0.f}, {0.f, 0.f, 0.f, 0.f}};
    unsigned msk[4];
#pragma unroll
    for (int d = 0; d < 4; ++d) msk[d] = (j >> 1) == d ? ((j & 1) ? 0xffff0000u : 0x0000ffffu) : 0u;
    constexpr float C2 = 0.07216878364870322f * 1.4426950408889634f;

    auto consume = [&](auto G) {
      constexpr int t = decltype(G)::value;
      if constexpr (t < 16) {
        float a0 = 0.f, a1 = 0.f, a2 = 0.f;
#pragma unroll
        for (int e = 0; e < 4; ++e) { a0 = dot2(buf[3 * t][e], qv[0][e], a0); a1 = dot2(buf[3 * t + 1][e], qv[1][e], a1); a2 = dot2(buf[3 * t + 2][e], qv[2][e], a2); }
        float a = (a0 + a1) + a2;
        a += dpp<XOR1>(a); a += dpp<XOR2>(a); a += dpp<HMIR>(a);
        sc[t] = a;
        if constexpr (t == 15) {
          float m = sc[0];
#pragma unroll
          for (int u = 1; u < 16; ++u) m = fmaxf(m, sc[u]);
          m = fmaxf(m, dpp<ROR8>(m)); m = x16_max(m); m = x32_max(m);
          const float mc = m * C2;
          float l = 0.f;
#pragma unroll
          for (int u = 0; u < 16; ++u) { const float p = __builtin_amdgcn_exp2f(fmaf(sc[u], C2, -mc)); pd[u] = cvtpk(p, p); l += bfhi(pd[u]); }
          l += dpp<ROR8>(l); l = x16_sum(l); l = x32_sum(l);
          if (lane == 0) *(f32x2*)(l_ml + 2 * sw) = f32x2{mc, l};
          STAMP(3);
          WG_BARRIER();
        }
      } else {
        constexpr int u = t - 16;
        const u32x4 a = {pd[u] & msk[0], pd[u] & msk[1], pd[u] & msk[2], pd[u] & msk[3]};
#pragma unroll
        for (int i = 0; i < 3; ++i) acc[i] = mfma32(a, buf[3 * t + i], acc[i]);
      }
    };
    constexpr auto done_after = [](int n) constexpr {
      if (n < B1_AT) return 0;
      const int landed = (n - WIN + 1) / 3;
      const int cu = 1 + (CATCH * (n - B1_AT)) / 3;
      return landed < cu ? landed : cu;
    };
    static_for<0, 96>([&](auto N) {
      constexpr int n = decltype(N)::value;
      if constexpr (n == B1_AT) {
        STAMP(1);
        WG_BARRIER();
        STAMP(2);
#pragma unroll
        for (int i = 0; i < 3; ++i) qv[i] = *(const u32x4*)(l_q + 32 * i + 4 * j);
      }
      if constexpr (n == 0) FS(1);
      if constexpr (n == WIN) FS(2);
      if constexpr (n >= WIN) asm volatile("s_waitcnt vmcnt(%0)" ::"i"(WIN - 1) : "memory");
      if constexpr (n >= WIN && n < WIN + 45 && (n - WIN) % 5 == 0) FS(3 + (n - WIN) / 5);
      constexpr int m = n < 48 ? n : n - 48;
      buf[n] = __builtin_amdgcn_raw_buffer_load_b128(n < 48 ? rk : rv, voff + (m % 3) * 128, (m / 3) * 12288, n < 48 ? AUX_K : AUX_V);
      SB();
      static_for<(n > 0 ? done_after(n - 1) : 0), done_after(n)>(consume);
      SB();
    });
    STAMP(4);
    WG_BARRIER();
    static_for<done_after(95), 32>([&](auto G) { consume(G); SB(); });
    if ((lane >> 5) == (g & 1)) {
      float* dst = l_acc + (sw * 2 + (g & 1)) * 192 + 8 * j + 4 * ((lane >> 4) & 1);
#pragma unroll
      for (int i = 0; i < 3; ++i) *(f32x4*)(dst + 64 * i) = acc[i];
    }
    STAMP(5);
    WG_BARRIER();
    if (sw < 3 && lane < 32) {
      const unsigned tag = *l_tag;
      const f32x4 c = *(const f32x4*)l_c;
      const float* src = l_acc + 64 * sw + 8 * j + 2 * g;
      float o0 = 0.f, o1 = 0.f;
#pragma unroll
      for (int w = 0; w < 4; ++w) {
        const f32x2 u = *(const f32x2*)(src + (2 * w) * 192), v = *(const f32x2*)(src + (2 * w + 1) * 192);
        o0 = fmaf(c[w], u[0] + v[0], o0); o1 = fmaf(c[w], u[1] + v[1], o1);
      }
      st_pair(rws, lane * 16, WS_OP + ((h * 2 + b) * 4 + s) * 1536 + sw * 512, o0, o1, tag);
    }
    STAMP(6);
    asm volatile("s_waitcnt vmcnt(0)" ::: "memory");
    STAMP(7);
    WG_BARRIER();
    STAMP_FLUSH();
    return;
  }

  __builtin_amdgcn_s_setprio(2);
  const int w = wave;
  const int bl = (lane >> 2) & 1, e4 = lane & 3;
  const unsigned m0 = e4 == 0 ? 0x0000ffffu : e4 == 1 ? 0xffff0000u : 0u;
  const unsigned m1 = e4 == 2 ? 0x0000ffffu : e4 == 3 ? 0xffff0000u : 0u;
  const int st_voff = ((lane >> 4) & 1) * 1536 + (8 * j + 4 * (lane >> 5) + 2 * (g & 1)) * 8;
  const rsrc_t rx = mk_rsrc(x, 2560);
  const int uq = unit & 31;
  unsigned tag;
  {
    const unsigned ep = *(const __attribute__((address_space(4))) unsigned*)(ws + WS_EPOCH);
    const u32x4 xa = __builtin_amdgcn_raw_buffer_load_b128(rx, lane * 40, 0, AUX_X);
    const u32x4 xb = __builtin_amdgcn_raw_buffer_load_b128(rx, lane * 40 + 16, 0, AUX_X);
    const u32x2 xc = __builtin_amdgcn_raw_buffer_load_b64(rx, lane * 40 + 32, 0, AUX_X);
    auto front = [&](auto NN, rsrc_t rw, int row_bytes, int xoff, int rec) {
      constexpr int N = decltype(NN)::value;
      unsigned xr[N / 2];
      {
        const u32x4 t0 = __builtin_amdgcn_raw_buffer_load_b128(rx, bl * 1280 + g * (2 * N), xoff, AUX_X);
        xr[0] = t0[0]; xr[1] = t0[1]; xr[2] = t0[2]; xr[3] = t0[3];
        if constexpr (N == 10) xr[4] = __builtin_amdgcn_raw_buffer_load_b32(rx, bl * 1280 + g * (2 * N) + 16, xoff, AUX_X);
        else { const u32x4 t1 = __builtin_amdgcn_raw_buffer_load_b128(rx, bl * 1280 + g * (2 * N) + 16, xoff, AUX_X); xr[4] = t1[0]; xr[5] = t1[1]; xr[6] = t1[2]; xr[7] = t1[3]; }
      }
      FS(1);
      u32x4 wv[N];
      const int wvoff = g * N * row_bytes + 16 * j;
#pragma unroll
      for (int n = 0; n < N; ++n) wv[n] = ld_nt(rw, wvoff, n * row_bytes);
      SB();
      FS(2);
      float v[20];
#pragma unroll
      for (int e = 0; e < 4; ++e) { v[2 * e] = bflo(xa[e]); v[2 * e + 1] = bfhi(xa[e]); v[8 + 2 * e] = bflo(xb[e]); v[9 + 2 * e] = bfhi(xb[e]); }
      v[16] = bflo(xc[0]); v[17] = bfhi(xc[0]); v[18] = bflo(xc[1]); v[19] = bfhi(xc[1]);
      float sum = 0.f;
#pragma unroll
      for (int e = 0; e < 20; ++e) sum += v[e];
      sum = x16_sum(row_sum(sum));
      const float mean = sum * (1.f / 640.f);
      float sq = 0.f;
#pragma unroll
      for (int e = 0; e < 20; ++e) { const float d = v[e] - mean; sq = fmaf(d, d, sq); }
      sq = x16_sum(row_sum(sq));
      const float mb = rbf(mean), den = rbf(sqrtf(rbf(rbf(sq * (1.f / 640.f)) + 1e-5f)));
      const float inv = 1.f / den;
      const float mb0 = __builtin_bit_cast(float, __builtin_amdgcn_readlane(__builtin_bit_cast(int, mb), 0));
      const float mb1 = __builtin_bit_cast(float, __builtin_amdgcn_readlane(__builtin_bit_cast(int, mb), 32));
      const float iv0 = __builtin_bit_cast(float, __builtin_amdgcn_readlane(__builtin_bit_cast(int, inv), 0));
      const float iv1 = __builtin_bit_cast(float, __builtin_amdgcn_readlane(__builtin_bit_cast(int, inv), 32));
      const float mm = bl ? mb1 : mb0, ii = bl ? iv1 : iv0;
      FS(7);
      f32x4 acc0 = {0.f, 0.f, 0.f, 0.f}, acc1 = {0.f, 0.f, 0.f, 0.f};
#pragma unroll
      for (int n = 0; n < N; ++n) {
        const float xv = (n & 1) ? bfhi(xr[n >> 1]) : bflo(xr[n >> 1]);
        const float xn = rbf(xv - mm) * ii;
        const unsigned d = cvtpk(xn, xn), a0 = d & m0, a1 = d & m1;
        acc0 = mfma16(a0, a1, wv[n][0], wv[n][1], acc0);
        acc1 = mfma16(a0, a1, wv[n][2], wv[n][3], acc1);
      }
      float v0, v1;
      proj_epilogue(acc0, acc1, g & 1, v0, v1);
      st_pair(rws, st_voff, rec, v0, v1, ep + 1u);
    };
    if (w < 3) {
      front(std::integral_constant<int, 10>{}, mk_rsrc(wq + (size_t)(80 * j8) * 6144 + 192 * h + 64 * w, 79 * 12288 + 128), 12288, 160 * j8,
            WS_QP + (h * 8 + j8) * 3072 + w * 512);
    } else if (uq < 30) {
      const int r = uq / 6, lc = uq - 6 * r, mat = lc >= 3 ? 1 : 0, c3 = lc - 3 * mat;
      front(std::integral_constant<int, 16>{}, mk_rsrc((mat ? wu : wg) + (size_t)(128 * r) * 1536 + 192 * (h >> 2) + 64 * c3, 127 * 3072 + 128), 3072, 256 * r,
            WS_GU + (((h >> 2) * 5 + r) * 2 + mat) * 3072 + c3 * 512);
    }
    tag = ep + 1u;
  }
  STAMP(1); FS(8);
  asm volatile("s_waitcnt vmcnt(0)" ::: "memory");
  STAMP(11); FS(9);
  __builtin_amdgcn_s_sleep(QSLEEP);
  {
    u32x4 t[3];
    for (unsigned spin = 0;; ++spin) {
      MEMFENCE();
#pragma unroll
      for (int k = 0; k < 3; ++k) t[k] = ld_sc1(rwg, g * 3072 + 16 * j + 128 * k, WS_QP + h * 8 * 3072 + b * 1536 + w * 384);
      bool ok = true;
#pragma unroll
      for (int k = 0; k < 3; ++k) ok &= (t[k][1] == tag) & (t[k][3] == tag);
      if (GOK(__all(ok)) || spin > SPIN_CAP) { STAMPV(12, spin + 1); break; }
    }
#pragma unroll
    for (int k = 0; k < 3; ++k) {
      float a = __uint_as_float(t[k][0]), c = __uint_as_float(t[k][2]);
      a += dpp<ROR8>(a); c += dpp<ROR8>(c);
      a = x32_sum(x16_sum(a)); c = x32_sum(x16_sum(c));
      if (lane < 8) l_q[24 * w + 8 * k + j] = cvtpk(a, c);
    }
    if (tid == 0) *l_tag = tag;
  }
  STAMP(2); FS(10);
  WG_BARRIER();
  const int slot = 4 * j8 + w;
  const bool has_back = slot < 30;
  const int t3 = slot / 10, cc = slot - 10 * t3;
  if (PREWRITE) {
    if (w < 3 && lane < 32) st_pair(rws, lane * 16, WS_OP + ((h * 2 + b) * 4 + s) * 1536 + w * 512, 0.f, 0.f, tag - 1u);
    if (has_back) st_pair(rws, ((lane >> 4) & 1) * (96 * 512) + (8 * j + 4 * (lane >> 5) + 2 * (g & 1)) * 8, WS_YP + cc * 2 * 96 * 512 + (h * 3 + t3) * 512, 0.f, 0.f, tag - 1u);
    asm volatile("s_waitcnt vmcnt(0)" ::: "memory");
  }
  unsigned ha[2][2] = {{0u, 0u}, {0u, 0u}};
  if (has_back) {
    u32x4 t[3];
    const int so = WS_GU + (h >> 2) * 5 * 6144 + (h & 3) * 384 + t3 * 128;
    const int vo = bl * 1536 + 16 * g;
    for (unsigned spin = 0;; ++spin) {
      MEMFENCE();
      t[0] = ld_sc1(rwg, vo + e4 * 6144, so);
      t[1] = ld_sc1(rwg, vo + e4 * 6144 + 3072, so);
      t[2] = ld_sc1(rwg, vo + 4 * 6144 + (e4 & 1) * 3072, so);
      bool ok = true;
#pragma unroll
      for (int k = 0; k < 3; ++k) ok &= (t[k][1] == tag) & (t[k][3] == tag);
      if (GOK(__all(ok)) || spin > SPIN_CAP) { STAMPV(13, spin + 1); break; }
    }
#pragma unroll
    for (int z = 0; z < 2; ++z) {
      const float ex = __uint_as_float(t[2][2 * z]);
      const float gg = rbf(quad_sum(__uint_as_float(t[0][2 * z]) + (e4 == 0 ? ex : 0.f)));
      const float uu = rbf(quad_sum(__uint_as_float(t[1][2 * z]) + (e4 == 1 ? ex : 0.f)));
      const float zz = 0.7978845608028654f * (gg + 0.044715f * gg * gg * gg);
      const float ge = rbf(gg / (1.f + __builtin_amdgcn_exp2f(-2.8853900817779268f * zz)));
      const float hv = ge * uu;
      const unsigned d = cvtpk(hv, hv);
      ha[z][0] = d & m0; ha[z][1] = d & m1;
    }
  }
  const bool reducer = (w == 0) && (bid < 160);
  const int rb = bid / 80, rc = (bid - 80 * rb) >> 3, ro = bid & 7;
  unsigned xres = 0;
  if (reducer) xres = __builtin_amdgcn_raw_buffer_load_b32(rx, (lane & 3) * 4, rb * 1280 + rc * 128 + ro * 16, AUX_X);
  STAMP(3);
  WG_BARRIER();
  if (w == 0) {
    const int so = WS_ML + (h * 2 + b) * 256;
    if (lane < 4) { const f32x2 ml = *(const f32x2*)(l_ml + 2 * lane); st_pair(rws, lane * 16, so + s * 64, ml[0], ml[1], tag); }
    asm volatile("s_waitcnt vmcnt(0)" ::: "memory");
    __builtin_amdgcn_s_sleep(MLSLEEP);
    u32x4 t;
    for (unsigned spin = 0;; ++spin) {
      MEMFENCE();
      t = ld_sc1(rws, (lane & 15) * 16, so);
      if (__all((t[1] == tag) & (t[3] == tag)) || spin > SPIN_CAP) break;
    }
    const float mv = __uint_as_float(t[0]);
    const float M = row_max(mv);
    const float f = __builtin_amdgcn_exp2f(mv - M);
    const float L = row_sum(f * __uint_as_float(t[2]));
    if ((lane >> 2) == s) l_c[lane & 3] = f / L;
  }
  STAMP(4);
  WG_BARRIER();
  u32x4 wb[10];
  if (has_back) {
    const rsrc_t rd = mk_rsrc(wd + (size_t)(48 * h + 16 * t3) * 640 + 64 * cc, 15 * 1280 + 128);
    const rsrc_t ro_ = mk_rsrc(wo + (size_t)(192 * h + 64 * t3) * 640 + 64 * cc, 63 * 1280 + 128);
#pragma unroll
    for (int n = 0; n < 2; ++n) { wb[8 + n] = ld_nt(rd, g * 2560 + 16 * j + n * 1280, 0); SB(); if (PACE) __builtin_amdgcn_s_sleep(PACE); SB(); }
#pragma unroll
    for (int n = 0; n < 8; ++n) { wb[n] = ld_nt(ro_, g * 10240 + 16 * j, n * 1280); SB(); if (PACE && n < 7) __builtin_amdgcn_s_sleep(PACE); SB(); }
    SB();
  }
  STAMP(5);
  WG_BARRIER();
  WG_BARRIER();
  if (H1SLEEP) __builtin_amdgcn_s_sleep(H1SLEEP);
  STAMP(6);
  if (has_back) {
    u32x4 t[4];
    const int so = WS_OP + h * 8 * 1536 + t3 * 512;
    const int vo = (bl * 4 + e4) * 1536 + 16 * g;
    for (unsigned spin = 0;; ++spin) {
      MEMFENCE();
#pragma unroll
      for (int k = 0; k < 4; ++k) t[k] = ld_sc1(rws, vo + 128 * k, so);
      bool ok = true;
#pragma unroll
      for (int k = 0; k < 4; ++k) ok &= (t[k][1] == tag) & (t[k][3] == tag);
      if (__all(ok) || spin > SPIN_CAP) { STAMPV(14, spin + 1); break; }
    }
    STAMP(7);
    f32x4 acc0 = {0.f, 0.f, 0.f, 0.f}, acc1 = {0.f, 0.f, 0.f, 0.f};
#pragma unroll
    for (int z = 0; z < 2; ++z) {
      acc0 = mfma16(ha[z][0], ha[z][1], wb[8 + z][0], wb[8 + z][1], acc0);
      acc1 = mfma16(ha[z][0], ha[z][1], wb[8 + z][2], wb[8 + z][3], acc1);
    }
#pragma unroll
    for (int n = 0; n < 8; ++n) {
      const float ov = quad_sum(__uint_as_float(t[n >> 1][2 * (n & 1)]));
      const unsigned d = cvtpk(ov, ov), a0 = d & m0, a1 = d & m1;
      acc0 = mfma16(a0, a1, wb[n][0], wb[n][1], acc0);
      acc1 = mfma16(a0, a1, wb[n][2], wb[n][3], acc1);
    }
    float v0, v1;
    proj_epilogue(acc0, acc1, g & 1, v0, v1);
    st_pair(rws, ((lane >> 4) & 1) * (96 * 512) + (8 * j + 4 * (lane >> 5) + 2 * (g & 1)) * 8, WS_YP + cc * 2 * 96 * 512 + (h * 3 + t3) * 512, v0, v1, tag);
  }
  STAMP(8);
  if (reducer) {
    u32x4 t[6];
    const int so = WS_YP + (rc * 2 + rb) * 96 * 512 + ro * 64;
    const int vo = (lane >> 2) * 512 + (lane & 3) * 16;
    for (unsigned spin = 0;; ++spin) {
      MEMFENCE();
#pragma unroll
      for (int k = 0; k < 6; ++k) t[k] = ld_sc1(rws, vo, so + k * 16 * 512);
      bool ok = true;
#pragma unroll
      for (int k = 0; k < 6; ++k) ok &= (t[k][1] == tag) & (t[k][3] == tag);
      if (__all(ok) || spin > SPIN_CAP) { STAMPV(15, spin + 1); break; }
    }
    STAMP(9);
    float y0 = 0.f, y1 = 0.f;
#pragma unroll
    for (int k = 0; k < 6; ++k) { y0 += __uint_as_float(t[k][0]); y1 += __uint_as_float(t[k][2]); }
    y0 += dpp<ROR4>(y0); y1 += dpp<ROR4>(y1);
    y0 += dpp<ROR8>(y0); y1 += dpp<ROR8>(y1);
    y0 = x32_sum(x16_sum(y0)); y1 = x32_sum(x16_sum(y1));
    if (lane < 4) ((unsigned*)out)[rb * 320 + rc * 32 + ro * 4 + lane] = cvtpk(bflo(xres) + y0, bfhi(xres) + y1);
    if (bid == 0 && lane == 0) __builtin_amdgcn_raw_buffer_store_b32(tag, rws, 0, WS_EPOCH, AUX_SC1);
    STAMP(10);
  }
  STAMP_FLUSH();
}

extern "C" void kernel_launch(void* const* d_in, const int* in_sizes, int n_in, void* d_out, int out_size, void* d_ws, size_t ws_size,
                              hipStream_t stream) {
  fused_decode<<<256, 512, 0, stream>>>((const u16*)d_in[0], (const u16*)d_in[1], (const u16*)d_in[2], (const u16*)d_in[3], (const u16*)d_in[4],
                                        (const u16*)d_in[6], (const u16*)d_in[7], (const u16*)d_in[8], (u16*)d_out, (unsigned char*)d_ws + WS_BASE);
}
```

```cpp
#define B1_AT 48
#define PROBE 0
#include <hip/hip_runtime.h>
#include <hip/hip_bf16.h>
#include <stdint.h>
#include <type_traits>

typedef unsigned short u16;
typedef unsigned u32x4 __attribute__((ext_vector_type(4)));
typedef unsigned u32x2 __attribute__((ext_vector_type(2)));
typedef float f32x4 __attribute__((ext_vector_type(4)));
typedef float f32x2 __attribute__((ext_vector_type(2)));
typedef short s16x4 __attribute__((ext_vector_type(4)));
typedef __bf16 bf16x8 __attribute__((ext_vector_type(8)));
typedef __bf16 bf16x2 __attribute__((ext_vector_type(2)));
typedef __amdgpu_buffer_rsrc_t rsrc_t;

#define WIN 8

#define CATCH 3
#define AUX_X 2
#define QSLEEP 16
#define MLSLEEP 32
#define PACE 3
#define PREWRITE 0
#define CODEPF 0
#define H1SLEEP 0
#define WS_BASE 0
#define AUX_K 2
#define AUX_V 2
#define AUX_W 2
constexpr int AUX_NT = 2, AUX_SC1 = 16;
constexpr int RSRC_W3 = 0x00020000;
constexpr unsigned SPIN_CAP = 1u << 18;

constexpr int WS_EPOCH = 0;
constexpr int WS_QP = 4096;
constexpr int WS_GU = WS_QP + 32 * 8 * 2 * 192 * 8;
constexpr int WS_ML = WS_GU + 8 * 5 * 2 * 2 * 192 * 8;
constexpr int WS_OP = WS_ML + 32 * 2 * 4 * 4 * 16;
constexpr int WS_YP = WS_OP + 32 * 2 * 4 * 192 * 8;
constexpr int WS_END = WS_YP + 10 * 2 * 96 * 64 * 8;

template <int I, int N, class F> __device__ __forceinline__ void static_for(F&& f) {
  if constexpr (I < N) { f(std::integral_constant<int, I>{}); static_for<I + 1, N>(f); }
}
__device__ __forceinline__ rsrc_t mk_rsrc(const void* p, int bytes) {
  return __builtin_amdgcn_make_buffer_rsrc(const_cast<void*>(p), 0, bytes, RSRC_W3);
}
__device__ __forceinline__ u32x4 ld_nt(rsrc_t r, int voff, int soff) { return __builtin_amdgcn_raw_buffer_load_b128(r, voff, soff, AUX_W); }
__device__ __forceinline__ u32x4 ld_sc1(rsrc_t r, int voff, int soff) { return __builtin_amdgcn_raw_buffer_load_b128(r, voff, soff, AUX_SC1); }
__device__ __forceinline__ void st_pair(rsrc_t r, int voff, int soff, float a, float b, unsigned tag) {
  u32x4 v = {__float_as_uint(a), tag, __float_as_uint(b), tag};
  __builtin_amdgcn_raw_buffer_store_b128(v, r, voff, soff, AUX_SC1);
}
__device__ __forceinline__ float bflo(unsigned v) { return __uint_as_float(v << 16); }
__device__ __forceinline__ float bfhi(unsigned v) { return __uint_as_float(v & 0xffff0000u); }
__device__ __forceinline__ unsigned cvtpk(float lo, float hi) {
  bf16x2 r = {(__bf16)lo, (__bf16)hi};
  return __builtin_bit_cast(unsigned, r);
}
__device__ __forceinline__ float rbf(float x) { return (float)(__bf16)x; }
template <int CTRL> __device__ __forceinline__ float dpp(float x) {
  return __builtin_bit_cast(float, __builtin_amdgcn_mov_dpp(__builtin_bit_cast(int, x), CTRL, 0xf, 0xf, true));
}
constexpr int XOR1 = 0xB1, XOR2 = 0x4E, HMIR = 0x141, ROR8 = 0x128, ROR4 = 0x124;
__device__ __forceinline__ float x16_sum(float x) { auto s = __builtin_amdgcn_permlane16_swap(__float_as_uint(x), __float_as_uint(x), false, false); return __uint_as_float(s[0]) + __uint_as_float(s[1]); }
__device__ __forceinline__ float x32_sum(float x) { auto s = __builtin_amdgcn_permlane32_swap(__float_as_uint(x), __float_as_uint(x), false, false); return __uint_as_float(s[0]) + __uint_as_float(s[1]); }
__device__ __forceinline__ float x16_max(float x) { auto s = __builtin_amdgcn_permlane16_swap(__float_as_uint(x), __float_as_uint(x), false, false); return fmaxf(__uint_as_float(s[0]), __uint_as_float(s[1])); }
__device__ __forceinline__ float x32_max(float x) { auto s = __builtin_amdgcn_permlane32_swap(__float_as_uint(x), __float_as_uint(x), false, false); return fmaxf(__uint_as_float(s[0]), __uint_as_float(s[1])); }
__device__ __forceinline__ float row_sum(float x) { x += dpp<XOR1>(x); x += dpp<XOR2>(x); x += dpp<HMIR>(x); x += dpp<ROR8>(x); return x; }
__device__ __forceinline__ float row_max(float x) { x = fmaxf(x, dpp<XOR1>(x)); x = fmaxf(x, dpp<XOR2>(x)); x = fmaxf(x, dpp<HMIR>(x)); x = fmaxf(x, dpp<ROR8>(x)); return x; }
__device__ __forceinline__ float quad_sum(float x) { x += dpp<XOR1>(x); x += dpp<XOR2>(x); return x; }
__device__ __forceinline__ float dot2(unsigned a, unsigned b, float c) {
  return __builtin_amdgcn_fdot2_f32_bf16(__builtin_bit_cast(bf16x2, a), __builtin_bit_cast(bf16x2, b), c, false);
}
__device__ __forceinline__ f32x4 mfma16(unsigned a0, unsigned a1, unsigned b0, unsigned b1, f32x4 c) {
  u32x2 a = {a0, a1}, b = {b0, b1};
  return __builtin_amdgcn_mfma_f32_16x16x16bf16_1k(__builtin_bit_cast(s16x4, a), __builtin_bit_cast(s16x4, b), c, 0, 0, 0);
}
__device__ __forceinline__ f32x4 mfma32(u32x4 a, u32x4 b, f32x4 c) {
  return __builtin_amdgcn_mfma_f32_16x16x32_bf16(__builtin_bit_cast(bf16x8, a), __builtin_bit_cast(bf16x8, b), c, 0, 0, 0);
}
#define SB() __builtin_amdgcn_sched_barrier(0)
#define WG_BARRIER() do { asm volatile("s_waitcnt lgkmcnt(0)\n\ts_barrier" ::: "memory"); } while (0)
#define MEMFENCE() asm volatile("" ::: "memory")

#define FS(i)
#define STAMP(i)
#define STAMPV(i, v)
#define STAMP_DECL
#define STAMP_FLUSH()

#define PB(i) do { if ((i) == PROBE) { SB(); unsigned long long t_; asm volatile("s_memrealtime %0\n\ts_waitcnt lgkmcnt(0)" : "=s"(t_) :: "memory"); pbt = (unsigned)t_; pbon = 1; SB(); } } while (0)
#define PB_DECL unsigned pbt = 0, pbon = 0
#define PB_END() do { if (pbon) { for (;;) { unsigned long long t_; asm volatile("s_memrealtime %0\n\ts_waitcnt lgkmcnt(0)" : "=s"(t_) :: "memory"); if ((int)((unsigned)t_ - pbt) >= 4000) break; __builtin_amdgcn_s_sleep(2); } } } while (0)
__device__ __forceinline__ void proj_epilogue(f32x4 acc0, f32x4 acc1, bool z, float& v0, float& v1) {
  float n0[4], n1[4];
#pragma unroll
  for (int r = 0; r < 4; ++r) {
    auto s = __builtin_amdgcn_permlane32_swap(__float_as_uint(acc0[r]), __float_as_uint(acc1[r]), false, false);
    n0[r] = __uint_as_float(s[0]); n1[r] = __uint_as_float(s[1]);
  }
  const float r0 = n0[0] + dpp<ROR8>(n1[0]), r1 = n0[1] + dpp<ROR8>(n1[1]);
  const float r2 = n1[2] + dpp<ROR8>(n0[2]), r3 = n1[3] + dpp<ROR8>(n0[3]);
  v0 = z ? r2 : r0; v1 = z ? r3 : r1;
}

__global__ void __launch_bounds__(512, 2)
fused_decode(const u16* __restrict__ x, const u16* __restrict__ kc, const u16* __restrict__ vc, const u16* __restrict__ wq,
             const u16* __restrict__ wo, const u16* __restrict__ wg, const u16* __restrict__ wu, const u16* __restrict__ wd,
             u16* __restrict__ out, unsigned char* __restrict__ ws) {
  __shared__ __attribute__((aligned(16))) float lds[96 + 8 + 4 + 4 + 8 * 192];
  unsigned* l_q = (unsigned*)lds;
  float* l_ml = lds + 96;
  float* l_c = lds + 104;
  unsigned* l_tag = (unsigned*)(lds + 108);
  float* l_acc = lds + 112;

  STAMP_DECL;
  PB_DECL;
  { unsigned long long t_ = __builtin_amdgcn_s_memrealtime(); pbt = (unsigned)t_; pbon = 1; SB(); }
  STAMP(0);
  asm volatile("" ::"s"(x), "s"(kc), "s"(vc), "s"(wq), "s"(wo), "s"(wg), "s"(wu), "s"(wd), "s"(out), "s"(ws));
  PB(1);
  const int bid = blockIdx.x;
  const int unit = (bid & 7) * 32 + (bid >> 3);
  const int h = unit >> 3, j8 = unit & 7, b = j8 >> 2, s = j8 & 3;
  const int tid = threadIdx.x, lane = tid & 63, wave = __builtin_amdgcn_readfirstlane(tid >> 6);
  const int g = lane >> 3, j = lane & 7;
  const rsrc_t rws = mk_rsrc(ws, WS_END);
  const rsrc_t rwg = rws;
#define GOK(x) (x)

  if (CODEPF) {
    if (wave == 3) {
      unsigned long long pc; asm volatile("s_getpc_b64 %0" : "=s"(pc));
      const rsrc_t rc_ = mk_rsrc((const void*)(pc & ~255ull), 14848);
      unsigned d0 = __builtin_amdgcn_raw_buffer_load_b32(rc_, lane * 128, 0, CODEPF == 2 ? AUX_NT : 0), d1 = __builtin_amdgcn_raw_buffer_load_b32(rc_, lane * 128, 8192, CODEPF == 2 ? AUX_NT : 0);
      asm volatile("" ::"v"(d0), "v"(d1));
    }
  }
  if (wave >= 4) {
    const int sw = wave - 4;
    const size_t kvoff = ((size_t)(b * 32 + h) * 8192 + 6144 + 512 * s) * 192;
    const rsrc_t rk = mk_rsrc(kc + kvoff, 512 * 384), rv = mk_rsrc(vc + kvoff, 512 * 384);
    const int voff = (sw * 8 + g) * 384 + 16 * j;
    u32x4 buf[96];
    u32x4 qv[3];
    float sc[16];
    unsigned pd[16];
    f32x4 acc[3] = {{0.f, 0.f, 0.f, 0.f}, {0.f, 0.f, 0.f, 0.f}, {0.f, 0.f, 0.f, 0.f}};
    unsigned msk[4];
#pragma unroll
    for (int d = 0; d < 4; ++d) msk[d] = (j >> 1) == d ? ((j & 1) ? 0xffff0000u : 0x0000ffffu) : 0u;
    constexpr float C2 = 0.07216878364870322f * 1.4426950408889634f;

    auto consume = [&](auto G) {
      constexpr int t = decltype(G)::value;
      if constexpr (t < 16) {
        float a0 = 0.f, a1 = 0.f, a2 = 0.f;
#pragma unroll
        for (int e = 0; e < 4; ++e) { a0 = dot2(buf[3 * t][e], qv[0][e], a0); a1 = dot2(buf[3 * t + 1][e], qv[1][e], a1); a2 = dot2(buf[3 * t + 2][e], qv[2][e], a2); }
        float a = (a0 + a1) + a2;
        a += dpp<XOR1>(a); a += dpp<XOR2>(a); a += dpp<HMIR>(a);
        sc[t] = a;
        if constexpr (t == 15) {
          float m = sc[0];
#pragma unroll
          for (int u = 1; u < 16; ++u) m = fmaxf(m, sc[u]);
          m = fmaxf(m, dpp<ROR8>(m)); m = x16_max(m); m = x32_max(m);
          const float mc = m * C2;
          float l = 0.f;
#pragma unroll
          for (int u = 0; u < 16; ++u) { const float p = __builtin_amdgcn_exp2f(fmaf(sc[u], C2, -mc)); pd[u] = cvtpk(p, p); l += bfhi(pd[u]); }
          l += dpp<ROR8>(l); l = x16_sum(l); l = x32_sum(l);
          if (lane == 0) *(f32x2*)(l_ml + 2 * sw) = f32x2{mc, l};
          STAMP(3);
          WG_BARRIER();
        }
      } else {
        constexpr int u = t - 16;
        const u32x4 a = {pd[u] & msk[0], pd[u] & msk[1], pd[u] & msk[2], pd[u] & msk[3]};
#pragma unroll
        for (int i = 0; i < 3; ++i) acc[i] = mfma32(a, buf[3 * t + i], acc[i]);
      }
    };
    constexpr auto done_after = [](int n) constexpr {
      if (n < B1_AT) return 0;
      const int landed = (n - WIN + 1) / 3;
      const int cu = 1 + (CATCH * (n - B1_AT)) / 3;
      return landed < cu ? landed : cu;
    };
    static_for<0, 96>([&](auto N) {
      constexpr int n = decltype(N)::value;
      if constexpr (n == B1_AT) {
        STAMP(1); PB(3);
        WG_BARRIER();
        PB(6);
        STAMP(2);
#pragma unroll
        for (int i = 0; i < 3; ++i) qv[i] = *(const u32x4*)(l_q + 32 * i + 4 * j);
      }
      if constexpr (n == 0) FS(1);
      if constexpr (n == WIN) FS(2);
      if constexpr (n >= WIN) asm volatile("s_waitcnt vmcnt(%0)" ::"i"(WIN - 1) : "memory");
      if constexpr (n == WIN) PB(2);
      if constexpr (n >= WIN && n < WIN + 45 && (n - WIN) % 5 == 0) FS(3 + (n - WIN) / 5);
      constexpr int m = n < 48 ? n : n - 48;
      buf[n] = __builtin_amdgcn_raw_buffer_load_b128(n < 48 ? rk : rv, voff + (m % 3) * 128, (m / 3) * 12288, n < 48 ? AUX_K : AUX_V);
      SB();
      static_for<(n > 0 ? done_after(n - 1) : 0), done_after(n)>(consume);
      SB();
    });
    STAMP(4); PB(4);
    WG_BARRIER();
    static_for<done_after(95), 32>([&](auto G) { consume(G); SB(); });
    if ((lane >> 5) == (g & 1)) {
      float* dst = l_acc + (sw * 2 + (g & 1)) * 192 + 8 * j + 4 * ((lane >> 4) & 1);
#pragma unroll
      for (int i = 0; i < 3; ++i) *(f32x4*)(dst + 64 * i) = acc[i];
    }
    STAMP(5); PB(7);
    WG_BARRIER();
    if (sw < 3 && lane < 32) {
      const unsigned tag = *l_tag;
      const f32x4 c = *(const f32x4*)l_c;
      const float* src = l_acc + 64 * sw + 8 * j + 2 * g;
      float o0 = 0.f, o1 = 0.f;
#pragma unroll
      for (int w = 0; w < 4; ++w) {
        const f32x2 u = *(const f32x2*)(src + (2 * w) * 192), v = *(const f32x2*)(src + (2 * w + 1) * 192);
        o0 = fmaf(c[w], u[0] + v[0], o0); o1 = fmaf(c[w], u[1] + v[1], o1);
      }
      st_pair(rws, lane * 16, WS_OP + ((h * 2 + b) * 4 + s) * 1536 + sw * 512, o0, o1, tag);
    }
    STAMP(6);
    asm volatile("s_waitcnt vmcnt(0)" ::: "memory");
    STAMP(7);
    WG_BARRIER();
    STAMP_FLUSH(); PB_END();
    return;
  }

  __builtin_amdgcn_s_setprio(2);
  const int w = wave;
  const int bl = (lane >> 2) & 1, e4 = lane & 3;
  const unsigned m0 = e4 == 0 ? 0x0000ffffu : e4 == 1 ? 0xffff0000u : 0u;
  const unsigned m1 = e4 == 2 ? 0x0000ffffu : e4 == 3 ? 0xffff0000u : 0u;
  const int st_voff = ((lane >> 4) & 1) * 1536 + (8 * j + 4 * (lane >> 5) + 2 * (g & 1)) * 8;
  const rsrc_t rx = mk_rsrc(x, 2560);
  const int uq = unit & 31;
  unsigned tag;
  {
    const unsigned ep = *(const __attribute__((address_space(4))) unsigned*)(ws + WS_EPOCH);
    const u32x4 xa = __builtin_amdgcn_raw_buffer_load_b128(rx, lane * 40, 0, AUX_X);
    const u32x4 xb = __builtin_amdgcn_raw_buffer_load_b128(rx, lane * 40 + 16, 0, AUX_X);
    const u32x2 xc = __builtin_amdgcn_raw_buffer_load_b64(rx, lane * 40 + 32, 0, AUX_X);
    auto front = [&](auto NN, rsrc_t rw, int row_bytes, int xoff, int rec) {
      constexpr int N = decltype(NN)::value;
      unsigned xr[N / 2];
      {
        const u32x4 t0 = __builtin_amdgcn_raw_buffer_load_b128(rx, bl * 1280 + g * (2 * N), xoff, AUX_X);
        xr[0] = t0[0]; xr[1] = t0[1]; xr[2] = t0[2]; xr[3] = t0[3];
        if constexpr (N == 10) xr[4] = __builtin_amdgcn_raw_buffer_load_b32(rx, bl * 1280 + g * (2 * N) + 16, xoff, AUX_X);
        else { const u32x4 t1 = __builtin_amdgcn_raw_buffer_load_b128(rx, bl * 1280 + g * (2 * N) + 16, xoff, AUX_X); xr[4] = t1[0]; xr[5] = t1[1]; xr[6] = t1[2]; xr[7] = t1[3]; }
      }
      FS(1);
      u32x4 wv[N];
      const int wvoff = g * N * row_bytes + 16 * j;
#pragma unroll
      for (int n = 0; n < N; ++n) wv[n] = ld_nt(rw, wvoff, n * row_bytes);
      SB();
      FS(2);
      float v[20];
#pragma unroll
      for (int e = 0; e < 4; ++e) { v[2 * e] = bflo(xa[e]); v[2 * e + 1] = bfhi(xa[e]); v[8 + 2 * e] = bflo(xb[e]); v[9 + 2 * e] = bfhi(xb[e]); }
      v[16] = bflo(xc[0]); v[17] = bfhi(xc[0]); v[18] = bflo(xc[1]); v[19] = bfhi(xc[1]);
      float sum = 0.f;
#pragma unroll
      for (int e = 0; e < 20; ++e) sum += v[e];
      sum = x16_sum(row_sum(sum));
      const float mean = sum * (1.f / 640.f);
      float sq = 0.f;
#pragma unroll
      for (int e = 0; e < 20; ++e) { const float d = v[e] - mean; sq = fmaf(d, d, sq); }
      sq = x16_sum(row_sum(sq));
      const float mb = rbf(mean), den = rbf(sqrtf(rbf(rbf(sq * (1.f / 640.f)) + 1e-5f)));
      const float inv = 1.f / den;
      const float mb0 = __builtin_bit_cast(float, __builtin_amdgcn_readlane(__builtin_bit_cast(int, mb), 0));
      const float mb1 = __builtin_bit_cast(float, __builtin_amdgcn_readlane(__builtin_bit_cast(int, mb), 32));
      const float iv0 = __builtin_bit_cast(float, __builtin_amdgcn_readlane(__builtin_bit_cast(int, inv), 0));
      const float iv1 = __builtin_bit_cast(float, __builtin_amdgcn_readlane(__builtin_bit_cast(int, inv), 32));
      const float mm = bl ? mb1 : mb0, ii = bl ? iv1 : iv0;
      FS(7);
      f32x4 acc0 = {0.f, 0.f, 0.f, 0.f}, acc1 = {0.f, 0.f, 0.f, 0.f};
#pragma unroll
      for (int n = 0; n < N; ++n) {
        const float xv = (n & 1) ? bfhi(xr[n >> 1]) : bflo(xr[n >> 1]);
        const float xn = rbf(xv - mm) * ii;
        const unsigned d = cvtpk(xn, xn), a0 = d & m0, a1 = d & m1;
        acc0 = mfma16(a0, a1, wv[n][0], wv[n][1], acc0);
        acc1 = mfma16(a0, a1, wv[n][2], wv[n][3], acc1);
      }
      float v0, v1;
      proj_epilogue(acc0, acc1, g & 1, v0, v1);
      st_pair(rws, st_voff, rec, v0, v1, ep + 1u);
    };
    if (w < 3) {
      front(std::integral_constant<int, 10>{}, mk_rsrc(wq + (size_t)(80 * j8) * 6144 + 192 * h + 64 * w, 79 * 12288 + 128), 12288, 160 * j8,
            WS_QP + (h * 8 + j8) * 3072 + w * 512);
    } else if (uq < 30) {
      const int r = uq / 6, lc = uq - 6 * r, mat = lc >= 3 ? 1 : 0, c3 = lc - 3 * mat;
      front(std::integral_constant<int, 16>{}, mk_rsrc((mat ? wu : wg) + (size_t)(128 * r) * 1536 + 192 * (h >> 2) + 64 * c3, 127 * 3072 + 128), 3072, 256 * r,
            WS_GU + (((h >> 2) * 5 + r) * 2 + mat) * 3072 + c3 * 512);
    }
    tag = ep + 1u;
  }
  STAMP(1); FS(8); PB(9);
  asm volatile("s_waitcnt vmcnt(0)" ::: "memory");
  STAMP(11); FS(9);
  __builtin_amdgcn_s_sleep(QSLEEP);
  {
    u32x4 t[3];
    for (unsigned spin = 0;; ++spin) {
      MEMFENCE();
#pragma unroll
      for (int k = 0; k < 3; ++k) t[k] = ld_sc1(rwg, g * 3072 + 16 * j + 128 * k, WS_QP + h * 8 * 3072 + b * 1536 + w * 384);
      bool ok = true;
#pragma unroll
      for (int k = 0; k < 3; ++k) ok &= (t[k][1] == tag) & (t[k][3] == tag);
      if (GOK(__all(ok)) || spin > SPIN_CAP) { STAMPV(12, spin + 1); break; }
    }
#pragma unroll
    for (int k = 0; k < 3; ++k) {
      float a = __uint_as_float(t[k][0]), c = __uint_as_float(t[k][2]);
      a += dpp<ROR8>(a); c += dpp<ROR8>(c);
      a = x32_sum(x16_sum(a)); c = x32_sum(x16_sum(c));
      if (lane < 8) l_q[24 * w + 8 * k + j] = cvtpk(a, c);
    }
    if (tid == 0) *l_tag = tag;
  }
  STAMP(2); FS(10);
  WG_BARRIER();
  const int slot = 4 * j8 + w;
  const bool has_back = slot < 30;
  const int t3 = slot / 10, cc = slot - 10 * t3;
  if (PREWRITE) {
    if (w < 3 && lane < 32) st_pair(rws, lane * 16, WS_OP + ((h * 2 + b) * 4 + s) * 1536 + w * 512, 0.f, 0.f, tag - 1u);
    if (has_back) st_pair(rws, ((lane >> 4) & 1) * (96 * 512) + (8 * j + 4 * (lane >> 5) + 2 * (g & 1)) * 8, WS_YP + cc * 2 * 96 * 512 + (h * 3 + t3) * 512, 0.f, 0.f, tag - 1u);
    asm volatile("s_waitcnt vmcnt(0)" ::: "memory");
  }
  unsigned ha[2][2] = {{0u, 0u}, {0u, 0u}};
  if (has_back) {
    u32x4 t[3];
    const int so = WS_GU + (h >> 2) * 5 * 6144 + (h & 3) * 384 + t3 * 128;
    const int vo = bl * 1536 + 16 * g;
    for (unsigned spin = 0;; ++spin) {
      MEMFENCE();
      t[0] = ld_sc1(rwg, vo + e4 * 6144, so);
      t[1] = ld_sc1(rwg, vo + e4 * 6144 + 3072, so);
      t[2] = ld_sc1(rwg, vo + 4 * 6144 + (e4 & 1) * 3072, so);
      bool ok = true;
#pragma unroll
      for (int k = 0; k < 3; ++k) ok &= (t[k][1] == tag) & (t[k][3] == tag);
      if (GOK(__all(ok)) || spin > SPIN_CAP) { STAMPV(13, spin + 1); break; }
    }
#pragma unroll
    for (int z = 0; z < 2; ++z) {
      const float ex = __uint_as_float(t[2][2 * z]);
      const float gg = rbf(quad_sum(__uint_as_float(t[0][2 * z]) + (e4 == 0 ? ex : 0.f)));
      const float uu = rbf(quad_sum(__uint_as_float(t[1][2 * z]) + (e4 == 1 ? ex : 0.f)));
      const float zz = 0.7978845608028654f * (gg + 0.044715f * gg * gg * gg);
      const float ge = rbf(gg / (1.f + __builtin_amdgcn_exp2f(-2.8853900817779268f * zz)));
      const float hv = ge * uu;
      const unsigned d = cvtpk(hv, hv);
      ha[z][0] = d & m0; ha[z][1] = d & m1;
    }
  }
  const bool reducer = (w == 0) && (bid < 160);
  const int rb = bid / 80, rc = (bid - 80 * rb) >> 3, ro = bid & 7;
  unsigned xres = 0;
  if (reducer) xres = __builtin_amdgcn_raw_buffer_load_b32(rx, (lane & 3) * 4, rb * 1280 + rc * 128 + ro * 16, AUX_X);
  STAMP(3);
  WG_BARRIER();
  if (w == 0) {
    const int so = WS_ML + (h * 2 + b) * 256;
    if (lane < 4) { const f32x2 ml = *(const f32x2*)(l_ml + 2 * lane); st_pair(rws, lane * 16, so + s * 64, ml[0], ml[1], tag); }
    asm volatile("s_waitcnt vmcnt(0)" ::: "memory");
    __builtin_amdgcn_s_sleep(MLSLEEP);
    u32x4 t;
    for (unsigned spin = 0;; ++spin) {
      MEMFENCE();
      t = ld_sc1(rws, (lane & 15) * 16, so);
      if (__all((t[1] == tag) & (t[3] == tag)) || spin > SPIN_CAP) break;
    }
    const float mv = __uint_as_float(t[0]);
    const float M = row_max(mv);
    const float f = __builtin_amdgcn_exp2f(mv - M);
    const float L = row_sum(f * __uint_as_float(t[2]));
    if ((lane >> 2) == s) l_c[lane & 3] = f / L;
  }
  STAMP(4);
  WG_BARRIER();
  u32x4 wb[10];
  if (has_back) {
    const rsrc_t rd = mk_rsrc(wd + (size_t)(48 * h + 16 * t3) * 640 + 64 * cc, 15 * 1280 + 128);
    const rsrc_t ro_ = mk_rsrc(wo + (size_t)(192 * h + 64 * t3) * 640 + 64 * cc, 63 * 1280 + 128);
#pragma unroll
    for (int n = 0; n < 2; ++n) { wb[8 + n] = ld_nt(rd, g * 2560 + 16 * j + n * 1280, 0); SB(); if (PACE) __builtin_amdgcn_s_sleep(PACE); SB(); }
#pragma unroll
    for (int n = 0; n < 8; ++n) { wb[n] = ld_nt(ro_, g * 10240 + 16 * j, n * 1280); SB(); if (PACE && n < 7) __builtin_amdgcn_s_sleep(PACE); SB(); }
    SB();
  }
  STAMP(5);
  WG_BARRIER();
  WG_BARRIER();
  if (H1SLEEP) __builtin_amdgcn_s_sleep(H1SLEEP);
  STAMP(6);
  if (has_back) {
    u32x4 t[4];
    const int so = WS_OP + h * 8 * 1536 + t3 * 512;
    const int vo = (bl * 4 + e4) * 1536 + 16 * g;
    for (unsigned spin = 0;; ++spin) {
      MEMFENCE();
#pragma unroll
      for (int k = 0; k < 4; ++k) t[k] = ld_sc1(rws, vo + 128 * k, so);
      bool ok = true;
#pragma unroll
      for (int k = 0; k < 4; ++k) ok &= (t[k][1] == tag) & (t[k][3] == tag);
      if (__all(ok) || spin > SPIN_CAP) { STAMPV(14, spin + 1); break; }
    }
    STAMP(7); PB(5);
    f32x4 acc0 = {0.f, 0.f, 0.f, 0.f}, acc1 = {0.f, 0.f, 0.f, 0.f};
#pragma unroll
    for (int z = 0; z < 2; ++z) {
      acc0 = mfma16(ha[z][0], ha[z][1], wb[8 + z][0], wb[8 + z][1], acc0);
      acc1 = mfma16(ha[z][0], ha[z][1], wb[8 + z][2], wb[8 + z][3], acc1);
    }
#pragma unroll
    for (int n = 0; n < 8; ++n) {
      const float ov = quad_sum(__uint_as_float(t[n >> 1][2 * (n & 1)]));
      const unsigned d = cvtpk(ov, ov), a0 = d & m0, a1 = d & m1;
      acc0 = mfma16(a0, a1, wb[n][0], wb[n][1], acc0);
      acc1 = mfma16(a0, a1, wb[n][2], wb[n][3], acc1);
    }
    float v0, v1;
    proj_epilogue(acc0, acc1, g & 1, v0, v1);
    st_pair(rws, ((lane >> 4) & 1) * (96 * 512) + (8 * j + 4 * (lane >> 5) + 2 * (g & 1)) * 8, WS_YP + cc * 2 * 96 * 512 + (h * 3 + t3) * 512, v0, v1, tag);
  }
  STAMP(8);
  if (reducer) {
    u32x4 t[6];
    const int so = WS_YP + (rc * 2 + rb) * 96 * 512 + ro * 64;
    const int vo = (lane >> 2) * 512 + (lane & 3) * 16;
    for (unsigned spin = 0;; ++spin) {
      MEMFENCE();
#pragma unroll
      for (int k = 0; k < 6; ++k) t[k] = ld_sc1(rws, vo, so + k * 16 * 512);
      bool ok = true;
#pragma unroll
      for (int k = 0; k < 6; ++k) ok &= (t[k][1] == tag) & (t[k][3] == tag);
      if (__all(ok) || spin > SPIN_CAP) { STAMPV(15, spin + 1); break; }
    }
    STAMP(9);
    float y0 = 0.f, y1 = 0.f;
#pragma unroll
    for (int k = 0; k < 6; ++k) { y0 += __uint_as_float(t[k][0]); y1 += __uint_as_float(t[k][2]); }
    y0 += dpp<ROR4>(y0); y1 += dpp<ROR4>(y1);
    y0 += dpp<ROR8>(y0); y1 += dpp<ROR8>(y1);
    y0 = x32_sum(x16_sum(y0)); y1 = x32_sum(x16_sum(y1));
    if (lane < 4) ((unsigned*)out)[rb * 320 + rc * 32 + ro * 4 + lane] = cvtpk(bflo(xres) + y0, bfhi(xres) + y1);
    if (bid == 0 && lane == 0) __builtin_amdgcn_raw_buffer_store_b32(tag, rws, 0, WS_EPOCH, AUX_SC1);
    STAMP(10); PB(8);
  }
  STAMP_FLUSH(); PB_END();
}

extern "C" void kernel_launch(void* const* d_in, const int* in_sizes, int n_in, void* d_out, int out_size, void* d_ws, size_t ws_size,
                              hipStream_t stream) {
  fused_decode<<<256, 512, 0, stream>>>((const u16*)d_in[0], (const u16*)d_in[1], (const u16*)d_in[2], (const u16*)d_in[3], (const u16*)d_in[4],
                                        (const u16*)d_in[6], (const u16*)d_in[7], (const u16*)d_in[8], (u16*)d_out, (unsigned char*)d_ws + WS_BASE);
}
```

```cpp
#define B1_AT 48
#define PROBE 1
#define PBSEL (bid==0)
#include <hip/hip_runtime.h>
#include <hip/hip_bf16.h>
#include <stdint.h>
#include <type_traits>

typedef unsigned short u16;
typedef unsigned u32x4 __attribute__((ext_vector_type(4)));
typedef unsigned u32x2 __attribute__((ext_vector_type(2)));
typedef float f32x4 __attribute__((ext_vector_type(4)));
typedef float f32x2 __attribute__((ext_vector_type(2)));
typedef short s16x4 __attribute__((ext_vector_type(4)));
typedef __bf16 bf16x8 __attribute__((ext_vector_type(8)));
typedef __bf16 bf16x2 __attribute__((ext_vector_type(2)));
typedef __amdgpu_buffer_rsrc_t rsrc_t;

#define WIN 8

#define CATCH 3
#define AUX_X 2
#define QSLEEP 16
#define MLSLEEP 32
#define PACE 3
#define PREWRITE 0
#define CODEPF 0
#define H1SLEEP 0
#define WS_BASE 0
#define AUX_K 2
#define AUX_V 2
#define AUX_W 2
#define AUX_OUT 0
constexpr int AUX_NT = 2, AUX_SC1 = 16;
constexpr int RSRC_W3 = 0x00020000;
constexpr unsigned SPIN_CAP = 1u << 18;

constexpr int WS_EPOCH = 0;
constexpr int WS_QP = 4096;
constexpr int WS_GU = WS_QP + 32 * 8 * 2 * 192 * 8;
constexpr int WS_ML = WS_GU + 8 * 5 * 2 * 2 * 192 * 8;
constexpr int WS_OP = WS_ML + 32 * 2 * 4 * 4 * 16;
constexpr int WS_YP = WS_OP + 32 * 2 * 4 * 192 * 8;
constexpr int WS_END = WS_YP + 10 * 2 * 96 * 64 * 8;

template <int I, int N, class F> __device__ __forceinline__ void static_for(F&& f) {
  if constexpr (I < N) { f(std::integral_constant<int, I>{}); static_for<I + 1, N>(f); }
}
__device__ __forceinline__ rsrc_t mk_rsrc(const void* p, int bytes) {
  return __builtin_amdgcn_make_buffer_rsrc(const_cast<void*>(p), 0, bytes, RSRC_W3);
}
__device__ __forceinline__ u32x4 ld_nt(rsrc_t r, int voff, int soff) { return __builtin_amdgcn_raw_buffer_load_b128(r, voff, soff, AUX_W); }
__device__ __forceinline__ u32x4 ld_sc1(rsrc_t r, int voff, int soff) { return __builtin_amdgcn_raw_buffer_load_b128(r, voff, soff, AUX_SC1); }
__device__ __forceinline__ void st_pair(rsrc_t r, int voff, int soff, float a, float b, unsigned tag) {
  u32x4 v = {__float_as_uint(a), tag, __float_as_uint(b), tag};
  __builtin_amdgcn_raw_buffer_store_b128(v, r, voff, soff, AUX_SC1);
}
__device__ __forceinline__ float bflo(unsigned v) { return __uint_as_float(v << 16); }
__device__ __forceinline__ float bfhi(unsigned v) { return __uint_as_float(v & 0xffff0000u); }
__device__ __forceinline__ unsigned cvtpk(float lo, float hi) {
  bf16x2 r = {(__bf16)lo, (__bf16)hi};
  return __builtin_bit_cast(unsigned, r);
}
__device__ __forceinline__ float rbf(float x) { return (float)(__bf16)x; }
template <int CTRL> __device__ __forceinline__ float dpp(float x) {
  return __builtin_bit_cast(float, __builtin_amdgcn_mov_dpp(__builtin_bit_cast(int, x), CTRL, 0xf, 0xf, true));
}
constexpr int XOR1 = 0xB1, XOR2 = 0x4E, HMIR = 0x141, ROR8 = 0x128, ROR4 = 0x124;
__device__ __forceinline__ float x16_sum(float x) { auto s = __builtin_amdgcn_permlane16_swap(__float_as_uint(x), __float_as_uint(x), false, false); return __uint_as_float(s[0]) + __uint_as_float(s[1]); }
__device__ __forceinline__ float x32_sum(float x) { auto s = __builtin_amdgcn_permlane32_swap(__float_as_uint(x), __float_as_uint(x), false, false); return __uint_as_float(s[0]) + __uint_as_float(s[1]); }
__device__ __forceinline__ float x16_max(float x) { auto s = __builtin_amdgcn_permlane16_swap(__float_as_uint(x), __float_as_uint(x), false, false); return fmaxf(__uint_as_float(s[0]), __uint_as_float(s[1])); }
__device__ __forceinline__ float x32_max(float x) { auto s = __builtin_amdgcn_permlane32_swap(__float_as_uint(x), __float_as_uint(x), false, false); return fmaxf(__uint_as_float(s[0]), __uint_as_float(s[1])); }
__device__ __forceinline__ float row_sum(float x) { x += dpp<XOR1>(x); x += dpp<XOR2>(x); x += dpp<HMIR>(x); x += dpp<ROR8>(x); return x; }
__device__ __forceinline__ float row_max(float x) { x = fmaxf(x, dpp<XOR1>(x)); x = fmaxf(x, dpp<XOR2>(x)); x = fmaxf(x, dpp<HMIR>(x)); x = fmaxf(x, dpp<ROR8>(x)); return x; }
__device__ __forceinline__ float quad_sum(float x) { x += dpp<XOR1>(x); x += dpp<XOR2>(x); return x; }
__device__ __forceinline__ float dot2(unsigned a, unsigned b, float c) {
  return __builtin_amdgcn_fdot2_f32_bf16(__builtin_bit_cast(bf16x2, a), __builtin_bit_cast(bf16x2, b), c, false);
}
__device__ __forceinline__ f32x4 mfma16(unsigned a0, unsigned a1, unsigned b0, unsigned b1, f32x4 c) {
  u32x2 a = {a0, a1}, b = {b0, b1};
  return __builtin_amdgcn_mfma_f32_16x16x16bf16_1k(__builtin_bit_cast(s16x4, a), __builtin_bit_cast(s16x4, b), c, 0, 0, 0);
}
__device__ __forceinline__ f32x4 mfma32(u32x4 a, u32x4 b, f32x4 c) {
  return __builtin_amdgcn_mfma_f32_16x16x32_bf16(__builtin_bit_cast(bf16x8, a), __builtin_bit_cast(bf16x8, b), c, 0, 0, 0);
}
#define SB() __builtin_amdgcn_sched_barrier(0)
#define WG_BARRIER() do { asm volatile("s_waitcnt lgkmcnt(0)\n\ts_barrier" ::: "memory"); } while (0)
#define MEMFENCE() asm volatile("" ::: "memory")

#define FS(i)
#define STAMP(i)
#define STAMPV(i, v)
#define STAMP_DECL
#define STAMP_FLUSH()

#define PB(i) do { if ((i) == PROBE) { SB(); unsigned long long t_; asm volatile("s_memrealtime %0\n\ts_waitcnt lgkmcnt(0)" : "=s"(t_) :: "memory"); pbt = (unsigned)t_; pbon = 1; SB(); } } while (0)
#define PB_DECL unsigned pbt = 0, pbon = 0
#define PB_END() do { if (pbon && (PBSEL)) { for (;;) { unsigned long long t_; asm volatile("s_memrealtime %0\n\ts_waitcnt lgkmcnt(0)" : "=s"(t_) :: "memory"); if ((int)((unsigned)t_ - pbt) >= 4000) break; __builtin_amdgcn_s_sleep(2); } } } while (0)
__device__ __forceinline__ void proj_epilogue(f32x4 acc0, f32x4 acc1, bool z, float& v0, float& v1) {
  float n0[4], n1[4];
#pragma unroll
  for (int r = 0; r < 4; ++r) {
    auto s = __builtin_amdgcn_permlane32_swap(__float_as_uint(acc0[r]), __float_as_uint(acc1[r]), false, false);
    n0[r] = __uint_as_float(s[0]); n1[r] = __uint_as_float(s[1]);
  }
  const float r0 = n0[0] + dpp<ROR8>(n1[0]), r1 = n0[1] + dpp<ROR8>(n1[1]);
  const float r2 = n1[2] + dpp<ROR8>(n0[2]), r3 = n1[3] + dpp<ROR8>(n0[3]);
  v0 = z ? r2 : r0; v1 = z ? r3 : r1;
}

__global__ void __launch_bounds__(512, 2)
fused_decode(const u16* x_, const u16* kc_, const u16* vc_, const u16* wq_, const u16* wo_, const u16* wg_, const u16* wu_, const u16* wd_, u16* out_, unsigned char* ws_) {
  __shared__ __attribute__((aligned(16))) float lds[96 + 8 + 4 + 4 + 8 * 192];
  unsigned* l_q = (unsigned*)lds;
  float* l_ml = lds + 96;
  float* l_c = lds + 104;
  unsigned* l_tag = (unsigned*)(lds + 108);
  float* l_acc = lds + 112;

  STAMP_DECL;
  PB_DECL;
  PB(0);
  STAMP(0);
  typedef const __attribute__((address_space(4))) unsigned long long* kptr_t;
  kptr_t ka = (kptr_t)__builtin_amdgcn_kernarg_segment_ptr();
  asm volatile("" : "+s"(ka));
  const u16 *x = (const u16*)ka[0], *kc = (const u16*)ka[1], *vc = (const u16*)ka[2], *wq = (const u16*)ka[3], *wo = (const u16*)ka[4],
            *wg = (const u16*)ka[5], *wu = (const u16*)ka[6], *wd = (const u16*)ka[7];
  u16* out = (u16*)ka[8];
  unsigned char* ws = (unsigned char*)ka[9];
  asm volatile("" ::"s"(x), "s"(kc), "s"(vc), "s"(wq), "s"(wo), "s"(wg), "s"(wu), "s"(wd), "s"(out), "s"(ws));
  PB(1);
  const int bid = blockIdx.x;
  const int unit = (bid & 7) * 32 + (bid >> 3);
  const int h = unit >> 3, j8 = unit & 7, b = j8 >> 2, s = j8 & 3;
  const int tid = threadIdx.x, lane = tid & 63, wave = __builtin_amdgcn_readfirstlane(tid >> 6);
  const int g = lane >> 3, j = lane & 7;
  const rsrc_t rws = mk_rsrc(ws, WS_END);
  const rsrc_t rwg = rws;
#define GOK(x) (x)

  if (CODEPF) {
    if (wave == 3) {
      unsigned long long pc; asm volatile("s_getpc_b64 %0" : "=s"(pc));
      const rsrc_t rc_ = mk_rsrc((const void*)(pc & ~255ull), 14848);
      unsigned d0 = __builtin_amdgcn_raw_buffer_load_b32(rc_, lane * 128, 0, CODEPF == 2 ? AUX_NT : 0), d1 = __builtin_amdgcn_raw_buffer_load_b32(rc_, lane * 128, 8192, CODEPF == 2 ? AUX_NT : 0);
      asm volatile("" ::"v"(d0), "v"(d1));
    }
  }
  if (wave >= 4) {
    const int sw = wave - 4;
    const size_t kvoff = ((size_t)(b * 32 + h) * 8192 + 6144 + 512 * s) * 192;
    const rsrc_t rk = mk_rsrc(kc + kvoff, 512 * 384), rv = mk_rsrc(vc + kvoff, 512 * 384);
    const int voff = (sw * 8 + g) * 384 + 16 * j;
    u32x4 buf[96];
    u32x4 qv[3];
    float sc[16];
    unsigned pd[16];
    f32x4 acc[3] = {{0.f, 0.f, 0.f, 0.f}, {0.f, 0.f, 0.f, 0.f}, {0.f, 0.f, 0.f, 0.f}};
    unsigned msk[4];
#pragma unroll
    for (int d = 0; d < 4; ++d) msk[d] = (j >> 1) == d ? ((j & 1) ? 0xffff0000u : 0x0000ffffu) : 0u;
    constexpr float C2 = 0.07216878364870322f * 1.4426950408889634f;

    auto consume = [&](auto G) {
      constexpr int t = decltype(G)::value;
      if constexpr (t < 16) {
        float a0 = 0.f, a1 = 0.f, a2 = 0.f;
#pragma unroll
        for (int e = 0; e < 4; ++e) { a0 = dot2(buf[3 * t][e], qv[0][e], a0); a1 = dot2(buf[3 * t + 1][e], qv[1][e], a1); a2 = dot2(buf[3 * t + 2][e], qv[2][e], a2); }
        float a = (a0 + a1) + a2;
        a += dpp<XOR1>(a); a += dpp<XOR2>(a); a += dpp<HMIR>(a);
        sc[t] = a;
        if constexpr (t == 15) {
          float m = sc[0];
#pragma unroll
          for (int u = 1; u < 16; ++u) m = fmaxf(m, sc[u]);
          m = fmaxf(m, dpp<ROR8>(m)); m = x16_max(m); m = x32_max(m);
          const float mc = m * C2;
          float l = 0.f;
#pragma unroll
          for (int u = 0; u < 16; ++u) { const float p = __builtin_amdgcn_exp2f(fmaf(sc[u], C2, -mc)); pd[u] = cvtpk(p, p); l += bfhi(pd[u]); }
          l += dpp<ROR8>(l); l = x16_sum(l); l = x32_sum(l);
          if (lane == 0) *(f32x2*)(l_ml + 2 * sw) = f32x2{mc, l};
          STAMP(3);
          WG_BARRIER();
        }
      } else {
        constexpr int u = t - 16;
        const u32x4 a = {pd[u] & msk[0], pd[u] & msk[1], pd[u] & msk[2], pd[u] & msk[3]};
#pragma unroll
        for (int i = 0; i < 3; ++i) acc[i] = mfma32(a, buf[3 * t + i], acc[i]);
      }
    };
    constexpr auto done_after = [](int n) constexpr {
      if (n < B1_AT) return 0;
      const int landed = (n - WIN + 1) / 3;
      const int cu = 1 + (CATCH * (n - B1_AT)) / 3;
      return landed < cu ? landed : cu;
    };
    static_for<0, 96>([&](auto N) {
      constexpr int n = decltype(N)::value;
      if constexpr (n == B1_AT) {
        STAMP(1); PB(3);
        WG_BARRIER();
        PB(6);
        STAMP(2);
#pragma unroll
        for (int i = 0; i < 3; ++i) qv[i] = *(const u32x4*)(l_q + 32 * i + 4 * j);
      }
      if constexpr (n == 0) FS(1);
      if constexpr (n == WIN) FS(2);
      if constexpr (n >= WIN) asm volatile("s_waitcnt vmcnt(%0)" ::"i"(WIN - 1) : "memory");
      if constexpr (n == WIN) PB(2);
      if constexpr (n >= WIN && n < WIN + 45 && (n - WIN) % 5 == 0) FS(3 + (n - WIN) / 5);
      constexpr int m = n < 48 ? n : n - 48;
      buf[n] = __builtin_amdgcn_raw_buffer_load_b128(n < 48 ? rk : rv, voff + (m % 3) * 128, (m / 3) * 12288, n < 48 ? AUX_K : AUX_V);
      SB();
      static_for<(n > 0 ? done_after(n - 1) : 0), done_after(n)>(consume);
      SB();
    });
    STAMP(4); PB(4);
    WG_BARRIER();
    static_for<done_after(95), 32>([&](auto G) { consume(G); SB(); });
    if ((lane >> 5) == (g & 1)) {
      float* dst = l_acc + (sw * 2 + (g & 1)) * 192 + 8 * j + 4 * ((lane >> 4) & 1);
#pragma unroll
      for (int i = 0; i < 3; ++i) *(f32x4*)(dst + 64 * i) = acc[i];
    }
    STAMP(5); PB(7);
    WG_BARRIER();
    if (sw < 3 && lane < 32) {
      const unsigned tag = *l_tag;
      const f32x4 c = *(const f32x4*)l_c;
      const float* src = l_acc + 64 * sw + 8 * j + 2 * g;
      float o0 = 0.f, o1 = 0.f;
#pragma unroll
      for (int w = 0; w < 4; ++w) {
        const f32x2 u = *(const f32x2*)(src + (2 * w) * 192), v = *(const f32x2*)(src + (2 * w + 1) * 192);
        o0 = fmaf(c[w], u[0] + v[0], o0); o1 = fmaf(c[w], u[1] + v[1], o1);
      }
      st_pair(rws, lane * 16, WS_OP + ((h * 2 + b) * 4 + s) * 1536 + sw * 512, o0, o1, tag);
    }
    STAMP(6);
    asm volatile("s_waitcnt vmcnt(0)" ::: "memory");
    STAMP(7);
    WG_BARRIER();
    STAMP_FLUSH(); PB_END();
    return;
  }

  __builtin_amdgcn_s_setprio(2);
  const int w = wave;
  const int bl = (lane >> 2) & 1, e4 = lane & 3;
  const unsigned m0 = e4 == 0 ? 0x0000ffffu : e4 == 1 ? 0xffff0000u : 0u;
  const unsigned m1 = e4 == 2 ? 0x0000ffffu : e4 == 3 ? 0xffff0000u : 0u;
  const int st_voff = ((lane >> 4) & 1) * 1536 + (8 * j + 4 * (lane >> 5) + 2 * (g & 1)) * 8;
  const rsrc_t rx = mk_rsrc(x, 2560);
  const int uq = unit & 31;
  unsigned tag;
  {
    const unsigned ep = *(const __attribute__((address_space(4))) unsigned*)(ws + WS_EPOCH);
    const u32x4 xa = __builtin_amdgcn_raw_buffer_load_b128(rx, lane * 40, 0, AUX_X);
    const u32x4 xb = __builtin_amdgcn_raw_buffer_load_b128(rx, lane * 40 + 16, 0, AUX_X);
    const u32x2 xc = __builtin_amdgcn_raw_buffer_load_b64(rx, lane * 40 + 32, 0, AUX_X);
    auto front = [&](auto NN, rsrc_t rw, int row_bytes, int xoff, int rec) {
      constexpr int N = decltype(NN)::value;
      unsigned xr[N / 2];
      {
        const u32x4 t0 = __builtin_amdgcn_raw_buffer_load_b128(rx, bl * 1280 + g * (2 * N), xoff, AUX_X);
        xr[0] = t0[0]; xr[1] = t0[1]; xr[2] = t0[2]; xr[3] = t0[3];
        if constexpr (N == 10) xr[4] = __builtin_amdgcn_raw_buffer_load_b32(rx, bl * 1280 + g * (2 * N) + 16, xoff, AUX_X);
        else { const u32x4 t1 = __builtin_amdgcn_raw_buffer_load_b128(rx, bl * 1280 + g * (2 * N) + 16, xoff, AUX_X); xr[4] = t1[0]; xr[5] = t1[1]; xr[6] = t1[2]; xr[7] = t1[3]; }
      }
      FS(1);
      u32x4 wv[N];
      const int wvoff = g * N * row_bytes + 16 * j;
#pragma unroll
      for (int n = 0; n < N; ++n) wv[n] = ld_nt(rw, wvoff, n * row_bytes);
      SB();
      FS(2);
      float v[20];
#pragma unroll
      for (int e = 0; e < 4; ++e) { v[2 * e] = bflo(xa[e]); v[2 * e + 1] = bfhi(xa[e]); v[8 + 2 * e] = bflo(xb[e]); v[9 + 2 * e] = bfhi(xb[e]); }
      v[16] = bflo(xc[0]); v[17] = bfhi(xc[0]); v[18] = bflo(xc[1]); v[19] = bfhi(xc[1]);
      float sum = 0.f;
#pragma unroll
      for (int e = 0; e < 20; ++e) sum += v[e];
      sum = x16_sum(row_sum(sum));
      const float mean = sum * (1.f / 640.f);
      float sq = 0.f;
#pragma unroll
      for (int e = 0; e < 20; ++e) { const float d = v[e] - mean; sq = fmaf(d, d, sq); }
      sq = x16_sum(row_sum(sq));
      const float mb = rbf(mean), den = rbf(sqrtf(rbf(rbf(sq * (1.f / 640.f)) + 1e-5f)));
      const float inv = 1.f / den;
      const float mb0 = __builtin_bit_cast(float, __builtin_amdgcn_readlane(__builtin_bit_cast(int, mb), 0));
      const float mb1 = __builtin_bit_cast(float, __builtin_amdgcn_readlane(__builtin_bit_cast(int, mb), 32));
      const float iv0 = __builtin_bit_cast(float, __builtin_amdgcn_readlane(__builtin_bit_cast(int, inv), 0));
      const float iv1 = __builtin_bit_cast(float, __builtin_amdgcn_readlane(__builtin_bit_cast(int, inv), 32));
      const float mm = bl ? mb1 : mb0, ii = bl ? iv1 : iv0;
      FS(7);
      f32x4 acc0 = {0.f, 0.f, 0.f, 0.f}, acc1 = {0.f, 0.f, 0.f, 0.f};
#pragma unroll
      for (int n = 0; n < N; ++n) {
        const float xv = (n & 1) ? bfhi(xr[n >> 1]) : bflo(xr[n >> 1]);
        const float xn = rbf(xv - mm) * ii;
        const unsigned d = cvtpk(xn, xn), a0 = d & m0, a1 = d & m1;
        acc0 = mfma16(a0, a1, wv[n][0], wv[n][1], acc0);
        acc1 = mfma16(a0, a1, wv[n][2], wv[n][3], acc1);
      }
      float v0, v1;
      proj_epilogue(acc0, acc1, g & 1, v0, v1);
      st_pair(rws, st_voff, rec, v0, v1, ep + 1u);
    };
    if (w < 3) {
      front(std::integral_constant<int, 10>{}, mk_rsrc(wq + (size_t)(80 * j8) * 6144 + 192 * h + 64 * w, 79 * 12288 + 128), 12288, 160 * j8,
            WS_QP + (h * 8 + j8) * 3072 + w * 512);
    } else if (uq < 30) {
      const int r = uq / 6, lc = uq - 6 * r, mat = lc >= 3 ? 1 : 0, c3 = lc - 3 * mat;
      front(std::integral_constant<int, 16>{}, mk_rsrc((mat ? wu : wg) + (size_t)(128 * r) * 1536 + 192 * (h >> 2) + 64 * c3, 127 * 3072 + 128), 3072, 256 * r,
            WS_GU + (((h >> 2) * 5 + r) * 2 + mat) * 3072 + c3 * 512);
    }
    tag = ep + 1u;
  }
  STAMP(1); FS(8); PB(9);
  asm volatile("s_waitcnt vmcnt(0)" ::: "memory");
  STAMP(11); FS(9);
  __builtin_amdgcn_s_sleep(QSLEEP);
  {
    u32x4 t[3];
    for (unsigned spin = 0;; ++spin) {
      MEMFENCE();
#pragma unroll
      for (int k = 0; k < 3; ++k) t[k] = ld_sc1(rwg, g * 3072 + 16 * j + 128 * k, WS_QP + h * 8 * 3072 + b * 1536 + w * 384);
      bool ok = true;
#pragma unroll
      for (int k = 0; k < 3; ++k) ok &= (t[k][1] == tag) & (t[k][3] == tag);
      if (GOK(__all(ok)) || spin > SPIN_CAP) { STAMPV(12, spin + 1); break; }
    }
#pragma unroll
    for (int k = 0; k < 3; ++k) {
      float a = __uint_as_float(t[k][0]), c = __uint_as_float(t[k][2]);
      a += dpp<ROR8>(a); c += dpp<ROR8>(c);
      a = x32_sum(x16_sum(a)); c = x32_sum(x16_sum(c));
      if (lane < 8) l_q[24 * w + 8 * k + j] = cvtpk(a, c);
    }
    if (tid == 0) *l_tag = tag;
  }
  STAMP(2); FS(10);
  WG_BARRIER();
  const int slot = 4 * j8 + w;
  const bool has_back = slot < 30;
  const int t3 = slot / 10, cc = slot - 10 * t3;
  if (PREWRITE) {
    if (w < 3 && lane < 32) st_pair(rws, lane * 16, WS_OP + ((h * 2 + b) * 4 + s) * 1536 + w * 512, 0.f, 0.f, tag - 1u);
    if (has_back) st_pair(rws, ((lane >> 4) & 1) * (96 * 512) + (8 * j + 4 * (lane >> 5) + 2 * (g & 1)) * 8, WS_YP + cc * 2 * 96 * 512 + (h * 3 + t3) * 512, 0.f, 0.f, tag - 1u);
    asm volatile("s_waitcnt vmcnt(0)" ::: "memory");
  }
  unsigned ha[2][2] = {{0u, 0u}, {0u, 0u}};
  if (has_back) {
    u32x4 t[3];
    const int so = WS_GU + (h >> 2) * 5 * 6144 + (h & 3) * 384 + t3 * 128;
    const int vo = bl * 1536 + 16 * g;
    for (unsigned spin = 0;; ++spin) {
      MEMFENCE();
      t[0] = ld_sc1(rwg, vo + e4 * 6144, so);
      t[1] = ld_sc1(rwg, vo + e4 * 6144 + 3072, so);
      t[2] = ld_sc1(rwg, vo + 4 * 6144 + (e4 & 1) * 3072, so);
      bool ok = true;
#pragma unroll
      for (int k = 0; k < 3; ++k) ok &= (t[k][1] == tag) & (t[k][3] == tag);
      if (GOK(__all(ok)) || spin > SPIN_CAP) { STAMPV(13, spin + 1); break; }
    }
#pragma unroll
    for (int z = 0; z < 2; ++z) {
      const float ex = __uint_as_float(t[2][2 * z]);
      const float gg = rbf(quad_sum(__uint_as_float(t[0][2 * z]) + (e4 == 0 ? ex : 0.f)));
      const float uu = rbf(quad_sum(__uint_as_float(t[1][2 * z]) + (e4 == 1 ? ex : 0.f)));
      const float zz = 0.7978845608028654f * (gg + 0.044715f * gg * gg * gg);
      const float ge = rbf(gg / (1.f + __builtin_amdgcn_exp2f(-2.8853900817779268f * zz)));
      const float hv = ge * uu;
      const unsigned d = cvtpk(hv, hv);
      ha[z][0] = d & m0; ha[z][1] = d & m1;
    }
  }
  const bool reducer = (w == 0) && (bid < 160);
  const int rb = bid / 80, rc = (bid - 80 * rb) >> 3, ro = bid & 7;
  unsigned xres = 0;
  if (reducer) xres = __builtin_amdgcn_raw_buffer_load_b32(rx, (lane & 3) * 4, rb * 1280 + rc * 128 + ro * 16, AUX_X);
  STAMP(3);
  WG_BARRIER();
  if (w == 0) {
    const int so = WS_ML + (h * 2 + b) * 256;
    if (lane < 4) { const f32x2 ml = *(const f32x2*)(l_ml + 2 * lane); st_pair(rws, lane * 16, so + s * 64, ml[0], ml[1], tag); }
    asm volatile("s_waitcnt vmcnt(0)" ::: "memory");
    __builtin_amdgcn_s_sleep(MLSLEEP);
    u32x4 t;
    for (unsigned spin = 0;; ++spin) {
      MEMFENCE();
      t = ld_sc1(rws, (lane & 15) * 16, so);
      if (__all((t[1] == tag) & (t[3] == tag)) || spin > SPIN_CAP) break;
    }
    const float mv = __uint_as_float(t[0]);
    const float M = row_max(mv);
    const float f = __builtin_amdgcn_exp2f(mv - M);
    const float L = row_sum(f * __uint_as_float(t[2]));
    if ((lane >> 2) == s) l_c[lane & 3] = f / L;
  }
  STAMP(4);
  WG_BARRIER();
  u32x4 wb[10];
  if (has_back) {
    const rsrc_t rd = mk_rsrc(wd + (size_t)(48 * h + 16 * t3) * 640 + 64 * cc, 15 * 1280 + 128);
    const rsrc_t ro_ = mk_rsrc(wo + (size_t)(192 * h + 64 * t3) * 640 + 64 * cc, 63 * 1280 + 128);
#pragma unroll
    for (int n = 0; n < 2; ++n) { wb[8 + n] = ld_nt(rd, g * 2560 + 16 * j + n * 1280, 0); SB(); if (PACE) __builtin_amdgcn_s_sleep(PACE); SB(); }
#pragma unroll
    for (int n = 0; n < 8; ++n) { wb[n] = ld_nt(ro_, g * 10240 + 16 * j, n * 1280); SB(); if (PACE && n < 7) __builtin_amdgcn_s_sleep(PACE); SB(); }
    SB();
  }
  STAMP(5);
  WG_BARRIER();
  WG_BARRIER();
  if (H1SLEEP) __builtin_amdgcn_s_sleep(H1SLEEP);
  STAMP(6);
  if (has_back) {
    u32x4 t[4];
    const int so = WS_OP + h * 8 * 1536 + t3 * 512;
    const int vo = (bl * 4 + e4) * 1536 + 16 * g;
    for (unsigned spin = 0;; ++spin) {
      MEMFENCE();
#pragma unroll
      for (int k = 0; k < 4; ++k) t[k] = ld_sc1(rws, vo + 128 * k, so);
      bool ok = true;
#pragma unroll
      for (int k = 0; k < 4; ++k) ok &= (t[k][1] == tag) & (t[k][3] == tag);
      if (__all(ok) || spin > SPIN_CAP) { STAMPV(14, spin + 1); break; }
    }
    STAMP(7); PB(5);
    f32x4 acc0 = {0.f, 0.f, 0.f, 0.f}, acc1 = {0.f, 0.f, 0.f, 0.f};
#pragma unroll
    for (int z = 0; z < 2; ++z) {
      acc0 = mfma16(ha[z][0], ha[z][1], wb[8 + z][0], wb[8 + z][1], acc0);
      acc1 = mfma16(ha[z][0], ha[z][1], wb[8 + z][2], wb[8 + z][3], acc1);
    }
#pragma unroll
    for (int n = 0; n < 8; ++n) {
      const float ov = quad_sum(__uint_as_float(t[n >> 1][2 * (n & 1)]));
      const unsigned d = cvtpk(ov, ov), a0 = d & m0, a1 = d & m1;
      acc0 = mfma16(a0, a1, wb[n][0], wb[n][1], acc0);
      acc1 = mfma16(a0, a1, wb[n][2], wb[n][3], acc1);
    }
    float v0, v1;
    proj_epilogue(acc0, acc1, g & 1, v0, v1);
    st_pair(rws, ((lane >> 4) & 1) * (96 * 512) + (8 * j + 4 * (lane >> 5) + 2 * (g & 1)) * 8, WS_YP + cc * 2 * 96 * 512 + (h * 3 + t3) * 512, v0, v1, tag);
  }
  STAMP(8);
  if (reducer) {
    u32x4 t[6];
    const int so = WS_YP + (rc * 2 + rb) * 96 * 512 + ro * 64;
    const int vo = (lane >> 2) * 512 + (lane & 3) * 16;
    for (unsigned spin = 0;; ++spin) {
      MEMFENCE();
#pragma unroll
      for (int k = 0; k < 6; ++k) t[k] = ld_sc1(rws, vo, so + k * 16 * 512);
      bool ok = true;
#pragma unroll
      for (int k = 0; k < 6; ++k) ok &= (t[k][1] == tag) & (t[k][3] == tag);
      if (__all(ok) || spin > SPIN_CAP) { STAMPV(15, spin + 1); break; }
    }
    STAMP(9);
    float y0 = 0.f, y1 = 0.f;
#pragma unroll
    for (int k = 0; k < 6; ++k) { y0 += __uint_as_float(t[k][0]); y1 += __uint_as_float(t[k][2]); }
    y0 += dpp<ROR4>(y0); y1 += dpp<ROR4>(y1);
    y0 += dpp<ROR8>(y0); y1 += dpp<ROR8>(y1);
    y0 = x32_sum(x16_sum(y0)); y1 = x32_sum(x16_sum(y1));
    if (lane < 4) __builtin_amdgcn_raw_buffer_store_b32(cvtpk(bflo(xres) + y0, bfhi(xres) + y1), mk_rsrc(out, 2560), lane * 4, rb * 1280 + rc * 128 + ro * 16, AUX_OUT);
    if (bid == 0 && lane == 0) __builtin_amdgcn_raw_buffer_store_b32(tag, rws, 0, WS_EPOCH, AUX_SC1);
    STAMP(10); PB(8);
  }
  STAMP_FLUSH(); PB_END();
}

extern "C" void kernel_launch(void* const* d_in, const int* in_sizes, int n_in, void* d_out, int out_size, void* d_ws, size_t ws_size,
                              hipStream_t stream) {
  fused_decode<<<256, 512, 0, stream>>>((const u16*)d_in[0], (const u16*)d_in[1], (const u16*)d_in[2], (const u16*)d_in[3], (const u16*)d_in[4],
                                        (const u16*)d_in[6], (const u16*)d_in[7], (const u16*)d_in[8], (u16*)d_out, (unsigned char*)d_ws + WS_BASE);
}
```

```cpp
#define H1DELAY 20
#define H2DELAY 40
#define BDELAY 30
#define PACING 1
#define TEND0 1630
#define DRYRUN 0
#include <hip/hip_runtime.h>
#include <hip/hip_bf16.h>
#include <stdint.h>
#include <type_traits>

typedef unsigned short u16;
typedef unsigned u32x4 __attribute__((ext_vector_type(4)));
typedef unsigned u32x2 __attribute__((ext_vector_type(2)));
typedef float f32x4 __attribute__((ext_vector_type(4)));
typedef float f32x2 __attribute__((ext_vector_type(2)));
typedef short s16x4 __attribute__((ext_vector_type(4)));
typedef __bf16 bf16x8 __attribute__((ext_vector_type(8)));
typedef __bf16 bf16x2 __attribute__((ext_vector_type(2)));
typedef __amdgpu_buffer_rsrc_t rsrc_t;

#define WIN 9
#define B1_AT 48
#define CATCH 3
#define QSLEEP 16
#define MLSLEEP 32
#define BPACE 5
#define H1SLEEP 0
#define KC 10
#define TEND1 910
#define FB_AT 95
#define RATE16 229
#define SMP0 550
#define WT0 200
#define SMP1 500
#define WT1 100
#define CODEPF 0

constexpr int AUX_NT = 2, AUX_SC1 = 16;
constexpr int RSRC_W3 = 0x00020000;
constexpr unsigned SPIN_CAP = 1u << 18;
constexpr int OOB = 0x40000000;

constexpr int WS_EPOCH = 0;
constexpr int WS_TS = 1024;
constexpr int WS_QP = 4096;
constexpr int WS_GU = WS_QP + 32 * 5 * 2 * 192 * 8;
constexpr int WS_ML = WS_GU + 8 * 5 * 2 * 2 * 192 * 8;
constexpr int WS_OP = WS_ML + 32 * 2 * 4 * 4 * 16;
constexpr int WS_YP = WS_OP + 32 * 2 * 4 * 96 * 8;
constexpr int WS_END = WS_YP + 10 * 2 * 96 * 64 * 8;

template <int I, int N, class F> __device__ __forceinline__ void static_for(F&& f) {
  if constexpr (I < N) { f(std::integral_constant<int, I>{}); static_for<I + 1, N>(f); }
}
__device__ __forceinline__ rsrc_t mk_rsrc(const void* p, int bytes) {
  return __builtin_amdgcn_make_buffer_rsrc(const_cast<void*>(p), 0, bytes, RSRC_W3);
}
__device__ __forceinline__ u32x4 ld_nt(rsrc_t r, int voff, int soff) { return __builtin_amdgcn_raw_buffer_load_b128(r, voff, soff, AUX_NT); }
__device__ __forceinline__ u32x4 ld_sc1(rsrc_t r, int voff, int soff) { return __builtin_amdgcn_raw_buffer_load_b128(r, voff, soff, AUX_SC1); }
__device__ __forceinline__ void st_pair(rsrc_t r, int voff, int soff, float a, float b, unsigned tag) {
  u32x4 v = {__float_as_uint(a), tag, __float_as_uint(b), tag};
  __builtin_amdgcn_raw_buffer_store_b128(v, r, voff, soff, AUX_SC1);
}
__device__ __forceinline__ float bflo(unsigned v) { return __uint_as_float(v << 16); }
__device__ __forceinline__ float bfhi(unsigned v) { return __uint_as_float(v & 0xffff0000u); }
__device__ __forceinline__ unsigned cvtpk(float lo, float hi) {
  bf16x2 r = {(__bf16)lo, (__bf16)hi};
  return __builtin_bit_cast(unsigned, r);
}
__device__ __forceinline__ float rbf(float x) { return (float)(__bf16)x; }
template <int CTRL> __device__ __forceinline__ float dpp(float x) {
  return __builtin_bit_cast(float, __builtin_amdgcn_mov_dpp(__builtin_bit_cast(int, x), CTRL, 0xf, 0xf, true));
}
constexpr int XOR1 = 0xB1, XOR2 = 0x4E, HMIR = 0x141, ROR8 = 0x128, ROR4 = 0x124;
__device__ __forceinline__ float x16_sum(float x) { auto s = __builtin_amdgcn_permlane16_swap(__float_as_uint(x), __float_as_uint(x), false, false); return __uint_as_float(s[0]) + __uint_as_float(s[1]); }
__device__ __forceinline__ float x32_sum(float x) { auto s = __builtin_amdgcn_permlane32_swap(__float_as_uint(x), __float_as_uint(x), false, false); return __uint_as_float(s[0]) + __uint_as_float(s[1]); }
__device__ __forceinline__ float x16_max(float x) { auto s = __builtin_amdgcn_permlane16_swap(__float_as_uint(x), __float_as_uint(x), false, false); return fmaxf(__uint_as_float(s[0]), __uint_as_float(s[1])); }
__device__ __forceinline__ float x32_max(float x) { auto s = __builtin_amdgcn_permlane32_swap(__float_as_uint(x), __float_as_uint(x), false, false); return fmaxf(__uint_as_float(s[0]), __uint_as_float(s[1])); }
__device__ __forceinline__ float row_sum(float x) { x += dpp<XOR1>(x); x += dpp<XOR2>(x); x += dpp<HMIR>(x); x += dpp<ROR8>(x); return x; }
__device__ __forceinline__ float row_max(float x) { x = fmaxf(x, dpp<XOR1>(x)); x = fmaxf(x, dpp<XOR2>(x)); x = fmaxf(x, dpp<HMIR>(x)); x = fmaxf(x, dpp<ROR8>(x)); return x; }
__device__ __forceinline__ float quad_sum(float x) { x += dpp<XOR1>(x); x += dpp<XOR2>(x); return x; }
__device__ __forceinline__ float dot2(unsigned a, unsigned b, float c) {
  return __builtin_amdgcn_fdot2_f32_bf16(__builtin_bit_cast(bf16x2, a), __builtin_bit_cast(bf16x2, b), c, false);
}
__device__ __forceinline__ f32x4 mfma16(unsigned a0, unsigned a1, unsigned b0, unsigned b1, f32x4 c) {
  u32x2 a = {a0, a1}, b = {b0, b1};
  return __builtin_amdgcn_mfma_f32_16x16x16bf16_1k(__builtin_bit_cast(s16x4, a), __builtin_bit_cast(s16x4, b), c, 0, 0, 0);
}
__device__ __forceinline__ f32x4 mfma32(u32x4 a, u32x4 b, f32x4 c) {
  return __builtin_amdgcn_mfma_f32_16x16x32_bf16(__builtin_bit_cast(bf16x8, a), __builtin_bit_cast(bf16x8, b), c, 0, 0, 0);
}
template <int RECIP> __device__ __forceinline__ unsigned pace_step(unsigned& tgt, unsigned tend) {
  unsigned d, now;
  asm volatile("1:\n\ts_memrealtime vcc\n\ts_waitcnt lgkmcnt(0)\n\ts_sub_u32 %0, vcc_lo, %2\n\ts_cmp_lt_i32 %0, 0\n\ts_cbranch_scc1 1b\n\t"
               "s_sub_i32 %0, %3, vcc_lo\n\ts_max_i32 %0, %0, 0\n\ts_min_i32 %0, %0, 0x7d0\n\ts_mul_i32 %0, %0, %4\n\ts_lshr_b32 %0, %0, 16\n\t"
               "s_add_u32 %2, vcc_lo, %0\n\ts_mov_b32 %1, vcc_lo"
               : "=&s"(d), "=&s"(now), "+s"(tgt) : "s"(tend), "i"(RECIP) : "vcc", "scc");
  return now;
}
__device__ __forceinline__ void wait_until(unsigned tgt) {
  asm volatile("1:\n\ts_memrealtime vcc\n\ts_waitcnt lgkmcnt(0)\n\ts_sub_u32 vcc_lo, vcc_lo, %0\n\ts_cmp_lt_i32 vcc_lo, 0\n\ts_cbranch_scc1 1b" :: "s"(tgt) : "vcc", "scc", "memory");
}
#define SB() __builtin_amdgcn_sched_barrier(0)
#define WG_BARRIER() do { asm volatile("s_waitcnt lgkmcnt(0)\n\ts_barrier" ::: "memory"); } while (0)
#define MEMFENCE() asm volatile("" ::: "memory")

#define FS(i)
#define STAMP(i)
#define STAMPV(i, v)
#define STAMP_DECL
#define STAMP_FLUSH()

__device__ __forceinline__ void delay_ticks(int n) {
  if (n > 0) wait_until((unsigned)__builtin_amdgcn_s_memrealtime() + n);
}

typedef __attribute__((address_space(3))) float lfloat;
typedef __attribute__((address_space(3))) f32x4 lf32x4;
typedef __attribute__((address_space(3))) unsigned luint;
typedef __attribute__((address_space(3))) unsigned char lbyte;
template <int SLP> __device__ __forceinline__ void wait_lds(unsigned a, unsigned want) {
  unsigned v, sv;
  asm volatile("s_branch 2f\n1:\n\ts_sleep %4\n2:\n\tds_read_b32 %0, %2\n\ts_waitcnt lgkmcnt(0)\n\tv_readfirstlane_b32 %1, %0\n\ts_cmp_lg_u32 %1, %3\n\ts_cbranch_scc1 1b"
               : "=&v"(v), "=&s"(sv) : "v"(a), "s"(want), "i"(SLP) : "scc", "memory");
}

__device__ __forceinline__ void proj_epilogue(f32x4 acc0, f32x4 acc1, bool z, float& v0, float& v1) {
  float n0[4], n1[4];
#pragma unroll
  for (int r = 0; r < 4; ++r) {
    auto s = __builtin_amdgcn_permlane32_swap(__float_as_uint(acc0[r]), __float_as_uint(acc1[r]), false, false);
    n0[r] = __uint_as_float(s[0]); n1[r] = __uint_as_float(s[1]);
  }
  const float r0 = n0[0] + dpp<ROR8>(n1[0]), r1 = n0[1] + dpp<ROR8>(n1[1]);
  const float r2 = n1[2] + dpp<ROR8>(n0[2]), r3 = n1[3] + dpp<ROR8>(n0[3]);
  v0 = z ? r2 : r0; v1 = z ? r3 : r1;
}

__global__ void __launch_bounds__(512, 2)
fused_decode(const u16* __restrict__ x, const u16* __restrict__ kc, const u16* __restrict__ vc, const u16* __restrict__ wq,
             const u16* __restrict__ wo, const u16* __restrict__ wg, const u16* __restrict__ wu, const u16* __restrict__ wd,
             u16* __restrict__ out, unsigned char* __restrict__ ws
             ) {
  constexpr int tn0 = TEND0, tn1 = TEND1, tn2 = H1DELAY, tn3 = H2DELAY, tn4 = BDELAY, tn5 = RATE16;
  __shared__ __attribute__((aligned(16))) float lds[96 + 8 + 4 + 4 + 20 + 8 * 192];
  unsigned* l_q = (unsigned*)lds;
  float* l_ml = lds + 96;
  float* l_c = lds + 104;
  unsigned* l_est = (unsigned*)(lds + 108);
  unsigned char* l_f = (unsigned char*)(lds + 112);
  constexpr int FA = 0, FB = 4, FC = 8, FD = 12, FE = 16, FF = 20, FG = 24;
  unsigned* l_tend = (unsigned*)(lds + 119);
  const unsigned fbase = (unsigned)(size_t)(lbyte*)l_f;
  float* l_acc = lds + 132;

  STAMP_DECL;
  STAMP(0);
  unsigned long long pc;
  asm volatile("s_getpc_b64 %0" : "=s"(pc));
  const unsigned tstart = (unsigned)__builtin_amdgcn_s_memrealtime();
  const int bid = blockIdx.x;
  const int xq = bid & 7, uq = bid >> 3;
  const int h = xq * 4 + (uq >> 3), j8 = uq & 7, b = j8 >> 2, s = j8 & 3;
  const int tid = threadIdx.x, lane = tid & 63, wave = __builtin_amdgcn_readfirstlane(tid >> 6);
  const int g = lane >> 3, j = lane & 7;
  const int bl = (lane >> 2) & 1, e4 = lane & 3;
  const bool isS = wave >= 4, isQ = wave < 2;
  if (tid < 12) l_est[tid] = tid == 9 ? 0x01010101u : 0u;

  unsigned u6q = 2 * (2 * (7 - j8) + wave), u6f = 31 - uq;
  asm volatile("" : "+s"(u6q), "+s"(u6f));
  const unsigned u6 = isQ ? u6q : u6f;
  const unsigned ur = (u6 * 43) >> 8, uc = u6 - 6 * ur;
  const bool has_front = (wave < 3) & (u6 < 30);
  const unsigned up = uc >= 3 ? 1 : 0;
  const unsigned rot = 0;
  unsigned kv_off = ((b * 32 + h) * 8192 + 6144 + 512 * s + 8 * (wave - 4)) * 384u;
  unsigned q_off = ur * (128u * 12288u) + 384u * h + 64u * uc;
  unsigned f_off = ur * (128u * 3072u) + 384u * xq + 128u * (uc - 3 * up);
  asm volatile("" : "+s"(kv_off), "+s"(q_off), "+s"(f_off));
  const unsigned off0 = isS ? kv_off : (isQ ? q_off : f_off);
  const unsigned rowb = isQ ? 12288 : 3072;
  const unsigned gstr = isS ? 384 : 16 * rowb;
  const int nrec = isS ? 2 * 32 * 8192 * 384 : (has_front ? 640 * rowb : 0);
  const int voff = g * gstr + 16 * j + off0;
  asm volatile("s_waitcnt lgkmcnt(0)\n\ts_barrier" ::"s"(x), "s"(kc), "s"(vc), "s"(wq), "s"(wo), "s"(wg), "s"(wu), "s"(wd), "s"(out), "s"(ws), "s"(nrec), "v"(voff) : "memory");
  const rsrc_t r0 = mk_rsrc(isS ? kc : (isQ ? wq : (up ? wu : wg)), nrec);
  u32x4 buf[96];
  if (nrec) {
    const int stepA = isS ? 128 : rowb, stepB = isS ? 12288 - 256 : rowb;
    int so = isS ? rot * 12288 : 0;
#pragma unroll
    for (int n = 0; n < 9; ++n) { buf[n] = ld_nt(r0, voff, so); so += (n % 3 == 2) ? stepB : stepA; SB(); }
  }
  else if (CODEPF && wave == 3) {
    unsigned d0 = __builtin_amdgcn_raw_buffer_load_b32(mk_rsrc((const void*)(pc & ~255ull), CODEPF), lane < 5 ? lane * 4096 + uq * 128 : OOB, 0, 0);
    asm volatile("" ::"v"(d0));
  }
  FS(1);
  unsigned tag, ep;
  asm volatile("s_load_dword %0, %1, 0x0" : "=s"(ep) : "s"(ws));
#define TAG_READY() asm volatile("s_waitcnt lgkmcnt(0)\n\ts_add_u32 %0, %1, 1" : "=s"(tag) : "s"(ep) : "scc")
  const rsrc_t rws = mk_rsrc(ws, WS_END);

  if (isS) {
    const int sw = wave - 4;
    const rsrc_t rv = mk_rsrc(vc, 2 * 32 * 8192 * 384);
    u32x4 qv[3];
    float sc[16];
    unsigned pd[16];
    f32x4 acc[3] = {{0.f, 0.f, 0.f, 0.f}, {0.f, 0.f, 0.f, 0.f}, {0.f, 0.f, 0.f, 0.f}};
    unsigned msk[4];
#pragma unroll
    for (int d = 0; d < 4; ++d) msk[d] = (j >> 1) == d ? ((j & 1) ? 0xffff0000u : 0x0000ffffu) : 0u;
    constexpr float C2 = 0.07216878364870322f * 1.4426950408889634f;
    unsigned tend = 0, tgt = 0, tendv = 0;
    if (PACING) { tgt = tstart; tend = tgt + tn0; }

    auto consume = [&](auto G) {
      constexpr int t = decltype(G)::value;
      if constexpr (t < 16) {
        float a0 = 0.f, a1 = 0.f, a2 = 0.f;
#pragma unroll
        for (int e = 0; e < 4; ++e) { a0 = dot2(buf[3 * t][e], qv[0][e], a0); a1 = dot2(buf[3 * t + 1][e], qv[1][e], a1); a2 = dot2(buf[3 * t + 2][e], qv[2][e], a2); }
        float a = (a0 + a1) + a2;
        a += dpp<XOR1>(a); a += dpp<XOR2>(a); a += dpp<HMIR>(a);
        sc[t] = a;
        if constexpr (t == 15) {
          float m = sc[0];
#pragma unroll
          for (int u = 1; u < 16; ++u) m = fmaxf(m, sc[u]);
          m = fmaxf(m, dpp<ROR8>(m)); m = x16_max(m); m = x32_max(m);
          const float mc = m * C2;
          float l = 0.f;
#pragma unroll
          for (int u = 0; u < 16; ++u) { const float p = __builtin_amdgcn_exp2f(fmaf(sc[u], C2, -mc)); pd[u] = cvtpk(p, p); l += bfhi(pd[u]); }
          l += dpp<ROR8>(l); l = x16_sum(l); l = x32_sum(l);
          if (lane == 0) { *(f32x2*)(l_ml + 2 * sw) = f32x2{mc, l}; MEMFENCE(); l_f[FA + sw] = 1; }
          STAMP(3);
        }
      } else {
        constexpr int u = t - 16;
        const u32x4 a = {pd[u] & msk[0], pd[u] & msk[1], pd[u] & msk[2], pd[u] & msk[3]};
#pragma unroll
        for (int i = 0; i < 3; ++i) acc[i] = mfma32(a, buf[3 * t + i], acc[i]);
      }
    };
    constexpr auto done_after = [](int n) constexpr {
      if (n < B1_AT) return 0;
      const int landed = (n - WIN + 1) / 3;
      const int cu = 1 + (CATCH * (n - B1_AT)) / 3;
      return landed < cu ? landed : cu;
    };
    static_for<9, 96>([&](auto N) {
      constexpr int n = decltype(N)::value;
      if constexpr (n == B1_AT) {
        STAMP(1);
        wait_lds<0>(fbase + FG, 0x00010101u);
        STAMP(2);
#pragma unroll
        for (int i = 0; i < 3; ++i) qv[i] = *(const u32x4*)(l_q + 32 * i + 4 * j);
      }
      if constexpr (n >= WIN) asm volatile("s_waitcnt vmcnt(%0)" ::"i"(WIN - 1) : "memory");
      if constexpr (n == 9) TAG_READY();
      if constexpr (n >= 9 && n < 9 + 45 && (n - 9) % 5 == 0) FS(2 + (n - 9) / 5);
      if constexpr (PACING && n % 3 == 0) {
        constexpr int k = n / 3;
        if constexpr (PACING == 2 && k > 12) { const unsigned tv = __builtin_amdgcn_readfirstlane(tendv); tend = tv ? tv : tend; }
        const unsigned now = pace_step<(k < 31 ? 65536 / (31 - k) : 0)>(tgt, tend);
        if constexpr (PACING == 2 && k >= 6 && k < 26) {
          unsigned vm;
          asm volatile("s_getreg_b32 %0, hwreg(HW_REG_IB_STS, 0, 4)" : "=s"(vm));
          l_est[sw] = (now + (((vm + 3 * (31 - k)) * tn5) >> 4)) | 1u;
        }
        if constexpr (PACING == 2 && k >= 12 && k < 30) tendv = *l_tend;
      }
      if constexpr (n == FB_AT) { if (lane == 0) l_f[FB + sw] = 1; }
      constexpr int m = n < 48 ? n : n - 48;
      buf[n] = ld_nt(n < 48 ? r0 : rv, voff + (m % 3) * 128, ((m / 3 + rot) & 15) * 12288);
      SB();
      static_for<done_after(n - 1), done_after(n)>(consume);
      SB();
    });
    STAMP(4);
    __builtin_amdgcn_s_setprio(3);
    wait_lds<0>(fbase + FE, 0x01010101u);
    float cq = l_c[lane >> 4];
    unsigned dst = (unsigned)(size_t)(lfloat*)(l_acc + (sw * 2 + (g & 1)) * 192 + 8 * j + 4 * ((lane >> 4) & 1));
    unsigned src = (unsigned)(size_t)(lfloat*)(l_acc + (lane >> 4) * 384 + 64 * sw + 8 * j + 4 * (g & 1));
    unsigned fla = fbase + FC + sw, one = 1u, flr = fbase + FC;
    int pvo = (lane & 16) ? OOB : (g & 1) * 128 + j * 16 + (lane >> 5) * 8;
    int pso = WS_OP + ((h * 2 + b) * 4 + s) * 768 + sw * 256;
    unsigned tagv = tag;
    asm volatile("" : "+v"(cq), "+v"(dst), "+v"(src), "+v"(fla), "+v"(flr), "+v"(one), "+v"(pvo), "+s"(pso), "+v"(tagv));
    const bool wr = (lane >> 5) == (g & 1);
    SB();
    static_for<done_after(95), 32>([&](auto G) { consume(G); SB(); });
    if (wr) {
#pragma unroll
      for (int i = 0; i < 3; ++i) *(lf32x4*)(size_t)(dst + 256 * i) = acc[i];
      MEMFENCE();
      *(lbyte*)(size_t)fla = (unsigned char)one;
    }
    STAMP(5);
    if (sw < 3) {
      f32x4 u0, u1;
      unsigned fv, fs;
      asm volatile("1:\n\tds_read_b32 %0, %4\n\tds_read_b128 %2, %5\n\tds_read_b128 %3, %5 offset:768\n\ts_waitcnt lgkmcnt(2)\n\tv_readfirstlane_b32 %1, %0\n\t"
                   "s_cmp_lg_u32 %1, 0x01010101\n\ts_waitcnt lgkmcnt(0)\n\ts_cbranch_scc1 1b"
                   : "=&v"(fv), "=&s"(fs), "=&v"(u0), "=&v"(u1) : "v"(flr), "v"(src) : "scc", "memory");
      float o[4];
#pragma unroll
      for (int e = 0; e < 4; ++e) o[e] = cq * (u0[e] + u1[e]);
      const auto s0 = __builtin_amdgcn_permlane32_swap(__float_as_uint(o[0]), __float_as_uint(o[2]), false, false);
      const auto s1 = __builtin_amdgcn_permlane32_swap(__float_as_uint(o[1]), __float_as_uint(o[3]), false, false);
      const float p0 = x16_sum(__uint_as_float(s0[0]) + __uint_as_float(s0[1])), p1 = x16_sum(__uint_as_float(s1[0]) + __uint_as_float(s1[1]));
      const u32x2 pv = {cvtpk(p0, p1), tagv};
      __builtin_amdgcn_raw_buffer_store_b64(pv, rws, pvo, pso, AUX_SC1);
    }
    MEMFENCE();
    *(lbyte*)(size_t)(fla + (FD - FC)) = (unsigned char)one;
    STAMP(6);
    STAMP_FLUSH();
    return;
  }

  __builtin_amdgcn_s_setprio(2);
  const int w = wave;
  const unsigned m0 = e4 == 0 ? 0x0000ffffu : e4 == 1 ? 0xffff0000u : 0u;
  const unsigned m1 = e4 == 2 ? 0x0000ffffu : e4 == 3 ? 0xffff0000u : 0u;
  const int st_col = (8 * j + 4 * (lane >> 5) + 2 * (g & 1)) * 8;
  const rsrc_t rx = mk_rsrc(x, 2560);
  if (has_front) {
    const int xo = (lane >> 5) * 1280 + (lane & 31) * 16;
    const bool x3 = (lane & 16) == 0;
    const u32x4 xa = __builtin_amdgcn_raw_buffer_load_b128(rx, xo, 0, 0), xb = __builtin_amdgcn_raw_buffer_load_b128(rx, xo + 512, 0, 0);
    const u32x4 xc = __builtin_amdgcn_raw_buffer_load_b128(rx, x3 ? xo + 1024 : OOB, 0, 0);
    const u32x4 xr0 = __builtin_amdgcn_raw_buffer_load_b128(rx, bl * 1280 + g * 32, 256 * ur, 0);
    const u32x4 xr1 = __builtin_amdgcn_raw_buffer_load_b128(rx, bl * 1280 + g * 32 + 16, 256 * ur, 0);
    SB();
#pragma unroll
    for (int n = 9; n < 16; ++n) buf[n] = ld_nt(r0, voff, n * rowb);
    SB();
    FS(2);
    TAG_READY();
    float v[24];
#pragma unroll
    for (int e = 0; e < 4; ++e) { v[2 * e] = bflo(xa[e]); v[2 * e + 1] = bfhi(xa[e]); v[8 + 2 * e] = bflo(xb[e]); v[9 + 2 * e] = bfhi(xb[e]); v[16 + 2 * e] = bflo(xc[e]); v[17 + 2 * e] = bfhi(xc[e]); }
    float sum = 0.f;
#pragma unroll
    for (int e = 0; e < 24; ++e) sum += v[e];
    sum = x16_sum(row_sum(sum));
    const float mean = sum * (1.f / 640.f);
    float sq = 0.f, sq3 = 0.f;
#pragma unroll
    for (int e = 0; e < 16; ++e) { const float d = v[e] - mean; sq = fmaf(d, d, sq); }
#pragma unroll
    for (int e = 16; e < 24; ++e) { const float d = v[e] - mean; sq3 = fmaf(d, d, sq3); }
    sq = x16_sum(row_sum(sq + (x3 ? sq3 : 0.f)));
    const float mb = rbf(mean), den = rbf(__builtin_amdgcn_sqrtf(rbf(rbf(sq * (1.f / 640.f)) + 1e-5f)));
    const float inv = __builtin_amdgcn_rcpf(den);
    const float mb0 = __builtin_bit_cast(float, __builtin_amdgcn_readlane(__builtin_bit_cast(int, mb), 0));
    const float mb1 = __builtin_bit_cast(float, __builtin_amdgcn_readlane(__builtin_bit_cast(int, mb), 32));
    const float iv0 = __builtin_bit_cast(float, __builtin_amdgcn_readlane(__builtin_bit_cast(int, inv), 0));
    const float iv1 = __builtin_bit_cast(float, __builtin_amdgcn_readlane(__builtin_bit_cast(int, inv), 32));
    const float mm = bl ? mb1 : mb0, ii = bl ? iv1 : iv0;
    FS(3);
    f32x4 acc0 = {0.f, 0.f, 0.f, 0.f}, acc1 = {0.f, 0.f, 0.f, 0.f};
#pragma unroll
    for (int n = 0; n < 16; ++n) {
      const unsigned xw = n < 8 ? xr0[n >> 1] : xr1[(n - 8) >> 1];
      const float xv = (n & 1) ? bfhi(xw) : bflo(xw);
      const float xn = rbf(xv - mm) * ii;
      const unsigned d = cvtpk(xn, xn), a0 = d & m0, a1 = d & m1;
      acc0 = mfma16(a0, a1, buf[n][0], buf[n][1], acc0);
      acc1 = mfma16(a0, a1, buf[n][2], buf[n][3], acc1);
    }
    float v0, v1;
    proj_epilogue(acc0, acc1, g & 1, v0, v1);
    const int rec = w < 2 ? WS_QP + (h * 5 + ur) * 3072 + uc * 256 : WS_GU + ((xq * 5 + ur) * 2 + up) * 3072 + (uc - 3 * up) * 512;
    st_pair(rws, ((lane >> 4) & 1) * 1536 + st_col, rec, v0, v1, tag);
    STAMP(1); FS(4);
    asm volatile("s_waitcnt vmcnt(0)" ::: "memory");
    STAMP(11); FS(5);
  } else {
    TAG_READY();
    if (CODEPF) wait_until(tstart + 120);
  }
  const int slot = w * 8 + j8;
  const bool has_back = slot < 30;
  const int t3 = slot / 10, cc = slot - 10 * t3;
  const int rb = bid / 80, rc = (bid - 80 * rb) >> 3, ro = bid & 7;
  {
    const unsigned sLo = (e4 & 1) ? 0x01000c0cu : 0x0c0c0100u, sHi = (e4 & 1) ? 0x03020c0cu : 0x0c0c0302u;
    unsigned sel0 = e4 < 2 ? sLo : 0x0c0c0c0cu, sel1 = e4 < 2 ? 0x0c0c0c0cu : sLo, sel2 = e4 < 2 ? sHi : 0x0c0c0c0cu, sel3 = e4 < 2 ? 0x0c0c0c0cu : sHi;
    int op_so = WS_OP + h * 8 * 768 + t3 * 256, op_vo = (bl * 4 + e4) * 768 + 16 * g;
    int d_vo = ((48 * h + 16 * t3 + 2 * g) * 640 + 64 * cc) * 2 + 16 * j, o_vo = ((192 * h + 64 * t3 + 8 * g) * 640 + 64 * cc) * 2 + 16 * j;
    int yvo = ((lane >> 4) & 1) * (96 * 512) + st_col, yso = WS_YP + cc * 2 * 96 * 512 + (h * 3 + t3) * 512;
    int r_vo = (lane >> 2) * 512 + (lane & 3) * 16 + WS_YP + (rc * 2 + rb) * 96 * 512 + ro * 64;
    int out_vo = lane < 4 ? lane * 4 + rb * 1280 + rc * 128 + ro * 16 : OOB;
    unsigned tagv = tag;
    asm volatile("" : "+v"(sel0), "+v"(sel1), "+v"(sel2), "+v"(sel3), "+s"(op_so), "+v"(op_vo), "+v"(d_vo), "+v"(o_vo), "+v"(yvo), "+s"(yso), "+v"(r_vo), "+v"(out_vo), "+v"(tagv));
    int pass = DRYRUN ? __builtin_amdgcn_readfirstlane(w == 3 ? 0 : 1) : 1;
    asm volatile("" : "+s"(pass));
    unsigned ha[2][2] = {{0u, 0u}, {0u, 0u}};
    unsigned xres = 0;
#pragma nounroll
    for (; pass < 2; ++pass) {
      const bool reducer = !pass || ((w == 0) && (bid < 160));
      if (pass) {
        unsigned tend = tstart + tn0;
        auto pace_round = [&](unsigned t_sample, unsigned t_read, int so) {
          wait_until(t_sample);
          u32x4 e;
          do { MEMFENCE(); e = *(const u32x4*)l_est; } while (!__builtin_amdgcn_readfirstlane(e[0] & e[1] & e[2] & e[3] & 1u));
          const u32x2 pv = {tstart + (unsigned)((int)((e[0] - tstart) + (e[1] - tstart) + (e[2] - tstart) + (e[3] - tstart)) >> 2), tag};
          __builtin_amdgcn_raw_buffer_store_b64(pv, rws, (lane == 0 && bid < 32) ? bid * 8 : OOB, so, AUX_SC1);
          wait_until(t_read);
          MEMFENCE();
          const u32x2 tv = __builtin_amdgcn_raw_buffer_load_b64(rws, (lane & 31) * 8, so, AUX_SC1);
          if (__all(tv[1] == tag)) {
            const float d = x16_sum(row_sum((float)(int)(tv[0] - tstart)));
            int di = (int)(d * (1.f / 32.f)) - tn1;
            di = di < 0 ? 0 : (di > 2500 ? 2500 : di);
            tend = (tstart + __builtin_amdgcn_readfirstlane(di)) | 1u;
            *l_tend = tend;
          }
        };
        if (w < 3) {
          if (has_front) __builtin_amdgcn_s_sleep(QSLEEP); else wait_until(tstart + 520);
          u32x4 t[3];
          const int vo = (lane >> 5) * 3072 + (lane & 31) * 16, so = WS_QP + h * 5 * 3072 + b * 1536 + w * 512;
          for (unsigned spin = 0;; ++spin) {
            MEMFENCE();
            t[0] = ld_sc1(rws, vo, so); t[1] = ld_sc1(rws, vo, so + 6144); t[2] = ld_sc1(rws, lane < 32 ? vo : OOB, so + 12288);
            const bool ok = (t[0][1] == tag) & (t[0][3] == tag) & (t[1][1] == tag) & (t[1][3] == tag) & (((t[2][1] == tag) & (t[2][3] == tag)) | (lane >= 32));
            if (__all(ok) || spin > SPIN_CAP) { STAMPV(12, spin + 1); break; }
          }
          const float a = x32_sum((__uint_as_float(t[0][0]) + __uint_as_float(t[1][0])) + __uint_as_float(t[2][0]));
          const float c = x32_sum((__uint_as_float(t[0][2]) + __uint_as_float(t[1][2])) + __uint_as_float(t[2][2]));
          if (lane < 32) { l_q[32 * w + lane] = cvtpk(a, c); MEMFENCE(); l_f[FG + w] = 1; }
        } else if (PACING == 2) {
          pace_round(tstart + SMP0, tstart + SMP0 + WT0, WS_TS);
        } else {
          wait_until(tstart + 700);
        }
        STAMP(2); FS(6);
        if (has_back) {
          u32x4 t[3];
          const int so = WS_GU + xq * 5 * 6144 + (h & 3) * 384 + t3 * 128;
          const int vo = bl * 1536 + 16 * g;
          for (unsigned spin = 0;; ++spin) {
            MEMFENCE();
            t[0] = ld_sc1(rws, vo + e4 * 6144, so);
            t[1] = ld_sc1(rws, vo + e4 * 6144 + 3072, so);
            t[2] = ld_sc1(rws, vo + 4 * 6144 + (e4 & 1) * 3072, so);
            bool ok = true;
#pragma unroll
            for (int k = 0; k < 3; ++k) ok &= (t[k][1] == tag) & (t[k][3] == tag);
            if (__all(ok) || spin > SPIN_CAP) { STAMPV(13, spin + 1); break; }
          }
#pragma unroll
          for (int z = 0; z < 2; ++z) {
            const float ex = __uint_as_float(t[2][2 * z]);
            const float gg = rbf(quad_sum(__uint_as_float(t[0][2 * z]) + (e4 == 0 ? ex : 0.f)));
            const float uu = rbf(quad_sum(__uint_as_float(t[1][2 * z]) + (e4 == 1 ? ex : 0.f)));
            const float zz = 0.7978845608028654f * (gg + 0.044715f * gg * gg * gg);
            const float ge = rbf(gg * __builtin_amdgcn_rcpf(1.f + __builtin_amdgcn_exp2f(-2.8853900817779268f * zz)));
            const float hv = ge * uu;
            const unsigned d = cvtpk(hv, hv);
            ha[z][0] = d & m0; ha[z][1] = d & m1;
          }
        }
        if (reducer) xres = __builtin_amdgcn_raw_buffer_load_b32(rx, (lane & 3) * 4, rb * 1280 + rc * 128 + ro * 16, 0);
        STAMP(3);
        if (w == 1) {
          wait_lds<4>(fbase + FA, 0x01010101u);
          const int so = WS_ML + (h * 2 + b) * 256;
          if (lane < 4) { const f32x2 ml = *(const f32x2*)(l_ml + 2 * lane); st_pair(rws, lane * 16, so + s * 64, ml[0], ml[1], tag); }
          asm volatile("s_waitcnt vmcnt(0)" ::: "memory");
          __builtin_amdgcn_s_sleep(MLSLEEP);
          u32x4 t;
          for (unsigned spin = 0;; ++spin) {
            MEMFENCE();
            t = ld_sc1(rws, (lane & 15) * 16, so);
            if (__all((t[1] == tag) & (t[3] == tag)) || spin > SPIN_CAP) break;
          }
          const float mv = __uint_as_float(t[0]);
          const float M = row_max(mv);
          const float f = __builtin_amdgcn_exp2f(mv - M);
          const float L = row_sum(f * __uint_as_float(t[2]));
          if ((lane >> 2) == s) { l_c[lane & 3] = f / L; MEMFENCE(); l_f[FE + (lane & 3)] = 1; }
        }
        if (PACING == 2 && w == 3) {
          pace_round(tend - SMP1, tend - SMP1 + WT1, WS_TS + 256);
        }
        STAMP(4);
        if (!has_back) break;
      }
      const int live = pass ? -1 : 0;
      const rsrc_t rd = mk_rsrc(wd, 1536 * 1280 & live), ro_ = mk_rsrc(wo, 6144 * 1280 & live), rw2 = mk_rsrc(ws, WS_END & live), rout = mk_rsrc(out, 2560 & live);
      const unsigned want = tag & live;
      unsigned fb = fbase + (pass ? FB : FF), fd = fbase + (pass ? FD : FF);
      asm volatile("" : "+v"(fb), "+v"(fd));
      wait_lds<1>(fb, 0x01010101u);
      delay_ticks(tn4 & live);
      u32x4 wb[10];
#pragma unroll
      for (int n = 0; n < 2; ++n) { wb[8 + n] = ld_nt(rd, d_vo + n * 1280, 0); SB(); if (BPACE) __builtin_amdgcn_s_sleep(BPACE); SB(); }
#pragma unroll
      for (int n = 0; n < 8; ++n) { wb[n] = ld_nt(ro_, o_vo, n * 1280); SB(); if (BPACE && n < 7) __builtin_amdgcn_s_sleep(BPACE); SB(); }
      STAMP(5);
      wait_lds<1>(fd, 0x01010101u);
      delay_ticks(tn2 & live);
      STAMP(6);
      u32x4 t0 = ld_sc1(rw2, op_vo, op_so), t1 = ld_sc1(rw2, op_vo + 128, op_so);
      SB();
      u32x4 bq[10];
#pragma unroll
      for (int p = 0; p < 5; ++p) {
        bq[2 * p] = u32x4{wb[2 * p][0], wb[2 * p][1], wb[2 * p + 1][0], wb[2 * p + 1][1]};
        bq[2 * p + 1] = u32x4{wb[2 * p][2], wb[2 * p][3], wb[2 * p + 1][2], wb[2 * p + 1][3]};
      }
      const u32x4 hA = {ha[0][0], ha[0][1], ha[1][0], ha[1][1]};
      f32x4 acc0 = mfma32(hA, bq[8], f32x4{0.f, 0.f, 0.f, 0.f}), acc1 = mfma32(hA, bq[9], f32x4{0.f, 0.f, 0.f, 0.f});
      asm volatile("" : "+v"(bq[0]), "+v"(bq[1]), "+v"(bq[2]), "+v"(bq[3]), "+v"(bq[4]), "+v"(bq[5]), "+v"(bq[6]), "+v"(bq[7]), "+v"(acc0), "+v"(acc1));
      SB();
      unsigned np1 = 1;
      while (!__all((t0[1] == want) & (t0[3] == want) & (t1[1] == want) & (t1[3] == want)) && np1 < SPIN_CAP) {
        ++np1;
        MEMFENCE();
        t0 = ld_sc1(rw2, op_vo, op_so); t1 = ld_sc1(rw2, op_vo + 128, op_so);
      }
      STAMPV(14, np1);
      STAMP(7);
      float ov[8] = {bflo(t0[0]), bfhi(t0[0]), bflo(t0[2]), bfhi(t0[2]), bflo(t1[0]), bfhi(t1[0]), bflo(t1[2]), bfhi(t1[2])};
#pragma unroll
      for (int n = 0; n < 8; ++n) ov[n] += dpp<XOR1>(ov[n]);
      SB();
#pragma unroll
      for (int n = 0; n < 8; ++n) ov[n] += dpp<XOR2>(ov[n]);
      SB();
#pragma unroll
      for (int p = 0; p < 4; ++p) {
        const unsigned d = cvtpk(ov[2 * p], ov[2 * p + 1]);
        const u32x4 a = {__builtin_amdgcn_perm(0u, d, sel0), __builtin_amdgcn_perm(0u, d, sel1), __builtin_amdgcn_perm(0u, d, sel2), __builtin_amdgcn_perm(0u, d, sel3)};
        acc0 = mfma32(a, bq[2 * p], acc0);
        acc1 = mfma32(a, bq[2 * p + 1], acc1);
      }
      float v0, v1;
      proj_epilogue(acc0, acc1, g & 1, v0, v1);
      st_pair(rw2, yvo, yso, v0, v1, tagv);
      STAMP(8);
      if (reducer) {
        delay_ticks(tn3 & live);
        u32x4 t[6];
        unsigned np2 = 0;
        for (;;) {
          ++np2;
          MEMFENCE();
#pragma unroll
          for (int k = 0; k < 6; ++k) t[k] = ld_sc1(rw2, r_vo, k * 16 * 512);
          bool ok = true;
#pragma unroll
          for (int k = 0; k < 6; ++k) ok &= (t[k][1] == want) & (t[k][3] == want);
          if (__all(ok) || np2 > SPIN_CAP) break;
        }
        STAMPV(15, np2);
        STAMP(9);
        float y0 = ((__uint_as_float(t[0][0]) + __uint_as_float(t[1][0])) + (__uint_as_float(t[2][0]) + __uint_as_float(t[3][0]))) + (__uint_as_float(t[4][0]) + __uint_as_float(t[5][0]));
        float y1 = ((__uint_as_float(t[0][2]) + __uint_as_float(t[1][2])) + (__uint_as_float(t[2][2]) + __uint_as_float(t[3][2]))) + (__uint_as_float(t[4][2]) + __uint_as_float(t[5][2]));
        y0 += dpp<ROR4>(y0); y1 += dpp<ROR4>(y1);
        y0 += dpp<ROR8>(y0); y1 += dpp<ROR8>(y1);
        y0 = x32_sum(x16_sum(y0)); y1 = x32_sum(x16_sum(y1));
        __builtin_amdgcn_raw_buffer_store_b32(cvtpk(bflo(xres) + y0, bfhi(xres) + y1), rout, out_vo, 0, 0);
        if (bid == 0) __builtin_amdgcn_raw_buffer_store_b32(tagv, rw2, lane == 0 ? WS_EPOCH : OOB, 0, AUX_SC1);
        STAMP(10);
      }
    }
  }
  STAMP_FLUSH();
}

extern "C" void kernel_launch(void* const* d_in, const int* in_sizes, int n_in, void* d_out, int out_size, void* d_ws, size_t ws_size,
                              hipStream_t stream) {
  fused_decode<<<256, 512, 0, stream>>>((const u16*)d_in[0], (const u16*)d_in[1], (const u16*)d_in[2], (const u16*)d_in[3], (const u16*)d_in[4],
                                        (const u16*)d_in[6], (const u16*)d_in[7], (const u16*)d_in[8], (u16*)d_out, (unsigned char*)d_ws
                                        );
}
```

```cpp
#define H1DELAY 20
#define H2DELAY 40
#define BDELAY 30
#define PACING 2
#define TEND0 1540
#define SMP0 600
#define WT0 250
#define PRETOUCH 250
#define TEND1 (-70)
#define CODEPF 15872
#include <hip/hip_runtime.h>
#include <hip/hip_bf16.h>
#include <stdint.h>
#include <type_traits>

typedef unsigned short u16;
typedef unsigned u32x4 __attribute__((ext_vector_type(4)));
typedef unsigned u32x2 __attribute__((ext_vector_type(2)));
typedef float f32x4 __attribute__((ext_vector_type(4)));
typedef float f32x2 __attribute__((ext_vector_type(2)));
typedef short s16x4 __attribute__((ext_vector_type(4)));
typedef __bf16 bf16x8 __attribute__((ext_vector_type(8)));
typedef __bf16 bf16x2 __attribute__((ext_vector_type(2)));
typedef __amdgpu_buffer_rsrc_t rsrc_t;

#define WIN 9
#define B1_AT 48
#define CATCH 3
#define QSLEEP 16
#define MLSLEEP 32
#define BPACE 5
#define H1SLEEP 0
#define KC 10
#define FB_AT 95
#define DRYRUN 1
#define RATE16 229
#define SMP1 500
#define WT1 100

constexpr int AUX_NT = 2, AUX_SC1 = 16;
constexpr int RSRC_W3 = 0x00020000;
constexpr unsigned SPIN_CAP = 1u << 18;
constexpr int OOB = 0x40000000;

constexpr int WS_EPOCH = 0;
constexpr int WS_TS = 1024;
constexpr int WS_QP = 4096;
constexpr int WS_GU = WS_QP + 32 * 5 * 2 * 192 * 8;
constexpr int WS_ML = WS_GU + 8 * 5 * 2 * 2 * 192 * 8;
constexpr int WS_OP = WS_ML + 32 * 2 * 4 * 4 * 16;
constexpr int WS_YP = WS_OP + 32 * 2 * 4 * 96 * 8;
constexpr int WS_END = WS_YP + 10 * 2 * 96 * 64 * 8;
constexpr int WS_PAD = 512, WS_SPAN = WS_END & ~1023;

template <int I, int N, class F> __device__ __forceinline__ void static_for(F&& f) {
  if constexpr (I < N) { f(std::integral_constant<int, I>{}); static_for<I + 1, N>(f); }
}
__device__ __forceinline__ rsrc_t mk_rsrc(const void* p, int bytes) {
  return __builtin_amdgcn_make_buffer_rsrc(const_cast<void*>(p), 0, bytes, RSRC_W3);
}
__device__ __forceinline__ u32x4 ld_nt(rsrc_t r, int voff, int soff) { return __builtin_amdgcn_raw_buffer_load_b128(r, voff, soff, AUX_NT); }
__device__ __forceinline__ u32x4 ld_sc1(rsrc_t r, int voff, int soff) { return __builtin_amdgcn_raw_buffer_load_b128(r, voff, soff, AUX_SC1); }
__device__ __forceinline__ void st_pair(rsrc_t r, int voff, int soff, float a, float b, unsigned tag) {
  u32x4 v = {__float_as_uint(a), tag, __float_as_uint(b), tag};
  __builtin_amdgcn_raw_buffer_store_b128(v, r, voff, soff, AUX_SC1);
}
__device__ __forceinline__ float bflo(unsigned v) { return __uint_as_float(v << 16); }
__device__ __forceinline__ float bfhi(unsigned v) { return __uint_as_float(v & 0xffff0000u); }
__device__ __forceinline__ unsigned cvtpk(float lo, float hi) {
  bf16x2 r = {(__bf16)lo, (__bf16)hi};
  return __builtin_bit_cast(unsigned, r);
}
__device__ __forceinline__ float rbf(float x) { return (float)(__bf16)x; }
template <int CTRL> __device__ __forceinline__ float dpp(float x) {
  return __builtin_bit_cast(float, __builtin_amdgcn_mov_dpp(__builtin_bit_cast(int, x), CTRL, 0xf, 0xf, true));
}
constexpr int XOR1 = 0xB1, XOR2 = 0x4E, HMIR = 0x141, ROR8 = 0x128, ROR4 = 0x124;
__device__ __forceinline__ float x16_sum(float x) { auto s = __builtin_amdgcn_permlane16_swap(__float_as_uint(x), __float_as_uint(x), false, false); return __uint_as_float(s[0]) + __uint_as_float(s[1]); }
__device__ __forceinline__ float x32_sum(float x) { auto s = __builtin_amdgcn_permlane32_swap(__float_as_uint(x), __float_as_uint(x), false, false); return __uint_as_float(s[0]) + __uint_as_float(s[1]); }
__device__ __forceinline__ float x16_max(float x) { auto s = __builtin_amdgcn_permlane16_swap(__float_as_uint(x), __float_as_uint(x), false, false); return fmaxf(__uint_as_float(s[0]), __uint_as_float(s[1])); }
__device__ __forceinline__ float x32_max(float x) { auto s = __builtin_amdgcn_permlane32_swap(__float_as_uint(x), __float_as_uint(x), false, false); return fmaxf(__uint_as_float(s[0]), __uint_as_float(s[1])); }
__device__ __forceinline__ float row_sum(float x) { x += dpp<XOR1>(x); x += dpp<XOR2>(x); x += dpp<HMIR>(x); x += dpp<ROR8>(x); return x; }
__device__ __forceinline__ float row_max(float x) { x = fmaxf(x, dpp<XOR1>(x)); x = fmaxf(x, dpp<XOR2>(x)); x = fmaxf(x, dpp<HMIR>(x)); x = fmaxf(x, dpp<ROR8>(x)); return x; }
__device__ __forceinline__ float quad_sum(float x) { x += dpp<XOR1>(x); x += dpp<XOR2>(x); return x; }
__device__ __forceinline__ float dot2(unsigned a, unsigned b, float c) {
  return __builtin_amdgcn_fdot2_f32_bf16(__builtin_bit_cast(bf16x2, a), __builtin_bit_cast(bf16x2, b), c, false);
}
__device__ __forceinline__ f32x4 mfma16(unsigned a0, unsigned a1, unsigned b0, unsigned b1, f32x4 c) {
  u32x2 a = {a0, a1}, b = {b0, b1};
  return __builtin_amdgcn_mfma_f32_16x16x16bf16_1k(__builtin_bit_cast(s16x4, a), __builtin_bit_cast(s16x4, b), c, 0, 0, 0);
}
__device__ __forceinline__ f32x4 mfma32(u32x4 a, u32x4 b, f32x4 c) {
  return __builtin_amdgcn_mfma_f32_16x16x32_bf16(__builtin_bit_cast(bf16x8, a), __builtin_bit_cast(bf16x8, b), c, 0, 0, 0);
}
template <int RECIP> __device__ __forceinline__ unsigned pace_step(unsigned& tgt, unsigned tend) {
  unsigned d, now;
  asm volatile("1:\n\ts_memrealtime vcc\n\ts_waitcnt lgkmcnt(0)\n\ts_sub_u32 %0, vcc_lo, %2\n\ts_cmp_lt_i32 %0, 0\n\ts_cbranch_scc1 1b\n\t"
               "s_sub_i32 %0, %3, vcc_lo\n\ts_max_i32 %0, %0, 0\n\ts_min_i32 %0, %0, 0x7d0\n\ts_mul_i32 %0, %0, %4\n\ts_lshr_b32 %0, %0, 16\n\t"
               "s_add_u32 %2, vcc_lo, %0\n\ts_mov_b32 %1, vcc_lo"
               : "=&s"(d), "=&s"(now), "+s"(tgt) : "s"(tend), "i"(RECIP) : "vcc", "scc");
  return now;
}
__device__ __forceinline__ void wait_until(unsigned tgt) {
  asm volatile("1:\n\ts_memrealtime vcc\n\ts_waitcnt lgkmcnt(0)\n\ts_sub_u32 vcc_lo, vcc_lo, %0\n\ts_cmp_lt_i32 vcc_lo, 0\n\ts_cbranch_scc1 1b" :: "s"(tgt) : "vcc", "scc", "memory");
}
#define SB() __builtin_amdgcn_sched_barrier(0)
#define WG_BARRIER() do { asm volatile("s_waitcnt lgkmcnt(0)\n\ts_barrier" ::: "memory"); } while (0)
#define MEMFENCE() asm volatile("" ::: "memory")

#define FS(i)
#define STAMP(i)
#define STAMPV(i, v)
#define STAMP_DECL
#define STAMP_FLUSH()

__device__ __forceinline__ void delay_ticks(int n) {
  if (n > 0) wait_until((unsigned)__builtin_amdgcn_s_memrealtime() + n);
}

typedef __attribute__((address_space(3))) float lfloat;
typedef __attribute__((address_space(3))) f32x4 lf32x4;
typedef __attribute__((address_space(3))) unsigned luint;
typedef __attribute__((address_space(3))) unsigned char lbyte;
template <int SLP> __device__ __forceinline__ void wait_lds(unsigned a, unsigned want) {
  unsigned v, sv;
  asm volatile("s_branch 2f\n1:\n\ts_sleep %4\n2:\n\tds_read_b32 %0, %2\n\ts_waitcnt lgkmcnt(0)\n\tv_readfirstlane_b32 %1, %0\n\ts_cmp_lg_u32 %1, %3\n\ts_cbranch_scc1 1b"
               : "=&v"(v), "=&s"(sv) : "v"(a), "s"(want), "i"(SLP) : "scc", "memory");
}

__device__ __forceinline__ void proj_epilogue(f32x4 acc0, f32x4 acc1, bool z, float& v0, float& v1) {
  float n0[4], n1[4];
#pragma unroll
  for (int r = 0; r < 4; ++r) {
    auto s = __builtin_amdgcn_permlane32_swap(__float_as_uint(acc0[r]), __float_as_uint(acc1[r]), false, false);
    n0[r] = __uint_as_float(s[0]); n1[r] = __uint_as_float(s[1]);
  }
  const float r0 = n0[0] + dpp<ROR8>(n1[0]), r1 = n0[1] + dpp<ROR8>(n1[1]);
  const float r2 = n1[2] + dpp<ROR8>(n0[2]), r3 = n1[3] + dpp<ROR8>(n0[3]);
  v0 = z ? r2 : r0; v1 = z ? r3 : r1;
}

__global__ void __launch_bounds__(512, 2)
fused_decode(const u16* x_, const u16* kc_, const u16* vc_, const u16* wq_, const u16* wo_, const u16* wg_, const u16* wu_, const u16* wd_, u16* out_, unsigned char* ws_
             ) {
  constexpr int tn0 = TEND0, tn1 = TEND1, tn2 = H1DELAY, tn3 = H2DELAY, tn4 = BDELAY, tn5 = RATE16, tn6 = SMP0, tn7 = WT0;
  __shared__ __attribute__((aligned(16))) float lds[96 + 8 + 4 + 4 + 20 + 8 * 192];
  unsigned* l_q = (unsigned*)lds;
  float* l_ml = lds + 96;
  float* l_c = lds + 104;
  unsigned* l_est = (unsigned*)(lds + 108);
  unsigned char* l_f = (unsigned char*)(lds + 112);
  constexpr int FA = 0, FB = 4, FC = 8, FD = 12, FE = 16, FF = 20, FG = 24;
  unsigned* l_tend = (unsigned*)(lds + 119);
  const unsigned fbase = (unsigned)(size_t)(lbyte*)l_f;
  float* l_acc = lds + 132;

  STAMP_DECL;
  STAMP(0);
  typedef unsigned long long u64;
  typedef u64 u64x8 __attribute__((ext_vector_type(8)));
  typedef u64 u64x2 __attribute__((ext_vector_type(2)));
  u64x8 ka; u64x2 kb; u64 tst, pc;
  asm volatile("s_load_dwordx16 %0, %4, 0x0\n\ts_load_dwordx4 %1, %4, 0x40\n\ts_memrealtime %2\n\ts_getpc_b64 %3"
               : "=&s"(ka), "=&s"(kb), "=&s"(tst), "=&s"(pc) : "s"(__builtin_amdgcn_kernarg_segment_ptr()));
  const int bid = blockIdx.x;
  const int xq = bid & 7, uq = bid >> 3;
  const int h = xq * 4 + (uq >> 3), j8 = uq & 7, b = j8 >> 2, s = j8 & 3;
  const int tid = threadIdx.x, lane = tid & 63, wave = __builtin_amdgcn_readfirstlane(tid >> 6);
  const int g = lane >> 3, j = lane & 7;
  const int bl = (lane >> 2) & 1, e4 = lane & 3;
  const bool isS = wave >= 4, isQ = wave < 2;
  if (tid < 12) l_est[tid] = tid == 9 ? 0x01010101u : 0u;

  unsigned u6q = 2 * (2 * (7 - j8) + wave), u6f = 31 - uq;
  asm volatile("" : "+s"(u6q), "+s"(u6f));
  const unsigned u6 = isQ ? u6q : u6f;
  const unsigned ur = (u6 * 43) >> 8, uc = u6 - 6 * ur;
  const bool has_front = (wave < 3) & (u6 < 30);
  const unsigned up = uc >= 3 ? 1 : 0;
  unsigned kv_off = ((b * 32 + h) * 8192 + 6144 + 512 * s + 8 * (wave - 4)) * 384u;
  unsigned q_off = ur * (128u * 12288u) + 384u * h + 64u * uc;
  unsigned f_off = ur * (128u * 3072u) + 384u * xq + 128u * (uc - 3 * up);
  asm volatile("" : "+s"(kv_off), "+s"(q_off), "+s"(f_off));
  const unsigned off0 = isS ? kv_off : (isQ ? q_off : f_off);
  const unsigned rowb = isQ ? 12288 : 3072;
  const unsigned gstr = isS ? 384 : 16 * rowb;
  const int nrec = isS ? 2 * 32 * 8192 * 384 : (has_front ? 640 * rowb : 0);
  const int voff = g * gstr + 16 * j + off0;
  const int stepA = isS ? 128 : rowb, stepB = isS ? 12288 - 256 : rowb;
  asm volatile("s_waitcnt lgkmcnt(0)\n\ts_barrier" : "+s"(ka), "+s"(kb), "+s"(tst)
               : "s"(nrec), "v"(voff), "s"(stepA), "s"(stepB) : "memory");
  const u16 *x = (const u16*)ka[0], *kc = (const u16*)ka[1], *vc = (const u16*)ka[2], *wq = (const u16*)ka[3], *wo = (const u16*)ka[4],
            *wg = (const u16*)ka[5], *wu = (const u16*)ka[6], *wd = (const u16*)ka[7];
  u16* out = (u16*)kb[0];
  unsigned char* ws = (unsigned char*)kb[1];
  const unsigned tstart = (unsigned)tst;
  const rsrc_t r0 = mk_rsrc(isS ? kc : (isQ ? wq : (up ? wu : wg)), nrec);
  u32x4 buf[96];
  if (nrec) {
    int so = 0;
#pragma unroll
    for (int n = 0; n < 9; ++n) { buf[n] = ld_nt(r0, voff, so); so += (n % 3 == 2) ? stepB : stepA; SB(); }
  }
  FS(1);
  unsigned tag, ep;
  asm volatile("s_load_dword %0, %1, 0x0" : "=s"(ep) : "s"(ws));
#define TAG_READY() asm volatile("s_waitcnt lgkmcnt(0)\n\ts_add_u32 %0, %1, 1" : "=s"(tag) : "s"(ep) : "scc")
  const rsrc_t rws = mk_rsrc(ws, WS_END);

  if (isS) {
    const int sw = wave - 4;
    const rsrc_t rv = mk_rsrc(vc, 2 * 32 * 8192 * 384);
    u32x4 qv[3];
    float sc[16];
    unsigned pd[16];
    f32x4 acc[3] = {{0.f, 0.f, 0.f, 0.f}, {0.f, 0.f, 0.f, 0.f}, {0.f, 0.f, 0.f, 0.f}};
    unsigned msk[4];
#pragma unroll
    for (int d = 0; d < 4; ++d) msk[d] = (j >> 1) == d ? ((j & 1) ? 0xffff0000u : 0x0000ffffu) : 0u;
    constexpr float C2 = 0.07216878364870322f * 1.4426950408889634f;
    unsigned tend = 0, tgt = 0, tendv = 0;
    if (PACING) { tgt = tstart; tend = tgt + tn0; }

    auto consume = [&](auto G) {
      constexpr int t = decltype(G)::value;
      if constexpr (t < 16) {
        float a0 = 0.f, a1 = 0.f, a2 = 0.f;
#pragma unroll
        for (int e = 0; e < 4; ++e) { a0 = dot2(buf[3 * t][e], qv[0][e], a0); a1 = dot2(buf[3 * t + 1][e], qv[1][e], a1); a2 = dot2(buf[3 * t + 2][e], qv[2][e], a2); }
        float a = (a0 + a1) + a2;
        a += dpp<XOR1>(a); a += dpp<XOR2>(a); a += dpp<HMIR>(a);
        sc[t] = a;
        if constexpr (t == 15) {
          float m = sc[0];
#pragma unroll
          for (int u = 1; u < 16; ++u) m = fmaxf(m, sc[u]);
          m = fmaxf(m, dpp<ROR8>(m)); m = x16_max(m); m = x32_max(m);
          const float mc = m * C2;
          float l = 0.f;
#pragma unroll
          for (int u = 0; u < 16; ++u) { const float p = __builtin_amdgcn_exp2f(fmaf(sc[u], C2, -mc)); pd[u] = cvtpk(p, p); l += bfhi(pd[u]); }
          l += dpp<ROR8>(l); l = x16_sum(l); l = x32_sum(l);
          if (lane == 0) { *(f32x2*)(l_ml + 2 * sw) = f32x2{mc, l}; MEMFENCE(); l_f[FA + sw] = 1; }
          STAMP(3);
        }
      } else {
        constexpr int u = t - 16;
        const u32x4 a = {pd[u] & msk[0], pd[u] & msk[1], pd[u] & msk[2], pd[u] & msk[3]};
#pragma unroll
        for (int i = 0; i < 3; ++i) acc[i] = mfma32(a, buf[3 * t + i], acc[i]);
      }
    };
    constexpr auto done_after = [](int n) constexpr {
      if (n < B1_AT) return 0;
      const int landed = (n - WIN + 1) / 3;
      const int cu = 1 + (CATCH * (n - B1_AT)) / 3;
      return landed < cu ? landed : cu;
    };
    static_for<9, 96>([&](auto N) {
      constexpr int n = decltype(N)::value;
      if constexpr (n == B1_AT) {
        STAMP(1);
        wait_lds<0>(fbase + FG, 0x00010101u);
        STAMP(2);
#pragma unroll
        for (int i = 0; i < 3; ++i) qv[i] = *(const u32x4*)(l_q + 32 * i + 4 * j);
      }
      if constexpr (n >= WIN) asm volatile("s_waitcnt vmcnt(%0)" ::"i"(WIN - 1) : "memory");
      if constexpr (n == 9) TAG_READY();
      if constexpr (n >= 9 && n < 9 + 45 && (n - 9) % 5 == 0) FS(2 + (n - 9) / 5);
      if constexpr (PACING && n % 3 == 0) {
        constexpr int k = n / 3;
        if constexpr (PACING == 2 && k > 12) { const unsigned tv = __builtin_amdgcn_readfirstlane(tendv); tend = tv ? tv : tend; }
        const unsigned now = pace_step<(k < 31 ? 65536 / (31 - k) : 0)>(tgt, tend);
        if constexpr (PACING == 2 && k >= 6 && k < 26) {
          unsigned vm;
          asm volatile("s_getreg_b32 %0, hwreg(HW_REG_IB_STS, 0, 4)" : "=s"(vm));
          l_est[sw] = (now + (((vm + 3 * (31 - k)) * tn5) >> 4)) | 1u;
        }
        if constexpr (PACING == 2 && k >= 12 && k < 30) tendv = *l_tend;
      }
      if constexpr (n == FB_AT) { if (lane == 0) l_f[FB + sw] = 1; }
      constexpr int m = n < 48 ? n : n - 48;
      buf[n] = ld_nt(n < 48 ? r0 : rv, voff + (m % 3) * 128, (m / 3) * 12288);
      SB();
      static_for<done_after(n - 1), done_after(n)>(consume);
      SB();
    });
    STAMP(4);
    __builtin_amdgcn_s_setprio(3);
    wait_lds<0>(fbase + FE, 0x01010101u);
    float cq = l_c[lane >> 4];
    unsigned dst = (unsigned)(size_t)(lfloat*)(l_acc + (sw * 2 + (g & 1)) * 192 + 8 * j + 4 * ((lane >> 4) & 1));
    unsigned src = (unsigned)(size_t)(lfloat*)(l_acc + (lane >> 4) * 384 + 64 * sw + 8 * j + 4 * (g & 1));
    unsigned fla = fbase + FC + sw, one = 1u, flr = fbase + FC;
    int pvo = (lane & 16) ? OOB : (g & 1) * 128 + j * 16 + (lane >> 5) * 8;
    int pso = WS_OP + ((h * 2 + b) * 4 + s) * 768 + sw * 256;
    unsigned tagv = tag;
    asm volatile("" : "+v"(cq), "+v"(dst), "+v"(src), "+v"(fla), "+v"(flr), "+v"(one), "+v"(pvo), "+s"(pso), "+v"(tagv));
    const bool wr = (lane >> 5) == (g & 1);
    SB();
    static_for<done_after(95), 32>([&](auto G) { consume(G); SB(); });
    if (wr) {
#pragma unroll
      for (int i = 0; i < 3; ++i) *(lf32x4*)(size_t)(dst + 256 * i) = acc[i];
      MEMFENCE();
      *(lbyte*)(size_t)fla = (unsigned char)one;
    }
    STAMP(5);
    if (sw < 3) {
      f32x4 u0, u1;
      unsigned fv, fs;
      asm volatile("1:\n\tds_read_b32 %0, %4\n\tds_read_b128 %2, %5\n\tds_read_b128 %3, %5 offset:768\n\ts_waitcnt lgkmcnt(2)\n\tv_readfirstlane_b32 %1, %0\n\t"
                   "s_cmp_lg_u32 %1, 0x01010101\n\ts_waitcnt lgkmcnt(0)\n\ts_cbranch_scc1 1b"
                   : "=&v"(fv), "=&s"(fs), "=&v"(u0), "=&v"(u1) : "v"(flr), "v"(src) : "scc", "memory");
      float o[4];
#pragma unroll
      for (int e = 0; e < 4; ++e) o[e] = cq * (u0[e] + u1[e]);
      const auto s0 = __builtin_amdgcn_permlane32_swap(__float_as_uint(o[0]), __float_as_uint(o[2]), false, false);
      const auto s1 = __builtin_amdgcn_permlane32_swap(__float_as_uint(o[1]), __float_as_uint(o[3]), false, false);
      const float p0 = x16_sum(__uint_as_float(s0[0]) + __uint_as_float(s0[1])), p1 = x16_sum(__uint_as_float(s1[0]) + __uint_as_float(s1[1]));
      const u32x2 pv = {cvtpk(p0, p1), tagv};
      __builtin_amdgcn_raw_buffer_store_b64(pv, rws, pvo, pso, AUX_SC1);
    }
    MEMFENCE();
    *(lbyte*)(size_t)(fla + (FD - FC)) = (unsigned char)one;
    STAMP(6);
    STAMP_FLUSH();
    return;
  }

  __builtin_amdgcn_s_setprio(2);
  const int w = wave;
  const unsigned m0 = e4 == 0 ? 0x0000ffffu : e4 == 1 ? 0xffff0000u : 0u;
  const unsigned m1 = e4 == 2 ? 0x0000ffffu : e4 == 3 ? 0xffff0000u : 0u;
  const int st_col = (8 * j + 4 * (lane >> 5) + 2 * (g & 1)) * 8;
  const rsrc_t rx = mk_rsrc(x, 2560);
  if (has_front) {
    const int xo = (lane >> 5) * 1280 + (lane & 31) * 16;
    const bool x3 = (lane & 16) == 0;
    const u32x4 xa = __builtin_amdgcn_raw_buffer_load_b128(rx, xo, 0, 0), xb = __builtin_amdgcn_raw_buffer_load_b128(rx, xo + 512, 0, 0);
    const u32x4 xc = __builtin_amdgcn_raw_buffer_load_b128(rx, x3 ? xo + 1024 : OOB, 0, 0);
    const u32x4 xr0 = __builtin_amdgcn_raw_buffer_load_b128(rx, bl * 1280 + g * 32, 256 * ur, 0);
    const u32x4 xr1 = __builtin_amdgcn_raw_buffer_load_b128(rx, bl * 1280 + g * 32 + 16, 256 * ur, 0);
    SB();
#pragma unroll
    for (int n = 9; n < 16; ++n) buf[n] = ld_nt(r0, voff, n * rowb);
    SB();
    FS(2);
    TAG_READY();
    float v[24];
#pragma unroll
    for (int e = 0; e < 4; ++e) { v[2 * e] = bflo(xa[e]); v[2 * e + 1] = bfhi(xa[e]); v[8 + 2 * e] = bflo(xb[e]); v[9 + 2 * e] = bfhi(xb[e]); v[16 + 2 * e] = bflo(xc[e]); v[17 + 2 * e] = bfhi(xc[e]); }
    float sum = 0.f;
#pragma unroll
    for (int e = 0; e < 24; ++e) sum += v[e];
    sum = x16_sum(row_sum(sum));
    const float mean = sum * (1.f / 640.f);
    float sq = 0.f, sq3 = 0.f;
#pragma unroll
    for (int e = 0; e < 16; ++e) { const float d = v[e] - mean; sq = fmaf(d, d, sq); }
#pragma unroll
    for (int e = 16; e < 24; ++e) { const float d = v[e] - mean; sq3 = fmaf(d, d, sq3); }
    sq = x16_sum(row_sum(sq + (x3 ? sq3 : 0.f)));
    const float mb = rbf(mean), den = rbf(__builtin_amdgcn_sqrtf(rbf(rbf(sq * (1.f / 640.f)) + 1e-5f)));
    const float inv = __builtin_amdgcn_rcpf(den);
    const float mb0 = __builtin_bit_cast(float, __builtin_amdgcn_readlane(__builtin_bit_cast(int, mb), 0));
    const float mb1 = __builtin_bit_cast(float, __builtin_amdgcn_readlane(__builtin_bit_cast(int, mb), 32));
    const float iv0 = __builtin_bit_cast(float, __builtin_amdgcn_readlane(__builtin_bit_cast(int, inv), 0));
    const float iv1 = __builtin_bit_cast(float, __builtin_amdgcn_readlane(__builtin_bit_cast(int, inv), 32));
    const float mm = bl ? mb1 : mb0, ii = bl ? iv1 : iv0;
    FS(3);
    f32x4 acc0 = {0.f, 0.f, 0.f, 0.f}, acc1 = {0.f, 0.f, 0.f, 0.f};
#pragma unroll
    for (int n = 0; n < 16; ++n) {
      const unsigned xw = n < 8 ? xr0[n >> 1] : xr1[(n - 8) >> 1];
      const float xv = (n & 1) ? bfhi(xw) : bflo(xw);
      const float xn = rbf(xv - mm) * ii;
      const unsigned d = cvtpk(xn, xn), a0 = d & m0, a1 = d & m1;
      acc0 = mfma16(a0, a1, buf[n][0], buf[n][1], acc0);
      acc1 = mfma16(a0, a1, buf[n][2], buf[n][3], acc1);
    }
    float v0, v1;
    proj_epilogue(acc0, acc1, g & 1, v0, v1);
    const int rec = w < 2 ? WS_QP + (h * 5 + ur) * 3072 + uc * 256 : WS_GU + ((xq * 5 + ur) * 2 + up) * 3072 + (uc - 3 * up) * 512;
    st_pair(rws, ((lane >> 4) & 1) * 1536 + st_col, rec, v0, v1, tag);
    STAMP(1); FS(4);
    asm volatile("s_waitcnt vmcnt(0)" ::: "memory");
    STAMP(11); FS(5);
  } else {
    if (w == 3) {
      pc &= ~255ull;
      unsigned c0, c1, c2, c3;
      if (CODEPF) {
        const unsigned o = uq * 128, o3 = o + 12288 < CODEPF ? o + 12288 : o;
        asm volatile("s_load_dword %0, %4, %5\n\ts_load_dword %1, %4, %6\n\ts_load_dword %2, %4, %7\n\ts_load_dword %3, %4, %8"
                     : "=&s"(c0), "=&s"(c1), "=&s"(c2), "=&s"(c3) : "s"(pc), "s"(o), "s"(o + 4096), "s"(o + 8192), "s"(o3));
      }
      if (CODEPF) asm volatile("s_waitcnt lgkmcnt(0)" ::"s"(c0), "s"(c1), "s"(c2), "s"(c3));
      if (PRETOUCH) {
        wait_until(tstart + PRETOUCH);
        const unsigned kv0 = kv_off + 8 * 384, kv1 = kv0 + 512 * 384 - 64, o0 = h * (192 * 1280), o1 = o0 + 192 * 1280 - 64, d0_ = h * (48 * 1280);
        unsigned t0, t1, t2, t3_, t4, t5, t6, t7, t8;
        asm volatile("s_load_dword %0, %9, %14\n\ts_load_dword %1, %10, %15\n\ts_load_dword %2, %10, %14\n\ts_load_dword %3, %11, %16\n\ts_load_dword %4, %11, %17\n\t"
                     "s_load_dword %5, %12, %18\n\ts_load_dword %6, %13, 0x0\n\ts_load_dword %7, %19, %20\n\ts_load_dword %8, %19, %21\n\ts_waitcnt lgkmcnt(0)"
                     : "=&s"(t0), "=&s"(t1), "=&s"(t2), "=&s"(t3_), "=&s"(t4), "=&s"(t5), "=&s"(t6), "=&s"(t7), "=&s"(t8)
                     : "s"(kc), "s"(vc), "s"(wo), "s"(wd), "s"(out), "s"(kv1), "s"(kv0), "s"(o0), "s"(o1), "s"(d0_), "s"(ws), "s"(WS_END), "s"(WS_END / 2 & ~63));
      }
    }
    TAG_READY();
  }
  const int slot = w * 8 + j8;
  const bool has_back = slot < 30;
  const int t3 = slot / 10, cc = slot - 10 * t3;
  const int rb = bid / 80, rc = (bid - 80 * rb) >> 3, ro = bid & 7;
  {
    const unsigned sLo = (e4 & 1) ? 0x01000c0cu : 0x0c0c0100u, sHi = (e4 & 1) ? 0x03020c0cu : 0x0c0c0302u;
    unsigned sel0 = e4 < 2 ? sLo : 0x0c0c0c0cu, sel1 = e4 < 2 ? 0x0c0c0c0cu : sLo, sel2 = e4 < 2 ? sHi : 0x0c0c0c0cu, sel3 = e4 < 2 ? 0x0c0c0c0cu : sHi;
    int op_so = WS_OP + h * 8 * 768 + t3 * 256, op_vo = (bl * 4 + e4) * 768 + 16 * g;
    int d_vo = ((48 * h + 16 * t3 + 2 * g) * 640 + 64 * cc) * 2 + 16 * j, o_vo = ((192 * h + 64 * t3 + 8 * g) * 640 + 64 * cc) * 2 + 16 * j;
    int yvo = ((lane >> 4) & 1) * (96 * 512) + st_col, yso = WS_YP + cc * 2 * 96 * 512 + (h * 3 + t3) * 512;
    int r_vo = (lane >> 2) * 512 + (lane & 3) * 16 + WS_YP + (rc * 2 + rb) * 96 * 512 + ro * 64;
    int out_vo = lane < 4 ? lane * 4 + rb * 1280 + rc * 128 + ro * 16 : OOB;
    unsigned tagv = tag;
    asm volatile("" : "+v"(sel0), "+v"(sel1), "+v"(sel2), "+v"(sel3), "+s"(op_so), "+v"(op_vo), "+v"(d_vo), "+v"(o_vo), "+v"(yvo), "+s"(yso), "+v"(r_vo), "+v"(out_vo), "+v"(tagv));
    int pass = DRYRUN ? __builtin_amdgcn_readfirstlane(w == 3 ? 0 : 1) : 1;
    asm volatile("" : "+s"(pass));
    unsigned ha[2][2] = {{0u, 0u}, {0u, 0u}};
    unsigned xres = 0;
        unsigned tend = tstart + tn0;
        auto pace_round = [&](unsigned t_sample, unsigned t_read, int so) {
          wait_until(t_sample);
          u32x4 e;
          do { MEMFENCE(); e = *(const u32x4*)l_est; } while (!__builtin_amdgcn_readfirstlane(e[0] & e[1] & e[2] & e[3] & 1u));
          const u32x2 pv = {tstart + (unsigned)((int)((e[0] - tstart) + (e[1] - tstart) + (e[2] - tstart) + (e[3] - tstart)) >> 2), tag};
          __builtin_amdgcn_raw_buffer_store_b64(pv, rws, (lane == 0 && bid < 32) ? bid * 8 : OOB, so, AUX_SC1);
          wait_until(t_read);
          MEMFENCE();
          const u32x2 tv = __builtin_amdgcn_raw_buffer_load_b64(rws, (lane & 31) * 8, so, AUX_SC1);
          if (__all(tv[1] == tag)) {
            const float d = x16_sum(row_sum((float)(int)(tv[0] - tstart)));
            int di = (int)(d * (1.f / 32.f)) - tn1;
            di = di < 0 ? 0 : (di > 2500 ? 2500 : di);
            tend = (tstart + __builtin_amdgcn_readfirstlane(di)) | 1u;
            *l_tend = tend;
          }
        };
    if (PACING == 2 && w == 3) pace_round(tstart + tn6, tstart + tn6 + tn7, WS_TS);
#pragma nounroll
    for (; pass < 2; ++pass) {
      const bool reducer = !pass || ((w == 0) && (bid < 160));
      if (pass) {
        if (w < 3) {
          if (has_front) __builtin_amdgcn_s_sleep(QSLEEP); else wait_until(tstart + 520);
          u32x4 t[3];
          const int vo = (lane >> 5) * 3072 + (lane & 31) * 16, so = WS_QP + h * 5 * 3072 + b * 1536 + w * 512;
          for (unsigned spin = 0;; ++spin) {
            MEMFENCE();
            t[0] = ld_sc1(rws, vo, so); t[1] = ld_sc1(rws, vo, so + 6144); t[2] = ld_sc1(rws, lane < 32 ? vo : OOB, so + 12288);
            const bool ok = (t[0][1] == tag) & (t[0][3] == tag) & (t[1][1] == tag) & (t[1][3] == tag) & (((t[2][1] == tag) & (t[2][3] == tag)) | (lane >= 32));
            if (__all(ok) || spin > SPIN_CAP) { STAMPV(12, spin + 1); break; }
          }
          const float a = x32_sum((__uint_as_float(t[0][0]) + __uint_as_float(t[1][0])) + __uint_as_float(t[2][0]));
          const float c = x32_sum((__uint_as_float(t[0][2]) + __uint_as_float(t[1][2])) + __uint_as_float(t[2][2]));
          if (lane < 32) { l_q[32 * w + lane] = cvtpk(a, c); MEMFENCE(); l_f[FG + w] = 1; }
        } else if (PACING != 2) {
          wait_until(tstart + 700);
        }
        STAMP(2); FS(6);
        if (has_back) {
          u32x4 t[3];
          const int so = WS_GU + xq * 5 * 6144 + (h & 3) * 384 + t3 * 128;
          const int vo = bl * 1536 + 16 * g;
          for (unsigned spin = 0;; ++spin) {
            MEMFENCE();
            t[0] = ld_sc1(rws, vo + e4 * 6144, so);
            t[1] = ld_sc1(rws, vo + e4 * 6144 + 3072, so);
            t[2] = ld_sc1(rws, vo + 4 * 6144 + (e4 & 1) * 3072, so);
            bool ok = true;
#pragma unroll
            for (int k = 0; k < 3; ++k) ok &= (t[k][1] == tag) & (t[k][3] == tag);
            if (__all(ok) || spin > SPIN_CAP) { STAMPV(13, spin + 1); break; }
          }
#pragma unroll
          for (int z = 0; z < 2; ++z) {
            const float ex = __uint_as_float(t[2][2 * z]);
            const float gg = rbf(quad_sum(__uint_as_float(t[0][2 * z]) + (e4 == 0 ? ex : 0.f)));
            const float uu = rbf(quad_sum(__uint_as_float(t[1][2 * z]) + (e4 == 1 ? ex : 0.f)));
            const float zz = 0.7978845608028654f * (gg + 0.044715f * gg * gg * gg);
            const float ge = rbf(gg * __builtin_amdgcn_rcpf(1.f + __builtin_amdgcn_exp2f(-2.8853900817779268f * zz)));
            const float hv = ge * uu;
            const unsigned d = cvtpk(hv, hv);
            ha[z][0] = d & m0; ha[z][1] = d & m1;
          }
        }
        if (reducer) xres = __builtin_amdgcn_raw_buffer_load_b32(rx, (lane & 3) * 4, rb * 1280 + rc * 128 + ro * 16, 0);
        STAMP(3);
        if (w == 1) {
          wait_lds<4>(fbase + FA, 0x01010101u);
          const int so = WS_ML + (h * 2 + b) * 256;
          if (lane < 4) { const f32x2 ml = *(const f32x2*)(l_ml + 2 * lane); st_pair(rws, lane * 16, so + s * 64, ml[0], ml[1], tag); }
          asm volatile("s_waitcnt vmcnt(0)" ::: "memory");
          __builtin_amdgcn_s_sleep(MLSLEEP);
          u32x4 t;
          for (unsigned spin = 0;; ++spin) {
            MEMFENCE();
            t = ld_sc1(rws, (lane & 15) * 16, so);
            if (__all((t[1] == tag) & (t[3] == tag)) || spin > SPIN_CAP) break;
          }
          const float mv = __uint_as_float(t[0]);
          const float M = row_max(mv);
          const float f = __builtin_amdgcn_exp2f(mv - M);
          const float L = row_sum(f * __uint_as_float(t[2]));
          if ((lane >> 2) == s) { l_c[lane & 3] = f / L; MEMFENCE(); l_f[FE + (lane & 3)] = 1; }
        }
        STAMP(4);
        if (!has_back) break;
      }
      const int live = pass ? -1 : 0;
      const rsrc_t rd = mk_rsrc(wd, 1536 * 1280 & live), ro_ = mk_rsrc(wo, 6144 * 1280 & live), rw2 = mk_rsrc(ws, WS_END & live), rout = mk_rsrc(out, 2560 & live);
      const unsigned want = tag & live;
      unsigned fb = fbase + (pass ? FB : FF), fd = fbase + (pass ? FD : FF);
      asm volatile("" : "+v"(fb), "+v"(fd));
      wait_lds<1>(fb, 0x01010101u);
      delay_ticks(tn4 & live);
      u32x4 wb[10];
#pragma unroll
      for (int n = 0; n < 2; ++n) { wb[8 + n] = ld_nt(rd, d_vo + n * 1280, 0); SB(); if (BPACE) __builtin_amdgcn_s_sleep(BPACE); SB(); }
#pragma unroll
      for (int n = 0; n < 8; ++n) { wb[n] = ld_nt(ro_, o_vo, n * 1280); SB(); if (BPACE && n < 7) __builtin_amdgcn_s_sleep(BPACE); SB(); }
      STAMP(5);
      wait_lds<1>(fd, 0x01010101u);
      delay_ticks(tn2 & live);
      STAMP(6);
      u32x4 t0 = ld_sc1(rw2, op_vo, op_so), t1 = ld_sc1(rw2, op_vo + 128, op_so);
      SB();
      u32x4 bq[10];
#pragma unroll
      for (int p = 0; p < 5; ++p) {
        bq[2 * p] = u32x4{wb[2 * p][0], wb[2 * p][1], wb[2 * p + 1][0], wb[2 * p + 1][1]};
        bq[2 * p + 1] = u32x4{wb[2 * p][2], wb[2 * p][3], wb[2 * p + 1][2], wb[2 * p + 1][3]};
      }
      const u32x4 hA = {ha[0][0], ha[0][1], ha[1][0], ha[1][1]};
      f32x4 acc0 = mfma32(hA, bq[8], f32x4{0.f, 0.f, 0.f, 0.f}), acc1 = mfma32(hA, bq[9], f32x4{0.f, 0.f, 0.f, 0.f});
      asm volatile("" : "+v"(bq[0]), "+v"(bq[1]), "+v"(bq[2]), "+v"(bq[3]), "+v"(bq[4]), "+v"(bq[5]), "+v"(bq[6]), "+v"(bq[7]), "+v"(acc0), "+v"(acc1));
      SB();
      unsigned np1 = 1;
      while (!__all((t0[1] == want) & (t0[3] == want) & (t1[1] == want) & (t1[3] == want)) && np1 < SPIN_CAP) {
        ++np1;
        MEMFENCE();
        t0 = ld_sc1(rw2, op_vo, op_so); t1 = ld_sc1(rw2, op_vo + 128, op_so);
      }
      STAMPV(14, np1);
      STAMP(7);
      float ov[8] = {bflo(t0[0]), bfhi(t0[0]), bflo(t0[2]), bfhi(t0[2]), bflo(t1[0]), bfhi(t1[0]), bflo(t1[2]), bfhi(t1[2])};
#pragma unroll
      for (int n = 0; n < 8; ++n) ov[n] += dpp<XOR1>(ov[n]);
      SB();
#pragma unroll
      for (int n = 0; n < 8; ++n) ov[n] += dpp<XOR2>(ov[n]);
      SB();
#pragma unroll
      for (int p = 0; p < 4; ++p) {
        const unsigned d = cvtpk(ov[2 * p], ov[2 * p + 1]);
        const u32x4 a = {__builtin_amdgcn_perm(0u, d, sel0), __builtin_amdgcn_perm(0u, d, sel1), __builtin_amdgcn_perm(0u, d, sel2), __builtin_amdgcn_perm(0u, d, sel3)};
        acc0 = mfma32(a, bq[2 * p], acc0);
        acc1 = mfma32(a, bq[2 * p + 1], acc1);
      }
      float v0, v1;
      proj_epilogue(acc0, acc1, g & 1, v0, v1);
      st_pair(rw2, yvo, yso, v0, v1, tagv);
      STAMP(8);
      if (reducer) {
        delay_ticks(tn3 & live);
        u32x4 t[6];
        unsigned np2 = 0;
        for (;;) {
          ++np2;
          MEMFENCE();
#pragma unroll
          for (int k = 0; k < 6; ++k) t[k] = ld_sc1(rw2, r_vo, k * 16 * 512);
          bool ok = true;
#pragma unroll
          for (int k = 0; k < 6; ++k) ok &= (t[k][1] == want) & (t[k][3] == want);
          if (__all(ok) || np2 > SPIN_CAP) break;
        }
        STAMPV(15, np2);
        STAMP(9);
        float y0 = ((__uint_as_float(t[0][0]) + __uint_as_float(t[1][0])) + (__uint_as_float(t[2][0]) + __uint_as_float(t[3][0]))) + (__uint_as_float(t[4][0]) + __uint_as_float(t[5][0]));
        float y1 = ((__uint_as_float(t[0][2]) + __uint_as_float(t[1][2])) + (__uint_as_float(t[2][2]) + __uint_as_float(t[3][2]))) + (__uint_as_float(t[4][2]) + __uint_as_float(t[5][2]));
        y0 += dpp<ROR4>(y0); y1 += dpp<ROR4>(y1);
        y0 += dpp<ROR8>(y0); y1 += dpp<ROR8>(y1);
        y0 = x32_sum(x16_sum(y0)); y1 = x32_sum(x16_sum(y1));
        __builtin_amdgcn_raw_buffer_store_b32(cvtpk(bflo(xres) + y0, bfhi(xres) + y1), rout, out_vo, 0, 0);
        if (bid == 0) __builtin_amdgcn_raw_buffer_store_b32(tagv, rw2, lane == 0 ? WS_EPOCH : OOB, 0, AUX_SC1);
        STAMP(10);
      }
    }
  }
  STAMP_FLUSH();
}

extern "C" void kernel_launch(void* const* d_in, const int* in_sizes, int n_in, void* d_out, int out_size, void* d_ws, size_t ws_size,
                              hipStream_t stream) {
  fused_decode<<<256, 512, 0, stream>>>((const u16*)d_in[0], (const u16*)d_in[1], (const u16*)d_in[2], (const u16*)d_in[3], (const u16*)d_in[4],
                                        (const u16*)d_in[6], (const u16*)d_in[7], (const u16*)d_in[8], (u16*)d_out, (unsigned char*)d_ws
                                        );
}
```

```cpp
#define PACING 2
#define CODEPF 16128
#define PRETOUCH 250
#define SCAT 0
#define STAG 11
#define POLLPOS 8
#define FDLOOK 1
#define TEND0 1500
#define H1DELAY 20
#define H2DELAY 40
#define BDELAY 10
#define SMP0 600
#define WT0 250
#define TFIN 0
#define TEND1 (-60)
#define SMP1 480
#define WT1 150
#define BIAS1 (-10)
#include <hip/hip_runtime.h>
#include <hip/hip_bf16.h>
#include <stdint.h>
#include <type_traits>

typedef unsigned short u16;
typedef unsigned u32x4 __attribute__((ext_vector_type(4)));
typedef unsigned u32x2 __attribute__((ext_vector_type(2)));
typedef float f32x4 __attribute__((ext_vector_type(4)));
typedef float f32x2 __attribute__((ext_vector_type(2)));
typedef short s16x4 __attribute__((ext_vector_type(4)));
typedef __bf16 bf16x8 __attribute__((ext_vector_type(8)));
typedef __bf16 bf16x2 __attribute__((ext_vector_type(2)));
typedef __amdgpu_buffer_rsrc_t rsrc_t;

#define WIN 9
#define B1_AT 48
#define CATCH 3
#define QSLEEP 16
#define MLSLEEP 32
#define BPACE 5
#define H1SLEEP 0
#define KC 10
#define FB_AT 95
#define DRYRUN 1
#define RATE16 229
#define STOUCH 0
#define AUX_OUT 0
#define KVW 8
#define KVG 384
#define KVT 12288
#define WOSEQ 0

constexpr int AUX_NT = 2, AUX_SC1 = 16;
constexpr int RSRC_W3 = 0x00020000;
constexpr unsigned SPIN_CAP = 1u << 18;
constexpr int OOB = 0x40000000;

constexpr int WS_EPOCH = 0;
constexpr int WS_TS = 1024;
constexpr int WS_QP = 4096;
constexpr int WS_GU = WS_QP + 32 * 5 * 2 * 192 * 8;
constexpr int WS_ML = WS_GU + 8 * 5 * 2 * 2 * 192 * 8;
constexpr int WS_OP = WS_ML + 32 * 2 * 4 * 4 * 16;
constexpr int WS_YP = WS_QP;
constexpr int WS_END = WS_OP + 32 * 2 * 4 * 96 * 8;

template <int I, int N, class F> __device__ __forceinline__ void static_for(F&& f) {
  if constexpr (I < N) { f(std::integral_constant<int, I>{}); static_for<I + 1, N>(f); }
}
__device__ __forceinline__ rsrc_t mk_rsrc(const void* p, int bytes) {
  return __builtin_amdgcn_make_buffer_rsrc(const_cast<void*>(p), 0, bytes, RSRC_W3);
}
__device__ __forceinline__ u32x4 ld_nt(rsrc_t r, int voff, int soff) { return __builtin_amdgcn_raw_buffer_load_b128(r, voff, soff, AUX_NT); }
__device__ __forceinline__ u32x4 ld_sc1(rsrc_t r, int voff, int soff) { return __builtin_amdgcn_raw_buffer_load_b128(r, voff, soff, AUX_SC1); }
__device__ __forceinline__ void st_pair(rsrc_t r, int voff, int soff, float a, float b, unsigned tag) {
  u32x4 v = {__float_as_uint(a), tag, __float_as_uint(b), tag};
  __builtin_amdgcn_raw_buffer_store_b128(v, r, voff, soff, AUX_SC1);
}
__device__ __forceinline__ float bflo(unsigned v) { return __uint_as_float(v << 16); }
__device__ __forceinline__ float bfhi(unsigned v) { return __uint_as_float(v & 0xffff0000u); }
__device__ __forceinline__ unsigned cvtpk(float lo, float hi) {
  bf16x2 r = {(__bf16)lo, (__bf16)hi};
  return __builtin_bit_cast(unsigned, r);
}
__device__ __forceinline__ float rbf(float x) { return (float)(__bf16)x; }
template <int CTRL> __device__ __forceinline__ float dpp(float x) {
  return __builtin_bit_cast(float, __builtin_amdgcn_mov_dpp(__builtin_bit_cast(int, x), CTRL, 0xf, 0xf, true));
}
constexpr int XOR1 = 0xB1, XOR2 = 0x4E, HMIR = 0x141, ROR8 = 0x128, ROR4 = 0x124;
__device__ __forceinline__ float x16_sum(float x) { auto s = __builtin_amdgcn_permlane16_swap(__float_as_uint(x), __float_as_uint(x), false, false); return __uint_as_float(s[0]) + __uint_as_float(s[1]); }
__device__ __forceinline__ float x32_sum(float x) { auto s = __builtin_amdgcn_permlane32_swap(__float_as_uint(x), __float_as_uint(x), false, false); return __uint_as_float(s[0]) + __uint_as_float(s[1]); }
__device__ __forceinline__ float x16_max(float x) { auto s = __builtin_amdgcn_permlane16_swap(__float_as_uint(x), __float_as_uint(x), false, false); return fmaxf(__uint_as_float(s[0]), __uint_as_float(s[1])); }
__device__ __forceinline__ float x32_max(float x) { auto s = __builtin_amdgcn_permlane32_swap(__float_as_uint(x), __float_as_uint(x), false, false); return fmaxf(__uint_as_float(s[0]), __uint_as_float(s[1])); }
__device__ __forceinline__ float row_sum(float x) { x += dpp<XOR1>(x); x += dpp<XOR2>(x); x += dpp<HMIR>(x); x += dpp<ROR8>(x); return x; }
__device__ __forceinline__ float row_max(float x) { x = fmaxf(x, dpp<XOR1>(x)); x = fmaxf(x, dpp<XOR2>(x)); x = fmaxf(x, dpp<HMIR>(x)); x = fmaxf(x, dpp<ROR8>(x)); return x; }
__device__ __forceinline__ float quad_sum(float x) { x += dpp<XOR1>(x); x += dpp<XOR2>(x); return x; }
__device__ __forceinline__ float dot2(unsigned a, unsigned b, float c) {
  return __builtin_amdgcn_fdot2_f32_bf16(__builtin_bit_cast(bf16x2, a), __builtin_bit_cast(bf16x2, b), c, false);
}
__device__ __forceinline__ f32x4 mfma16(unsigned a0, unsigned a1, unsigned b0, unsigned b1, f32x4 c) {
  u32x2 a = {a0, a1}, b = {b0, b1};
  return __builtin_amdgcn_mfma_f32_16x16x16bf16_1k(__builtin_bit_cast(s16x4, a), __builtin_bit_cast(s16x4, b), c, 0, 0, 0);
}
__device__ __forceinline__ f32x4 mfma32(u32x4 a, u32x4 b, f32x4 c) {
  return __builtin_amdgcn_mfma_f32_16x16x32_bf16(__builtin_bit_cast(bf16x8, a), __builtin_bit_cast(bf16x8, b), c, 0, 0, 0);
}
template <int RECIP> __device__ __forceinline__ unsigned pace_step(unsigned& tgt, unsigned tend) {
  unsigned d, now;
  asm volatile("1:\n\ts_memrealtime vcc\n\ts_waitcnt lgkmcnt(0)\n\ts_sub_u32 %0, vcc_lo, %2\n\ts_cmp_lt_i32 %0, 0\n\ts_cbranch_scc1 1b\n\t"
               "s_sub_i32 %0, %3, vcc_lo\n\ts_max_i32 %0, %0, 0\n\ts_min_i32 %0, %0, 0x7d0\n\ts_mul_i32 %0, %0, %4\n\ts_lshr_b32 %0, %0, 16\n\t"
               "s_add_u32 %2, vcc_lo, %0\n\ts_mov_b32 %1, vcc_lo"
               : "=&s"(d), "=&s"(now), "+s"(tgt) : "s"(tend), "i"(RECIP) : "vcc", "scc");
  return now;
}
__device__ __forceinline__ void wait_until(unsigned tgt) {
  asm volatile("1:\n\ts_memrealtime vcc\n\ts_waitcnt lgkmcnt(0)\n\ts_sub_u32 vcc_lo, vcc_lo, %0\n\ts_cmp_lt_i32 vcc_lo, 0\n\ts_cbranch_scc1 1b" :: "s"(tgt) : "vcc", "scc", "memory");
}
#define SB() __builtin_amdgcn_sched_barrier(0)
#define WG_BARRIER() do { asm volatile("s_waitcnt lgkmcnt(0)\n\ts_barrier" ::: "memory"); } while (0)
#define MEMFENCE() asm volatile("" ::: "memory")

#define FS(i)
#define STAMP(i)
#define STAMPV(i, v)
#define STAMP_DECL
#define STAMP_FLUSH()

__device__ __forceinline__ void delay_ticks(int n) {
  if (n > 0) wait_until((unsigned)__builtin_amdgcn_s_memrealtime() + n);
}

typedef __attribute__((address_space(3))) float lfloat;
typedef __attribute__((address_space(3))) f32x4 lf32x4;
typedef __attribute__((address_space(3))) unsigned luint;
typedef __attribute__((address_space(3))) unsigned char lbyte;
template <int SLP> __device__ __forceinline__ void wait_lds(unsigned a, unsigned want) {
  unsigned v, sv;
  asm volatile("s_branch 2f\n1:\n\ts_sleep %4\n2:\n\tds_read_b32 %0, %2\n\ts_waitcnt lgkmcnt(0)\n\tv_readfirstlane_b32 %1, %0\n\ts_cmp_lg_u32 %1, %3\n\ts_cbranch_scc1 1b"
               : "=&v"(v), "=&s"(sv) : "v"(a), "s"(want), "i"(SLP) : "scc", "memory");
}

__device__ __forceinline__ void proj_epilogue(f32x4 acc0, f32x4 acc1, bool z, float& v0, float& v1) {
  float n0[4], n1[4];
#pragma unroll
  for (int r = 0; r < 4; ++r) {
    auto s = __builtin_amdgcn_permlane32_swap(__float_as_uint(acc0[r]), __float_as_uint(acc1[r]), false, false);
    n0[r] = __uint_as_float(s[0]); n1[r] = __uint_as_float(s[1]);
  }
  const float r0 = n0[0] + dpp<ROR8>(n1[0]), r1 = n0[1] + dpp<ROR8>(n1[1]);
  const float r2 = n1[2] + dpp<ROR8>(n0[2]), r3 = n1[3] + dpp<ROR8>(n0[3]);
  v0 = z ? r2 : r0; v1 = z ? r3 : r1;
}

__global__ void __launch_bounds__(512, 2)
fused_decode(const u16* x_, const u16* kc_, const u16* vc_, const u16* wq_, const u16* wo_, const u16* wg_, const u16* wu_, const u16* wd_, u16* out_, unsigned char* ws_
             ) {
  constexpr int tn0 = TEND0, tn1 = TEND1, tn2 = H1DELAY, tn3 = H2DELAY, tn4 = BDELAY, tn5 = RATE16, tn6 = SMP0, tn7 = WT0, tn8 = SMP1, tn9 = WT1, tn10 = BIAS1, tn11 = TFIN;
  __shared__ __attribute__((aligned(16))) float lds[96 + 8 + 4 + 4 + 20 + 8 * 192];
  unsigned* l_q = (unsigned*)lds;
  float* l_ml = lds + 96;
  float* l_c = lds + 104;
  unsigned* l_est = (unsigned*)(lds + 108);
  unsigned char* l_f = (unsigned char*)(lds + 112);
  constexpr int FA = 0, FB = 4, FC = 8, FD = 12, FE = 16, FF = 20, FG = 24;
  unsigned* l_tend = (unsigned*)(lds + 119);
  const unsigned fbase = (unsigned)(size_t)(lbyte*)l_f;
  float* l_acc = lds + 132;

  STAMP_DECL;
  STAMP(0);
  typedef unsigned long long u64;
  typedef u64 u64x8 __attribute__((ext_vector_type(8)));
  typedef u64 u64x2 __attribute__((ext_vector_type(2)));
  u64x8 ka; u64x2 kb; u64 tst, pc;
  asm volatile("s_load_dwordx16 %0, %4, 0x0\n\ts_load_dwordx4 %1, %4, 0x40\n\ts_memrealtime %2\n\ts_getpc_b64 %3"
               : "=&s"(ka), "=&s"(kb), "=&s"(tst), "=&s"(pc) : "s"(__builtin_amdgcn_kernarg_segment_ptr()));
  const int bid = blockIdx.x;
  const int xq = bid & 7, uq = bid >> 3;
  const int h = xq * 4 + (uq >> 3), j8 = uq & 7, b = j8 >> 2, s = j8 & 3;
  const int tid = threadIdx.x, lane = tid & 63, wave = __builtin_amdgcn_readfirstlane(tid >> 6);
  const int g = lane >> 3, j = lane & 7;
  const int bl = (lane >> 2) & 1, e4 = lane & 3;
  const bool isS = wave >= 4, isQ = wave < 2;
  if (tid < 12) l_est[tid] = tid == 9 ? 0x01010101u : 0u;

  unsigned u6q = 2 * (2 * (7 - j8) + wave), u6f = 31 - uq;
  asm volatile("" : "+s"(u6q), "+s"(u6f));
  const unsigned u6 = isQ ? u6q : u6f;
  const unsigned ur = (u6 * 43) >> 8, uc = u6 - 6 * ur;
  const bool has_front = (wave < 3) & (u6 < 30);
  const unsigned up = uc >= 3 ? 1 : 0;
  unsigned kv_off = ((b * 32 + h) * 8192 + 6144 + 512 * s + KVW * (wave - 4)) * 384u;
  unsigned q_off = ur * (128u * 12288u) + 384u * h + 64u * uc;
  unsigned f_off = ur * (128u * 3072u) + 384u * xq + 128u * (uc - 3 * up);
  asm volatile("" : "+s"(kv_off), "+s"(q_off), "+s"(f_off));
  const unsigned off0 = isS ? kv_off : (isQ ? q_off : f_off);
  const unsigned rowb = isQ ? 12288 : 3072;
  const unsigned gstr = isS ? KVG : 16 * rowb;
  const int nrec = isS ? 2 * 32 * 8192 * 384 : (has_front ? 640 * rowb : 0);
  const int voff = g * gstr + 16 * j + off0;
  const int stepA = isS ? 128 : rowb, stepB = isS ? KVT - 256 : rowb;
  pc &= ~255ull;
  unsigned pco = ((7 - wave) * 32 + uq) * 64;
  pco = pco < CODEPF ? pco : 0;
  asm volatile("s_waitcnt lgkmcnt(0)\n\ts_barrier" : "+s"(ka), "+s"(kb), "+s"(tst)
               : "s"(nrec), "v"(voff), "s"(stepA), "s"(stepB), "s"(pco), "s"(pc) : "memory");
  const u16 *x = (const u16*)ka[0], *kc = (const u16*)ka[1], *vc = (const u16*)ka[2], *wq = (const u16*)ka[3], *wo = (const u16*)ka[4],
            *wg = (const u16*)ka[5], *wu = (const u16*)ka[6], *wd = (const u16*)ka[7];
  u16* out = (u16*)kb[0];
  unsigned char* ws = (unsigned char*)kb[1];
  const unsigned tstart = (unsigned)tst;
  const rsrc_t r0 = mk_rsrc(isS ? kc : (isQ ? wq : (up ? wu : wg)), nrec);
  u32x4 buf[96];
  FS(11);
  unsigned tag, ep;
  unsigned cpf;
  asm volatile("s_load_dword %0, %2, 0x0\n\ts_load_dword %1, %3, %4" : "=&s"(ep), "=&s"(cpf) : "s"(ws), "s"(pc), "s"(pco));
#define TAG_READY() asm volatile("s_waitcnt lgkmcnt(0)\n\ts_add_u32 %0, %1, 1" : "=s"(tag) : "s"(ep), "s"(cpf) : "scc")
  if (nrec) {
    int so = 0;
#pragma unroll
    for (int n = 0; n < 9; ++n) { buf[n] = ld_nt(r0, voff, so); so += (n % 3 == 2) ? stepB : stepA; SB(); }
  }
  FS(1);
  const rsrc_t rws = mk_rsrc(ws, WS_END);

  if (isS) {
    const int sw = wave - 4;
    const rsrc_t rv = mk_rsrc(vc, 2 * 32 * 8192 * 384);
    u32x4 qv[3];
    float sc[16];
    unsigned pd[16];
    f32x4 acc[3] = {{0.f, 0.f, 0.f, 0.f}, {0.f, 0.f, 0.f, 0.f}, {0.f, 0.f, 0.f, 0.f}};
    unsigned msk[4];
#pragma unroll
    for (int d = 0; d < 4; ++d) msk[d] = (j >> 1) == d ? ((j & 1) ? 0xffff0000u : 0x0000ffffu) : 0u;
    constexpr float C2 = 0.07216878364870322f * 1.4426950408889634f;
    unsigned tend = 0, tgt = 0, tendv = 0;
    const int stag = ((2 * sw - 3) * STAG) >> 1;
    if (PACING) { tgt = tstart; tend = tgt + tn0 + stag; }

    auto consume = [&](auto G) {
      constexpr int t = decltype(G)::value;
      if constexpr (t < 16) {
        float a0 = 0.f, a1 = 0.f, a2 = 0.f;
#pragma unroll
        for (int e = 0; e < 4; ++e) { a0 = dot2(buf[3 * t][e], qv[0][e], a0); a1 = dot2(buf[3 * t + 1][e], qv[1][e], a1); a2 = dot2(buf[3 * t + 2][e], qv[2][e], a2); }
        float a = (a0 + a1) + a2;
        a += dpp<XOR1>(a); a += dpp<XOR2>(a); a += dpp<HMIR>(a);
        sc[t] = a;
        if constexpr (t == 15) {
          float m = sc[0];
#pragma unroll
          for (int u = 1; u < 16; ++u) m = fmaxf(m, sc[u]);
          m = fmaxf(m, dpp<ROR8>(m)); m = x16_max(m); m = x32_max(m);
          const float mc = m * C2;
          float l = 0.f;
#pragma unroll
          for (int u = 0; u < 16; ++u) { const float p = __builtin_amdgcn_exp2f(fmaf(sc[u], C2, -mc)); pd[u] = cvtpk(p, p); l += bfhi(pd[u]); }
          l += dpp<ROR8>(l); l = x16_sum(l); l = x32_sum(l);
          if (lane == 0) { *(f32x2*)(l_ml + 2 * sw) = f32x2{mc, l}; MEMFENCE(); l_f[FA + sw] = 1; }
          STAMP(3);
        }
      } else {
        constexpr int u = t - 16;
        const u32x4 a = {pd[u] & msk[0], pd[u] & msk[1], pd[u] & msk[2], pd[u] & msk[3]};
#pragma unroll
        for (int i = 0; i < 3; ++i) acc[i] = mfma32(a, buf[3 * t + i], acc[i]);
      }
    };
    constexpr auto done_after = [](int n) constexpr {
      if (n < B1_AT) return 0;
      const int landed = (n - WIN + 1) / 3;
      const int cu = 1 + (CATCH * (n - B1_AT)) / 3;
      return landed < cu ? landed : cu;
    };
    static_for<9, 96>([&](auto N) {
      constexpr int n = decltype(N)::value;
      if constexpr (n == B1_AT) {
        STAMP(1);
        wait_lds<0>(fbase + FG, 0x00010101u);
        STAMP(2);
#pragma unroll
        for (int i = 0; i < 3; ++i) qv[i] = *(const u32x4*)(l_q + 32 * i + 4 * j);
      }
      if constexpr (n >= WIN) asm volatile("s_waitcnt vmcnt(%0)" ::"i"(WIN - 1) : "memory");
      if constexpr (n == 9) TAG_READY();
      if constexpr (n >= 9 && n < 9 + 45 && (n - 9) % 5 == 0) FS(2 + (n - 9) / 5);
      if constexpr (PACING && n % 3 == 0) {
        constexpr int k = n / 3;
        if constexpr (PACING == 2 && k > 12) { const unsigned tv = __builtin_amdgcn_readfirstlane(tendv); tend = tv ? tv + stag : tend; }
        const unsigned now = pace_step<(k < 31 ? 65536 / (31 - k) : 0)>(tgt, tend);
        if constexpr (PACING == 2 && k >= 6 && k < 26) {
          unsigned vm;
          asm volatile("s_getreg_b32 %0, hwreg(HW_REG_IB_STS, 0, 4)" : "=s"(vm));
          l_est[sw] = (now + (((vm + 3 * (31 - k)) * tn5) >> 4)) | 1u;
        }
        if constexpr (PACING == 2 && k >= 12 && k < 30) tendv = *l_tend;
      }
      if constexpr (n == FB_AT) { if (lane == 0) l_f[FB + sw] = 1; }
      constexpr int m = n < 48 ? n : n - 48;
      buf[n] = ld_nt(n < 48 ? r0 : rv, voff + (m % 3) * 128, (m / 3) * KVT);
      SB();
      static_for<done_after(n - 1), done_after(n)>(consume);
      SB();
    });
    STAMP(4);
    __builtin_amdgcn_s_setprio(3);
    wait_lds<0>(fbase + FE, 0x01010101u);
    float cq = l_c[lane >> 4];
    unsigned dst = (unsigned)(size_t)(lfloat*)(l_acc + (sw * 2 + (g & 1)) * 192 + 8 * j + 4 * ((lane >> 4) & 1));
    unsigned src = (unsigned)(size_t)(lfloat*)(l_acc + (lane >> 4) * 384 + 64 * sw + 8 * j + 4 * (g & 1));
    unsigned fla = fbase + FC + sw, one = 1u, flr = fbase + FC;
    int pvo = (lane & 16) ? OOB : (g & 1) * 128 + j * 16 + (lane >> 5) * 8;
    int pso = WS_OP + ((h * 2 + b) * 4 + s) * 768 + sw * 256;
    unsigned tagv = tag;
    asm volatile("" : "+v"(cq), "+v"(dst), "+v"(src), "+v"(fla), "+v"(flr), "+v"(one), "+v"(pvo), "+s"(pso), "+v"(tagv));
    const bool wr = (lane >> 5) == (g & 1);
    SB();
    static_for<done_after(95), 32>([&](auto G) { consume(G); SB(); });
    if (wr) {
#pragma unroll
      for (int i = 0; i < 3; ++i) *(lf32x4*)(size_t)(dst + 256 * i) = acc[i];
      MEMFENCE();
      *(lbyte*)(size_t)fla = (unsigned char)one;
    }
    STAMP(5);
    if (sw < 3) {
      f32x4 u0, u1;
      unsigned fv, fs;
      asm volatile("1:\n\tds_read_b32 %0, %4\n\tds_read_b128 %2, %5\n\tds_read_b128 %3, %5 offset:768\n\ts_waitcnt lgkmcnt(2)\n\tv_readfirstlane_b32 %1, %0\n\t"
                   "s_cmp_lg_u32 %1, 0x01010101\n\ts_waitcnt lgkmcnt(0)\n\ts_cbranch_scc1 1b"
                   : "=&v"(fv), "=&s"(fs), "=&v"(u0), "=&v"(u1) : "v"(flr), "v"(src) : "scc", "memory");
      float o[4];
#pragma unroll
      for (int e = 0; e < 4; ++e) o[e] = cq * (u0[e] + u1[e]);
      const auto s0 = __builtin_amdgcn_permlane32_swap(__float_as_uint(o[0]), __float_as_uint(o[2]), false, false);
      const auto s1 = __builtin_amdgcn_permlane32_swap(__float_as_uint(o[1]), __float_as_uint(o[3]), false, false);
      const float p0 = x16_sum(__uint_as_float(s0[0]) + __uint_as_float(s0[1])), p1 = x16_sum(__uint_as_float(s1[0]) + __uint_as_float(s1[1]));
      const u32x2 pv = {cvtpk(p0, p1), tagv};
      __builtin_amdgcn_raw_buffer_store_b64(pv, rws, pvo, pso, AUX_SC1);
    }
    MEMFENCE();
    *(lbyte*)(size_t)(fla + (FD - FC)) = (unsigned char)one;
    STAMP(6);
    STAMP_FLUSH();
    return;
  }

  __builtin_amdgcn_s_setprio(2);
  const int w = wave;
  const unsigned m0 = e4 == 0 ? 0x0000ffffu : e4 == 1 ? 0xffff0000u : 0u;
  const unsigned m1 = e4 == 2 ? 0x0000ffffu : e4 == 3 ? 0xffff0000u : 0u;
  const int st_col = (8 * j + 4 * (lane >> 5) + 2 * (g & 1)) * 8;
  const rsrc_t rx = mk_rsrc(x, 2560);
  if (has_front) {
    const int xo = (lane >> 5) * 1280 + (lane & 31) * 16;
    const bool x3 = (lane & 16) == 0;
    const u32x4 xa = __builtin_amdgcn_raw_buffer_load_b128(rx, xo, 0, 0), xb = __builtin_amdgcn_raw_buffer_load_b128(rx, xo + 512, 0, 0);
    const u32x4 xc = __builtin_amdgcn_raw_buffer_load_b128(rx, x3 ? xo + 1024 : OOB, 0, 0);
    const u32x4 xr0 = __builtin_amdgcn_raw_buffer_load_b128(rx, bl * 1280 + g * 32, 256 * ur, 0);
    const u32x4 xr1 = __builtin_amdgcn_raw_buffer_load_b128(rx, bl * 1280 + g * 32 + 16, 256 * ur, 0);
    SB();
#pragma unroll
    for (int n = 9; n < 16; ++n) buf[n] = ld_nt(r0, voff, n * rowb);
    SB();
    FS(2);
    TAG_READY();
    float v[24];
#pragma unroll
    for (int e = 0; e < 4; ++e) { v[2 * e] = bflo(xa[e]); v[2 * e + 1] = bfhi(xa[e]); v[8 + 2 * e] = bflo(xb[e]); v[9 + 2 * e] = bfhi(xb[e]); v[16 + 2 * e] = bflo(xc[e]); v[17 + 2 * e] = bfhi(xc[e]); }
    float sum = 0.f;
#pragma unroll
    for (int e = 0; e < 24; ++e) sum += v[e];
    sum = x16_sum(row_sum(sum));
    const float mean = sum * (1.f / 640.f);
    float sq = 0.f, sq3 = 0.f;
#pragma unroll
    for (int e = 0; e < 16; ++e) { const float d = v[e] - mean; sq = fmaf(d, d, sq); }
#pragma unroll
    for (int e = 16; e < 24; ++e) { const float d = v[e] - mean; sq3 = fmaf(d, d, sq3); }
    sq = x16_sum(row_sum(sq + (x3 ? sq3 : 0.f)));
    const float mb = rbf(mean), den = rbf(__builtin_amdgcn_sqrtf(rbf(rbf(sq * (1.f / 640.f)) + 1e-5f)));
    const float inv = __builtin_amdgcn_rcpf(den);
    const float mb0 = __builtin_bit_cast(float, __builtin_amdgcn_readlane(__builtin_bit_cast(int, mb), 0));
    const float mb1 = __builtin_bit_cast(float, __builtin_amdgcn_readlane(__builtin_bit_cast(int, mb), 32));
    const float iv0 = __builtin_bit_cast(float, __builtin_amdgcn_readlane(__builtin_bit_cast(int, inv), 0));
    const float iv1 = __builtin_bit_cast(float, __builtin_amdgcn_readlane(__builtin_bit_cast(int, inv), 32));
    const float mm = bl ? mb1 : mb0, ii = bl ? iv1 : iv0;
    FS(3);
    f32x4 acc0 = {0.f, 0.f, 0.f, 0.f}, acc1 = {0.f, 0.f, 0.f, 0.f};
#pragma unroll
    for (int n = 0; n < 16; ++n) {
      const unsigned xw = n < 8 ? xr0[n >> 1] : xr1[(n - 8) >> 1];
      const float xv = (n & 1) ? bfhi(xw) : bflo(xw);
      const float xn = rbf(xv - mm) * ii;
      const unsigned d = cvtpk(xn, xn), a0 = d & m0, a1 = d & m1;
      acc0 = mfma16(a0, a1, buf[n][0], buf[n][1], acc0);
      acc1 = mfma16(a0, a1, buf[n][2], buf[n][3], acc1);
    }
    float v0, v1;
    proj_epilogue(acc0, acc1, g & 1, v0, v1);
    const int rec = w < 2 ? WS_QP + (h * 5 + ur) * 3072 + uc * 256 : WS_GU + ((xq * 5 + ur) * 2 + up) * 3072 + (uc - 3 * up) * 512;
    st_pair(rws, ((lane >> 4) & 1) * 1536 + st_col, rec, v0, v1, tag);
    STAMP(1); FS(4);
    asm volatile("s_waitcnt vmcnt(0)" ::: "memory");
    STAMP(11); FS(5);
  } else {
    if (w == 3) {
      if (PRETOUCH) {
        wait_until(tstart + PRETOUCH);
        const unsigned kv0 = kv_off + KVW * 384, kv1 = kv0 + 512 * 384 - 64, o0 = h * (192 * 1280), o1 = o0 + 192 * 1280 - 64, d0_ = h * (48 * 1280);
        unsigned t0, t1, t2, t3_, t4, t5, t6, t7, t8;
        asm volatile("s_load_dword %0, %9, %14\n\ts_load_dword %1, %10, %15\n\ts_load_dword %2, %10, %14\n\ts_load_dword %3, %11, %16\n\ts_load_dword %4, %11, %17\n\t"
                     "s_load_dword %5, %12, %18\n\ts_load_dword %6, %13, 0x0\n\ts_load_dword %7, %19, %20\n\ts_load_dword %8, %19, %21\n\ts_waitcnt lgkmcnt(0)"
                     : "=&s"(t0), "=&s"(t1), "=&s"(t2), "=&s"(t3_), "=&s"(t4), "=&s"(t5), "=&s"(t6), "=&s"(t7), "=&s"(t8)
                     : "s"(kc), "s"(vc), "s"(wo), "s"(wd), "s"(out), "s"(kv1), "s"(kv0), "s"(o0), "s"(o1), "s"(d0_), "s"(ws), "s"(WS_END), "s"(512));
      }
    }
    TAG_READY();
  }
  const int slot = w * 8 + j8;
  const bool has_back = slot < 30;
  const int t3 = slot / 10, cc = slot - 10 * t3;
  const int rb = bid / 80, rc = (bid - 80 * rb) >> 3, ro = bid & 7;
  const bool reducer = (w == 0) && (bid < 160);
  unsigned ha[2][2] = {{0u, 0u}, {0u, 0u}};
        unsigned tend = tstart + tn0;
        auto pace_round = [&](unsigned t_sample, unsigned t_read, int so, int bias) {
          wait_until(t_sample);
          u32x4 e;
          do { MEMFENCE(); e = *(const u32x4*)l_est; } while (!__builtin_amdgcn_readfirstlane(e[0] & e[1] & e[2] & e[3] & 1u));
          const u32x2 pv = {tstart + (unsigned)((int)((e[0] - tstart) + (e[1] - tstart) + (e[2] - tstart) + (e[3] - tstart)) >> 2), tag};
          __builtin_amdgcn_raw_buffer_store_b64(pv, rws, (lane == 0 && bid < 32) ? bid * 8 : OOB, so, AUX_SC1);
          wait_until(t_read);
          MEMFENCE();
          const u32x2 tv = __builtin_amdgcn_raw_buffer_load_b64(rws, (lane & 31) * 8, so, AUX_SC1);
          if (__all(tv[1] == tag)) {
            const float d = x16_sum(row_sum((float)(int)(tv[0] - tstart)));
            int di = (int)(d * (1.f / 32.f)) - bias;
            di = di < 0 ? 0 : (di > 2500 ? 2500 : di);
            tend = (tstart + __builtin_amdgcn_readfirstlane(di)) | 1u;
            *l_tend = tend;
          }
        };
        if (w < 3) {
          if (has_front) __builtin_amdgcn_s_sleep(QSLEEP); else wait_until(tstart + 520);
          u32x4 t[3];
          const int vo = (lane >> 5) * 3072 + (lane & 31) * 16, so = WS_QP + h * 5 * 3072 + b * 1536 + w * 512;
          for (unsigned spin = 0;; ++spin) {
            MEMFENCE();
            t[0] = ld_sc1(rws, vo, so); t[1] = ld_sc1(rws, vo, so + 6144); t[2] = ld_sc1(rws, lane < 32 ? vo : OOB, so + 12288);
            const bool ok = (t[0][1] == tag) & (t[0][3] == tag) & (t[1][1] == tag) & (t[1][3] == tag) & (((t[2][1] == tag) & (t[2][3] == tag)) | (lane >= 32));
            if (__all(ok) || spin > SPIN_CAP) { STAMPV(12, spin + 1); break; }
          }
          const float a = x32_sum((__uint_as_float(t[0][0]) + __uint_as_float(t[1][0])) + __uint_as_float(t[2][0]));
          const float c = x32_sum((__uint_as_float(t[0][2]) + __uint_as_float(t[1][2])) + __uint_as_float(t[2][2]));
          if (lane < 32) { l_q[32 * w + lane] = cvtpk(a, c); MEMFENCE(); l_f[FG + w] = 1; }
        } else if (PACING == 2) {
          pace_round(tstart + tn6, tstart + tn6 + tn7, WS_TS, tn1);
        } else {
          wait_until(tstart + 700);
        }
        STAMP(2); FS(6);
        if (has_back) {
          u32x4 t[3];
          const int so = WS_GU + xq * 5 * 6144 + (h & 3) * 384 + t3 * 128;
          const int vo = bl * 1536 + 16 * g;
          for (unsigned spin = 0;; ++spin) {
            MEMFENCE();
            t[0] = ld_sc1(rws, vo + e4 * 6144, so);
            t[1] = ld_sc1(rws, vo + e4 * 6144 + 3072, so);
            t[2] = ld_sc1(rws, vo + 4 * 6144 + (e4 & 1) * 3072, so);
            bool ok = true;
#pragma unroll
            for (int k = 0; k < 3; ++k) ok &= (t[k][1] == tag) & (t[k][3] == tag);
            if (__all(ok) || spin > SPIN_CAP) { STAMPV(13, spin + 1); break; }
          }
#pragma unroll
          for (int z = 0; z < 2; ++z) {
            const float ex = __uint_as_float(t[2][2 * z]);
            const float gg = rbf(quad_sum(__uint_as_float(t[0][2 * z]) + (e4 == 0 ? ex : 0.f)));
            const float uu = rbf(quad_sum(__uint_as_float(t[1][2 * z]) + (e4 == 1 ? ex : 0.f)));
            const float zz = 0.7978845608028654f * (gg + 0.044715f * gg * gg * gg);
            const float ge = rbf(gg * __builtin_amdgcn_rcpf(1.f + __builtin_amdgcn_exp2f(-2.8853900817779268f * zz)));
            const float hv = ge * uu;
            const unsigned d = cvtpk(hv, hv);
            ha[z][0] = d & m0; ha[z][1] = d & m1;
          }
        }
  const int oc = 4 * (lane & 1) + 2 * (lane >> 5) + ((lane >> 4) & 1);
  float xres = 0.f;
  if (reducer) xres = bflo(__builtin_amdgcn_raw_buffer_load_b16(rx, oc * 2, rb * 1280 + rc * 128 + ro * 16, 0));
  STAMP(3);
        if (w == 1) {
          wait_lds<4>(fbase + FA, 0x01010101u);
          const int so = WS_ML + (h * 2 + b) * 256;
          if (lane < 4) { const f32x2 ml = *(const f32x2*)(l_ml + 2 * lane); st_pair(rws, lane * 16, so + s * 64, ml[0], ml[1], tag); }
          asm volatile("s_waitcnt vmcnt(0)" ::: "memory");
          __builtin_amdgcn_s_sleep(MLSLEEP);
          u32x4 t;
          for (unsigned spin = 0;; ++spin) {
            MEMFENCE();
            t = ld_sc1(rws, (lane & 15) * 16, so);
            if (__all((t[1] == tag) & (t[3] == tag)) || spin > SPIN_CAP) break;
          }
          const float mv = __uint_as_float(t[0]);
          const float M = row_max(mv);
          const float f = __builtin_amdgcn_exp2f(mv - M);
          const float L = row_sum(f * __uint_as_float(t[2]));
          if ((lane >> 2) == s) { l_c[lane & 3] = f / L; MEMFENCE(); l_f[FE + (lane & 3)] = 1; }
        }
  if (PACING == 2 && w == 3 && tn8) {
    pace_round(tend - tn8, tend - tn8 + tn9, WS_TS + 256, tn10);
  }
  if (has_back) {
    const unsigned sLo = (e4 & 1) ? 0x01000c0cu : 0x0c0c0100u, sHi = (e4 & 1) ? 0x03020c0cu : 0x0c0c0302u;
    unsigned sel0 = e4 < 2 ? sLo : 0x0c0c0c0cu, sel1 = e4 < 2 ? 0x0c0c0c0cu : sLo, sel2 = e4 < 2 ? sHi : 0x0c0c0c0cu, sel3 = e4 < 2 ? 0x0c0c0c0cu : sHi;
    int op_so = WS_OP + h * 8 * 768 + t3 * 256, op_vo = (bl * 4 + e4) * 768 + 16 * g;
    int d_vo = ((48 * h + 16 * t3 + 2 * g) * 640 + 64 * cc) * 2 + 16 * j, o_vo = ((192 * h + 64 * t3 + (WOSEQ ? 1 : 8) * g) * 640 + 64 * cc) * 2 + 16 * j;
    int yvo = ((lane >> 4) & 1) * 256 + st_col / 2, yso = WS_YP + ((h * 3 + t3) * 10 + cc) * 512;
    int r_vo = (lane >> 1) * 5120 + (lane & 1) * 16 + WS_YP + rc * 512 + rb * 256 + ro * 32;
    int out_vo = (lane & 14) == 0 ? oc * 2 + rb * 1280 + rc * 128 + ro * 16 : OOB;
    unsigned tag2v = tag + 1u, tda = (unsigned)(size_t)(luint*)l_tend;
    asm volatile("" : "+v"(sel0), "+v"(sel1), "+v"(sel2), "+v"(sel3), "+s"(op_so), "+v"(op_vo), "+v"(d_vo), "+v"(o_vo), "+v"(yvo), "+s"(yso), "+v"(r_vo), "+v"(out_vo), "+v"(tag2v), "+v"(xres), "+v"(tda));
    int pass = DRYRUN ? (w + 3) >> 2 : 1;
    asm volatile("" : "+s"(pass));
#pragma nounroll
    for (; pass < 2; ++pass) {
      STAMP(4);
      const int live = pass ? -1 : 0;
      const rsrc_t rd = mk_rsrc(wd, 1536 * 1280 & live), ro_ = mk_rsrc(wo, 6144 * 1280 & live), rw2 = mk_rsrc(ws, WS_END & live), rout = mk_rsrc(out, 2560 & live);
      const unsigned want = tag & live, want2 = (tag + 1u) & live;
      unsigned fb = fbase + (pass ? FB : FF), fd = fbase + (pass ? FD : FF);
      asm volatile("" : "+v"(fb), "+v"(fd));
      wait_lds<1>(fb, 0x01010101u);
      const unsigned tfin = __builtin_amdgcn_readfirstlane(*(luint*)(size_t)tda) & live;
      delay_ticks(tn4 & live);
      u32x4 wb[10], t0, t1;
      unsigned fdv = 0;
#pragma unroll
      for (int n = 0; n < 10; ++n) {
        if (n == POLLPOS) {
          const rsrc_t rp = mk_rsrc(ws, __builtin_amdgcn_readfirstlane(fdv) == 0x01010101u ? (WS_END & live) : 0);
          t0 = ld_sc1(rp, op_vo, op_so); t1 = ld_sc1(rp, op_vo + 128, op_so);
          SB();
        }
        if (n < 2) wb[8 + n] = ld_nt(rd, d_vo + n * 1280, 0); else wb[n - 2] = ld_nt(ro_, o_vo, (n - 2) * (WOSEQ ? 10240 : 1280));
        if (n == POLLPOS - FDLOOK) { MEMFENCE(); fdv = *(luint*)(size_t)fd; }
        SB();
        if (n < 9) __builtin_amdgcn_s_sleep(BPACE);
        SB();
      }
      if (POLLPOS == 10) {
        const rsrc_t rp = mk_rsrc(ws, __builtin_amdgcn_readfirstlane(fdv) == 0x01010101u ? (WS_END & live) : 0);
        t0 = ld_sc1(rp, op_vo, op_so); t1 = ld_sc1(rp, op_vo + 128, op_so);
        SB();
      }
      STAMP(5);
      u32x4 bq[10];
#define PAIR(p) do { bq[2 * (p)] = u32x4{wb[2 * (p)][0], wb[2 * (p)][1], wb[2 * (p) + 1][0], wb[2 * (p) + 1][1]}; \
                     bq[2 * (p) + 1] = u32x4{wb[2 * (p)][2], wb[2 * (p)][3], wb[2 * (p) + 1][2], wb[2 * (p) + 1][3]}; } while (0)
      PAIR(4); PAIR(0); PAIR(1); PAIR(2);
      if (POLLPOS == 10) PAIR(3);
      const u32x4 hA = {ha[0][0], ha[0][1], ha[1][0], ha[1][1]};
      f32x4 acc0 = mfma32(hA, bq[8], f32x4{0.f, 0.f, 0.f, 0.f}), acc1 = mfma32(hA, bq[9], f32x4{0.f, 0.f, 0.f, 0.f});
      asm volatile("" : "+v"(bq[0]), "+v"(bq[1]), "+v"(bq[2]), "+v"(bq[3]), "+v"(bq[4]), "+v"(bq[5]), "+v"(acc0), "+v"(acc1));
      if (POLLPOS == 10) asm volatile("" : "+v"(bq[6]), "+v"(bq[7]));
      SB();
      unsigned np1 = 1;
      if (__builtin_expect(!__all((t0[1] == want) & (t0[3] == want) & (t1[1] == want) & (t1[3] == want)), 0)) {
        do {
          ++np1;
          wait_lds<1>(fd, 0x01010101u);
          delay_ticks(tn2);
          t0 = ld_sc1(rw2, op_vo, op_so); t1 = ld_sc1(rw2, op_vo + 128, op_so);
        } while (!__all((t0[1] == want) & (t0[3] == want) & (t1[1] == want) & (t1[3] == want)) && np1 < SPIN_CAP);
      }
      STAMPV(14, np1);
      STAMP(7);
      unsigned pk[4];
      {
        float u0, u1, u2, u3, u4, u5, u6, u7;
#define DPPADD(r, P) "v_add_f32_dpp " r ", " r ", " r " quad_perm:" P " row_mask:0xf bank_mask:0xf\n\t"
#define DPP8(P) DPPADD("%4", P) DPPADD("%5", P) DPPADD("%6", P) DPPADD("%7", P) DPPADD("%8", P) DPPADD("%9", P) DPPADD("%10", P) DPPADD("%11", P)
        asm volatile("v_lshlrev_b32 %4, 16, %12\n\tv_and_b32 %5, 0xffff0000, %12\n\tv_lshlrev_b32 %6, 16, %13\n\tv_and_b32 %7, 0xffff0000, %13\n\t"
                     "v_lshlrev_b32 %8, 16, %14\n\tv_and_b32 %9, 0xffff0000, %14\n\tv_lshlrev_b32 %10, 16, %15\n\tv_and_b32 %11, 0xffff0000, %15\n\t"
                     DPP8("[1,0,3,2]") DPP8("[2,3,0,1]")
                     "v_cvt_pk_bf16_f32 %0, %4, %5\n\tv_cvt_pk_bf16_f32 %1, %6, %7\n\tv_cvt_pk_bf16_f32 %2, %8, %9\n\tv_cvt_pk_bf16_f32 %3, %10, %11"
                     : "=&v"(pk[0]), "=&v"(pk[1]), "=&v"(pk[2]), "=&v"(pk[3]), "=&v"(u0), "=&v"(u1), "=&v"(u2), "=&v"(u3), "=&v"(u4), "=&v"(u5), "=&v"(u6), "=&v"(u7)
                     : "v"(t0[0]), "v"(t0[2]), "v"(t1[0]), "v"(t1[2]));
      }
      u32x4 aq[4];
#pragma unroll
      for (int p = 0; p < 4; ++p)
        aq[p] = u32x4{__builtin_amdgcn_perm(0u, pk[p], sel0), __builtin_amdgcn_perm(0u, pk[p], sel1), __builtin_amdgcn_perm(0u, pk[p], sel2), __builtin_amdgcn_perm(0u, pk[p], sel3)};
#pragma unroll
      for (int p = 0; p < 3; ++p) { acc0 = mfma32(aq[p], bq[2 * p], acc0); acc1 = mfma32(aq[p], bq[2 * p + 1], acc1); }
      if (POLLPOS < 10) { asm volatile("" : "+v"(aq[3]), "+v"(acc0), "+v"(acc1)); SB(); PAIR(3); }
      acc0 = mfma32(aq[3], bq[6], acc0); acc1 = mfma32(aq[3], bq[7], acc1);
      float v0, v1;
      proj_epilogue(acc0, acc1, g & 1, v0, v1);
      __builtin_amdgcn_raw_buffer_store_b64(u32x2{cvtpk(v0, v1), tag2v}, rw2, yvo, yso, AUX_SC1);
      STAMP(8);
      if (reducer) {
        if (pass) {
          const unsigned tn = (unsigned)__builtin_amdgcn_s_memrealtime();
          int wt = tfin ? (int)(tfin + tn11 - tn) : 40;
          asm("s_max_i32 %0, %0, %1\n\ts_min_i32 %0, %0, 0x64" : "+s"(wt) : "s"(tn3) : "scc");
          wait_until(tn + wt);
        }
        u32x4 t[3];
        unsigned np2 = 0;
        for (;;) {
          ++np2;
          MEMFENCE();
#pragma unroll
          for (int k = 0; k < 3; ++k) t[k] = ld_sc1(rw2, r_vo, k * 32 * 5120);
          bool ok = true;
#pragma unroll
          for (int k = 0; k < 3; ++k) ok &= (t[k][1] == want2) & (t[k][3] == want2);
          if (__all(ok) || np2 > SPIN_CAP) break;
        }
        STAMPV(15, np2);
        STAMP(9);
        const float y0 = (bflo(t[0][0]) + bflo(t[1][0])) + bflo(t[2][0]), y1 = (bfhi(t[0][0]) + bfhi(t[1][0])) + bfhi(t[2][0]);
        const float y2 = (bflo(t[0][2]) + bflo(t[1][2])) + bflo(t[2][2]), y3 = (bfhi(t[0][2]) + bfhi(t[1][2])) + bfhi(t[2][2]);
        const auto s0 = __builtin_amdgcn_permlane32_swap(__float_as_uint(y0), __float_as_uint(y2), false, false);
        const auto s1 = __builtin_amdgcn_permlane32_swap(__float_as_uint(y1), __float_as_uint(y3), false, false);
        const float z0 = __uint_as_float(s0[0]) + __uint_as_float(s0[1]), z1 = __uint_as_float(s1[0]) + __uint_as_float(s1[1]);
        const auto s2 = __builtin_amdgcn_permlane16_swap(__float_as_uint(z0), __float_as_uint(z1), false, false);
        float y = __uint_as_float(s2[0]) + __uint_as_float(s2[1]);
        y += dpp<ROR8>(y); y += dpp<ROR4>(y); y += dpp<XOR2>(y);
        __builtin_amdgcn_raw_buffer_store_b16((unsigned short)cvtpk(xres + y, 0.f), rout, out_vo, 0, AUX_OUT);
        if (bid == 0) __builtin_amdgcn_raw_buffer_store_b32(tag2v, rw2, lane == 0 ? WS_EPOCH : OOB, 0, AUX_SC1);
        STAMP(10);
      }
    }
  }
  STAMP_FLUSH();
}

extern "C" void kernel_launch(void* const* d_in, const int* in_sizes, int n_in, void* d_out, int out_size, void* d_ws, size_t ws_size,
                              hipStream_t stream) {
  fused_decode<<<256, 512, 0, stream>>>((const u16*)d_in[0], (const u16*)d_in[1], (const u16*)d_in[2], (const u16*)d_in[3], (const u16*)d_in[4],
                                        (const u16*)d_in[6], (const u16*)d_in[7], (const u16*)d_in[8], (u16*)d_out, (unsigned char*)d_ws
                                        );
}
```

```cpp
#define PACING 2
#define CODEPF 16384
#define PRETOUCH 250
#define SCAT 0
#define STAG 11
#define POLLPOS 8
#define FDLOOK 1
#define TEND0 1500
#define H1DELAY 20
#define H2DELAY 40
#define BDELAY 12
#define SMP0 600
#define WT0 250
#define TFIN 0
#define TEND1 20
#define SMP1 480
#define WT1 150
#define BIAS1 15
#define SLATE 1
#define SDELAY 160
#define AMP 0
#include <hip/hip_runtime.h>
#include <hip/hip_bf16.h>
#include <stdint.h>
#include <type_traits>

typedef unsigned short u16;
typedef unsigned u32x4 __attribute__((ext_vector_type(4)));
typedef unsigned u32x2 __attribute__((ext_vector_type(2)));
typedef float f32x4 __attribute__((ext_vector_type(4)));
typedef float f32x2 __attribute__((ext_vector_type(2)));
typedef short s16x4 __attribute__((ext_vector_type(4)));
typedef __bf16 bf16x8 __attribute__((ext_vector_type(8)));
typedef __bf16 bf16x2 __attribute__((ext_vector_type(2)));
typedef __amdgpu_buffer_rsrc_t rsrc_t;

#define WIN 9
#define B1_AT 48
#define CATCH 3
#define QSLEEP 16
#define MLSLEEP 32
#define BPACE 5
#define H1SLEEP 0
#define KC 10
#define FB_AT 95
#define DRYRUN 1
#define RATE16 229
#define STOUCH 0
#define AUX_OUT 0
#define KVW 8
#define KVG 384
#define KVT 12288
#define WOSEQ 0
#define TPOLL 155
#define PBID 8

constexpr int AUX_NT = 2, AUX_SC1 = 16;
constexpr int RSRC_W3 = 0x00020000;
constexpr unsigned SPIN_CAP = 1u << 18;
constexpr int OOB = 0x40000000;

constexpr int WS_EPOCH = 0;
constexpr int WS_TS = 1024;
constexpr int WS_QP = 4096;
constexpr int WS_GU = WS_QP + 32 * 5 * 2 * 192 * 8;
constexpr int WS_ML = WS_GU + 8 * 5 * 2 * 2 * 192 * 8;
constexpr int WS_OP = WS_ML + 32 * 2 * 4 * 4 * 16;
constexpr int WS_YP = WS_QP;
constexpr int WS_END = WS_OP + 32 * 2 * 4 * 96 * 8;

template <int I, int N, class F> __device__ __forceinline__ void static_for(F&& f) {
  if constexpr (I < N) { f(std::integral_constant<int, I>{}); static_for<I + 1, N>(f); }
}
__device__ __forceinline__ rsrc_t mk_rsrc(const void* p, int bytes) {
  return __builtin_amdgcn_make_buffer_rsrc(const_cast<void*>(p), 0, bytes, RSRC_W3);
}
__device__ __forceinline__ u32x4 ld_nt(rsrc_t r, int voff, int soff) { return __builtin_amdgcn_raw_buffer_load_b128(r, voff, soff, AUX_NT); }
__device__ __forceinline__ u32x4 ld_sc1(rsrc_t r, int voff, int soff) { return __builtin_amdgcn_raw_buffer_load_b128(r, voff, soff, AUX_SC1); }
__device__ __forceinline__ void st_pair(rsrc_t r, int voff, int soff, float a, float b, unsigned tag) {
  u32x4 v = {__float_as_uint(a), tag, __float_as_uint(b), tag};
  __builtin_amdgcn_raw_buffer_store_b128(v, r, voff, soff, AUX_SC1);
}
__device__ __forceinline__ float bflo(unsigned v) { return __uint_as_float(v << 16); }
__device__ __forceinline__ float bfhi(unsigned v) { return __uint_as_float(v & 0xffff0000u); }
__device__ __forceinline__ unsigned cvtpk(float lo, float hi) {
  bf16x2 r = {(__bf16)lo, (__bf16)hi};
  return __builtin_bit_cast(unsigned, r);
}
__device__ __forceinline__ float rbf(float x) { return (float)(__bf16)x; }
template <int CTRL> __device__ __forceinline__ float dpp(float x) {
  return __builtin_bit_cast(float, __builtin_amdgcn_mov_dpp(__builtin_bit_cast(int, x), CTRL, 0xf, 0xf, true));
}
constexpr int XOR1 = 0xB1, XOR2 = 0x4E, HMIR = 0x141, ROR8 = 0x128, ROR4 = 0x124;
__device__ __forceinline__ float x16_sum(float x) { auto s = __builtin_amdgcn_permlane16_swap(__float_as_uint(x), __float_as_uint(x), false, false); return __uint_as_float(s[0]) + __uint_as_float(s[1]); }
__device__ __forceinline__ float x32_sum(float x) { auto s = __builtin_amdgcn_permlane32_swap(__float_as_uint(x), __float_as_uint(x), false, false); return __uint_as_float(s[0]) + __uint_as_float(s[1]); }
__device__ __forceinline__ float x16_max(float x) { auto s = __builtin_amdgcn_permlane16_swap(__float_as_uint(x), __float_as_uint(x), false, false); return fmaxf(__uint_as_float(s[0]), __uint_as_float(s[1])); }
__device__ __forceinline__ float x32_max(float x) { auto s = __builtin_amdgcn_permlane32_swap(__float_as_uint(x), __float_as_uint(x), false, false); return fmaxf(__uint_as_float(s[0]), __uint_as_float(s[1])); }
__device__ __forceinline__ float row_sum(float x) { x += dpp<XOR1>(x); x += dpp<XOR2>(x); x += dpp<HMIR>(x); x += dpp<ROR8>(x); return x; }
__device__ __forceinline__ float row_max(float x) { x = fmaxf(x, dpp<XOR1>(x)); x = fmaxf(x, dpp<XOR2>(x)); x = fmaxf(x, dpp<HMIR>(x)); x = fmaxf(x, dpp<ROR8>(x)); return x; }
__device__ __forceinline__ float quad_sum(float x) { x += dpp<XOR1>(x); x += dpp<XOR2>(x); return x; }
__device__ __forceinline__ float dot2(unsigned a, unsigned b, float c) {
  return __builtin_amdgcn_fdot2_f32_bf16(__builtin_bit_cast(bf16x2, a), __builtin_bit_cast(bf16x2, b), c, false);
}
__device__ __forceinline__ f32x4 mfma16(unsigned a0, unsigned a1, unsigned b0, unsigned b1, f32x4 c) {
  u32x2 a = {a0, a1}, b = {b0, b1};
  return __builtin_amdgcn_mfma_f32_16x16x16bf16_1k(__builtin_bit_cast(s16x4, a), __builtin_bit_cast(s16x4, b), c, 0, 0, 0);
}
__device__ __forceinline__ f32x4 mfma32(u32x4 a, u32x4 b, f32x4 c) {
  return __builtin_amdgcn_mfma_f32_16x16x32_bf16(__builtin_bit_cast(bf16x8, a), __builtin_bit_cast(bf16x8, b), c, 0, 0, 0);
}
template <int RECIP> __device__ __forceinline__ unsigned pace_step(unsigned& tgt, unsigned tend) {
  unsigned d, now;
  asm volatile("1:\n\ts_memrealtime vcc\n\ts_waitcnt lgkmcnt(0)\n\ts_sub_u32 %0, vcc_lo, %2\n\ts_cmp_lt_i32 %0, 0\n\ts_cbranch_scc1 1b\n\t"
               "s_sub_i32 %0, %3, vcc_lo\n\ts_max_i32 %0, %0, 0\n\ts_min_i32 %0, %0, 0x7d0\n\ts_mul_i32 %0, %0, %4\n\ts_lshr_b32 %0, %0, 16\n\t"
               "s_add_u32 %2, vcc_lo, %0\n\ts_mov_b32 %1, vcc_lo"
               : "=&s"(d), "=&s"(now), "+s"(tgt) : "s"(tend), "i"(RECIP) : "vcc", "scc");
  return now;
}
__device__ __forceinline__ void wait_until(unsigned tgt) {
  asm volatile("1:\n\ts_memrealtime vcc\n\ts_waitcnt lgkmcnt(0)\n\ts_sub_u32 vcc_lo, vcc_lo, %0\n\ts_cmp_lt_i32 vcc_lo, 0\n\ts_cbranch_scc1 1b" :: "s"(tgt) : "vcc", "scc", "memory");
}
#define SB() __builtin_amdgcn_sched_barrier(0)
#define WG_BARRIER() do { asm volatile("s_waitcnt lgkmcnt(0)\n\ts_barrier" ::: "memory"); } while (0)
#define MEMFENCE() asm volatile("" ::: "memory")

#define FS(i)
#define STAMP(i)
#define STAMPV(i, v)
#define STAMP_DECL
#define STAMP_FLUSH()

__device__ __forceinline__ void delay_ticks(int n) {
  if (n > 0) wait_until((unsigned)__builtin_amdgcn_s_memrealtime() + n);
}

typedef __attribute__((address_space(3))) float lfloat;
typedef __attribute__((address_space(3))) f32x4 lf32x4;
typedef __attribute__((address_space(3))) unsigned luint;
typedef __attribute__((address_space(3))) unsigned char lbyte;
template <int SLP> __device__ __forceinline__ void wait_lds(unsigned a, unsigned want) {
  unsigned v, sv;
  asm volatile("s_branch 2f\n1:\n\ts_sleep %4\n2:\n\tds_read_b32 %0, %2\n\ts_waitcnt lgkmcnt(0)\n\tv_readfirstlane_b32 %1, %0\n\ts_cmp_lg_u32 %1, %3\n\ts_cbranch_scc1 1b"
               : "=&v"(v), "=&s"(sv) : "v"(a), "s"(want), "i"(SLP) : "scc", "memory");
}

__device__ __forceinline__ void proj_epilogue(f32x4 acc0, f32x4 acc1, bool z, float& v0, float& v1) {
  float n0[4], n1[4];
#pragma unroll
  for (int r = 0; r < 4; ++r) {
    auto s = __builtin_amdgcn_permlane32_swap(__float_as_uint(acc0[r]), __float_as_uint(acc1[r]), false, false);
    n0[r] = __uint_as_float(s[0]); n1[r] = __uint_as_float(s[1]);
  }
  const float r0 = n0[0] + dpp<ROR8>(n1[0]), r1 = n0[1] + dpp<ROR8>(n1[1]);
  const float r2 = n1[2] + dpp<ROR8>(n0[2]), r3 = n1[3] + dpp<ROR8>(n0[3]);
  v0 = z ? r2 : r0; v1 = z ? r3 : r1;
}

__global__ void __launch_bounds__(512, 2)
fused_decode(const u16* x_, const u16* kc_, const u16* vc_, const u16* wq_, const u16* wo_, const u16* wg_, const u16* wu_, const u16* wd_, u16* out_, unsigned char* ws_
             ) {
  constexpr int tn0 = TEND0, tn1 = TEND1, tn2 = H1DELAY, tn3 = H2DELAY, tn4 = BDELAY, tn5 = RATE16, tn6 = SMP0, tn7 = WT0, tn8 = SMP1, tn9 = WT1, tn10 = BIAS1, tn11 = SDELAY, tn12 = TPOLL, tn13 = TFIN;
  __shared__ __attribute__((aligned(16))) float lds[96 + 8 + 4 + 4 + 20 + 8 * 192];
  unsigned* l_q = (unsigned*)lds;
  float* l_ml = lds + 96;
  float* l_c = lds + 104;
  unsigned* l_est = (unsigned*)(lds + 108);
  unsigned char* l_f = (unsigned char*)(lds + 112);
  constexpr int FA = 0, FB = 4, FC = 8, FD = 12, FE = 16, FF = 20, FG = 24;
  unsigned* l_tend = (unsigned*)(lds + 119);
  const unsigned fbase = (unsigned)(size_t)(lbyte*)l_f;
  float* l_acc = lds + 132;

  STAMP_DECL;
  STAMP(0);
  unsigned amp0, ampv = 0;
  { unsigned long long t_; asm volatile("s_memrealtime %0\n\ts_waitcnt lgkmcnt(0)" : "=s"(t_) :: "memory"); amp0 = (unsigned)t_; }
#define AMPNOW(k, base) do { if (AMP == (k)) { SB(); unsigned long long t_; asm volatile("s_memrealtime %0\n\ts_waitcnt lgkmcnt(0)" : "=s"(t_) :: "memory"); ampv = (unsigned)t_ - (base); SB(); } } while (0)
  typedef unsigned long long u64;
  typedef u64 u64x8 __attribute__((ext_vector_type(8)));
  typedef u64 u64x2 __attribute__((ext_vector_type(2)));
  u64x8 ka; u64x2 kb; u64 tst, pc;
  asm volatile("s_load_dwordx16 %0, %4, 0x0\n\ts_load_dwordx4 %1, %4, 0x40\n\ts_memrealtime %2\n\ts_getpc_b64 %3"
               : "=&s"(ka), "=&s"(kb), "=&s"(tst), "=&s"(pc) : "s"(__builtin_amdgcn_kernarg_segment_ptr()));
  const int bid = blockIdx.x;
  const int xq = bid & 7, uq = bid >> 3;
  const int h = xq * 4 + (uq >> 3), j8 = uq & 7, b = j8 >> 2, s = j8 & 3;
  const int tid = threadIdx.x, lane = tid & 63, wave = __builtin_amdgcn_readfirstlane(tid >> 6);
  const int g = lane >> 3, j = lane & 7;
  const int bl = (lane >> 2) & 1, e4 = lane & 3;
  const bool isS = wave >= 4, isQ = wave < 2;
  if (tid < 12) l_est[tid] = tid == 9 ? 0x01010101u : 0u;

  unsigned u6q = 2 * (2 * (7 - j8) + wave), u6f = 31 - uq;
  asm volatile("" : "+s"(u6q), "+s"(u6f));
  const unsigned u6 = isQ ? u6q : u6f;
  const unsigned ur = (u6 * 43) >> 8, uc = u6 - 6 * ur;
  const bool has_front = (wave < 3) & (u6 < 30);
  const unsigned up = uc >= 3 ? 1 : 0;
  unsigned kv_off = ((b * 32 + h) * 8192 + 6144 + 512 * s + KVW * (wave - 4)) * 384u;
  unsigned q_off = ur * (128u * 12288u) + 384u * h + 64u * uc;
  unsigned f_off = ur * (128u * 3072u) + 384u * xq + 128u * (uc - 3 * up);
  asm volatile("" : "+s"(kv_off), "+s"(q_off), "+s"(f_off));
  const unsigned off0 = isS ? kv_off : (isQ ? q_off : f_off);
  const unsigned rowb = isQ ? 12288 : 3072;
  const unsigned gstr = isS ? KVG : 16 * rowb;
  const int nrec = isS ? 2 * 32 * 8192 * 384 : (has_front ? 640 * rowb : 0);
  const int voff = g * gstr + 16 * j + off0;
  const int stepA = isS ? 128 : rowb, stepB = isS ? KVT - 256 : rowb;
  pc &= ~255ull;
  unsigned pco = ((7 - wave) * 32 + uq) * 64;
  pco = pco < CODEPF ? pco : 0;
  asm volatile("s_waitcnt lgkmcnt(0)\n\ts_barrier" : "+s"(ka), "+s"(kb), "+s"(tst)
               : "s"(nrec), "v"(voff), "s"(stepA), "s"(stepB), "s"(pco), "s"(pc) : "memory");
  const u16 *x = (const u16*)ka[0], *kc = (const u16*)ka[1], *vc = (const u16*)ka[2], *wq = (const u16*)ka[3], *wo = (const u16*)ka[4],
            *wg = (const u16*)ka[5], *wu = (const u16*)ka[6], *wd = (const u16*)ka[7];
  u16* out = (u16*)kb[0];
  unsigned char* ws = (unsigned char*)kb[1];
  const unsigned tstart = (unsigned)tst;
  const rsrc_t r0 = mk_rsrc(isS ? kc : (isQ ? wq : (up ? wu : wg)), nrec);
  u32x4 buf[96];
  FS(11);
  AMPNOW(1, amp0);
  unsigned tag, ep;
  unsigned cpf;
  asm volatile("s_load_dword %0, %2, 0x0\n\ts_load_dword %1, %3, %4" : "=&s"(ep), "=&s"(cpf) : "s"(ws), "s"(pc), "s"(pco));
#define TAG_READY() asm volatile("s_waitcnt lgkmcnt(0)\n\ts_add_u32 %0, %1, 1" : "=s"(tag) : "s"(ep), "s"(cpf) : "scc")
  if (SLATE ? has_front : nrec != 0) {
    int so = 0;
#pragma unroll
    for (int n = 0; n < 9; ++n) { buf[n] = ld_nt(r0, voff, so); so += (n % 3 == 2) ? stepB : stepA; SB(); }
  }
  FS(1);
  const rsrc_t rws = mk_rsrc(ws, WS_END);

  if (isS) {
    const int sw = wave - 4;
    const rsrc_t rv = mk_rsrc(vc, 2 * 32 * 8192 * 384);
    u32x4 qv[3];
    float sc[16];
    unsigned pd[16];
    f32x4 acc[3] = {{0.f, 0.f, 0.f, 0.f}, {0.f, 0.f, 0.f, 0.f}, {0.f, 0.f, 0.f, 0.f}};
    unsigned msk[4];
#pragma unroll
    for (int d = 0; d < 4; ++d) msk[d] = (j >> 1) == d ? ((j & 1) ? 0xffff0000u : 0x0000ffffu) : 0u;
    constexpr float C2 = 0.07216878364870322f * 1.4426950408889634f;
    unsigned tend = 0, tgt = 0, tendv = 0;
    if (SLATE) { unsigned kt; asm volatile("s_load_dword %0, %1, %2\n\ts_waitcnt lgkmcnt(0)" : "=&s"(kt) : "s"(kc), "s"(off0)); }
    const int stag = ((2 * sw - 3) * STAG) >> 1;
    if (PACING) { tgt = SLATE ? tstart + tn11 + stag : tstart; tend = tstart + tn0 + stag; }

    auto consume = [&](auto G) {
      constexpr int t = decltype(G)::value;
      if constexpr (t < 16) {
        float a0 = 0.f, a1 = 0.f, a2 = 0.f;
#pragma unroll
        for (int e = 0; e < 4; ++e) { a0 = dot2(buf[3 * t][e], qv[0][e], a0); a1 = dot2(buf[3 * t + 1][e], qv[1][e], a1); a2 = dot2(buf[3 * t + 2][e], qv[2][e], a2); }
        float a = (a0 + a1) + a2;
        a += dpp<XOR1>(a); a += dpp<XOR2>(a); a += dpp<HMIR>(a);
        sc[t] = a;
        if constexpr (t == 15) {
          float m = sc[0];
#pragma unroll
          for (int u = 1; u < 16; ++u) m = fmaxf(m, sc[u]);
          m = fmaxf(m, dpp<ROR8>(m)); m = x16_max(m); m = x32_max(m);
          const float mc = m * C2;
          float l = 0.f;
#pragma unroll
          for (int u = 0; u < 16; ++u) { const float p = __builtin_amdgcn_exp2f(fmaf(sc[u], C2, -mc)); pd[u] = cvtpk(p, p); l += bfhi(pd[u]); }
          l += dpp<ROR8>(l); l = x16_sum(l); l = x32_sum(l);
          if (lane == 0) { *(f32x2*)(l_ml + 2 * sw) = f32x2{mc, l}; MEMFENCE(); l_f[FA + sw] = 1; }
          STAMP(3);
        }
      } else {
        constexpr int u = t - 16;
        const u32x4 a = {pd[u] & msk[0], pd[u] & msk[1], pd[u] & msk[2], pd[u] & msk[3]};
#pragma unroll
        for (int i = 0; i < 3; ++i) acc[i] = mfma32(a, buf[3 * t + i], acc[i]);
      }
    };
    constexpr auto done_after = [](int n) constexpr {
      if (n < B1_AT) return 0;
      const int landed = (n - WIN + 1) / 3;
      const int cu = 1 + (CATCH * (n - B1_AT)) / 3;
      return landed < cu ? landed : cu;
    };
    static_for<(SLATE ? 0 : 9), 96>([&](auto N) {
      constexpr int n = decltype(N)::value;
      if constexpr (n == B1_AT) {
        STAMP(1);
        wait_lds<0>(fbase + FG, 0x00010101u);
        STAMP(2);
#pragma unroll
        for (int i = 0; i < 3; ++i) qv[i] = *(const u32x4*)(l_q + 32 * i + 4 * j);
      }
      if constexpr (n >= WIN) asm volatile("s_waitcnt vmcnt(%0)" ::"i"(WIN - 1) : "memory");
      if constexpr (n == 9) TAG_READY();
      if constexpr (n >= 9 && n < 9 + 45 && (n - 9) % 5 == 0) FS(2 + (n - 9) / 5);
      if constexpr (PACING && n % 3 == 0) {
        constexpr int k = n / 3;
        if constexpr (PACING == 2 && k > 12) { const unsigned tv = __builtin_amdgcn_readfirstlane(tendv); tend = tv ? tv + stag : tend; }
        const unsigned now = pace_step<(k < 31 ? 65536 / (31 - k) : 0)>(tgt, tend);
        if constexpr (PACING == 2 && k >= 6 && k < 26) {
          unsigned vm;
          asm volatile("s_getreg_b32 %0, hwreg(HW_REG_IB_STS, 0, 4)" : "=s"(vm));
          l_est[sw] = (now + (((vm + 3 * (31 - k)) * tn5) >> 4)) | 1u;
        }
        if constexpr (PACING == 2 && k >= 12 && k < 30) tendv = *l_tend;
      }
      if constexpr (n == FB_AT) { if (lane == 0) l_f[FB + sw] = 1; }
      constexpr int m = n < 48 ? n : n - 48;
      buf[n] = ld_nt(n < 48 ? r0 : rv, voff + (m % 3) * 128, (m / 3) * KVT);
      SB();
      static_for<done_after(n > 0 ? n - 1 : 0), done_after(n)>(consume);
      SB();
    });
    STAMP(4);
    __builtin_amdgcn_s_setprio(3);
    wait_lds<0>(fbase + FE, 0x01010101u);
    float cq = l_c[lane >> 4];
    unsigned dst = (unsigned)(size_t)(lfloat*)(l_acc + (sw * 2 + (g & 1)) * 192 + 8 * j + 4 * ((lane >> 4) & 1));
    unsigned src = (unsigned)(size_t)(lfloat*)(l_acc + (lane >> 4) * 384 + 64 * sw + 8 * j + 4 * (g & 1));
    unsigned fla = fbase + FC + sw, one = 1u, flr = fbase + FC;
    int pvo = (lane & 16) ? OOB : (g & 1) * 128 + j * 16 + (lane >> 5) * 8;
    int pso = WS_OP + ((h * 2 + b) * 4 + s) * 768 + sw * 256;
    unsigned tagv = tag;
    asm volatile("" : "+v"(cq), "+v"(dst), "+v"(src), "+v"(fla), "+v"(flr), "+v"(one), "+v"(pvo), "+s"(pso), "+v"(tagv));
    const bool wr = (lane >> 5) == (g & 1);
    SB();
    static_for<done_after(95), 32>([&](auto G) { consume(G); SB(); });
    if (wr) {
#pragma unroll
      for (int i = 0; i < 3; ++i) *(lf32x4*)(size_t)(dst + 256 * i) = acc[i];
      MEMFENCE();
      *(lbyte*)(size_t)fla = (unsigned char)one;
    }
    STAMP(5);
    if (sw < 3) {
      f32x4 u0, u1;
      unsigned fv, fs;
      asm volatile("1:\n\tds_read_b32 %0, %4\n\tds_read_b128 %2, %5\n\tds_read_b128 %3, %5 offset:768\n\ts_waitcnt lgkmcnt(2)\n\tv_readfirstlane_b32 %1, %0\n\t"
                   "s_cmp_lg_u32 %1, 0x01010101\n\ts_waitcnt lgkmcnt(0)\n\ts_cbranch_scc1 1b"
                   : "=&v"(fv), "=&s"(fs), "=&v"(u0), "=&v"(u1) : "v"(flr), "v"(src) : "scc", "memory");
      float o[4];
#pragma unroll
      for (int e = 0; e < 4; ++e) o[e] = cq * (u0[e] + u1[e]);
      const auto s0 = __builtin_amdgcn_permlane32_swap(__float_as_uint(o[0]), __float_as_uint(o[2]), false, false);
      const auto s1 = __builtin_amdgcn_permlane32_swap(__float_as_uint(o[1]), __float_as_uint(o[3]), false, false);
      const float p0 = x16_sum(__uint_as_float(s0[0]) + __uint_as_float(s0[1])), p1 = x16_sum(__uint_as_float(s1[0]) + __uint_as_float(s1[1]));
      const u32x2 pv = {cvtpk(p0, p1), tagv};
      __builtin_amdgcn_raw_buffer_store_b64(pv, rws, pvo, pso, AUX_SC1);
    }
    MEMFENCE();
    *(lbyte*)(size_t)(fla + (FD - FC)) = (unsigned char)one;
    STAMP(6);
    STAMP_FLUSH();
    return;
  }

  __builtin_amdgcn_s_setprio(2);
  const int w = wave;
  const unsigned m0 = e4 == 0 ? 0x0000ffffu : e4 == 1 ? 0xffff0000u : 0u;
  const unsigned m1 = e4 == 2 ? 0x0000ffffu : e4 == 3 ? 0xffff0000u : 0u;
  const int st_col = (8 * j + 4 * (lane >> 5) + 2 * (g & 1)) * 8;
  const rsrc_t rx = mk_rsrc(x, 2560);
  if (has_front) {
    const int xo = (lane >> 5) * 1280 + (lane & 31) * 16;
    const bool x3 = (lane & 16) == 0;
    const u32x4 xa = __builtin_amdgcn_raw_buffer_load_b128(rx, xo, 0, 0), xb = __builtin_amdgcn_raw_buffer_load_b128(rx, xo + 512, 0, 0);
    const u32x4 xc = __builtin_amdgcn_raw_buffer_load_b128(rx, x3 ? xo + 1024 : OOB, 0, 0);
    const u32x4 xr0 = __builtin_amdgcn_raw_buffer_load_b128(rx, bl * 1280 + g * 32, 256 * ur, 0);
    const u32x4 xr1 = __builtin_amdgcn_raw_buffer_load_b128(rx, bl * 1280 + g * 32 + 16, 256 * ur, 0);
    SB();
#pragma unroll
    for (int n = 9; n < 16; ++n) buf[n] = ld_nt(r0, voff, n * rowb);
    SB();
    FS(2);
    TAG_READY();
    float v[24];
#pragma unroll
    for (int e = 0; e < 4; ++e) { v[2 * e] = bflo(xa[e]); v[2 * e + 1] = bfhi(xa[e]); v[8 + 2 * e] = bflo(xb[e]); v[9 + 2 * e] = bfhi(xb[e]); v[16 + 2 * e] = bflo(xc[e]); v[17 + 2 * e] = bfhi(xc[e]); }
    float sum = 0.f;
#pragma unroll
    for (int e = 0; e < 24; ++e) sum += v[e];
    sum = x16_sum(row_sum(sum));
    const float mean = sum * (1.f / 640.f);
    float sq = 0.f, sq3 = 0.f;
#pragma unroll
    for (int e = 0; e < 16; ++e) { const float d = v[e] - mean; sq = fmaf(d, d, sq); }
#pragma unroll
    for (int e = 16; e < 24; ++e) { const float d = v[e] - mean; sq3 = fmaf(d, d, sq3); }
    sq = x16_sum(row_sum(sq + (x3 ? sq3 : 0.f)));
    const float mb = rbf(mean), den = rbf(__builtin_amdgcn_sqrtf(rbf(rbf(sq * (1.f / 640.f)) + 1e-5f)));
    const float inv = __builtin_amdgcn_rcpf(den);
    const float mb0 = __builtin_bit_cast(float, __builtin_amdgcn_readlane(__builtin_bit_cast(int, mb), 0));
    const float mb1 = __builtin_bit_cast(float, __builtin_amdgcn_readlane(__builtin_bit_cast(int, mb), 32));
    const float iv0 = __builtin_bit_cast(float, __builtin_amdgcn_readlane(__builtin_bit_cast(int, inv), 0));
    const float iv1 = __builtin_bit_cast(float, __builtin_amdgcn_readlane(__builtin_bit_cast(int, inv), 32));
    const float mm = bl ? mb1 : mb0, ii = bl ? iv1 : iv0;
    FS(3);
    f32x4 acc0 = {0.f, 0.f, 0.f, 0.f}, acc1 = {0.f, 0.f, 0.f, 0.f};
#pragma unroll
    for (int n = 0; n < 16; ++n) {
      const unsigned xw = n < 8 ? xr0[n >> 1] : xr1[(n - 8) >> 1];
      const float xv = (n & 1) ? bfhi(xw) : bflo(xw);
      const float xn = rbf(xv - mm) * ii;
      const unsigned d = cvtpk(xn, xn), a0 = d & m0, a1 = d & m1;
      acc0 = mfma16(a0, a1, buf[n][0], buf[n][1], acc0);
      acc1 = mfma16(a0, a1, buf[n][2], buf[n][3], acc1);
    }
    float v0, v1;
    proj_epilogue(acc0, acc1, g & 1, v0, v1);
    const int rec = w < 2 ? WS_QP + (h * 5 + ur) * 3072 + uc * 256 : WS_GU + ((xq * 5 + ur) * 2 + up) * 3072 + (uc - 3 * up) * 512;
    st_pair(rws, ((lane >> 4) & 1) * 1536 + st_col, rec, v0, v1, tag);
    STAMP(1); FS(4);
    asm volatile("s_waitcnt vmcnt(0)" ::: "memory");
    STAMP(11); FS(5);
  } else {
    if (w == 3) {
      if (PRETOUCH) {
        wait_until(tstart + PRETOUCH);
        const unsigned kv0 = kv_off + KVW * 384, kv1 = kv0 + 512 * 384 - 64, o0 = h * (192 * 1280), o1 = o0 + 192 * 1280 - 64, d0_ = h * (48 * 1280);
        unsigned t0, t1, t2, t3_, t4, t5, t6, t7, t8;
        asm volatile("s_load_dword %0, %9, %14\n\ts_load_dword %1, %10, %15\n\ts_load_dword %2, %10, %14\n\ts_load_dword %3, %11, %16\n\ts_load_dword %4, %11, %17\n\t"
                     "s_load_dword %5, %12, %18\n\ts_load_dword %6, %13, 0x0\n\ts_load_dword %7, %19, %20\n\ts_load_dword %8, %19, %21\n\ts_waitcnt lgkmcnt(0)"
                     : "=&s"(t0), "=&s"(t1), "=&s"(t2), "=&s"(t3_), "=&s"(t4), "=&s"(t5), "=&s"(t6), "=&s"(t7), "=&s"(t8)
                     : "s"(kc), "s"(vc), "s"(wo), "s"(wd), "s"(out), "s"(kv1), "s"(kv0), "s"(o0), "s"(o1), "s"(d0_), "s"(ws), "s"(WS_END), "s"(512));
      }
    }
    TAG_READY();
  }
  const int slot = w * 8 + j8;
  const bool has_back = slot < 30;
  const int t3 = slot / 10, cc = slot - 10 * t3;
  const int rb = bid / 80, rc = (bid - 80 * rb) >> 3, ro = bid & 7;
  const bool reducer = (w == 0) && (bid < 160);
  unsigned ha[2][2] = {{0u, 0u}, {0u, 0u}};
        unsigned tend = tstart + tn0;
        auto pace_round = [&](unsigned t_sample, unsigned t_read, int so, int bias) {
          wait_until(t_sample);
          u32x4 e;
          do { MEMFENCE(); e = *(const u32x4*)l_est; } while (!__builtin_amdgcn_readfirstlane(e[0] & e[1] & e[2] & e[3] & 1u));
          const u32x2 pv = {tstart + (unsigned)((int)((e[0] - tstart) + (e[1] - tstart) + (e[2] - tstart) + (e[3] - tstart)) >> 2), tag};
          __builtin_amdgcn_raw_buffer_store_b64(pv, rws, (lane == 0 && bid < 32) ? bid * 8 : OOB, so, AUX_SC1);
          wait_until(t_read);
          MEMFENCE();
          const u32x2 tv = __builtin_amdgcn_raw_buffer_load_b64(rws, (lane & 31) * 8, so, AUX_SC1);
          if (__all(tv[1] == tag)) {
            const float d = x16_sum(row_sum((float)(int)(tv[0] - tstart)));
            int di = (int)(d * (1.f / 32.f)) - bias;
            di = di < 0 ? 0 : (di > 2500 ? 2500 : di);
            tend = (tstart + __builtin_amdgcn_readfirstlane(di)) | 1u;
            *l_tend = tend;
          }
        };
        if (w < 3) {
          if (has_front) __builtin_amdgcn_s_sleep(QSLEEP); else wait_until(tstart + 520);
          u32x4 t[3];
          const int vo = (lane >> 5) * 3072 + (lane & 31) * 16, so = WS_QP + h * 5 * 3072 + b * 1536 + w * 512;
          for (unsigned spin = 0;; ++spin) {
            MEMFENCE();
            t[0] = ld_sc1(rws, vo, so); t[1] = ld_sc1(rws, vo, so + 6144); t[2] = ld_sc1(rws, lane < 32 ? vo : OOB, so + 12288);
            const bool ok = (t[0][1] == tag) & (t[0][3] == tag) & (t[1][1] == tag) & (t[1][3] == tag) & (((t[2][1] == tag) & (t[2][3] == tag)) | (lane >= 32));
            if (__all(ok) || spin > SPIN_CAP) { STAMPV(12, spin + 1); break; }
          }
          const float a = x32_sum((__uint_as_float(t[0][0]) + __uint_as_float(t[1][0])) + __uint_as_float(t[2][0]));
          const float c = x32_sum((__uint_as_float(t[0][2]) + __uint_as_float(t[1][2])) + __uint_as_float(t[2][2]));
          if (lane < 32) { l_q[32 * w + lane] = cvtpk(a, c); MEMFENCE(); l_f[FG + w] = 1; }
        } else if (PACING == 2) {
          pace_round(tstart + tn6, tstart + tn6 + tn7, WS_TS, tn1);
        } else {
          wait_until(tstart + 700);
        }
        STAMP(2); FS(6);
        if (has_back) {
          u32x4 t[3];
          const int so = WS_GU + xq * 5 * 6144 + (h & 3) * 384 + t3 * 128;
          const int vo = bl * 1536 + 16 * g;
          for (unsigned spin = 0;; ++spin) {
            MEMFENCE();
            t[0] = ld_sc1(rws, vo + e4 * 6144, so);
            t[1] = ld_sc1(rws, vo + e4 * 6144 + 3072, so);
            t[2] = ld_sc1(rws, vo + 4 * 6144 + (e4 & 1) * 3072, so);
            bool ok = true;
#pragma unroll
            for (int k = 0; k < 3; ++k) ok &= (t[k][1] == tag) & (t[k][3] == tag);
            if (__all(ok) || spin > SPIN_CAP) { STAMPV(13, spin + 1); break; }
          }
#pragma unroll
          for (int z = 0; z < 2; ++z) {
            const float ex = __uint_as_float(t[2][2 * z]);
            const float gg = rbf(quad_sum(__uint_as_float(t[0][2 * z]) + (e4 == 0 ? ex : 0.f)));
            const float uu = rbf(quad_sum(__uint_as_float(t[1][2 * z]) + (e4 == 1 ? ex : 0.f)));
            const float zz = 0.7978845608028654f * (gg + 0.044715f * gg * gg * gg);
            const float ge = rbf(gg * __builtin_amdgcn_rcpf(1.f + __builtin_amdgcn_exp2f(-2.8853900817779268f * zz)));
            const float hv = ge * uu;
            const unsigned d = cvtpk(hv, hv);
            ha[z][0] = d & m0; ha[z][1] = d & m1;
          }
        }
  const int oc = 4 * (lane & 1) + 2 * (lane >> 5) + ((lane >> 4) & 1);
  float xres = 0.f;
  if (reducer) xres = bflo(__builtin_amdgcn_raw_buffer_load_b16(rx, oc * 2, rb * 1280 + rc * 128 + ro * 16, 0));
  STAMP(3);
        if (w == 1) {
          wait_lds<4>(fbase + FA, 0x01010101u);
          const int so = WS_ML + (h * 2 + b) * 256;
          if (lane < 4) { const f32x2 ml = *(const f32x2*)(l_ml + 2 * lane); st_pair(rws, lane * 16, so + s * 64, ml[0], ml[1], tag); }
          asm volatile("s_waitcnt vmcnt(0)" ::: "memory");
          __builtin_amdgcn_s_sleep(MLSLEEP);
          u32x4 t;
          for (unsigned spin = 0;; ++spin) {
            MEMFENCE();
            t = ld_sc1(rws, (lane & 15) * 16, so);
            if (__all((t[1] == tag) & (t[3] == tag)) || spin > SPIN_CAP) break;
          }
          const float mv = __uint_as_float(t[0]);
          const float M = row_max(mv);
          const float f = __builtin_amdgcn_exp2f(mv - M);
          const float L = row_sum(f * __uint_as_float(t[2]));
          if ((lane >> 2) == s) { l_c[lane & 3] = f / L; MEMFENCE(); l_f[FE + (lane & 3)] = 1; }
        }
  if (PACING == 2 && w == 3 && tn8) {
    pace_round(tend - tn8, tend - tn8 + tn9, WS_TS + 256, tn10);
  }
  if (has_back) {
    const unsigned sLo = (e4 & 1) ? 0x01000c0cu : 0x0c0c0100u, sHi = (e4 & 1) ? 0x03020c0cu : 0x0c0c0302u;
    unsigned sel0 = e4 < 2 ? sLo : 0x0c0c0c0cu, sel1 = e4 < 2 ? 0x0c0c0c0cu : sLo, sel2 = e4 < 2 ? sHi : 0x0c0c0c0cu, sel3 = e4 < 2 ? 0x0c0c0c0cu : sHi;
    int op_so = WS_OP + h * 8 * 768 + t3 * 256, op_vo = (bl * 4 + e4) * 768 + 16 * g;
    int d_vo = ((48 * h + 16 * t3 + 2 * g) * 640 + 64 * cc) * 2 + 16 * j, o_vo = ((192 * h + 64 * t3 + (WOSEQ ? 1 : 8) * g) * 640 + 64 * cc) * 2 + 16 * j;
    int yvo = ((lane >> 4) & 1) * 256 + st_col / 2, yso = WS_YP + ((h * 3 + t3) * 10 + cc) * 512;
    int r_vo = (lane >> 1) * 5120 + (lane & 1) * 16 + WS_YP + rc * 512 + rb * 256 + ro * 32;
    int out_vo = (lane & 14) == 0 ? oc * 2 + rb * 1280 + rc * 128 + ro * 16 : OOB;
    unsigned tag2v = tag + 1u, tda = (unsigned)(size_t)(luint*)l_tend;
    asm volatile("" : "+v"(sel0), "+v"(sel1), "+v"(sel2), "+v"(sel3), "+s"(op_so), "+v"(op_vo), "+v"(d_vo), "+v"(o_vo), "+v"(yvo), "+s"(yso), "+v"(r_vo), "+v"(out_vo), "+v"(tag2v), "+v"(xres), "+v"(tda));
    int pass = DRYRUN ? (w + 3) >> 2 : 1;
    asm volatile("" : "+s"(pass));
#pragma nounroll
    for (; pass < 2; ++pass) {
      STAMP(4);
      const int live = pass ? -1 : 0;
      const rsrc_t rd = mk_rsrc(wd, 1536 * 1280 & live), ro_ = mk_rsrc(wo, 6144 * 1280 & live), rw2 = mk_rsrc(ws, WS_END & live), rout = mk_rsrc(out, 2560 & live);
      const unsigned want = tag & live, want2 = (tag + 1u) & live;
      unsigned fb = fbase + (pass ? FB : FF), fd = fbase + (pass ? FD : FF);
      asm volatile("" : "+v"(fb), "+v"(fd));
      wait_lds<1>(fb, 0x01010101u);
      const unsigned tfin = __builtin_amdgcn_readfirstlane(*(luint*)(size_t)tda) & live;
      delay_ticks(tn4 & live);
      u32x4 wb[10], t0, t1;
      unsigned fdv = 0;
#pragma unroll
      for (int n = 0; n < 10; ++n) {
        if (n == POLLPOS) {
          const rsrc_t rp = mk_rsrc(ws, __builtin_amdgcn_readfirstlane(fdv) == 0x01010101u ? (WS_END & live) : 0);
          t0 = ld_sc1(rp, op_vo, op_so); t1 = ld_sc1(rp, op_vo + 128, op_so);
          SB();
        }
        if (n < 2) wb[8 + n] = ld_nt(rd, d_vo + n * 1280, 0); else wb[n - 2] = ld_nt(ro_, o_vo, (n - 2) * (WOSEQ ? 10240 : 1280));
        if (n == POLLPOS - FDLOOK) { MEMFENCE(); fdv = *(luint*)(size_t)fd; }
        SB();
        if (n < 9 && BPACE) __builtin_amdgcn_s_sleep(BPACE);
        SB();
      }
      if (POLLPOS == 11) {
        if (pass) {
          wait_until(tfin ? tfin + tn12 - tn2 : tstart);
          wait_lds<1>(fd, 0x01010101u);
          delay_ticks(tn2);
        }
        t0 = ld_sc1(rw2, op_vo, op_so); t1 = ld_sc1(rw2, op_vo + 128, op_so);
        SB();
      }
      if (POLLPOS == 10) {
        const rsrc_t rp = mk_rsrc(ws, __builtin_amdgcn_readfirstlane(fdv) == 0x01010101u ? (WS_END & live) : 0);
        t0 = ld_sc1(rp, op_vo, op_so); t1 = ld_sc1(rp, op_vo + 128, op_so);
        SB();
      }
      STAMP(5);
      u32x4 bq[10];
#define PAIR(p) do { bq[2 * (p)] = u32x4{wb[2 * (p)][0], wb[2 * (p)][1], wb[2 * (p) + 1][0], wb[2 * (p) + 1][1]}; \
                     bq[2 * (p) + 1] = u32x4{wb[2 * (p)][2], wb[2 * (p)][3], wb[2 * (p) + 1][2], wb[2 * (p) + 1][3]}; } while (0)
      PAIR(4); PAIR(0); PAIR(1); PAIR(2);
      if (POLLPOS >= 10) PAIR(3);
      const u32x4 hA = {ha[0][0], ha[0][1], ha[1][0], ha[1][1]};
      f32x4 acc0 = mfma32(hA, bq[8], f32x4{0.f, 0.f, 0.f, 0.f}), acc1 = mfma32(hA, bq[9], f32x4{0.f, 0.f, 0.f, 0.f});
      asm volatile("" : "+v"(bq[0]), "+v"(bq[1]), "+v"(bq[2]), "+v"(bq[3]), "+v"(bq[4]), "+v"(bq[5]), "+v"(acc0), "+v"(acc1));
      if (POLLPOS >= 10) asm volatile("" : "+v"(bq[6]), "+v"(bq[7]));
      SB();
      unsigned np1 = 1;
      if (__builtin_expect(!__all((t0[1] == want) & (t0[3] == want) & (t1[1] == want) & (t1[3] == want)), 0)) {
        do {
          ++np1;
          wait_lds<1>(fd, 0x01010101u);
          delay_ticks(tn2);
          t0 = ld_sc1(rw2, op_vo, op_so); t1 = ld_sc1(rw2, op_vo + 128, op_so);
        } while (!__all((t0[1] == want) & (t0[3] == want) & (t1[1] == want) & (t1[3] == want)) && np1 < SPIN_CAP);
      }
      STAMPV(14, np1);
      STAMP(7);
      unsigned pk[4];
      {
        float u0, u1, u2, u3, u4, u5, u6, u7;
#define DPPADD(r, P) "v_add_f32_dpp " r ", " r ", " r " quad_perm:" P " row_mask:0xf bank_mask:0xf\n\t"
#define DPP8(P) DPPADD("%4", P) DPPADD("%5", P) DPPADD("%6", P) DPPADD("%7", P) DPPADD("%8", P) DPPADD("%9", P) DPPADD("%10", P) DPPADD("%11", P)
        asm volatile("v_lshlrev_b32 %4, 16, %12\n\tv_and_b32 %5, 0xffff0000, %12\n\tv_lshlrev_b32 %6, 16, %13\n\tv_and_b32 %7, 0xffff0000, %13\n\t"
                     "v_lshlrev_b32 %8, 16, %14\n\tv_and_b32 %9, 0xffff0000, %14\n\tv_lshlrev_b32 %10, 16, %15\n\tv_and_b32 %11, 0xffff0000, %15\n\t"
                     DPP8("[1,0,3,2]") DPP8("[2,3,0,1]")
                     "v_cvt_pk_bf16_f32 %0, %4, %5\n\tv_cvt_pk_bf16_f32 %1, %6, %7\n\tv_cvt_pk_bf16_f32 %2, %8, %9\n\tv_cvt_pk_bf16_f32 %3, %10, %11"
                     : "=&v"(pk[0]), "=&v"(pk[1]), "=&v"(pk[2]), "=&v"(pk[3]), "=&v"(u0), "=&v"(u1), "=&v"(u2), "=&v"(u3), "=&v"(u4), "=&v"(u5), "=&v"(u6), "=&v"(u7)
                     : "v"(t0[0]), "v"(t0[2]), "v"(t1[0]), "v"(t1[2]));
      }
      u32x4 aq[4];
#pragma unroll
      for (int p = 0; p < 4; ++p)
        aq[p] = u32x4{__builtin_amdgcn_perm(0u, pk[p], sel0), __builtin_amdgcn_perm(0u, pk[p], sel1), __builtin_amdgcn_perm(0u, pk[p], sel2), __builtin_amdgcn_perm(0u, pk[p], sel3)};
#pragma unroll
      for (int p = 0; p < 3; ++p) { acc0 = mfma32(aq[p], bq[2 * p], acc0); acc1 = mfma32(aq[p], bq[2 * p + 1], acc1); }
      if (POLLPOS < 10) { asm volatile("" : "+v"(aq[3]), "+v"(acc0), "+v"(acc1)); SB(); PAIR(3); }
      acc0 = mfma32(aq[3], bq[6], acc0); acc1 = mfma32(aq[3], bq[7], acc1);
      float v0, v1;
      proj_epilogue(acc0, acc1, g & 1, v0, v1);
      __builtin_amdgcn_raw_buffer_store_b64(u32x2{cvtpk(v0, v1), tag2v}, rw2, yvo, yso, AUX_SC1);
      STAMP(8);
      if (reducer) {
        if (pass) {
          const unsigned tn = (unsigned)__builtin_amdgcn_s_memrealtime();
          int wt = tfin ? (int)(tfin + tn13 - tn) : 40;
          asm("s_max_i32 %0, %0, %1\n\ts_min_i32 %0, %0, 0x64" : "+s"(wt) : "s"(tn3) : "scc");
          wait_until(tn + wt);
        }
        u32x4 t[3];
        unsigned np2 = 0;
        for (;;) {
          ++np2;
          MEMFENCE();
#pragma unroll
          for (int k = 0; k < 3; ++k) t[k] = ld_sc1(rw2, r_vo, k * 32 * 5120);
          bool ok = true;
#pragma unroll
          for (int k = 0; k < 3; ++k) ok &= (t[k][1] == want2) & (t[k][3] == want2);
          if (__all(ok) || np2 > SPIN_CAP) break;
        }
        STAMPV(15, np2);
        STAMP(9);
        const float y0 = (bflo(t[0][0]) + bflo(t[1][0])) + bflo(t[2][0]), y1 = (bfhi(t[0][0]) + bfhi(t[1][0])) + bfhi(t[2][0]);
        const float y2 = (bflo(t[0][2]) + bflo(t[1][2])) + bflo(t[2][2]), y3 = (bfhi(t[0][2]) + bfhi(t[1][2])) + bfhi(t[2][2]);
        const auto s0 = __builtin_amdgcn_permlane32_swap(__float_as_uint(y0), __float_as_uint(y2), false, false);
        const auto s1 = __builtin_amdgcn_permlane32_swap(__float_as_uint(y1), __float_as_uint(y3), false, false);
        const float z0 = __uint_as_float(s0[0]) + __uint_as_float(s0[1]), z1 = __uint_as_float(s1[0]) + __uint_as_float(s1[1]);
        const auto s2 = __builtin_amdgcn_permlane16_swap(__float_as_uint(z0), __float_as_uint(z1), false, false);
        float y = __uint_as_float(s2[0]) + __uint_as_float(s2[1]);
        y += dpp<ROR8>(y); y += dpp<ROR4>(y); y += dpp<XOR2>(y);
        __builtin_amdgcn_raw_buffer_store_b16((unsigned short)cvtpk(xres + y, 0.f), rout, out_vo, 0, AUX_OUT);
        if (bid == 0) __builtin_amdgcn_raw_buffer_store_b32(tag2v, rw2, lane == 0 ? WS_EPOCH : OOB, 0, AUX_SC1);
        STAMP(10);
        if (pass && bid == PBID) {
          if (AMP == 3) ampv = tfin - amp0 - 1500;
          AMPNOW(4, tfin + 300); AMPNOW(5, amp0 + 1800);
          if (AMP == 6) ampv = 10 * np2 + 40 * np1;
          wait_until(amp0 + 4000 + 10 * ((int)ampv < 0 ? 0 : ampv));
        }
      }
    }
  }
  STAMP_FLUSH();
}

extern "C" void kernel_launch(void* const* d_in, const int* in_sizes, int n_in, void* d_out, int out_size, void* d_ws, size_t ws_size,
                              hipStream_t stream) {
  fused_decode<<<256, 512, 0, stream>>>((const u16*)d_in[0], (const u16*)d_in[1], (const u16*)d_in[2], (const u16*)d_in[3], (const u16*)d_in[4],
                                        (const u16*)d_in[6], (const u16*)d_in[7], (const u16*)d_in[8], (u16*)d_out, (unsigned char*)d_ws
                                        );
}
```

```cpp
#define PACING 2
#define CODEPF 16896
#define PRETOUCH 250
#define SCAT 0
#define STAG 11
#define POLLPOS 8
#define FDLOOK 1
#define H1DELAY 20
#define H2DELAY 40
#define SMP0 600
#define WT0 250
#define TFIN 0
#define SMP1 480
#define WT1 150
#define SLATE 1
#define TEND0 1490
#define TEND1 20
#define BIAS1 15
#define SDELAY 135
#define BDELAY 12
#include <hip/hip_runtime.h>
#include <hip/hip_bf16.h>
#include <stdint.h>
#include <type_traits>

typedef unsigned short u16;
typedef unsigned u32x4 __attribute__((ext_vector_type(4)));
typedef unsigned u32x2 __attribute__((ext_vector_type(2)));
typedef float f32x4 __attribute__((ext_vector_type(4)));
typedef float f32x2 __attribute__((ext_vector_type(2)));
typedef short s16x4 __attribute__((ext_vector_type(4)));
typedef __bf16 bf16x8 __attribute__((ext_vector_type(8)));
typedef __bf16 bf16x2 __attribute__((ext_vector_type(2)));
typedef __amdgpu_buffer_rsrc_t rsrc_t;

#define WIN 9
#define B1_AT 48
#define CATCH 3
#define QSLEEP 16
#define MLSLEEP 32
#define BPACE 5
#define H1SLEEP 0
#define KC 10
#define FB_AT 95
#define DRYRUN 1
#define RATE16 229
#define STOUCH 0
#define AUX_OUT 0
#define KVW 8
#define KVG 384
#define KVT 12288
#define WOSEQ 0
#define TPOLL 155
#define PBID 8

constexpr int AUX_NT = 2, AUX_SC1 = 16;
constexpr int RSRC_W3 = 0x00020000;
constexpr unsigned SPIN_CAP = 1u << 18;
constexpr int OOB = 0x40000000;

constexpr int WS_EPOCH = 0;
constexpr int WS_TS = 1024;
constexpr int WS_QP = 4096;
constexpr int WS_GU = WS_QP + 32 * 5 * 2 * 192 * 8;
constexpr int WS_ML = WS_GU + 8 * 5 * 2 * 2 * 192 * 8;
constexpr int WS_OP = WS_ML + 32 * 2 * 4 * 4 * 16;
constexpr int WS_YP = WS_QP;
constexpr int WS_END = WS_OP + 32 * 2 * 4 * 96 * 8;

template <int I, int N, class F> __device__ __forceinline__ void static_for(F&& f) {
  if constexpr (I < N) { f(std::integral_constant<int, I>{}); static_for<I + 1, N>(f); }
}
__device__ __forceinline__ rsrc_t mk_rsrc(const void* p, int bytes) {
  return __builtin_amdgcn_make_buffer_rsrc(const_cast<void*>(p), 0, bytes, RSRC_W3);
}
__device__ __forceinline__ u32x4 ld_nt(rsrc_t r, int voff, int soff) { return __builtin_amdgcn_raw_buffer_load_b128(r, voff, soff, AUX_NT); }
__device__ __forceinline__ u32x4 ld_sc1(rsrc_t r, int voff, int soff) { return __builtin_amdgcn_raw_buffer_load_b128(r, voff, soff, AUX_SC1); }
__device__ __forceinline__ void st_pair(rsrc_t r, int voff, int soff, float a, float b, unsigned tag) {
  u32x4 v = {__float_as_uint(a), tag, __float_as_uint(b), tag};
  __builtin_amdgcn_raw_buffer_store_b128(v, r, voff, soff, AUX_SC1);
}
__device__ __forceinline__ float bflo(unsigned v) { return __uint_as_float(v << 16); }
__device__ __forceinline__ float bfhi(unsigned v) { return __uint_as_float(v & 0xffff0000u); }
__device__ __forceinline__ unsigned cvtpk(float lo, float hi) {
  bf16x2 r = {(__bf16)lo, (__bf16)hi};
  return __builtin_bit_cast(unsigned, r);
}
__device__ __forceinline__ float rbf(float x) { return (float)(__bf16)x; }
template <int CTRL> __device__ __forceinline__ float dpp(float x) {
  return __builtin_bit_cast(float, __builtin_amdgcn_mov_dpp(__builtin_bit_cast(int, x), CTRL, 0xf, 0xf, true));
}
constexpr int XOR1 = 0xB1, XOR2 = 0x4E, HMIR = 0x141, ROR8 = 0x128, ROR4 = 0x124;
__device__ __forceinline__ float x16_sum(float x) { auto s = __builtin_amdgcn_permlane16_swap(__float_as_uint(x), __float_as_uint(x), false, false); return __uint_as_float(s[0]) + __uint_as_float(s[1]); }
__device__ __forceinline__ float x32_sum(float x) { auto s = __builtin_amdgcn_permlane32_swap(__float_as_uint(x), __float_as_uint(x), false, false); return __uint_as_float(s[0]) + __uint_as_float(s[1]); }
__device__ __forceinline__ float x16_max(float x) { auto s = __builtin_amdgcn_permlane16_swap(__float_as_uint(x), __float_as_uint(x), false, false); return fmaxf(__uint_as_float(s[0]), __uint_as_float(s[1])); }
__device__ __forceinline__ float x32_max(float x) { auto s = __builtin_amdgcn_permlane32_swap(__float_as_uint(x), __float_as_uint(x), false, false); return fmaxf(__uint_as_float(s[0]), __uint_as_float(s[1])); }
__device__ __forceinline__ float row_sum(float x) { x += dpp<XOR1>(x); x += dpp<XOR2>(x); x += dpp<HMIR>(x); x += dpp<ROR8>(x); return x; }
__device__ __forceinline__ float row_max(float x) { x = fmaxf(x, dpp<XOR1>(x)); x = fmaxf(x, dpp<XOR2>(x)); x = fmaxf(x, dpp<HMIR>(x)); x = fmaxf(x, dpp<ROR8>(x)); return x; }
__device__ __forceinline__ float quad_sum(float x) { x += dpp<XOR1>(x); x += dpp<XOR2>(x); return x; }
__device__ __forceinline__ float dot2(unsigned a, unsigned b, float c) {
  return __builtin_amdgcn_fdot2_f32_bf16(__builtin_bit_cast(bf16x2, a), __builtin_bit_cast(bf16x2, b), c, false);
}
__device__ __forceinline__ f32x4 mfma16(unsigned a0, unsigned a1, unsigned b0, unsigned b1, f32x4 c) {
  u32x2 a = {a0, a1}, b = {b0, b1};
  return __builtin_amdgcn_mfma_f32_16x16x16bf16_1k(__builtin_bit_cast(s16x4, a), __builtin_bit_cast(s16x4, b), c, 0, 0, 0);
}
__device__ __forceinline__ f32x4 mfma32(u32x4 a, u32x4 b, f32x4 c) {
  return __builtin_amdgcn_mfma_f32_16x16x32_bf16(__builtin_bit_cast(bf16x8, a), __builtin_bit_cast(bf16x8, b), c, 0, 0, 0);
}
template <int RECIP> __device__ __forceinline__ unsigned pace_step(unsigned& tgt, unsigned tend) {
  unsigned d, now;
  asm volatile("1:\n\ts_memrealtime vcc\n\ts_waitcnt lgkmcnt(0)\n\ts_sub_u32 %0, vcc_lo, %2\n\ts_cmp_lt_i32 %0, 0\n\ts_cbranch_scc1 1b\n\t"
               "s_sub_i32 %0, %3, vcc_lo\n\ts_max_i32 %0, %0, 0\n\ts_min_i32 %0, %0, 0x7d0\n\ts_mul_i32 %0, %0, %4\n\ts_lshr_b32 %0, %0, 16\n\t"
               "s_add_u32 %2, vcc_lo, %0\n\ts_mov_b32 %1, vcc_lo"
               : "=&s"(d), "=&s"(now), "+s"(tgt) : "s"(tend), "i"(RECIP) : "vcc", "scc");
  return now;
}
__device__ __forceinline__ void wait_until(unsigned tgt) {
  asm volatile("1:\n\ts_memrealtime vcc\n\ts_waitcnt lgkmcnt(0)\n\ts_sub_u32 vcc_lo, vcc_lo, %0\n\ts_cmp_lt_i32 vcc_lo, 0\n\ts_cbranch_scc1 1b" :: "s"(tgt) : "vcc", "scc", "memory");
}
#define SB() __builtin_amdgcn_sched_barrier(0)
#define WG_BARRIER() do { asm volatile("s_waitcnt lgkmcnt(0)\n\ts_barrier" ::: "memory"); } while (0)
#define MEMFENCE() asm volatile("" ::: "memory")

#define FS(i)
#define STAMP(i)
#define STAMPV(i, v)
#define STAMP_DECL
#define STAMP_FLUSH()

__device__ __forceinline__ void delay_ticks(int n) {
  if (n > 0) wait_until((unsigned)__builtin_amdgcn_s_memrealtime() + n);
}

typedef __attribute__((address_space(3))) float lfloat;
typedef __attribute__((address_space(3))) f32x4 lf32x4;
typedef __attribute__((address_space(3))) unsigned luint;
typedef __attribute__((address_space(3))) unsigned char lbyte;
template <int SLP> __device__ __forceinline__ void wait_lds(unsigned a, unsigned want) {
  unsigned v, sv;
  asm volatile("s_branch 2f\n1:\n\ts_sleep %4\n2:\n\tds_read_b32 %0, %2\n\ts_waitcnt lgkmcnt(0)\n\tv_readfirstlane_b32 %1, %0\n\ts_cmp_lg_u32 %1, %3\n\ts_cbranch_scc1 1b"
               : "=&v"(v), "=&s"(sv) : "v"(a), "s"(want), "i"(SLP) : "scc", "memory");
}

__device__ __forceinline__ void proj_epilogue(f32x4 acc0, f32x4 acc1, bool z, float& v0, float& v1) {
  float n0[4], n1[4];
#pragma unroll
  for (int r = 0; r < 4; ++r) {
    auto s = __builtin_amdgcn_permlane32_swap(__float_as_uint(acc0[r]), __float_as_uint(acc1[r]), false, false);
    n0[r] = __uint_as_float(s[0]); n1[r] = __uint_as_float(s[1]);
  }
  const float r0 = n0[0] + dpp<ROR8>(n1[0]), r1 = n0[1] + dpp<ROR8>(n1[1]);
  const float r2 = n1[2] + dpp<ROR8>(n0[2]), r3 = n1[3] + dpp<ROR8>(n0[3]);
  v0 = z ? r2 : r0; v1 = z ? r3 : r1;
}

__global__ void __launch_bounds__(512, 2)
fused_decode(const u16* x_, const u16* kc_, const u16* vc_, const u16* wq_, const u16* wo_, const u16* wg_, const u16* wu_, const u16* wd_, u16* out_, unsigned char* ws_
             ) {
  constexpr int tn0 = TEND0, tn1 = TEND1, tn2 = H1DELAY, tn3 = H2DELAY, tn4 = BDELAY, tn5 = RATE16, tn6 = SMP0, tn7 = WT0, tn8 = SMP1, tn9 = WT1, tn10 = BIAS1, tn11 = SDELAY, tn12 = TPOLL, tn13 = TFIN;
  __shared__ __attribute__((aligned(16))) float lds[96 + 8 + 4 + 4 + 20 + 8 * 192];
  unsigned* l_q = (unsigned*)lds;
  float* l_ml = lds + 96;
  float* l_c = lds + 104;
  unsigned* l_est = (unsigned*)(lds + 108);
  unsigned char* l_f = (unsigned char*)(lds + 112);
  constexpr int FA = 0, FB = 4, FC = 8, FD = 12, FE = 16, FF = 20, FG = 24;
  unsigned* l_tend = (unsigned*)(lds + 119);
  const unsigned fbase = (unsigned)(size_t)(lbyte*)l_f;
  float* l_acc = lds + 132;

  STAMP_DECL;
  STAMP(0);
#define AMPNOW(k, base)
  typedef unsigned long long u64;
  typedef u64 u64x8 __attribute__((ext_vector_type(8)));
  typedef u64 u64x2 __attribute__((ext_vector_type(2)));
  u64x8 ka; u64x2 kb; u64 tst;
  u32x4 fw[16];
  const int bid = blockIdx.x, tid = threadIdx.x;
  {
    unsigned v1, v2;
    asm volatile("s_load_dwordx16 s[36:51], %18, 0x0\n\ts_load_dwordx4 s[52:55], %18, 0x40\n\ts_memrealtime s[56:57]\n\t"
               "v_readfirstlane_b32 s14, %20\n\t"
               "s_lshr_b32 s15, %19, 3\n\t"
               "s_cmpk_ge_u32 s14, 0xc0\n\ts_cbranch_scc1 1f\n\t"
               "s_setprio 2\n\t"
               "s_and_b32 s16, %19, 7\n\t"
               "s_lshr_b32 s17, %19, 6\n\ts_lshl2_add_u32 s17, s16, s17\n\t"
               "s_and_b32 s18, s15, 7\n\ts_lshr_b32 s19, s14, 6\n\ts_lshl1_add_u32 s18, s18, s19\n\t"
               "s_lshr_b32 s19, %19, 4\n\t"
               "v_bfe_u32 %16, %20, 3, 3\n\tv_and_b32 %17, 7, %20\n\t"
               "s_movk_i32 s13, 0x3000\n\t"
               "s_cmpk_lt_u32 s14, 0x80\n\t"
               "s_cselect_b32 s18, s18, s19\n\ts_cselect_b32 s17, s17, s16\n\ts_cselect_b32 s13, s13, 0xc00\n\t"
               "s_mul_i32 s19, s18, 11\n\ts_lshr_b32 s19, s19, 5\n\t"
               "v_mad_u32_u24 %16, %16, s13, %17\n\t"
               "s_sub_u32 s17, s17, s19\n\ts_mul_i32 s17, s17, 3\n\ts_add_u32 s17, s17, s18\n\t"
               "s_mul_i32 s19, s19, s13\n\ts_add_u32 s12, s17, s19\n\ts_lshl_b32 s12, s12, 7\n\t"
               "v_lshlrev_b32 %16, 4, %16\n\t"
               "s_mul_i32 s10, s13, 0x280\n\ts_mov_b32 s11, 0x20000\n\ts_cmp_eq_u32 s18, 15\n\ts_cselect_b32 s10, 0, s10\n\t"
               "s_bitcmp1_b32 s15, 0\n\t"
               "s_waitcnt lgkmcnt(0)\n\t"
               "s_cselect_b64 s[8:9], s[48:49], s[46:47]\n\t"
               "s_cmpk_lt_u32 s14, 0x80\n\ts_cselect_b64 s[8:9], s[42:43], s[8:9]\n\t"
               "s_and_b32 s9, s9, 0xffff\n\t"
               "buffer_load_dwordx4 %0, %16, s[8:11], s12 offen nt\n\t" "s_add_u32 s12, s12, s13\n\t"
               "buffer_load_dwordx4 %1, %16, s[8:11], s12 offen nt\n\t" "s_add_u32 s12, s12, s13\n\t"
               "buffer_load_dwordx4 %2, %16, s[8:11], s12 offen nt\n\t" "s_add_u32 s12, s12, s13\n\t"
               "buffer_load_dwordx4 %3, %16, s[8:11], s12 offen nt\n\t" "s_add_u32 s12, s12, s13\n\t"
               "buffer_load_dwordx4 %4, %16, s[8:11], s12 offen nt\n\t" "s_add_u32 s12, s12, s13\n\t"
               "buffer_load_dwordx4 %5, %16, s[8:11], s12 offen nt\n\t" "s_add_u32 s12, s12, s13\n\t"
               "buffer_load_dwordx4 %6, %16, s[8:11], s12 offen nt\n\t" "s_add_u32 s12, s12, s13\n\t"
               "buffer_load_dwordx4 %7, %16, s[8:11], s12 offen nt\n\t" "s_add_u32 s12, s12, s13\n\t"
               "buffer_load_dwordx4 %8, %16, s[8:11], s12 offen nt\n\t" "s_add_u32 s12, s12, s13\n\t"
               "buffer_load_dwordx4 %9, %16, s[8:11], s12 offen nt\n\t" "s_add_u32 s12, s12, s13\n\t"
               "buffer_load_dwordx4 %10, %16, s[8:11], s12 offen nt\n\t" "s_add_u32 s12, s12, s13\n\t"
               "buffer_load_dwordx4 %11, %16, s[8:11], s12 offen nt\n\t" "s_add_u32 s12, s12, s13\n\t" "s_barrier\n\t"
               "buffer_load_dwordx4 %12, %16, s[8:11], s12 offen nt\n\t" "s_add_u32 s12, s12, s13\n\t"
               "buffer_load_dwordx4 %13, %16, s[8:11], s12 offen nt\n\t" "s_add_u32 s12, s12, s13\n\t"
               "buffer_load_dwordx4 %14, %16, s[8:11], s12 offen nt\n\t" "s_add_u32 s12, s12, s13\n\t"
               "buffer_load_dwordx4 %15, %16, s[8:11], s12 offen nt\n\t"
               "s_branch 2f\n"
               "1:\n\t"
               "v_and_b32 %16, 15, %20\n\tv_lshl_add_u32 %16, %16, 2, %21\n\tv_mov_b32 %17, 0\n\tds_write_b32 %16, %17\n\t"
               "s_waitcnt lgkmcnt(0)\n\ts_barrier\n"
               "2:"
               : "=&v"(fw[0]), "=&v"(fw[1]), "=&v"(fw[2]), "=&v"(fw[3]), "=&v"(fw[4]), "=&v"(fw[5]), "=&v"(fw[6]), "=&v"(fw[7]),
                 "=&v"(fw[8]), "=&v"(fw[9]), "=&v"(fw[10]), "=&v"(fw[11]), "=&v"(fw[12]), "=&v"(fw[13]), "=&v"(fw[14]), "=&v"(fw[15]),
                 "=&v"(v1), "=&v"(v2)
               : "s"(__builtin_amdgcn_kernarg_segment_ptr()), "s"(bid), "v"(tid), "s"(fbase - 16)
               : "s8", "s9", "s10", "s11", "s12", "s13", "s14", "s15", "s16", "s17", "s18", "s19", "scc", "memory",
                 "s36", "s37", "s38", "s39", "s40", "s41", "s42", "s43", "s44", "s45", "s46", "s47", "s48", "s49", "s50", "s51", "s52", "s53", "s54", "s55", "s56", "s57");
    asm volatile("" : "={s[36:51]}"(ka), "={s[52:55]}"(kb), "={s[56:57]}"(tst));
  }
  const int xq = bid & 7, uq = bid >> 3;
  const int h = xq * 4 + (uq >> 3), j8 = uq & 7, b = j8 >> 2, s = j8 & 3;
  const int lane = tid & 63, wave = __builtin_amdgcn_readfirstlane(tid >> 6);
  const int g = lane >> 3, j = lane & 7;
  const int bl = (lane >> 2) & 1, e4 = lane & 3;
  const bool isS = wave >= 4;
  const u16 *x = (const u16*)ka[0], *kc = (const u16*)ka[1], *vc = (const u16*)ka[2], *wo = (const u16*)ka[4], *wd = (const u16*)ka[7];
  u16* out = (u16*)kb[0];
  unsigned char* ws = (unsigned char*)kb[1];
  const unsigned tstart = (unsigned)tst;
  unsigned tag, ep;
  unsigned cp0, cp1;
  {
    u64 pc;
    asm volatile("s_getpc_b64 %0" : "=s"(pc));
    unsigned pco = (((wave + 5) & 7) * 32 + uq) * 128;
    pco = pco < CODEPF - 1024 ? pco : 0;
    asm volatile("s_load_dword %0, %3, 0x0\n\ts_load_dword %1, %4, %5\n\ts_load_dword %2, %4, %6" : "=&s"(ep), "=&s"(cp0), "=&s"(cp1) : "s"(ws), "s"(pc & ~127ull), "s"(pco), "s"(pco + 64));
  }
#define TAG_READY() asm volatile("s_waitcnt lgkmcnt(0)\n\ts_add_u32 %0, %1, 1" : "=s"(tag) : "s"(ep), "s"(cp0), "s"(cp1) : "scc")
  FS(1);
  const rsrc_t rws = mk_rsrc(ws, WS_END);
  const unsigned kv0 = ((b * 32 + h) * 8192 + 6144 + 512 * s) * 384u;

  if (isS) {
    const int sw = wave - 4;
    const rsrc_t r0 = mk_rsrc(kc, 2 * 32 * 8192 * 384), rv = mk_rsrc(vc, 2 * 32 * 8192 * 384);
    const int voff = kv0 + KVW * sw * 384 + g * KVG + 16 * j;
    u32x4 buf[96];
    u32x4 qv[3];
    float sc[16];
    unsigned pd[16];
    f32x4 acc[3] = {{0.f, 0.f, 0.f, 0.f}, {0.f, 0.f, 0.f, 0.f}, {0.f, 0.f, 0.f, 0.f}};
    unsigned msk[4];
#pragma unroll
    for (int d = 0; d < 4; ++d) msk[d] = (j >> 1) == d ? ((j & 1) ? 0xffff0000u : 0x0000ffffu) : 0u;
    constexpr float C2 = 0.07216878364870322f * 1.4426950408889634f;
    unsigned tend = 0, tgt = 0, tendv = 0;
    if (SLATE) { unsigned kt; asm volatile("s_load_dword %0, %1, %2\n\ts_waitcnt lgkmcnt(0)" : "=&s"(kt) : "s"(kc), "s"(kv0)); }
    const int stag = ((2 * sw - 3) * STAG) >> 1;
    if (PACING) { tgt = SLATE ? tstart + tn11 + stag : tstart; tend = tstart + tn0 + stag; }

    auto consume = [&](auto G) {
      constexpr int t = decltype(G)::value;
      if constexpr (t < 16) {
        float a0 = 0.f, a1 = 0.f, a2 = 0.f;
#pragma unroll
        for (int e = 0; e < 4; ++e) { a0 = dot2(buf[3 * t][e], qv[0][e], a0); a1 = dot2(buf[3 * t + 1][e], qv[1][e], a1); a2 = dot2(buf[3 * t + 2][e], qv[2][e], a2); }
        float a = (a0 + a1) + a2;
        a += dpp<XOR1>(a); a += dpp<XOR2>(a); a += dpp<HMIR>(a);
        sc[t] = a;
        if constexpr (t == 15) {
          float m = sc[0];
#pragma unroll
          for (int u = 1; u < 16; ++u) m = fmaxf(m, sc[u]);
          m = fmaxf(m, dpp<ROR8>(m)); m = x16_max(m); m = x32_max(m);
          const float mc = m * C2;
          float l = 0.f;
#pragma unroll
          for (int u = 0; u < 16; ++u) { const float p = __builtin_amdgcn_exp2f(fmaf(sc[u], C2, -mc)); pd[u] = cvtpk(p, p); l += bfhi(pd[u]); }
          l += dpp<ROR8>(l); l = x16_sum(l); l = x32_sum(l);
          if (lane == 0) { *(f32x2*)(l_ml + 2 * sw) = f32x2{mc, l}; MEMFENCE(); l_f[FA + sw] = 1; }
          STAMP(3);
        }
      } else {
        constexpr int u = t - 16;
        const u32x4 a = {pd[u] & msk[0], pd[u] & msk[1], pd[u] & msk[2], pd[u] & msk[3]};
#pragma unroll
        for (int i = 0; i < 3; ++i) acc[i] = mfma32(a, buf[3 * t + i], acc[i]);
      }
    };
    constexpr auto done_after = [](int n) constexpr {
      if (n < B1_AT) return 0;
      const int landed = (n - WIN + 1) / 3;
      const int cu = 1 + (CATCH * (n - B1_AT)) / 3;
      return landed < cu ? landed : cu;
    };
    static_for<(SLATE ? 0 : 9), 96>([&](auto N) {
      constexpr int n = decltype(N)::value;
      if constexpr (n == B1_AT) {
        STAMP(1);
        wait_lds<0>(fbase + FG, 0x00010101u);
        STAMP(2);
#pragma unroll
        for (int i = 0; i < 3; ++i) qv[i] = *(const u32x4*)(l_q + 32 * i + 4 * j);
      }
      if constexpr (n >= WIN) asm volatile("s_waitcnt vmcnt(%0)" ::"i"(WIN - 1) : "memory");
      if constexpr (n == 9) TAG_READY();
      if constexpr (n >= 9 && n < 9 + 45 && (n - 9) % 5 == 0) FS(2 + (n - 9) / 5);
      if constexpr (PACING && n % 3 == 0) {
        constexpr int k = n / 3;
        if constexpr (PACING == 2 && k > 12) { const unsigned tv = __builtin_amdgcn_readfirstlane(tendv); tend = tv ? tv + stag : tend; }
        const unsigned now = pace_step<(k < 31 ? 65536 / (31 - k) : 0)>(tgt, tend);
        if constexpr (PACING == 2 && k >= 6 && k < 26) {
          unsigned vm;
          asm volatile("s_getreg_b32 %0, hwreg(HW_REG_IB_STS, 0, 4)" : "=s"(vm));
          l_est[sw] = (now + (((vm + 3 * (31 - k)) * tn5) >> 4)) | 1u;
        }
        if constexpr (PACING == 2 && k >= 12 && k < 30) tendv = *l_tend;
      }
      if constexpr (n == FB_AT) { if (lane == 0) l_f[FB + sw] = 1; }
      constexpr int m = n < 48 ? n : n - 48;
      buf[n] = ld_nt(n < 48 ? r0 : rv, voff + (m % 3) * 128, (m / 3) * KVT);
      SB();
      static_for<done_after(n > 0 ? n - 1 : 0), done_after(n)>(consume);
      SB();
    });
    STAMP(4);
    __builtin_amdgcn_s_setprio(3);
    wait_lds<0>(fbase + FE, 0x01010101u);
    float cq = l_c[lane >> 4];
    unsigned dst = (unsigned)(size_t)(lfloat*)(l_acc + (sw * 2 + (g & 1)) * 192 + 8 * j + 4 * ((lane >> 4) & 1));
    unsigned src = (unsigned)(size_t)(lfloat*)(l_acc + (lane >> 4) * 384 + 64 * sw + 8 * j + 4 * (g & 1));
    unsigned fla = fbase + FC + sw, one = 1u, flr = fbase + FC;
    int pvo = (lane & 16) ? OOB : (g & 1) * 128 + j * 16 + (lane >> 5) * 8;
    int pso = WS_OP + ((h * 2 + b) * 4 + s) * 768 + sw * 256;
    unsigned tagv = tag;
    asm volatile("" : "+v"(cq), "+v"(dst), "+v"(src), "+v"(fla), "+v"(flr), "+v"(one), "+v"(pvo), "+s"(pso), "+v"(tagv));
    const bool wr = (lane >> 5) == (g & 1);
    SB();
    static_for<done_after(95), 32>([&](auto G) { consume(G); SB(); });
    if (wr) {
#pragma unroll
      for (int i = 0; i < 3; ++i) *(lf32x4*)(size_t)(dst + 256 * i) = acc[i];
      MEMFENCE();
      *(lbyte*)(size_t)fla = (unsigned char)one;
    }
    STAMP(5);
    if (sw < 3) {
      f32x4 u0, u1;
      unsigned fv, fs;
      asm volatile("1:\n\tds_read_b32 %0, %4\n\tds_read_b128 %2, %5\n\tds_read_b128 %3, %5 offset:768\n\ts_waitcnt lgkmcnt(2)\n\tv_readfirstlane_b32 %1, %0\n\t"
                   "s_cmp_lg_u32 %1, 0x01010101\n\ts_waitcnt lgkmcnt(0)\n\ts_cbranch_scc1 1b"
                   : "=&v"(fv), "=&s"(fs), "=&v"(u0), "=&v"(u1) : "v"(flr), "v"(src) : "scc", "memory");
      float o[4];
#pragma unroll
      for (int e = 0; e < 4; ++e) o[e] = cq * (u0[e] + u1[e]);
      const auto s0 = __builtin_amdgcn_permlane32_swap(__float_as_uint(o[0]), __float_as_uint(o[2]), false, false);
      const auto s1 = __builtin_amdgcn_permlane32_swap(__float_as_uint(o[1]), __float_as_uint(o[3]), false, false);
      const float p0 = x16_sum(__uint_as_float(s0[0]) + __uint_as_float(s0[1])), p1 = x16_sum(__uint_as_float(s1[0]) + __uint_as_float(s1[1]));
      const u32x2 pv = {cvtpk(p0, p1), tagv};
      __builtin_amdgcn_raw_buffer_store_b64(pv, rws, pvo, pso, AUX_SC1);
    }
    MEMFENCE();
    *(lbyte*)(size_t)(fla + (FD - FC)) = (unsigned char)one;
    STAMP(6);
    STAMP_FLUSH();
    return;
  }

  __builtin_amdgcn_s_setprio(2);
  const int w = wave;
  const unsigned idx = w < 2 ? 2 * j8 + w : uq >> 1, ur = (idx * 11) >> 5, uc = idx - 3 * ur, up = uq & 1;
  const bool has_front = (w < 3) & (idx < 15);
  const unsigned m0 = e4 == 0 ? 0x0000ffffu : e4 == 1 ? 0xffff0000u : 0u;
  const unsigned m1 = e4 == 2 ? 0x0000ffffu : e4 == 3 ? 0xffff0000u : 0u;
  const int st_col = (8 * j + 4 * (lane >> 5) + 2 * (g & 1)) * 8;
  const rsrc_t rx = mk_rsrc(x, 2560);
  if (has_front) {
    const int xo = (lane >> 5) * 1280 + (lane & 31) * 16;
    const bool x3 = (lane & 16) == 0;
    const u32x4 xa = __builtin_amdgcn_raw_buffer_load_b128(rx, xo, 0, 0), xb = __builtin_amdgcn_raw_buffer_load_b128(rx, xo + 512, 0, 0);
    const u32x4 xc = __builtin_amdgcn_raw_buffer_load_b128(rx, x3 ? xo + 1024 : OOB, 0, 0);
    const u32x4 xr0 = __builtin_amdgcn_raw_buffer_load_b128(rx, bl * 1280 + g * 32, 256 * ur, 0);
    const u32x4 xr1 = __builtin_amdgcn_raw_buffer_load_b128(rx, bl * 1280 + g * 32 + 16, 256 * ur, 0);
    SB();
    FS(2);
    TAG_READY();
    float v[24];
#pragma unroll
    for (int e = 0; e < 4; ++e) { v[2 * e] = bflo(xa[e]); v[2 * e + 1] = bfhi(xa[e]); v[8 + 2 * e] = bflo(xb[e]); v[9 + 2 * e] = bfhi(xb[e]); v[16 + 2 * e] = bflo(xc[e]); v[17 + 2 * e] = bfhi(xc[e]); }
    float sum = 0.f;
#pragma unroll
    for (int e = 0; e < 24; ++e) sum += v[e];
    sum = x16_sum(row_sum(sum));
    const float mean = sum * (1.f / 640.f);
    float sq = 0.f, sq3 = 0.f;
#pragma unroll
    for (int e = 0; e < 16; ++e) { const float d = v[e] - mean; sq = fmaf(d, d, sq); }
#pragma unroll
    for (int e = 16; e < 24; ++e) { const float d = v[e] - mean; sq3 = fmaf(d, d, sq3); }
    sq = x16_sum(row_sum(sq + (x3 ? sq3 : 0.f)));
    const float mb = rbf(mean), den = rbf(__builtin_amdgcn_sqrtf(rbf(rbf(sq * (1.f / 640.f)) + 1e-5f)));
    const float inv = __builtin_amdgcn_rcpf(den);
    const float mb0 = __builtin_bit_cast(float, __builtin_amdgcn_readlane(__builtin_bit_cast(int, mb), 0));
    const float mb1 = __builtin_bit_cast(float, __builtin_amdgcn_readlane(__builtin_bit_cast(int, mb), 32));
    const float iv0 = __builtin_bit_cast(float, __builtin_amdgcn_readlane(__builtin_bit_cast(int, inv), 0));
    const float iv1 = __builtin_bit_cast(float, __builtin_amdgcn_readlane(__builtin_bit_cast(int, inv), 32));
    const float mm = bl ? mb1 : mb0, ii = bl ? iv1 : iv0;
    FS(3);
    asm volatile("s_waitcnt vmcnt(0)" : "+v"(fw[0]), "+v"(fw[1]), "+v"(fw[2]), "+v"(fw[3]), "+v"(fw[4]), "+v"(fw[5]), "+v"(fw[6]), "+v"(fw[7]),
                 "+v"(fw[8]), "+v"(fw[9]), "+v"(fw[10]), "+v"(fw[11]), "+v"(fw[12]), "+v"(fw[13]), "+v"(fw[14]), "+v"(fw[15]));
    f32x4 acc0 = {0.f, 0.f, 0.f, 0.f}, acc1 = {0.f, 0.f, 0.f, 0.f};
#pragma unroll
    for (int n = 0; n < 16; ++n) {
      const unsigned xw = n < 8 ? xr0[n >> 1] : xr1[(n - 8) >> 1];
      const float xv = (n & 1) ? bfhi(xw) : bflo(xw);
      const float xn = rbf(xv - mm) * ii;
      const unsigned d = cvtpk(xn, xn), a0 = d & m0, a1 = d & m1;
      acc0 = mfma16(a0, a1, fw[n][0], fw[n][1], acc0);
      acc1 = mfma16(a0, a1, fw[n][2], fw[n][3], acc1);
    }
    float v0, v1;
    proj_epilogue(acc0, acc1, g & 1, v0, v1);
    const int rec = (w < 2 ? WS_QP + (h * 5 + ur) * 3072 : WS_GU + ((xq * 5 + ur) * 2 + up) * 3072) + uc * 512;
    st_pair(rws, ((lane >> 4) & 1) * 1536 + st_col, rec, v0, v1, tag);
    STAMP(1); FS(4);
    asm volatile("s_waitcnt vmcnt(0)" ::: "memory");
    STAMP(11); FS(5);
  } else {
    asm volatile("s_waitcnt vmcnt(0)" ::"v"(fw[0]), "v"(fw[1]), "v"(fw[2]), "v"(fw[3]), "v"(fw[4]), "v"(fw[5]), "v"(fw[6]), "v"(fw[7]),
                 "v"(fw[8]), "v"(fw[9]), "v"(fw[10]), "v"(fw[11]), "v"(fw[12]), "v"(fw[13]), "v"(fw[14]), "v"(fw[15]));
    if (w == 3) {
      if (PRETOUCH) {
        wait_until(tstart + PRETOUCH);
        const unsigned kv1 = kv0 + 512 * 384 - 64, o0 = h * (192 * 1280), o1 = o0 + 192 * 1280 - 64, d0_ = h * (48 * 1280);
        unsigned t0, t1, t2, t3_, t4, t5, t6, t7, t8;
        asm volatile("s_load_dword %0, %9, %14\n\ts_load_dword %1, %10, %15\n\ts_load_dword %2, %10, %14\n\ts_load_dword %3, %11, %16\n\ts_load_dword %4, %11, %17\n\t"
                     "s_load_dword %5, %12, %18\n\ts_load_dword %6, %13, 0x0\n\ts_load_dword %7, %19, %20\n\ts_load_dword %8, %19, %21\n\ts_waitcnt lgkmcnt(0)"
                     : "=&s"(t0), "=&s"(t1), "=&s"(t2), "=&s"(t3_), "=&s"(t4), "=&s"(t5), "=&s"(t6), "=&s"(t7), "=&s"(t8)
                     : "s"(kc), "s"(vc), "s"(wo), "s"(wd), "s"(out), "s"(kv1), "s"(kv0), "s"(o0), "s"(o1), "s"(d0_), "s"(ws), "s"(WS_END), "s"(512));
      }
    }
    TAG_READY();
  }
  const int slot = w * 8 + j8;
  const bool has_back = slot < 30;
  const int t3 = slot / 10, cc = slot - 10 * t3;
  const int rb = bid / 80, rc = (bid - 80 * rb) >> 3, ro = bid & 7;
  const bool reducer = (w == 0) && (bid < 160);
  unsigned ha[2][2] = {{0u, 0u}, {0u, 0u}};
        unsigned tend = tstart + tn0;
        auto pace_round = [&](unsigned t_sample, unsigned t_read, int so, int bias) {
          wait_until(t_sample);
          u32x4 e;
          do { MEMFENCE(); e = *(const u32x4*)l_est; } while (!__builtin_amdgcn_readfirstlane(e[0] & e[1] & e[2] & e[3] & 1u));
          const u32x2 pv = {tstart + (unsigned)((int)((e[0] - tstart) + (e[1] - tstart) + (e[2] - tstart) + (e[3] - tstart)) >> 2), tag};
          __builtin_amdgcn_raw_buffer_store_b64(pv, rws, (lane == 0 && bid < 32) ? bid * 8 : OOB, so, AUX_SC1);
          wait_until(t_read);
          MEMFENCE();
          const u32x2 tv = __builtin_amdgcn_raw_buffer_load_b64(rws, (lane & 31) * 8, so, AUX_SC1);
          if (__all(tv[1] == tag)) {
            const float d = x16_sum(row_sum((float)(int)(tv[0] - tstart)));
            int di = (int)(d * (1.f / 32.f)) - bias;
            di = di < 0 ? 0 : (di > 2500 ? 2500 : di);
            tend = (tstart + __builtin_amdgcn_readfirstlane(di)) | 1u;
            *l_tend = tend;
          }
        };
        if (w < 3) {
          if (has_front) __builtin_amdgcn_s_sleep(QSLEEP); else wait_until(tstart + 420);
          u32x4 t[3];
          const int vo = (lane >> 5) * 3072 + (lane & 31) * 16, so = WS_QP + h * 5 * 3072 + b * 1536 + w * 512;
          for (unsigned spin = 0;; ++spin) {
            MEMFENCE();
            t[0] = ld_sc1(rws, vo, so); t[1] = ld_sc1(rws, vo, so + 6144); t[2] = ld_sc1(rws, lane < 32 ? vo : OOB, so + 12288);
            const bool ok = (t[0][1] == tag) & (t[0][3] == tag) & (t[1][1] == tag) & (t[1][3] == tag) & (((t[2][1] == tag) & (t[2][3] == tag)) | (lane >= 32));
            if (__all(ok) || spin > SPIN_CAP) { STAMPV(12, spin + 1); break; }
          }
          const float a = x32_sum((__uint_as_float(t[0][0]) + __uint_as_float(t[1][0])) + __uint_as_float(t[2][0]));
          const float c = x32_sum((__uint_as_float(t[0][2]) + __uint_as_float(t[1][2])) + __uint_as_float(t[2][2]));
          if (lane < 32) { l_q[32 * w + lane] = cvtpk(a, c); MEMFENCE(); l_f[FG + w] = 1; }
        } else if (PACING == 2) {
          pace_round(tstart + tn6, tstart + tn6 + tn7, WS_TS, tn1);
        } else {
          wait_until(tstart + 700);
        }
        STAMP(2); FS(6);
        if (has_back) {
          u32x4 t[3];
          const int so = WS_GU + xq * 5 * 6144 + (h & 3) * 384 + t3 * 128;
          const int vo = bl * 1536 + 16 * g;
          for (unsigned spin = 0;; ++spin) {
            MEMFENCE();
            t[0] = ld_sc1(rws, vo + e4 * 6144, so);
            t[1] = ld_sc1(rws, vo + e4 * 6144 + 3072, so);
            t[2] = ld_sc1(rws, vo + 4 * 6144 + (e4 & 1) * 3072, so);
            bool ok = true;
#pragma unroll
            for (int k = 0; k < 3; ++k) ok &= (t[k][1] == tag) & (t[k][3] == tag);
            if (__all(ok) || spin > SPIN_CAP) { STAMPV(13, spin + 1); break; }
          }
#pragma unroll
          for (int z = 0; z < 2; ++z) {
            const float ex = __uint_as_float(t[2][2 * z]);
            const float gg = rbf(quad_sum(__uint_as_float(t[0][2 * z]) + (e4 == 0 ? ex : 0.f)));
            const float uu = rbf(quad_sum(__uint_as_float(t[1][2 * z]) + (e4 == 1 ? ex : 0.f)));
            const float zz = 0.7978845608028654f * (gg + 0.044715f * gg * gg * gg);
            const float ge = rbf(gg * __builtin_amdgcn_rcpf(1.f + __builtin_amdgcn_exp2f(-2.8853900817779268f * zz)));
            const float hv = ge * uu;
            const unsigned d = cvtpk(hv, hv);
            ha[z][0] = d & m0; ha[z][1] = d & m1;
          }
        }
  const int oc = 4 * (lane & 1) + 2 * (lane >> 5) + ((lane >> 4) & 1);
  float xres = 0.f;
  if (reducer) xres = bflo(__builtin_amdgcn_raw_buffer_load_b16(rx, oc * 2, rb * 1280 + rc * 128 + ro * 16, 0));
  STAMP(3);
        if (w == 1) {
          wait_lds<4>(fbase + FA, 0x01010101u);
          const int so = WS_ML + (h * 2 + b) * 256;
          if (lane < 4) { const f32x2 ml = *(const f32x2*)(l_ml + 2 * lane); st_pair(rws, lane * 16, so + s * 64, ml[0], ml[1], tag); }
          asm volatile("s_waitcnt vmcnt(0)" ::: "memory");
          __builtin_amdgcn_s_sleep(MLSLEEP);
          u32x4 t;
          for (unsigned spin = 0;; ++spin) {
            MEMFENCE();
            t = ld_sc1(rws, (lane & 15) * 16, so);
            if (__all((t[1] == tag) & (t[3] == tag)) || spin > SPIN_CAP) break;
          }
          const float mv = __uint_as_float(t[0]);
          const float M = row_max(mv);
          const float f = __builtin_amdgcn_exp2f(mv - M);
          const float L = row_sum(f * __uint_as_float(t[2]));
          if ((lane >> 2) == s) { l_c[lane & 3] = f / L; MEMFENCE(); l_f[FE + (lane & 3)] = 1; }
        }
  if (PACING == 2 && w == 3 && tn8) {
    pace_round(tend - tn8, tend - tn8 + tn9, WS_TS + 256, tn10);
  }
  if (has_back) {
    const unsigned sLo = (e4 & 1) ? 0x01000c0cu : 0x0c0c0100u, sHi = (e4 & 1) ? 0x03020c0cu : 0x0c0c0302u;
    unsigned sel0 = e4 < 2 ? sLo : 0x0c0c0c0cu, sel1 = e4 < 2 ? 0x0c0c0c0cu : sLo, sel2 = e4 < 2 ? sHi : 0x0c0c0c0cu, sel3 = e4 < 2 ? 0x0c0c0c0cu : sHi;
    int op_so = WS_OP + h * 8 * 768 + t3 * 256, op_vo = (bl * 4 + e4) * 768 + 16 * g;
    int d_vo = ((48 * h + 16 * t3 + 2 * g) * 640 + 64 * cc) * 2 + 16 * j, o_vo = ((192 * h + 64 * t3 + (WOSEQ ? 1 : 8) * g) * 640 + 64 * cc) * 2 + 16 * j;
    int yvo = ((lane >> 4) & 1) * 256 + st_col / 2, yso = WS_YP + ((h * 3 + t3) * 10 + cc) * 512;
    int r_vo = (lane >> 1) * 5120 + (lane & 1) * 16 + WS_YP + rc * 512 + rb * 256 + ro * 32;
    int out_vo = (lane & 14) == 0 ? oc * 2 + rb * 1280 + rc * 128 + ro * 16 : OOB;
    unsigned tag2v = tag + 1u, tda = (unsigned)(size_t)(luint*)l_tend;
    asm volatile("" : "+v"(sel0), "+v"(sel1), "+v"(sel2), "+v"(sel3), "+s"(op_so), "+v"(op_vo), "+v"(d_vo), "+v"(o_vo), "+v"(yvo), "+s"(yso), "+v"(r_vo), "+v"(out_vo), "+v"(tag2v), "+v"(xres), "+v"(tda));
    int pass = DRYRUN ? (w + 3) >> 2 : 1;
    asm volatile("" : "+s"(pass));
#pragma nounroll
    for (; pass < 2; ++pass) {
      STAMP(4);
      const int live = pass ? -1 : 0;
      const rsrc_t rd = mk_rsrc(wd, 1536 * 1280 & live), ro_ = mk_rsrc(wo, 6144 * 1280 & live), rw2 = mk_rsrc(ws, WS_END & live), rout = mk_rsrc(out, 2560 & live);
      const unsigned want = tag & live, want2 = (tag + 1u) & live;
      unsigned fb = fbase + (pass ? FB : FF), fd = fbase + (pass ? FD : FF);
      asm volatile("" : "+v"(fb), "+v"(fd));
      wait_lds<1>(fb, 0x01010101u & live);
      const unsigned tfin = __builtin_amdgcn_readfirstlane(*(luint*)(size_t)tda) & live;
      delay_ticks(tn4 & live);
      u32x4 wb[10], t0, t1;
      unsigned fdv = 0;
#pragma unroll
      for (int n = 0; n < 10; ++n) {
        if (n == POLLPOS) {
          const rsrc_t rp = mk_rsrc(ws, __builtin_amdgcn_readfirstlane(fdv) == 0x01010101u ? (WS_END & live) : 0);
          t0 = ld_sc1(rp, op_vo, op_so); t1 = ld_sc1(rp, op_vo + 128, op_so);
          SB();
        }
        if (n < 2) wb[8 + n] = ld_nt(rd, d_vo + n * 1280, 0); else wb[n - 2] = ld_nt(ro_, o_vo, (n - 2) * (WOSEQ ? 10240 : 1280));
        if (n == POLLPOS - FDLOOK) { MEMFENCE(); fdv = *(luint*)(size_t)fd; }
        SB();
        if (n < 9 && BPACE) __builtin_amdgcn_s_sleep(BPACE);
        SB();
      }
      if (POLLPOS == 11) {
        if (pass) {
          wait_until(tfin ? tfin + tn12 - tn2 : tstart);
          wait_lds<1>(fd, 0x01010101u);
          delay_ticks(tn2);
        }
        t0 = ld_sc1(rw2, op_vo, op_so); t1 = ld_sc1(rw2, op_vo + 128, op_so);
        SB();
      }
      if (POLLPOS == 10) {
        const rsrc_t rp = mk_rsrc(ws, __builtin_amdgcn_readfirstlane(fdv) == 0x01010101u ? (WS_END & live) : 0);
        t0 = ld_sc1(rp, op_vo, op_so); t1 = ld_sc1(rp, op_vo + 128, op_so);
        SB();
      }
      STAMP(5);
      u32x4 bq[10];
#define PAIR(p) do { bq[2 * (p)] = u32x4{wb[2 * (p)][0], wb[2 * (p)][1], wb[2 * (p) + 1][0], wb[2 * (p) + 1][1]}; \
                     bq[2 * (p) + 1] = u32x4{wb[2 * (p)][2], wb[2 * (p)][3], wb[2 * (p) + 1][2], wb[2 * (p) + 1][3]}; } while (0)
      PAIR(4); PAIR(0); PAIR(1); PAIR(2);
      if (POLLPOS >= 10) PAIR(3);
      const u32x4 hA = {ha[0][0], ha[0][1], ha[1][0], ha[1][1]};
      f32x4 acc0 = mfma32(hA, bq[8], f32x4{0.f, 0.f, 0.f, 0.f}), acc1 = mfma32(hA, bq[9], f32x4{0.f, 0.f, 0.f, 0.f});
      asm volatile("" : "+v"(bq[0]), "+v"(bq[1]), "+v"(bq[2]), "+v"(bq[3]), "+v"(bq[4]), "+v"(bq[5]), "+v"(acc0), "+v"(acc1));
      if (POLLPOS >= 10) asm volatile("" : "+v"(bq[6]), "+v"(bq[7]));
      SB();
      unsigned np1 = 1;
      if (__builtin_expect(!__all((t0[1] == want) & (t0[3] == want) & (t1[1] == want) & (t1[3] == want)), 0)) {
        do {
          ++np1;
          wait_lds<1>(fd, 0x01010101u);
          delay_ticks(tn2);
          t0 = ld_sc1(rw2, op_vo, op_so); t1 = ld_sc1(rw2, op_vo + 128, op_so);
        } while (!__all((t0[1] == want) & (t0[3] == want) & (t1[1] == want) & (t1[3] == want)) && np1 < SPIN_CAP);
      }
      STAMPV(14, np1);
      STAMP(7);
      unsigned pk[4];
      {
        float u0, u1, u2, u3, u4, u5, u6, u7;
#define DPPADD(r, P) "v_add_f32_dpp " r ", " r ", " r " quad_perm:" P " row_mask:0xf bank_mask:0xf\n\t"
#define DPP8(P) DPPADD("%4", P) DPPADD("%5", P) DPPADD("%6", P) DPPADD("%7", P) DPPADD("%8", P) DPPADD("%9", P) DPPADD("%10", P) DPPADD("%11", P)
        asm volatile("v_lshlrev_b32 %4, 16, %12\n\tv_and_b32 %5, 0xffff0000, %12\n\tv_lshlrev_b32 %6, 16, %13\n\tv_and_b32 %7, 0xffff0000, %13\n\t"
                     "v_lshlrev_b32 %8, 16, %14\n\tv_and_b32 %9, 0xffff0000, %14\n\tv_lshlrev_b32 %10, 16, %15\n\tv_and_b32 %11, 0xffff0000, %15\n\t"
                     DPP8("[1,0,3,2]") DPP8("[2,3,0,1]")
                     "v_cvt_pk_bf16_f32 %0, %4, %5\n\tv_cvt_pk_bf16_f32 %1, %6, %7\n\tv_cvt_pk_bf16_f32 %2, %8, %9\n\tv_cvt_pk_bf16_f32 %3, %10, %11"
                     : "=&v"(pk[0]), "=&v"(pk[1]), "=&v"(pk[2]), "=&v"(pk[3]), "=&v"(u0), "=&v"(u1), "=&v"(u2), "=&v"(u3), "=&v"(u4), "=&v"(u5), "=&v"(u6), "=&v"(u7)
                     : "v"(t0[0]), "v"(t0[2]), "v"(t1[0]), "v"(t1[2]));
      }
      u32x4 aq[4];
#pragma unroll
      for (int p = 0; p < 4; ++p)
        aq[p] = u32x4{__builtin_amdgcn_perm(0u, pk[p], sel0), __builtin_amdgcn_perm(0u, pk[p], sel1), __builtin_amdgcn_perm(0u, pk[p], sel2), __builtin_amdgcn_perm(0u, pk[p], sel3)};
#pragma unroll
      for (int p = 0; p < 3; ++p) { acc0 = mfma32(aq[p], bq[2 * p], acc0); acc1 = mfma32(aq[p], bq[2 * p + 1], acc1); }
      if (POLLPOS < 10) { asm volatile("" : "+v"(aq[3]), "+v"(acc0), "+v"(acc1)); SB(); PAIR(3); }
      acc0 = mfma32(aq[3], bq[6], acc0); acc1 = mfma32(aq[3], bq[7], acc1);
      float v0, v1;
      proj_epilogue(acc0, acc1, g & 1, v0, v1);
      __builtin_amdgcn_raw_buffer_store_b64(u32x2{cvtpk(v0, v1), tag2v}, rw2, yvo, yso, AUX_SC1);
      STAMP(8);
      if (reducer) {
        if (pass) {
          const unsigned tn = (unsigned)__builtin_amdgcn_s_memrealtime();
          int wt = tfin ? (int)(tfin + tn13 - tn) : 40;
          asm("s_max_i32 %0, %0, %1\n\ts_min_i32 %0, %0, 0x64" : "+s"(wt) : "s"(tn3) : "scc");
          wait_until(tn + wt);
        }
        u32x4 t[3];
        unsigned np2 = 0;
        for (;;) {
          ++np2;
          MEMFENCE();
#pragma unroll
          for (int k = 0; k < 3; ++k) t[k] = ld_sc1(rw2, r_vo, k * 32 * 5120);
          bool ok = true;
#pragma unroll
          for (int k = 0; k < 3; ++k) ok &= (t[k][1] == want2) & (t[k][3] == want2);
          if (__all(ok) || np2 > SPIN_CAP) break;
        }
        STAMPV(15, np2);
        STAMP(9);
        const float y0 = (bflo(t[0][0]) + bflo(t[1][0])) + bflo(t[2][0]), y1 = (bfhi(t[0][0]) + bfhi(t[1][0])) + bfhi(t[2][0]);
        const float y2 = (bflo(t[0][2]) + bflo(t[1][2])) + bflo(t[2][2]), y3 = (bfhi(t[0][2]) + bfhi(t[1][2])) + bfhi(t[2][2]);
        const auto s0 = __builtin_amdgcn_permlane32_swap(__float_as_uint(y0), __float_as_uint(y2), false, false);
        const auto s1 = __builtin_amdgcn_permlane32_swap(__float_as_uint(y1), __float_as_uint(y3), false, false);
        const float z0 = __uint_as_float(s0[0]) + __uint_as_float(s0[1]), z1 = __uint_as_float(s1[0]) + __uint_as_float(s1[1]);
        const auto s2 = __builtin_amdgcn_permlane16_swap(__float_as_uint(z0), __float_as_uint(z1), false, false);
        float y = __uint_as_float(s2[0]) + __uint_as_float(s2[1]);
        y += dpp<ROR8>(y); y += dpp<ROR4>(y); y += dpp<XOR2>(y);
        __builtin_amdgcn_raw_buffer_store_b16((unsigned short)cvtpk(xres + y, 0.f), rout, out_vo, 0, AUX_OUT);
        if (bid == 0) __builtin_amdgcn_raw_buffer_store_b32(tag2v, rw2, lane == 0 ? WS_EPOCH : OOB, 0, AUX_SC1);
        STAMP(10);
      }
    }
  }
  STAMP_FLUSH();
}

extern "C" void kernel_launch(void* const* d_in, const int* in_sizes, int n_in, void* d_out, int out_size, void* d_ws, size_t ws_size,
                              hipStream_t stream) {
  fused_decode<<<256, 512, 0, stream>>>((const u16*)d_in[0], (const u16*)d_in[1], (const u16*)d_in[2], (const u16*)d_in[3], (const u16*)d_in[4],
                                        (const u16*)d_in[6], (const u16*)d_in[7], (const u16*)d_in[8], (u16*)d_out, (unsigned char*)d_ws
                                        );
}
```

```cpp
#define PACING 2
#define CODEPF 16896
#define PRETOUCH 250
#define SCAT 0
#define STAG 11
#define POLLPOS 8
#define FDLOOK 1
#define H1DELAY 20
#define H2DELAY 40
#define SMP0 600
#define WT0 250
#define TFIN 0
#define SMP1 480
#define WT1 150
#define SLATE 1
#define TEND0 1490
#define TEND1 20
#define BIAS1 15
#define SDELAY 135
#define BDELAY 12
#define AMP 4
#include <hip/hip_runtime.h>
#include <hip/hip_bf16.h>
#include <stdint.h>
#include <type_traits>

typedef unsigned short u16;
typedef unsigned u32x4 __attribute__((ext_vector_type(4)));
typedef unsigned u32x2 __attribute__((ext_vector_type(2)));
typedef float f32x4 __attribute__((ext_vector_type(4)));
typedef float f32x2 __attribute__((ext_vector_type(2)));
typedef short s16x4 __attribute__((ext_vector_type(4)));
typedef __bf16 bf16x8 __attribute__((ext_vector_type(8)));
typedef __bf16 bf16x2 __attribute__((ext_vector_type(2)));
typedef __amdgpu_buffer_rsrc_t rsrc_t;

#define WIN 9
#define B1_AT 48
#define CATCH 3
#define QSLEEP 16
#define MLSLEEP 32
#define BPACE 5
#define H1SLEEP 0
#define KC 10
#define FB_AT 95
#define DRYRUN 1
#define RATE16 229
#define STOUCH 0
#define AUX_OUT 0
#define KVW 8
#define KVG 384
#define KVT 12288
#define WOSEQ 0
#define TPOLL 155
#define PBID 8

constexpr int AUX_NT = 2, AUX_SC1 = 16;
constexpr int RSRC_W3 = 0x00020000;
constexpr unsigned SPIN_CAP = 1u << 18;
constexpr int OOB = 0x40000000;

constexpr int WS_EPOCH = 0;
constexpr int WS_TS = 1024;
constexpr int WS_QP = 4096;
constexpr int WS_GU = WS_QP + 32 * 5 * 2 * 192 * 8;
constexpr int WS_ML = WS_GU + 8 * 5 * 2 * 2 * 192 * 8;
constexpr int WS_OP = WS_ML + 32 * 2 * 4 * 4 * 16;
constexpr int WS_YP = WS_QP;
constexpr int WS_END = WS_OP + 32 * 2 * 4 * 96 * 8;

template <int I, int N, class F> __device__ __forceinline__ void static_for(F&& f) {
  if constexpr (I < N) { f(std::integral_constant<int, I>{}); static_for<I + 1, N>(f); }
}
__device__ __forceinline__ rsrc_t mk_rsrc(const void* p, int bytes) {
  return __builtin_amdgcn_make_buffer_rsrc(const_cast<void*>(p), 0, bytes, RSRC_W3);
}
__device__ __forceinline__ u32x4 ld_nt(rsrc_t r, int voff, int soff) { return __builtin_amdgcn_raw_buffer_load_b128(r, voff, soff, AUX_NT); }
__device__ __forceinline__ u32x4 ld_sc1(rsrc_t r, int voff, int soff) { return __builtin_amdgcn_raw_buffer_load_b128(r, voff, soff, AUX_SC1); }
__device__ __forceinline__ void st_pair(rsrc_t r, int voff, int soff, float a, float b, unsigned tag) {
  u32x4 v = {__float_as_uint(a), tag, __float_as_uint(b), tag};
  __builtin_amdgcn_raw_buffer_store_b128(v, r, voff, soff, AUX_SC1);
}
__device__ __forceinline__ float bflo(unsigned v) { return __uint_as_float(v << 16); }
__device__ __forceinline__ float bfhi(unsigned v) { return __uint_as_float(v & 0xffff0000u); }
__device__ __forceinline__ unsigned cvtpk(float lo, float hi) {
  bf16x2 r = {(__bf16)lo, (__bf16)hi};
  return __builtin_bit_cast(unsigned, r);
}
__device__ __forceinline__ float rbf(float x) { return (float)(__bf16)x; }
template <int CTRL> __device__ __forceinline__ float dpp(float x) {
  return __builtin_bit_cast(float, __builtin_amdgcn_mov_dpp(__builtin_bit_cast(int, x), CTRL, 0xf, 0xf, true));
}
constexpr int XOR1 = 0xB1, XOR2 = 0x4E, HMIR = 0x141, ROR8 = 0x128, ROR4 = 0x124;
__device__ __forceinline__ float x16_sum(float x) { auto s = __builtin_amdgcn_permlane16_swap(__float_as_uint(x), __float_as_uint(x), false, false); return __uint_as_float(s[0]) + __uint_as_float(s[1]); }
__device__ __forceinline__ float x32_sum(float x) { auto s = __builtin_amdgcn_permlane32_swap(__float_as_uint(x), __float_as_uint(x), false, false); return __uint_as_float(s[0]) + __uint_as_float(s[1]); }
__device__ __forceinline__ float x16_max(float x) { auto s = __builtin_amdgcn_permlane16_swap(__float_as_uint(x), __float_as_uint(x), false, false); return fmaxf(__uint_as_float(s[0]), __uint_as_float(s[1])); }
__device__ __forceinline__ float x32_max(float x) { auto s = __builtin_amdgcn_permlane32_swap(__float_as_uint(x), __float_as_uint(x), false, false); return fmaxf(__uint_as_float(s[0]), __uint_as_float(s[1])); }
__device__ __forceinline__ float row_sum(float x) { x += dpp<XOR1>(x); x += dpp<XOR2>(x); x += dpp<HMIR>(x); x += dpp<ROR8>(x); return x; }
__device__ __forceinline__ float row_max(float x) { x = fmaxf(x, dpp<XOR1>(x)); x = fmaxf(x, dpp<XOR2>(x)); x = fmaxf(x, dpp<HMIR>(x)); x = fmaxf(x, dpp<ROR8>(x)); return x; }
__device__ __forceinline__ float quad_sum(float x) { x += dpp<XOR1>(x); x += dpp<XOR2>(x); return x; }
__device__ __forceinline__ float dot2(unsigned a, unsigned b, float c) {
  return __builtin_amdgcn_fdot2_f32_bf16(__builtin_bit_cast(bf16x2, a), __builtin_bit_cast(bf16x2, b), c, false);
}
__device__ __forceinline__ f32x4 mfma16(unsigned a0, unsigned a1, unsigned b0, unsigned b1, f32x4 c) {
  u32x2 a = {a0, a1}, b = {b0, b1};
  return __builtin_amdgcn_mfma_f32_16x16x16bf16_1k(__builtin_bit_cast(s16x4, a), __builtin_bit_cast(s16x4, b), c, 0, 0, 0);
}
__device__ __forceinline__ f32x4 mfma32(u32x4 a, u32x4 b, f32x4 c) {
  return __builtin_amdgcn_mfma_f32_16x16x32_bf16(__builtin_bit_cast(bf16x8, a), __builtin_bit_cast(bf16x8, b), c, 0, 0, 0);
}
template <int RECIP> __device__ __forceinline__ unsigned pace_step(unsigned& tgt, unsigned tend) {
  unsigned d, now;
  asm volatile("1:\n\ts_memrealtime vcc\n\ts_waitcnt lgkmcnt(0)\n\ts_sub_u32 %0, vcc_lo, %2\n\ts_cmp_lt_i32 %0, 0\n\ts_cbranch_scc1 1b\n\t"
               "s_sub_i32 %0, %3, vcc_lo\n\ts_max_i32 %0, %0, 0\n\ts_min_i32 %0, %0, 0x7d0\n\ts_mul_i32 %0, %0, %4\n\ts_lshr_b32 %0, %0, 16\n\t"
               "s_add_u32 %2, vcc_lo, %0\n\ts_mov_b32 %1, vcc_lo"
               : "=&s"(d), "=&s"(now), "+s"(tgt) : "s"(tend), "i"(RECIP) : "vcc", "scc");
  return now;
}
__device__ __forceinline__ void wait_until(unsigned tgt) {
  asm volatile("1:\n\ts_memrealtime vcc\n\ts_waitcnt lgkmcnt(0)\n\ts_sub_u32 vcc_lo, vcc_lo, %0\n\ts_cmp_lt_i32 vcc_lo, 0\n\ts_cbranch_scc1 1b" :: "s"(tgt) : "vcc", "scc", "memory");
}
#define SB() __builtin_amdgcn_sched_barrier(0)
#define WG_BARRIER() do { asm volatile("s_waitcnt lgkmcnt(0)\n\ts_barrier" ::: "memory"); } while (0)
#define MEMFENCE() asm volatile("" ::: "memory")

#define FS(i)
#define STAMP(i)
#define STAMPV(i, v)
#define STAMP_DECL
#define STAMP_FLUSH()

__device__ __forceinline__ void delay_ticks(int n) {
  if (n > 0) wait_until((unsigned)__builtin_amdgcn_s_memrealtime() + n);
}

typedef __attribute__((address_space(3))) float lfloat;
typedef __attribute__((address_space(3))) f32x4 lf32x4;
typedef __attribute__((address_space(3))) unsigned luint;
typedef __attribute__((address_space(3))) unsigned char lbyte;
template <int SLP> __device__ __forceinline__ void wait_lds(unsigned a, unsigned want) {
  unsigned v, sv;
  asm volatile("s_branch 2f\n1:\n\ts_sleep %4\n2:\n\tds_read_b32 %0, %2\n\ts_waitcnt lgkmcnt(0)\n\tv_readfirstlane_b32 %1, %0\n\ts_cmp_lg_u32 %1, %3\n\ts_cbranch_scc1 1b"
               : "=&v"(v), "=&s"(sv) : "v"(a), "s"(want), "i"(SLP) : "scc", "memory");
}

__device__ __forceinline__ void proj_epilogue(f32x4 acc0, f32x4 acc1, bool z, float& v0, float& v1) {
  float n0[4], n1[4];
#pragma unroll
  for (int r = 0; r < 4; ++r) {
    auto s = __builtin_amdgcn_permlane32_swap(__float_as_uint(acc0[r]), __float_as_uint(acc1[r]), false, false);
    n0[r] = __uint_as_float(s[0]); n1[r] = __uint_as_float(s[1]);
  }
  const float r0 = n0[0] + dpp<ROR8>(n1[0]), r1 = n0[1] + dpp<ROR8>(n1[1]);
  const float r2 = n1[2] + dpp<ROR8>(n0[2]), r3 = n1[3] + dpp<ROR8>(n0[3]);
  v0 = z ? r2 : r0; v1 = z ? r3 : r1;
}

__global__ void __launch_bounds__(512, 2)
fused_decode(const u16* x_, const u16* kc_, const u16* vc_, const u16* wq_, const u16* wo_, const u16* wg_, const u16* wu_, const u16* wd_, u16* out_, unsigned char* ws_
             ) {
  constexpr int tn0 = TEND0, tn1 = TEND1, tn2 = H1DELAY, tn3 = H2DELAY, tn4 = BDELAY, tn5 = RATE16, tn6 = SMP0, tn7 = WT0, tn8 = SMP1, tn9 = WT1, tn10 = BIAS1, tn11 = SDELAY, tn12 = TPOLL, tn13 = TFIN;
  __shared__ __attribute__((aligned(16))) float lds[96 + 8 + 4 + 4 + 20 + 8 * 192];
  unsigned* l_q = (unsigned*)lds;
  float* l_ml = lds + 96;
  float* l_c = lds + 104;
  unsigned* l_est = (unsigned*)(lds + 108);
  unsigned char* l_f = (unsigned char*)(lds + 112);
  constexpr int FA = 0, FB = 4, FC = 8, FD = 12, FE = 16, FF = 20, FG = 24;
  unsigned* l_tend = (unsigned*)(lds + 119);
  const unsigned fbase = (unsigned)(size_t)(lbyte*)l_f;
  float* l_acc = lds + 132;

  STAMP_DECL;
  STAMP(0);
  unsigned amp0, ampv = 0;
  { unsigned long long t_; asm volatile("s_memrealtime %0\n\ts_waitcnt lgkmcnt(0)" : "=s"(t_) :: "memory"); amp0 = (unsigned)t_; }
#define AMPNOW(k, base) do { if (AMP == (k)) { SB(); unsigned long long t_; asm volatile("s_memrealtime %0\n\ts_waitcnt lgkmcnt(0)" : "=s"(t_) :: "memory"); ampv = (unsigned)t_ - (base); SB(); } } while (0)
  typedef unsigned long long u64;
  typedef u64 u64x8 __attribute__((ext_vector_type(8)));
  typedef u64 u64x2 __attribute__((ext_vector_type(2)));
  u64x8 ka; u64x2 kb; u64 tst;
  u32x4 fw[16];
  const int bid = blockIdx.x, tid = threadIdx.x;
  {
    unsigned v1, v2;
    asm volatile("s_load_dwordx16 s[36:51], %18, 0x0\n\ts_load_dwordx4 s[52:55], %18, 0x40\n\ts_memrealtime s[56:57]\n\t"
               "v_readfirstlane_b32 s14, %20\n\t"
               "s_lshr_b32 s15, %19, 3\n\t"
               "s_cmpk_ge_u32 s14, 0xc0\n\ts_cbranch_scc1 1f\n\t"
               "s_setprio 2\n\t"
               "s_and_b32 s16, %19, 7\n\t"
               "s_lshr_b32 s17, %19, 6\n\ts_lshl2_add_u32 s17, s16, s17\n\t"
               "s_and_b32 s18, s15, 7\n\ts_lshr_b32 s19, s14, 6\n\ts_lshl1_add_u32 s18, s18, s19\n\t"
               "s_lshr_b32 s19, %19, 4\n\t"
               "v_bfe_u32 %16, %20, 3, 3\n\tv_and_b32 %17, 7, %20\n\t"
               "s_movk_i32 s13, 0x3000\n\t"
               "s_cmpk_lt_u32 s14, 0x80\n\t"
               "s_cselect_b32 s18, s18, s19\n\ts_cselect_b32 s17, s17, s16\n\ts_cselect_b32 s13, s13, 0xc00\n\t"
               "s_mul_i32 s19, s18, 11\n\ts_lshr_b32 s19, s19, 5\n\t"
               "v_mad_u32_u24 %16, %16, s13, %17\n\t"
               "s_sub_u32 s17, s17, s19\n\ts_mul_i32 s17, s17, 3\n\ts_add_u32 s17, s17, s18\n\t"
               "s_mul_i32 s19, s19, s13\n\ts_add_u32 s12, s17, s19\n\ts_lshl_b32 s12, s12, 7\n\t"
               "v_lshlrev_b32 %16, 4, %16\n\t"
               "s_mul_i32 s10, s13, 0x280\n\ts_mov_b32 s11, 0x20000\n\ts_cmp_eq_u32 s18, 15\n\ts_cselect_b32 s10, 0, s10\n\t"
               "s_bitcmp1_b32 s15, 0\n\t"
               "s_waitcnt lgkmcnt(0)\n\t"
               "s_cselect_b64 s[8:9], s[48:49], s[46:47]\n\t"
               "s_cmpk_lt_u32 s14, 0x80\n\ts_cselect_b64 s[8:9], s[42:43], s[8:9]\n\t"
               "s_and_b32 s9, s9, 0xffff\n\t"
               "buffer_load_dwordx4 %0, %16, s[8:11], s12 offen nt\n\t" "s_add_u32 s12, s12, s13\n\t"
               "buffer_load_dwordx4 %1, %16, s[8:11], s12 offen nt\n\t" "s_add_u32 s12, s12, s13\n\t"
               "buffer_load_dwordx4 %2, %16, s[8:11], s12 offen nt\n\t" "s_add_u32 s12, s12, s13\n\t"
               "buffer_load_dwordx4 %3, %16, s[8:11], s12 offen nt\n\t" "s_add_u32 s12, s12, s13\n\t"
               "buffer_load_dwordx4 %4, %16, s[8:11], s12 offen nt\n\t" "s_add_u32 s12, s12, s13\n\t"
               "buffer_load_dwordx4 %5, %16, s[8:11], s12 offen nt\n\t" "s_add_u32 s12, s12, s13\n\t"
               "buffer_load_dwordx4 %6, %16, s[8:11], s12 offen nt\n\t" "s_add_u32 s12, s12, s13\n\t"
               "buffer_load_dwordx4 %7, %16, s[8:11], s12 offen nt\n\t" "s_add_u32 s12, s12, s13\n\t"
               "buffer_load_dwordx4 %8, %16, s[8:11], s12 offen nt\n\t" "s_add_u32 s12, s12, s13\n\t"
               "buffer_load_dwordx4 %9, %16, s[8:11], s12 offen nt\n\t" "s_add_u32 s12, s12, s13\n\t"
               "buffer_load_dwordx4 %10, %16, s[8:11], s12 offen nt\n\t" "s_add_u32 s12, s12, s13\n\t"
               "buffer_load_dwordx4 %11, %16, s[8:11], s12 offen nt\n\t" "s_add_u32 s12, s12, s13\n\t" "s_barrier\n\t"
               "buffer_load_dwordx4 %12, %16, s[8:11], s12 offen nt\n\t" "s_add_u32 s12, s12, s13\n\t"
               "buffer_load_dwordx4 %13, %16, s[8:11], s12 offen nt\n\t" "s_add_u32 s12, s12, s13\n\t"
               "buffer_load_dwordx4 %14, %16, s[8:11], s12 offen nt\n\t" "s_add_u32 s12, s12, s13\n\t"
               "buffer_load_dwordx4 %15, %16, s[8:11], s12 offen nt\n\t"
               "s_branch 2f\n"
               "1:\n\t"
               "v_and_b32 %16, 15, %20\n\tv_lshl_add_u32 %16, %16, 2, %21\n\tv_mov_b32 %17, 0\n\tds_write_b32 %16, %17\n\t"
               "s_waitcnt lgkmcnt(0)\n\ts_barrier\n"
               "2:"
               : "=&v"(fw[0]), "=&v"(fw[1]), "=&v"(fw[2]), "=&v"(fw[3]), "=&v"(fw[4]), "=&v"(fw[5]), "=&v"(fw[6]), "=&v"(fw[7]),
                 "=&v"(fw[8]), "=&v"(fw[9]), "=&v"(fw[10]), "=&v"(fw[11]), "=&v"(fw[12]), "=&v"(fw[13]), "=&v"(fw[14]), "=&v"(fw[15]),
                 "=&v"(v1), "=&v"(v2)
               : "s"(__builtin_amdgcn_kernarg_segment_ptr()), "s"(bid), "v"(tid), "s"(fbase - 16)
               : "s8", "s9", "s10", "s11", "s12", "s13", "s14", "s15", "s16", "s17", "s18", "s19", "scc", "memory",
                 "s36", "s37", "s38", "s39", "s40", "s41", "s42", "s43", "s44", "s45", "s46", "s47", "s48", "s49", "s50", "s51", "s52", "s53", "s54", "s55", "s56", "s57");
    asm volatile("" : "={s[36:51]}"(ka), "={s[52:55]}"(kb), "={s[56:57]}"(tst));
  }
  const int xq = bid & 7, uq = bid >> 3;
  const int h = xq * 4 + (uq >> 3), j8 = uq & 7, b = j8 >> 2, s = j8 & 3;
  const int lane = tid & 63, wave = __builtin_amdgcn_readfirstlane(tid >> 6);
  const int g = lane >> 3, j = lane & 7;
  const int bl = (lane >> 2) & 1, e4 = lane & 3;
  const bool isS = wave >= 4;
  const u16 *x = (const u16*)ka[0], *kc = (const u16*)ka[1], *vc = (const u16*)ka[2], *wo = (const u16*)ka[4], *wd = (const u16*)ka[7];
  u16* out = (u16*)kb[0];
  unsigned char* ws = (unsigned char*)kb[1];
  const unsigned tstart = (unsigned)tst;
  unsigned tag, ep;
  unsigned cp0, cp1;
  {
    u64 pc;
    asm volatile("s_getpc_b64 %0" : "=s"(pc));
    unsigned pco = (((wave + 5) & 7) * 32 + uq) * 128;
    pco = pco < CODEPF - 1024 ? pco : 0;
    asm volatile("s_load_dword %0, %3, 0x0\n\ts_load_dword %1, %4, %5\n\ts_load_dword %2, %4, %6" : "=&s"(ep), "=&s"(cp0), "=&s"(cp1) : "s"(ws), "s"(pc & ~127ull), "s"(pco), "s"(pco + 64));
  }
#define TAG_READY() asm volatile("s_waitcnt lgkmcnt(0)\n\ts_add_u32 %0, %1, 1" : "=s"(tag) : "s"(ep), "s"(cp0), "s"(cp1) : "scc")
  const unsigned ampx = ampv;
  FS(1);
  const rsrc_t rws = mk_rsrc(ws, WS_END);
  const unsigned kv0 = ((b * 32 + h) * 8192 + 6144 + 512 * s) * 384u;

  if (isS) {
    const int sw = wave - 4;
    const rsrc_t r0 = mk_rsrc(kc, 2 * 32 * 8192 * 384), rv = mk_rsrc(vc, 2 * 32 * 8192 * 384);
    const int voff = kv0 + KVW * sw * 384 + g * KVG + 16 * j;
    u32x4 buf[96];
    u32x4 qv[3];
    float sc[16];
    unsigned pd[16];
    f32x4 acc[3] = {{0.f, 0.f, 0.f, 0.f}, {0.f, 0.f, 0.f, 0.f}, {0.f, 0.f, 0.f, 0.f}};
    unsigned msk[4];
#pragma unroll
    for (int d = 0; d < 4; ++d) msk[d] = (j >> 1) == d ? ((j & 1) ? 0xffff0000u : 0x0000ffffu) : 0u;
    constexpr float C2 = 0.07216878364870322f * 1.4426950408889634f;
    unsigned tend = 0, tgt = 0, tendv = 0;
    if (SLATE) { unsigned kt; asm volatile("s_load_dword %0, %1, %2\n\ts_waitcnt lgkmcnt(0)" : "=&s"(kt) : "s"(kc), "s"(kv0)); }
    const int stag = ((2 * sw - 3) * STAG) >> 1;
    if (PACING) { tgt = SLATE ? tstart + tn11 + stag : tstart; tend = tstart + tn0 + stag; }

    auto consume = [&](auto G) {
      constexpr int t = decltype(G)::value;
      if constexpr (t < 16) {
        float a0 = 0.f, a1 = 0.f, a2 = 0.f;
#pragma unroll
        for (int e = 0; e < 4; ++e) { a0 = dot2(buf[3 * t][e], qv[0][e], a0); a1 = dot2(buf[3 * t + 1][e], qv[1][e], a1); a2 = dot2(buf[3 * t + 2][e], qv[2][e], a2); }
        float a = (a0 + a1) + a2;
        a += dpp<XOR1>(a); a += dpp<XOR2>(a); a += dpp<HMIR>(a);
        sc[t] = a;
        if constexpr (t == 15) {
          float m = sc[0];
#pragma unroll
          for (int u = 1; u < 16; ++u) m = fmaxf(m, sc[u]);
          m = fmaxf(m, dpp<ROR8>(m)); m = x16_max(m); m = x32_max(m);
          const float mc = m * C2;
          float l = 0.f;
#pragma unroll
          for (int u = 0; u < 16; ++u) { const float p = __builtin_amdgcn_exp2f(fmaf(sc[u], C2, -mc)); pd[u] = cvtpk(p, p); l += bfhi(pd[u]); }
          l += dpp<ROR8>(l); l = x16_sum(l); l = x32_sum(l);
          if (lane == 0) { *(f32x2*)(l_ml + 2 * sw) = f32x2{mc, l}; MEMFENCE(); l_f[FA + sw] = 1; }
          STAMP(3);
        }
      } else {
        constexpr int u = t - 16;
        const u32x4 a = {pd[u] & msk[0], pd[u] & msk[1], pd[u] & msk[2], pd[u] & msk[3]};
#pragma unroll
        for (int i = 0; i < 3; ++i) acc[i] = mfma32(a, buf[3 * t + i], acc[i]);
      }
    };
    constexpr auto done_after = [](int n) constexpr {
      if (n < B1_AT) return 0;
      const int landed = (n - WIN + 1) / 3;
      const int cu = 1 + (CATCH * (n - B1_AT)) / 3;
      return landed < cu ? landed : cu;
    };
    static_for<(SLATE ? 0 : 9), 96>([&](auto N) {
      constexpr int n = decltype(N)::value;
      if constexpr (n == B1_AT) {
        STAMP(1);
        wait_lds<0>(fbase + FG, 0x00010101u);
        STAMP(2);
#pragma unroll
        for (int i = 0; i < 3; ++i) qv[i] = *(const u32x4*)(l_q + 32 * i + 4 * j);
      }
      if constexpr (n >= WIN) asm volatile("s_waitcnt vmcnt(%0)" ::"i"(WIN - 1) : "memory");
      if constexpr (n == 9) TAG_READY();
      if constexpr (n >= 9 && n < 9 + 45 && (n - 9) % 5 == 0) FS(2 + (n - 9) / 5);
      if constexpr (PACING && n % 3 == 0) {
        constexpr int k = n / 3;
        if constexpr (PACING == 2 && k > 12) { const unsigned tv = __builtin_amdgcn_readfirstlane(tendv); tend = tv ? tv + stag : tend; }
        const unsigned now = pace_step<(k < 31 ? 65536 / (31 - k) : 0)>(tgt, tend);
        if constexpr (PACING == 2 && k >= 6 && k < 26) {
          unsigned vm;
          asm volatile("s_getreg_b32 %0, hwreg(HW_REG_IB_STS, 0, 4)" : "=s"(vm));
          l_est[sw] = (now + (((vm + 3 * (31 - k)) * tn5) >> 4)) | 1u;
        }
        if constexpr (PACING == 2 && k >= 12 && k < 30) tendv = *l_tend;
      }
      if constexpr (n == FB_AT) { if (lane == 0) l_f[FB + sw] = 1; }
      constexpr int m = n < 48 ? n : n - 48;
      buf[n] = ld_nt(n < 48 ? r0 : rv, voff + (m % 3) * 128, (m / 3) * KVT);
      SB();
      static_for<done_after(n > 0 ? n - 1 : 0), done_after(n)>(consume);
      SB();
    });
    STAMP(4);
    __builtin_amdgcn_s_setprio(3);
    wait_lds<0>(fbase + FE, 0x01010101u);
    float cq = l_c[lane >> 4];
    unsigned dst = (unsigned)(size_t)(lfloat*)(l_acc + (sw * 2 + (g & 1)) * 192 + 8 * j + 4 * ((lane >> 4) & 1));
    unsigned src = (unsigned)(size_t)(lfloat*)(l_acc + (lane >> 4) * 384 + 64 * sw + 8 * j + 4 * (g & 1));
    unsigned fla = fbase + FC + sw, one = 1u, flr = fbase + FC;
    int pvo = (lane & 16) ? OOB : (g & 1) * 128 + j * 16 + (lane >> 5) * 8;
    int pso = WS_OP + ((h * 2 + b) * 4 + s) * 768 + sw * 256;
    unsigned tagv = tag;
    asm volatile("" : "+v"(cq), "+v"(dst), "+v"(src), "+v"(fla), "+v"(flr), "+v"(one), "+v"(pvo), "+s"(pso), "+v"(tagv));
    const bool wr = (lane >> 5) == (g & 1);
    SB();
    static_for<done_after(95), 32>([&](auto G) { consume(G); SB(); });
    if (wr) {
#pragma unroll
      for (int i = 0; i < 3; ++i) *(lf32x4*)(size_t)(dst + 256 * i) = acc[i];
      MEMFENCE();
      *(lbyte*)(size_t)fla = (unsigned char)one;
    }
    STAMP(5);
    if (sw < 3) {
      f32x4 u0, u1;
      unsigned fv, fs;
      asm volatile("1:\n\tds_read_b32 %0, %4\n\tds_read_b128 %2, %5\n\tds_read_b128 %3, %5 offset:768\n\ts_waitcnt lgkmcnt(2)\n\tv_readfirstlane_b32 %1, %0\n\t"
                   "s_cmp_lg_u32 %1, 0x01010101\n\ts_waitcnt lgkmcnt(0)\n\ts_cbranch_scc1 1b"
                   : "=&v"(fv), "=&s"(fs), "=&v"(u0), "=&v"(u1) : "v"(flr), "v"(src) : "scc", "memory");
      float o[4];
#pragma unroll
      for (int e = 0; e < 4; ++e) o[e] = cq * (u0[e] + u1[e]);
      const auto s0 = __builtin_amdgcn_permlane32_swap(__float_as_uint(o[0]), __float_as_uint(o[2]), false, false);
      const auto s1 = __builtin_amdgcn_permlane32_swap(__float_as_uint(o[1]), __float_as_uint(o[3]), false, false);
      const float p0 = x16_sum(__uint_as_float(s0[0]) + __uint_as_float(s0[1])), p1 = x16_sum(__uint_as_float(s1[0]) + __uint_as_float(s1[1]));
      const u32x2 pv = {cvtpk(p0, p1), tagv};
      __builtin_amdgcn_raw_buffer_store_b64(pv, rws, pvo, pso, AUX_SC1);
    }
    MEMFENCE();
    *(lbyte*)(size_t)(fla + (FD - FC)) = (unsigned char)one;
    STAMP(6);
    STAMP_FLUSH();
    return;
  }

  __builtin_amdgcn_s_setprio(2);
  const int w = wave;
  const unsigned idx = w < 2 ? 2 * j8 + w : uq >> 1, ur = (idx * 11) >> 5, uc = idx - 3 * ur, up = uq & 1;
  const bool has_front = (w < 3) & (idx < 15);
  const unsigned m0 = e4 == 0 ? 0x0000ffffu : e4 == 1 ? 0xffff0000u : 0u;
  const unsigned m1 = e4 == 2 ? 0x0000ffffu : e4 == 3 ? 0xffff0000u : 0u;
  const int st_col = (8 * j + 4 * (lane >> 5) + 2 * (g & 1)) * 8;
  const rsrc_t rx = mk_rsrc(x, 2560);
  if (has_front) {
    const int xo = (lane >> 5) * 1280 + (lane & 31) * 16;
    const bool x3 = (lane & 16) == 0;
    const u32x4 xa = __builtin_amdgcn_raw_buffer_load_b128(rx, xo, 0, 0), xb = __builtin_amdgcn_raw_buffer_load_b128(rx, xo + 512, 0, 0);
    const u32x4 xc = __builtin_amdgcn_raw_buffer_load_b128(rx, x3 ? xo + 1024 : OOB, 0, 0);
    const u32x4 xr0 = __builtin_amdgcn_raw_buffer_load_b128(rx, bl * 1280 + g * 32, 256 * ur, 0);
    const u32x4 xr1 = __builtin_amdgcn_raw_buffer_load_b128(rx, bl * 1280 + g * 32 + 16, 256 * ur, 0);
    SB();
    FS(2);
    TAG_READY();
    float v[24];
#pragma unroll
    for (int e = 0; e < 4; ++e) { v[2 * e] = bflo(xa[e]); v[2 * e + 1] = bfhi(xa[e]); v[8 + 2 * e] = bflo(xb[e]); v[9 + 2 * e] = bfhi(xb[e]); v[16 + 2 * e] = bflo(xc[e]); v[17 + 2 * e] = bfhi(xc[e]); }
    float sum = 0.f;
#pragma unroll
    for (int e = 0; e < 24; ++e) sum += v[e];
    sum = x16_sum(row_sum(sum));
    const float mean = sum * (1.f / 640.f);
    float sq = 0.f, sq3 = 0.f;
#pragma unroll
    for (int e = 0; e < 16; ++e) { const float d = v[e] - mean; sq = fmaf(d, d, sq); }
#pragma unroll
    for (int e = 16; e < 24; ++e) { const float d = v[e] - mean; sq3 = fmaf(d, d, sq3); }
    sq = x16_sum(row_sum(sq + (x3 ? sq3 : 0.f)));
    const float mb = rbf(mean), den = rbf(__builtin_amdgcn_sqrtf(rbf(rbf(sq * (1.f / 640.f)) + 1e-5f)));
    const float inv = __builtin_amdgcn_rcpf(den);
    const float mb0 = __builtin_bit_cast(float, __builtin_amdgcn_readlane(__builtin_bit_cast(int, mb), 0));
    const float mb1 = __builtin_bit_cast(float, __builtin_amdgcn_readlane(__builtin_bit_cast(int, mb), 32));
    const float iv0 = __builtin_bit_cast(float, __builtin_amdgcn_readlane(__builtin_bit_cast(int, inv), 0));
    const float iv1 = __builtin_bit_cast(float, __builtin_amdgcn_readlane(__builtin_bit_cast(int, inv), 32));
    const float mm = bl ? mb1 : mb0, ii = bl ? iv1 : iv0;
    FS(3);
    asm volatile("s_waitcnt vmcnt(0)" : "+v"(fw[0]), "+v"(fw[1]), "+v"(fw[2]), "+v"(fw[3]), "+v"(fw[4]), "+v"(fw[5]), "+v"(fw[6]), "+v"(fw[7]),
                 "+v"(fw[8]), "+v"(fw[9]), "+v"(fw[10]), "+v"(fw[11]), "+v"(fw[12]), "+v"(fw[13]), "+v"(fw[14]), "+v"(fw[15]));
    f32x4 acc0 = {0.f, 0.f, 0.f, 0.f}, acc1 = {0.f, 0.f, 0.f, 0.f};
#pragma unroll
    for (int n = 0; n < 16; ++n) {
      const unsigned xw = n < 8 ? xr0[n >> 1] : xr1[(n - 8) >> 1];
      const float xv = (n & 1) ? bfhi(xw) : bflo(xw);
      const float xn = rbf(xv - mm) * ii;
      const unsigned d = cvtpk(xn, xn), a0 = d & m0, a1 = d & m1;
      acc0 = mfma16(a0, a1, fw[n][0], fw[n][1], acc0);
      acc1 = mfma16(a0, a1, fw[n][2], fw[n][3], acc1);
    }
    float v0, v1;
    proj_epilogue(acc0, acc1, g & 1, v0, v1);
    const int rec = (w < 2 ? WS_QP + (h * 5 + ur) * 3072 : WS_GU + ((xq * 5 + ur) * 2 + up) * 3072) + uc * 512;
    st_pair(rws, ((lane >> 4) & 1) * 1536 + st_col, rec, v0, v1, tag);
    STAMP(1); FS(4);
    asm volatile("s_waitcnt vmcnt(0)" ::: "memory");
    STAMP(11); FS(5);
  } else {
    asm volatile("s_waitcnt vmcnt(0)" ::"v"(fw[0]), "v"(fw[1]), "v"(fw[2]), "v"(fw[3]), "v"(fw[4]), "v"(fw[5]), "v"(fw[6]), "v"(fw[7]),
                 "v"(fw[8]), "v"(fw[9]), "v"(fw[10]), "v"(fw[11]), "v"(fw[12]), "v"(fw[13]), "v"(fw[14]), "v"(fw[15]));
    if (w == 3) {
      if (PRETOUCH) {
        wait_until(tstart + PRETOUCH);
        const unsigned kv1 = kv0 + 512 * 384 - 64, o0 = h * (192 * 1280), o1 = o0 + 192 * 1280 - 64, d0_ = h * (48 * 1280);
        unsigned t0, t1, t2, t3_, t4, t5, t6, t7, t8;
        asm volatile("s_load_dword %0, %9, %14\n\ts_load_dword %1, %10, %15\n\ts_load_dword %2, %10, %14\n\ts_load_dword %3, %11, %16\n\ts_load_dword %4, %11, %17\n\t"
                     "s_load_dword %5, %12, %18\n\ts_load_dword %6, %13, 0x0\n\ts_load_dword %7, %19, %20\n\ts_load_dword %8, %19, %21\n\ts_waitcnt lgkmcnt(0)"
                     : "=&s"(t0), "=&s"(t1), "=&s"(t2), "=&s"(t3_), "=&s"(t4), "=&s"(t5), "=&s"(t6), "=&s"(t7), "=&s"(t8)
                     : "s"(kc), "s"(vc), "s"(wo), "s"(wd), "s"(out), "s"(kv1), "s"(kv0), "s"(o0), "s"(o1), "s"(d0_), "s"(ws), "s"(WS_END), "s"(512));
      }
    }
    TAG_READY();
  }
  const int slot = w * 8 + j8;
  const bool has_back = slot < 30;
  const int t3 = slot / 10, cc = slot - 10 * t3;
  const int rb = bid / 80, rc = (bid - 80 * rb) >> 3, ro = bid & 7;
  const bool reducer = (w == 0) && (bid < 160);
  unsigned ha[2][2] = {{0u, 0u}, {0u, 0u}};
        unsigned tend = tstart + tn0;
        auto pace_round = [&](unsigned t_sample, unsigned t_read, int so, int bias) {
          wait_until(t_sample);
          u32x4 e;
          do { MEMFENCE(); e = *(const u32x4*)l_est; } while (!__builtin_amdgcn_readfirstlane(e[0] & e[1] & e[2] & e[3] & 1u));
          const u32x2 pv = {tstart + (unsigned)((int)((e[0] - tstart) + (e[1] - tstart) + (e[2] - tstart) + (e[3] - tstart)) >> 2), tag};
          __builtin_amdgcn_raw_buffer_store_b64(pv, rws, (lane == 0 && bid < 32) ? bid * 8 : OOB, so, AUX_SC1);
          wait_until(t_read);
          MEMFENCE();
          const u32x2 tv = __builtin_amdgcn_raw_buffer_load_b64(rws, (lane & 31) * 8, so, AUX_SC1);
          if (__all(tv[1] == tag)) {
            const float d = x16_sum(row_sum((float)(int)(tv[0] - tstart)));
            int di = (int)(d * (1.f / 32.f)) - bias;
            di = di < 0 ? 0 : (di > 2500 ? 2500 : di);
            tend = (tstart + __builtin_amdgcn_readfirstlane(di)) | 1u;
            *l_tend = tend;
          }
        };
        if (w < 3) {
          if (has_front) __builtin_amdgcn_s_sleep(QSLEEP); else wait_until(tstart + 420);
          u32x4 t[3];
          const int vo = (lane >> 5) * 3072 + (lane & 31) * 16, so = WS_QP + h * 5 * 3072 + b * 1536 + w * 512;
          for (unsigned spin = 0;; ++spin) {
            MEMFENCE();
            t[0] = ld_sc1(rws, vo, so); t[1] = ld_sc1(rws, vo, so + 6144); t[2] = ld_sc1(rws, lane < 32 ? vo : OOB, so + 12288);
            const bool ok = (t[0][1] == tag) & (t[0][3] == tag) & (t[1][1] == tag) & (t[1][3] == tag) & (((t[2][1] == tag) & (t[2][3] == tag)) | (lane >= 32));
            if (__all(ok) || spin > SPIN_CAP) { STAMPV(12, spin + 1); break; }
          }
          const float a = x32_sum((__uint_as_float(t[0][0]) + __uint_as_float(t[1][0])) + __uint_as_float(t[2][0]));
          const float c = x32_sum((__uint_as_float(t[0][2]) + __uint_as_float(t[1][2])) + __uint_as_float(t[2][2]));
          if (lane < 32) { l_q[32 * w + lane] = cvtpk(a, c); MEMFENCE(); l_f[FG + w] = 1; }
        } else if (PACING == 2) {
          pace_round(tstart + tn6, tstart + tn6 + tn7, WS_TS, tn1);
        } else {
          wait_until(tstart + 700);
        }
        STAMP(2); FS(6);
        if (has_back) {
          u32x4 t[3];
          const int so = WS_GU + xq * 5 * 6144 + (h & 3) * 384 + t3 * 128;
          const int vo = bl * 1536 + 16 * g;
          for (unsigned spin = 0;; ++spin) {
            MEMFENCE();
            t[0] = ld_sc1(rws, vo + e4 * 6144, so);
            t[1] = ld_sc1(rws, vo + e4 * 6144 + 3072, so);
            t[2] = ld_sc1(rws, vo + 4 * 6144 + (e4 & 1) * 3072, so);
            bool ok = true;
#pragma unroll
            for (int k = 0; k < 3; ++k) ok &= (t[k][1] == tag) & (t[k][3] == tag);
            if (__all(ok) || spin > SPIN_CAP) { STAMPV(13, spin + 1); break; }
          }
#pragma unroll
          for (int z = 0; z < 2; ++z) {
            const float ex = __uint_as_float(t[2][2 * z]);
            const float gg = rbf(quad_sum(__uint_as_float(t[0][2 * z]) + (e4 == 0 ? ex : 0.f)));
            const float uu = rbf(quad_sum(__uint_as_float(t[1][2 * z]) + (e4 == 1 ? ex : 0.f)));
            const float zz = 0.7978845608028654f * (gg + 0.044715f * gg * gg * gg);
            const float ge = rbf(gg * __builtin_amdgcn_rcpf(1.f + __builtin_amdgcn_exp2f(-2.8853900817779268f * zz)));
            const float hv = ge * uu;
            const unsigned d = cvtpk(hv, hv);
            ha[z][0] = d & m0; ha[z][1] = d & m1;
          }
        }
  const int oc = 4 * (lane & 1) + 2 * (lane >> 5) + ((lane >> 4) & 1);
  float xres = 0.f;
  if (reducer) xres = bflo(__builtin_amdgcn_raw_buffer_load_b16(rx, oc * 2, rb * 1280 + rc * 128 + ro * 16, 0));
  STAMP(3);
        if (w == 1) {
          wait_lds<4>(fbase + FA, 0x01010101u);
          const int so = WS_ML + (h * 2 + b) * 256;
          if (lane < 4) { const f32x2 ml = *(const f32x2*)(l_ml + 2 * lane); st_pair(rws, lane * 16, so + s * 64, ml[0], ml[1], tag); }
          asm volatile("s_waitcnt vmcnt(0)" ::: "memory");
          __builtin_amdgcn_s_sleep(MLSLEEP);
          u32x4 t;
          for (unsigned spin = 0;; ++spin) {
            MEMFENCE();
            t = ld_sc1(rws, (lane & 15) * 16, so);
            if (__all((t[1] == tag) & (t[3] == tag)) || spin > SPIN_CAP) break;
          }
          const float mv = __uint_as_float(t[0]);
          const float M = row_max(mv);
          const float f = __builtin_amdgcn_exp2f(mv - M);
          const float L = row_sum(f * __uint_as_float(t[2]));
          if ((lane >> 2) == s) { l_c[lane & 3] = f / L; MEMFENCE(); l_f[FE + (lane & 3)] = 1; }
        }
  if (PACING == 2 && w == 3 && tn8) {
    pace_round(tend - tn8, tend - tn8 + tn9, WS_TS + 256, tn10);
  }
  if (has_back) {
    const unsigned sLo = (e4 & 1) ? 0x01000c0cu : 0x0c0c0100u, sHi = (e4 & 1) ? 0x03020c0cu : 0x0c0c0302u;
    unsigned sel0 = e4 < 2 ? sLo : 0x0c0c0c0cu, sel1 = e4 < 2 ? 0x0c0c0c0cu : sLo, sel2 = e4 < 2 ? sHi : 0x0c0c0c0cu, sel3 = e4 < 2 ? 0x0c0c0c0cu : sHi;
    int op_so = WS_OP + h * 8 * 768 + t3 * 256, op_vo = (bl * 4 + e4) * 768 + 16 * g;
    int d_vo = ((48 * h + 16 * t3 + 2 * g) * 640 + 64 * cc) * 2 + 16 * j, o_vo = ((192 * h + 64 * t3 + (WOSEQ ? 1 : 8) * g) * 640 + 64 * cc) * 2 + 16 * j;
    int yvo = ((lane >> 4) & 1) * 256 + st_col / 2, yso = WS_YP + ((h * 3 + t3) * 10 + cc) * 512;
    int r_vo = (lane >> 1) * 5120 + (lane & 1) * 16 + WS_YP + rc * 512 + rb * 256 + ro * 32;
    int out_vo = (lane & 14) == 0 ? oc * 2 + rb * 1280 + rc * 128 + ro * 16 : OOB;
    unsigned tag2v = tag + 1u, tda = (unsigned)(size_t)(luint*)l_tend;
    asm volatile("" : "+v"(sel0), "+v"(sel1), "+v"(sel2), "+v"(sel3), "+s"(op_so), "+v"(op_vo), "+v"(d_vo), "+v"(o_vo), "+v"(yvo), "+s"(yso), "+v"(r_vo), "+v"(out_vo), "+v"(tag2v), "+v"(xres), "+v"(tda));
    int pass = DRYRUN ? (w + 3) >> 2 : 1;
    asm volatile("" : "+s"(pass));
#pragma nounroll
    for (; pass < 2; ++pass) {
      STAMP(4);
      const int live = pass ? -1 : 0;
      const rsrc_t rd = mk_rsrc(wd, 1536 * 1280 & live), ro_ = mk_rsrc(wo, 6144 * 1280 & live), rw2 = mk_rsrc(ws, WS_END & live), rout = mk_rsrc(out, 2560 & live);
      const unsigned want = tag & live, want2 = (tag + 1u) & live;
      unsigned fb = fbase + (pass ? FB : FF), fd = fbase + (pass ? FD : FF);
      asm volatile("" : "+v"(fb), "+v"(fd));
      wait_lds<1>(fb, 0x01010101u & live);
      const unsigned tfin = __builtin_amdgcn_readfirstlane(*(luint*)(size_t)tda) & live;
      delay_ticks(tn4 & live);
      u32x4 wb[10], t0, t1;
      unsigned fdv = 0;
#pragma unroll
      for (int n = 0; n < 10; ++n) {
        if (n == POLLPOS) {
          const rsrc_t rp = mk_rsrc(ws, __builtin_amdgcn_readfirstlane(fdv) == 0x01010101u ? (WS_END & live) : 0);
          t0 = ld_sc1(rp, op_vo, op_so); t1 = ld_sc1(rp, op_vo + 128, op_so);
          SB();
        }
        if (n < 2) wb[8 + n] = ld_nt(rd, d_vo + n * 1280, 0); else wb[n - 2] = ld_nt(ro_, o_vo, (n - 2) * (WOSEQ ? 10240 : 1280));
        if (n == POLLPOS - FDLOOK) { MEMFENCE(); fdv = *(luint*)(size_t)fd; }
        SB();
        if (n < 9 && BPACE) __builtin_amdgcn_s_sleep(BPACE);
        SB();
      }
      if (POLLPOS == 11) {
        if (pass) {
          wait_until(tfin ? tfin + tn12 - tn2 : tstart);
          wait_lds<1>(fd, 0x01010101u);
          delay_ticks(tn2);
        }
        t0 = ld_sc1(rw2, op_vo, op_so); t1 = ld_sc1(rw2, op_vo + 128, op_so);
        SB();
      }
      if (POLLPOS == 10) {
        const rsrc_t rp = mk_rsrc(ws, __builtin_amdgcn_readfirstlane(fdv) == 0x01010101u ? (WS_END & live) : 0);
        t0 = ld_sc1(rp, op_vo, op_so); t1 = ld_sc1(rp, op_vo + 128, op_so);
        SB();
      }
      STAMP(5);
      u32x4 bq[10];
#define PAIR(p) do { bq[2 * (p)] = u32x4{wb[2 * (p)][0], wb[2 * (p)][1], wb[2 * (p) + 1][0], wb[2 * (p) + 1][1]}; \
                     bq[2 * (p) + 1] = u32x4{wb[2 * (p)][2], wb[2 * (p)][3], wb[2 * (p) + 1][2], wb[2 * (p) + 1][3]}; } while (0)
      PAIR(4); PAIR(0); PAIR(1); PAIR(2);
      if (POLLPOS >= 10) PAIR(3);
      const u32x4 hA = {ha[0][0], ha[0][1], ha[1][0], ha[1][1]};
      f32x4 acc0 = mfma32(hA, bq[8], f32x4{0.f, 0.f, 0.f, 0.f}), acc1 = mfma32(hA, bq[9], f32x4{0.f, 0.f, 0.f, 0.f});
      asm volatile("" : "+v"(bq[0]), "+v"(bq[1]), "+v"(bq[2]), "+v"(bq[3]), "+v"(bq[4]), "+v"(bq[5]), "+v"(acc0), "+v"(acc1));
      if (POLLPOS >= 10) asm volatile("" : "+v"(bq[6]), "+v"(bq[7]));
      SB();
      unsigned np1 = 1;
      if (__builtin_expect(!__all((t0[1] == want) & (t0[3] == want) & (t1[1] == want) & (t1[3] == want)), 0)) {
        do {
          ++np1;
          wait_lds<1>(fd, 0x01010101u);
          delay_ticks(tn2);
          t0 = ld_sc1(rw2, op_vo, op_so); t1 = ld_sc1(rw2, op_vo + 128, op_so);
        } while (!__all((t0[1] == want) & (t0[3] == want) & (t1[1] == want) & (t1[3] == want)) && np1 < SPIN_CAP);
      }
      STAMPV(14, np1);
      STAMP(7);
      unsigned pk[4];
      {
        float u0, u1, u2, u3, u4, u5, u6, u7;
#define DPPADD(r, P) "v_add_f32_dpp " r ", " r ", " r " quad_perm:" P " row_mask:0xf bank_mask:0xf\n\t"
#define DPP8(P) DPPADD("%4", P) DPPADD("%5", P) DPPADD("%6", P) DPPADD("%7", P) DPPADD("%8", P) DPPADD("%9", P) DPPADD("%10", P) DPPADD("%11", P)
        asm volatile("v_lshlrev_b32 %4, 16, %12\n\tv_and_b32 %5, 0xffff0000, %12\n\tv_lshlrev_b32 %6, 16, %13\n\tv_and_b32 %7, 0xffff0000, %13\n\t"
                     "v_lshlrev_b32 %8, 16, %14\n\tv_and_b32 %9, 0xffff0000, %14\n\tv_lshlrev_b32 %10, 16, %15\n\tv_and_b32 %11, 0xffff0000, %15\n\t"
                     DPP8("[1,0,3,2]") DPP8("[2,3,0,1]")
                     "v_cvt_pk_bf16_f32 %0, %4, %5\n\tv_cvt_pk_bf16_f32 %1, %6, %7\n\tv_cvt_pk_bf16_f32 %2, %8, %9\n\tv_cvt_pk_bf16_f32 %3, %10, %11"
                     : "=&v"(pk[0]), "=&v"(pk[1]), "=&v"(pk[2]), "=&v"(pk[3]), "=&v"(u0), "=&v"(u1), "=&v"(u2), "=&v"(u3), "=&v"(u4), "=&v"(u5), "=&v"(u6), "=&v"(u7)
                     : "v"(t0[0]), "v"(t0[2]), "v"(t1[0]), "v"(t1[2]));
      }
      u32x4 aq[4];
#pragma unroll
      for (int p = 0; p < 4; ++p)
        aq[p] = u32x4{__builtin_amdgcn_perm(0u, pk[p], sel0), __builtin_amdgcn_perm(0u, pk[p], sel1), __builtin_amdgcn_perm(0u, pk[p], sel2), __builtin_amdgcn_perm(0u, pk[p], sel3)};
#pragma unroll
      for (int p = 0; p < 3; ++p) { acc0 = mfma32(aq[p], bq[2 * p], acc0); acc1 = mfma32(aq[p], bq[2 * p + 1], acc1); }
      if (POLLPOS < 10) { asm volatile("" : "+v"(aq[3]), "+v"(acc0), "+v"(acc1)); SB(); PAIR(3); }
      acc0 = mfma32(aq[3], bq[6], acc0); acc1 = mfma32(aq[3], bq[7], acc1);
      float v0, v1;
      proj_epilogue(acc0, acc1, g & 1, v0, v1);
      __builtin_amdgcn_raw_buffer_store_b64(u32x2{cvtpk(v0, v1), tag2v}, rw2, yvo, yso, AUX_SC1);
      STAMP(8);
      if (reducer) {
        if (pass) {
          const unsigned tn = (unsigned)__builtin_amdgcn_s_memrealtime();
          int wt = tfin ? (int)(tfin + tn13 - tn) : 40;
          asm("s_max_i32 %0, %0, %1\n\ts_min_i32 %0, %0, 0x64" : "+s"(wt) : "s"(tn3) : "scc");
          wait_until(tn + wt);
        }
        u32x4 t[3];
        unsigned np2 = 0;
        for (;;) {
          ++np2;
          MEMFENCE();
#pragma unroll
          for (int k = 0; k < 3; ++k) t[k] = ld_sc1(rw2, r_vo, k * 32 * 5120);
          bool ok = true;
#pragma unroll
          for (int k = 0; k < 3; ++k) ok &= (t[k][1] == want2) & (t[k][3] == want2);
          if (__all(ok) || np2 > SPIN_CAP) break;
        }
        STAMPV(15, np2);
        STAMP(9);
        const float y0 = (bflo(t[0][0]) + bflo(t[1][0])) + bflo(t[2][0]), y1 = (bfhi(t[0][0]) + bfhi(t[1][0])) + bfhi(t[2][0]);
        const float y2 = (bflo(t[0][2]) + bflo(t[1][2])) + bflo(t[2][2]), y3 = (bfhi(t[0][2]) + bfhi(t[1][2])) + bfhi(t[2][2]);
        const auto s0 = __builtin_amdgcn_permlane32_swap(__float_as_uint(y0), __float_as_uint(y2), false, false);
        const auto s1 = __builtin_amdgcn_permlane32_swap(__float_as_uint(y1), __float_as_uint(y3), false, false);
        const float z0 = __uint_as_float(s0[0]) + __uint_as_float(s0[1]), z1 = __uint_as_float(s1[0]) + __uint_as_float(s1[1]);
        const auto s2 = __builtin_amdgcn_permlane16_swap(__float_as_uint(z0), __float_as_uint(z1), false, false);
        float y = __uint_as_float(s2[0]) + __uint_as_float(s2[1]);
        y += dpp<ROR8>(y); y += dpp<ROR4>(y); y += dpp<XOR2>(y);
        __builtin_amdgcn_raw_buffer_store_b16((unsigned short)cvtpk(xres + y, 0.f), rout, out_vo, 0, AUX_OUT);
        if (bid == 0) __builtin_amdgcn_raw_buffer_store_b32(tag2v, rw2, lane == 0 ? WS_EPOCH : OOB, 0, AUX_SC1);
        STAMP(10);
        if (pass && bid == PBID) {
          if (AMP == 3) ampv = tfin - amp0 - 1500;
          AMPNOW(4, tfin + 300); AMPNOW(5, amp0 + 1800);
          if (AMP == 6) ampv = 10 * np2 + 40 * np1;
          if (AMP >= 7) {
            const u32x2 tv = __builtin_amdgcn_raw_buffer_load_b64(rws, lane * 8, WS_TS + 512, AUX_SC1);
            const float d = AMP >= 9 ? (float)tv[0] : (float)(int)(tv[0] - amp0);
            const float mx = x32_max(x16_max(row_max(d))), mn = -x32_max(x16_max(row_max(-d))), av = x32_sum(x16_sum(row_sum(d))) * (1.f / 64.f);
            ampv = __builtin_amdgcn_readfirstlane((int)(AMP == 7 ? mx - mn : AMP == 8 ? av - mn : AMP == 9 ? mx : av));
          }
          wait_until(amp0 + 4000 + 10 * ((int)ampv < 0 ? 0 : ampv));
        }
      }
    }
  }
  STAMP_FLUSH();
}

extern "C" void kernel_launch(void* const* d_in, const int* in_sizes, int n_in, void* d_out, int out_size, void* d_ws, size_t ws_size,
                              hipStream_t stream) {
  fused_decode<<<256, 512, 0, stream>>>((const u16*)d_in[0], (const u16*)d_in[1], (const u16*)d_in[2], (const u16*)d_in[3], (const u16*)d_in[4],
                                        (const u16*)d_in[6], (const u16*)d_in[7], (const u16*)d_in[8], (u16*)d_out, (unsigned char*)d_ws
                                        );
}
```

```cpp
#define PACING 2
#define CODEPF 16896
#define PRETOUCH 250
#define SCAT 0
#define STAG 11
#define POLLPOS 8
#define FDLOOK 1
#define H1DELAY 20
#define H2DELAY 40
#define SMP0 600
#define WT0 250
#define TFIN 0
#define SMP1 480
#define WT1 150
#define SLATE 1
#define TEND0 1490
#define TEND1 20
#define BIAS1 15
#define SDELAY 135
#define BDELAY 12
#define AMP 8
#include <hip/hip_runtime.h>
#include <hip/hip_bf16.h>
#include <stdint.h>
#include <type_traits>

typedef unsigned short u16;
typedef unsigned u32x4 __attribute__((ext_vector_type(4)));
typedef unsigned u32x2 __attribute__((ext_vector_type(2)));
typedef float f32x4 __attribute__((ext_vector_type(4)));
typedef float f32x2 __attribute__((ext_vector_type(2)));
typedef short s16x4 __attribute__((ext_vector_type(4)));
typedef __bf16 bf16x8 __attribute__((ext_vector_type(8)));
typedef __bf16 bf16x2 __attribute__((ext_vector_type(2)));
typedef __amdgpu_buffer_rsrc_t rsrc_t;

#define WIN 9
#define B1_AT 48
#define CATCH 3
#define QSLEEP 16
#define MLSLEEP 32
#define BPACE 5
#define H1SLEEP 0
#define KC 10
#define FB_AT 95
#define DRYRUN 1
#define RATE16 229
#define STOUCH 0
#define AUX_OUT 0
#define KVW 8
#define KVG 384
#define KVT 12288
#define WOSEQ 0
#define TPOLL 155
#define PBID 8

constexpr int AUX_NT = 2, AUX_SC1 = 16;
constexpr int RSRC_W3 = 0x00020000;
constexpr unsigned SPIN_CAP = 1u << 18;
constexpr int OOB = 0x40000000;

constexpr int WS_EPOCH = 0;
constexpr int WS_TS = 1024;
constexpr int WS_QP = 4096;
constexpr int WS_GU = WS_QP + 32 * 5 * 2 * 192 * 8;
constexpr int WS_ML = WS_GU + 8 * 5 * 2 * 2 * 192 * 8;
constexpr int WS_OP = WS_ML + 32 * 2 * 4 * 4 * 16;
constexpr int WS_YP = WS_QP;
constexpr int WS_END = WS_OP + 32 * 2 * 4 * 96 * 8;

template <int I, int N, class F> __device__ __forceinline__ void static_for(F&& f) {
  if constexpr (I < N) { f(std::integral_constant<int, I>{}); static_for<I + 1, N>(f); }
}
__device__ __forceinline__ rsrc_t mk_rsrc(const void* p, int bytes) {
  return __builtin_amdgcn_make_buffer_rsrc(const_cast<void*>(p), 0, bytes, RSRC_W3);
}
__device__ __forceinline__ u32x4 ld_nt(rsrc_t r, int voff, int soff) { return __builtin_amdgcn_raw_buffer_load_b128(r, voff, soff, AUX_NT); }
__device__ __forceinline__ u32x4 ld_sc1(rsrc_t r, int voff, int soff) { return __builtin_amdgcn_raw_buffer_load_b128(r, voff, soff, AUX_SC1); }
__device__ __forceinline__ void st_pair(rsrc_t r, int voff, int soff, float a, float b, unsigned tag) {
  u32x4 v = {__float_as_uint(a), tag, __float_as_uint(b), tag};
  __builtin_amdgcn_raw_buffer_store_b128(v, r, voff, soff, AUX_SC1);
}
__device__ __forceinline__ float bflo(unsigned v) { return __uint_as_float(v << 16); }
__device__ __forceinline__ float bfhi(unsigned v) { return __uint_as_float(v & 0xffff0000u); }
__device__ __forceinline__ unsigned cvtpk(float lo, float hi) {
  bf16x2 r = {(__bf16)lo, (__bf16)hi};
  return __builtin_bit_cast(unsigned, r);
}
__device__ __forceinline__ float rbf(float x) { return (float)(__bf16)x; }
template <int CTRL> __device__ __forceinline__ float dpp(float x) {
  return __builtin_bit_cast(float, __builtin_amdgcn_mov_dpp(__builtin_bit_cast(int, x), CTRL, 0xf, 0xf, true));
}
constexpr int XOR1 = 0xB1, XOR2 = 0x4E, HMIR = 0x141, ROR8 = 0x128, ROR4 = 0x124;
__device__ __forceinline__ float x16_sum(float x) { auto s = __builtin_amdgcn_permlane16_swap(__float_as_uint(x), __float_as_uint(x), false, false); return __uint_as_float(s[0]) + __uint_as_float(s[1]); }
__device__ __forceinline__ float x32_sum(float x) { auto s = __builtin_amdgcn_permlane32_swap(__float_as_uint(x), __float_as_uint(x), false, false); return __uint_as_float(s[0]) + __uint_as_float(s[1]); }
__device__ __forceinline__ float x16_max(float x) { auto s = __builtin_amdgcn_permlane16_swap(__float_as_uint(x), __float_as_uint(x), false, false); return fmaxf(__uint_as_float(s[0]), __uint_as_float(s[1])); }
__device__ __forceinline__ float x32_max(float x) { auto s = __builtin_amdgcn_permlane32_swap(__float_as_uint(x), __float_as_uint(x), false, false); return fmaxf(__uint_as_float(s[0]), __uint_as_float(s[1])); }
__device__ __forceinline__ float row_sum(float x) { x += dpp<XOR1>(x); x += dpp<XOR2>(x); x += dpp<HMIR>(x); x += dpp<ROR8>(x); return x; }
__device__ __forceinline__ float row_max(float x) { x = fmaxf(x, dpp<XOR1>(x)); x = fmaxf(x, dpp<XOR2>(x)); x = fmaxf(x, dpp<HMIR>(x)); x = fmaxf(x, dpp<ROR8>(x)); return x; }
__device__ __forceinline__ float quad_sum(float x) { x += dpp<XOR1>(x); x += dpp<XOR2>(x); return x; }
__device__ __forceinline__ float dot2(unsigned a, unsigned b, float c) {
  return __builtin_amdgcn_fdot2_f32_bf16(__builtin_bit_cast(bf16x2, a), __builtin_bit_cast(bf16x2, b), c, false);
}
__device__ __forceinline__ f32x4 mfma16(unsigned a0, unsigned a1, unsigned b0, unsigned b1, f32x4 c) {
  u32x2 a = {a0, a1}, b = {b0, b1};
  return __builtin_amdgcn_mfma_f32_16x16x16bf16_1k(__builtin_bit_cast(s16x4, a), __builtin_bit_cast(s16x4, b), c, 0, 0, 0);
}
__device__ __forceinline__ f32x4 mfma32(u32x4 a, u32x4 b, f32x4 c) {
  return __builtin_amdgcn_mfma_f32_16x16x32_bf16(__builtin_bit_cast(bf16x8, a), __builtin_bit_cast(bf16x8, b), c, 0, 0, 0);
}
template <int RECIP> __device__ __forceinline__ unsigned pace_step(unsigned& tgt, unsigned tend) {
  unsigned d, now;
  asm volatile("1:\n\ts_memrealtime vcc\n\ts_waitcnt lgkmcnt(0)\n\ts_sub_u32 %0, vcc_lo, %2\n\ts_cmp_lt_i32 %0, 0\n\ts_cbranch_scc1 1b\n\t"
               "s_sub_i32 %0, %3, vcc_lo\n\ts_max_i32 %0, %0, 0\n\ts_min_i32 %0, %0, 0x7d0\n\ts_mul_i32 %0, %0, %4\n\ts_lshr_b32 %0, %0, 16\n\t"
               "s_add_u32 %2, vcc_lo, %0\n\ts_mov_b32 %1, vcc_lo"
               : "=&s"(d), "=&s"(now), "+s"(tgt) : "s"(tend), "i"(RECIP) : "vcc", "scc");
  return now;
}
__device__ __forceinline__ void wait_until(unsigned tgt) {
  asm volatile("1:\n\ts_memrealtime vcc\n\ts_waitcnt lgkmcnt(0)\n\ts_sub_u32 vcc_lo, vcc_lo, %0\n\ts_cmp_lt_i32 vcc_lo, 0\n\ts_cbranch_scc1 1b" :: "s"(tgt) : "vcc", "scc", "memory");
}
#define SB() __builtin_amdgcn_sched_barrier(0)
#define WG_BARRIER() do { asm volatile("s_waitcnt lgkmcnt(0)\n\ts_barrier" ::: "memory"); } while (0)
#define MEMFENCE() asm volatile("" ::: "memory")

#define FS(i)
#define STAMP(i)
#define STAMPV(i, v)
#define STAMP_DECL
#define STAMP_FLUSH()

__device__ __forceinline__ void delay_ticks(int n) {
  if (n > 0) wait_until((unsigned)__builtin_amdgcn_s_memrealtime() + n);
}

typedef __attribute__((address_space(3))) float lfloat;
typedef __attribute__((address_space(3))) f32x4 lf32x4;
typedef __attribute__((address_space(3))) unsigned luint;
typedef __attribute__((address_space(3))) unsigned char lbyte;
template <int SLP> __device__ __forceinline__ void wait_lds(unsigned a, unsigned want) {
  unsigned v, sv;
  asm volatile("s_branch 2f\n1:\n\ts_sleep %4\n2:\n\tds_read_b32 %0, %2\n\ts_waitcnt lgkmcnt(0)\n\tv_readfirstlane_b32 %1, %0\n\ts_cmp_lg_u32 %1, %3\n\ts_cbranch_scc1 1b"
               : "=&v"(v), "=&s"(sv) : "v"(a), "s"(want), "i"(SLP) : "scc", "memory");
}

__device__ __forceinline__ void proj_epilogue(f32x4 acc0, f32x4 acc1, bool z, float& v0, float& v1) {
  float n0[4], n1[4];
#pragma unroll
  for (int r = 0; r < 4; ++r) {
    auto s = __builtin_amdgcn_permlane32_swap(__float_as_uint(acc0[r]), __float_as_uint(acc1[r]), false, false);
    n0[r] = __uint_as_float(s[0]); n1[r] = __uint_as_float(s[1]);
  }
  const float r0 = n0[0] + dpp<ROR8>(n1[0]), r1 = n0[1] + dpp<ROR8>(n1[1]);
  const float r2 = n1[2] + dpp<ROR8>(n0[2]), r3 = n1[3] + dpp<ROR8>(n0[3]);
  v0 = z ? r2 : r0; v1 = z ? r3 : r1;
}

__global__ void __launch_bounds__(512, 2)
fused_decode(const u16* x_, const u16* kc_, const u16* vc_, const u16* wq_, const u16* wo_, const u16* wg_, const u16* wu_, const u16* wd_, u16* out_, unsigned char* ws_
             ) {
  constexpr int tn0 = TEND0, tn1 = TEND1, tn2 = H1DELAY, tn3 = H2DELAY, tn4 = BDELAY, tn5 = RATE16, tn6 = SMP0, tn7 = WT0, tn8 = SMP1, tn9 = WT1, tn10 = BIAS1, tn11 = SDELAY, tn12 = TPOLL, tn13 = TFIN;
  __shared__ __attribute__((aligned(16))) float lds[96 + 8 + 4 + 4 + 20 + 8 * 192];
  unsigned* l_q = (unsigned*)lds;
  float* l_ml = lds + 96;
  float* l_c = lds + 104;
  unsigned* l_est = (unsigned*)(lds + 108);
  unsigned char* l_f = (unsigned char*)(lds + 112);
  constexpr int FA = 0, FB = 4, FC = 8, FD = 12, FE = 16, FF = 20, FG = 24;
  unsigned* l_tend = (unsigned*)(lds + 119);
  const unsigned fbase = (unsigned)(size_t)(lbyte*)l_f;
  float* l_acc = lds + 132;

  STAMP_DECL;
  STAMP(0);
  unsigned amp0, ampv = 0;
  { unsigned long long t_; asm volatile("s_memrealtime %0\n\ts_waitcnt lgkmcnt(0)" : "=s"(t_) :: "memory"); amp0 = (unsigned)t_; }
#define AMPNOW(k, base) do { if (AMP == (k)) { SB(); unsigned long long t_; asm volatile("s_memrealtime %0\n\ts_waitcnt lgkmcnt(0)" : "=s"(t_) :: "memory"); ampv = (unsigned)t_ - (base); SB(); } } while (0)
  typedef unsigned long long u64;
  typedef u64 u64x8 __attribute__((ext_vector_type(8)));
  typedef u64 u64x2 __attribute__((ext_vector_type(2)));
  u64x8 ka; u64x2 kb; u64 tst;
  u32x4 fw[16];
  const int bid = blockIdx.x, tid = threadIdx.x;
  {
    unsigned v1, v2;
    asm volatile("s_load_dwordx16 s[36:51], %18, 0x0\n\ts_load_dwordx4 s[52:55], %18, 0x40\n\ts_memrealtime s[56:57]\n\t"
               "v_readfirstlane_b32 s14, %20\n\t"
               "s_lshr_b32 s15, %19, 3\n\t"
               "s_cmpk_ge_u32 s14, 0xc0\n\ts_cbranch_scc1 1f\n\t"
               "s_setprio 2\n\t"
               "s_and_b32 s16, %19, 7\n\t"
               "s_lshr_b32 s17, %19, 6\n\ts_lshl2_add_u32 s17, s16, s17\n\t"
               "s_and_b32 s18, s15, 7\n\ts_lshr_b32 s19, s14, 6\n\ts_lshl1_add_u32 s18, s18, s19\n\t"
               "s_lshr_b32 s19, %19, 4\n\t"
               "v_bfe_u32 %16, %20, 3, 3\n\tv_and_b32 %17, 7, %20\n\t"
               "s_movk_i32 s13, 0x3000\n\t"
               "s_cmpk_lt_u32 s14, 0x80\n\t"
               "s_cselect_b32 s18, s18, s19\n\ts_cselect_b32 s17, s17, s16\n\ts_cselect_b32 s13, s13, 0xc00\n\t"
               "s_mul_i32 s19, s18, 11\n\ts_lshr_b32 s19, s19, 5\n\t"
               "v_mad_u32_u24 %16, %16, s13, %17\n\t"
               "s_sub_u32 s17, s17, s19\n\ts_mul_i32 s17, s17, 3\n\ts_add_u32 s17, s17, s18\n\t"
               "s_mul_i32 s19, s19, s13\n\ts_add_u32 s12, s17, s19\n\ts_lshl_b32 s12, s12, 7\n\t"
               "v_lshlrev_b32 %16, 4, %16\n\t"
               "s_mul_i32 s10, s13, 0x280\n\ts_mov_b32 s11, 0x20000\n\ts_cmp_eq_u32 s18, 15\n\ts_cselect_b32 s10, 0, s10\n\t"
               "s_bitcmp1_b32 s15, 0\n\t"
               "s_waitcnt lgkmcnt(0)\n\t"
               "s_cselect_b64 s[8:9], s[48:49], s[46:47]\n\t"
               "s_cmpk_lt_u32 s14, 0x80\n\ts_cselect_b64 s[8:9], s[42:43], s[8:9]\n\t"
               "s_and_b32 s9, s9, 0xffff\n\t"
               "buffer_load_dwordx4 %0, %16, s[8:11], s12 offen nt\n\t" "s_add_u32 s12, s12, s13\n\t"
               "buffer_load_dwordx4 %1, %16, s[8:11], s12 offen nt\n\t" "s_add_u32 s12, s12, s13\n\t"
               "buffer_load_dwordx4 %2, %16, s[8:11], s12 offen nt\n\t" "s_add_u32 s12, s12, s13\n\t"
               "buffer_load_dwordx4 %3, %16, s[8:11], s12 offen nt\n\t" "s_add_u32 s12, s12, s13\n\t"
               "buffer_load_dwordx4 %4, %16, s[8:11], s12 offen nt\n\t" "s_add_u32 s12, s12, s13\n\t"
               "buffer_load_dwordx4 %5, %16, s[8:11], s12 offen nt\n\t" "s_add_u32 s12, s12, s13\n\t"
               "buffer_load_dwordx4 %6, %16, s[8:11], s12 offen nt\n\t" "s_add_u32 s12, s12, s13\n\t"
               "buffer_load_dwordx4 %7, %16, s[8:11], s12 offen nt\n\t" "s_add_u32 s12, s12, s13\n\t"
               "buffer_load_dwordx4 %8, %16, s[8:11], s12 offen nt\n\t" "s_add_u32 s12, s12, s13\n\t"
               "buffer_load_dwordx4 %9, %16, s[8:11], s12 offen nt\n\t" "s_add_u32 s12, s12, s13\n\t"
               "buffer_load_dwordx4 %10, %16, s[8:11], s12 offen nt\n\t" "s_add_u32 s12, s12, s13\n\t"
               "buffer_load_dwordx4 %11, %16, s[8:11], s12 offen nt\n\t" "s_add_u32 s12, s12, s13\n\t" "s_barrier\n\t"
               "buffer_load_dwordx4 %12, %16, s[8:11], s12 offen nt\n\t" "s_add_u32 s12, s12, s13\n\t"
               "buffer_load_dwordx4 %13, %16, s[8:11], s12 offen nt\n\t" "s_add_u32 s12, s12, s13\n\t"
               "buffer_load_dwordx4 %14, %16, s[8:11], s12 offen nt\n\t" "s_add_u32 s12, s12, s13\n\t"
               "buffer_load_dwordx4 %15, %16, s[8:11], s12 offen nt\n\t"
               "s_branch 2f\n"
               "1:\n\t"
               "v_and_b32 %16, 15, %20\n\tv_lshl_add_u32 %16, %16, 2, %21\n\tv_mov_b32 %17, 0\n\tds_write_b32 %16, %17\n\t"
               "s_waitcnt lgkmcnt(0)\n\ts_barrier\n"
               "2:"
               : "=&v"(fw[0]), "=&v"(fw[1]), "=&v"(fw[2]), "=&v"(fw[3]), "=&v"(fw[4]), "=&v"(fw[5]), "=&v"(fw[6]), "=&v"(fw[7]),
                 "=&v"(fw[8]), "=&v"(fw[9]), "=&v"(fw[10]), "=&v"(fw[11]), "=&v"(fw[12]), "=&v"(fw[13]), "=&v"(fw[14]), "=&v"(fw[15]),
                 "=&v"(v1), "=&v"(v2)
               : "s"(__builtin_amdgcn_kernarg_segment_ptr()), "s"(bid), "v"(tid), "s"(fbase - 16)
               : "s8", "s9", "s10", "s11", "s12", "s13", "s14", "s15", "s16", "s17", "s18", "s19", "scc", "memory",
                 "s36", "s37", "s38", "s39", "s40", "s41", "s42", "s43", "s44", "s45", "s46", "s47", "s48", "s49", "s50", "s51", "s52", "s53", "s54", "s55", "s56", "s57");
    asm volatile("" : "={s[36:51]}"(ka), "={s[52:55]}"(kb), "={s[56:57]}"(tst));
  }
  const int xq = bid & 7, uq = bid >> 3;
  const int h = xq * 4 + (uq >> 3), j8 = uq & 7, b = j8 >> 2, s = j8 & 3;
  const int lane = tid & 63, wave = __builtin_amdgcn_readfirstlane(tid >> 6);
  const int g = lane >> 3, j = lane & 7;
  const int bl = (lane >> 2) & 1, e4 = lane & 3;
  const bool isS = wave >= 4;
  const u16 *x = (const u16*)ka[0], *kc = (const u16*)ka[1], *vc = (const u16*)ka[2], *wo = (const u16*)ka[4], *wd = (const u16*)ka[7];
  u16* out = (u16*)kb[0];
  unsigned char* ws = (unsigned char*)kb[1];
  const unsigned tstart = (unsigned)tst;
  unsigned tag, ep;
  unsigned cp0, cp1;
  {
    u64 pc;
    asm volatile("s_getpc_b64 %0" : "=s"(pc));
    unsigned pco = (((wave + 5) & 7) * 32 + uq) * 128;
    pco = pco < CODEPF - 1024 ? pco : 0;
    asm volatile("s_load_dword %0, %3, 0x0\n\ts_load_dword %1, %4, %5\n\ts_load_dword %2, %4, %6" : "=&s"(ep), "=&s"(cp0), "=&s"(cp1) : "s"(ws), "s"(pc & ~127ull), "s"(pco), "s"(pco + 64));
  }
#define TAG_READY() asm volatile("s_waitcnt lgkmcnt(0)\n\ts_add_u32 %0, %1, 1" : "=s"(tag) : "s"(ep), "s"(cp0), "s"(cp1) : "scc")
  const unsigned ampx = ampv;
  FS(1);
  const rsrc_t rws = mk_rsrc(ws, WS_END);
  const unsigned kv0 = ((b * 32 + h) * 8192 + 6144 + 512 * s) * 384u;

  if (isS) {
    const int sw = wave - 4;
    const rsrc_t r0 = mk_rsrc(kc, 2 * 32 * 8192 * 384), rv = mk_rsrc(vc, 2 * 32 * 8192 * 384);
    const int voff = kv0 + KVW * sw * 384 + g * KVG + 16 * j;
    u32x4 buf[96];
    u32x4 qv[3];
    float sc[16];
    unsigned pd[16];
    f32x4 acc[3] = {{0.f, 0.f, 0.f, 0.f}, {0.f, 0.f, 0.f, 0.f}, {0.f, 0.f, 0.f, 0.f}};
    unsigned msk[4];
#pragma unroll
    for (int d = 0; d < 4; ++d) msk[d] = (j >> 1) == d ? ((j & 1) ? 0xffff0000u : 0x0000ffffu) : 0u;
    constexpr float C2 = 0.07216878364870322f * 1.4426950408889634f;
    unsigned tend = 0, tgt = 0, tendv = 0;
    if (SLATE) { unsigned kt; asm volatile("s_load_dword %0, %1, %2\n\ts_waitcnt lgkmcnt(0)" : "=&s"(kt) : "s"(kc), "s"(kv0)); }
    const int stag = ((2 * sw - 3) * STAG) >> 1;
    if (PACING) { tgt = SLATE ? tstart + tn11 + stag : tstart; tend = tstart + tn0 + stag; }

    auto consume = [&](auto G) {
      constexpr int t = decltype(G)::value;
      if constexpr (t < 16) {
        float a0 = 0.f, a1 = 0.f, a2 = 0.f;
#pragma unroll
        for (int e = 0; e < 4; ++e) { a0 = dot2(buf[3 * t][e], qv[0][e], a0); a1 = dot2(buf[3 * t + 1][e], qv[1][e], a1); a2 = dot2(buf[3 * t + 2][e], qv[2][e], a2); }
        float a = (a0 + a1) + a2;
        a += dpp<XOR1>(a); a += dpp<XOR2>(a); a += dpp<HMIR>(a);
        sc[t] = a;
        if constexpr (t == 15) {
          float m = sc[0];
#pragma unroll
          for (int u = 1; u < 16; ++u) m = fmaxf(m, sc[u]);
          m = fmaxf(m, dpp<ROR8>(m)); m = x16_max(m); m = x32_max(m);
          const float mc = m * C2;
          float l = 0.f;
#pragma unroll
          for (int u = 0; u < 16; ++u) { const float p = __builtin_amdgcn_exp2f(fmaf(sc[u], C2, -mc)); pd[u] = cvtpk(p, p); l += bfhi(pd[u]); }
          l += dpp<ROR8>(l); l = x16_sum(l); l = x32_sum(l);
          if (lane == 0) { *(f32x2*)(l_ml + 2 * sw) = f32x2{mc, l}; MEMFENCE(); l_f[FA + sw] = 1; }
          STAMP(3);
        }
      } else {
        constexpr int u = t - 16;
        const u32x4 a = {pd[u] & msk[0], pd[u] & msk[1], pd[u] & msk[2], pd[u] & msk[3]};
#pragma unroll
        for (int i = 0; i < 3; ++i) acc[i] = mfma32(a, buf[3 * t + i], acc[i]);
      }
    };
    constexpr auto done_after = [](int n) constexpr {
      if (n < B1_AT) return 0;
      const int landed = (n - WIN + 1) / 3;
      const int cu = 1 + (CATCH * (n - B1_AT)) / 3;
      return landed < cu ? landed : cu;
    };
    static_for<(SLATE ? 0 : 9), 96>([&](auto N) {
      constexpr int n = decltype(N)::value;
      if constexpr (n == B1_AT) {
        STAMP(1);
        wait_lds<0>(fbase + FG, 0x00010101u);
        STAMP(2);
#pragma unroll
        for (int i = 0; i < 3; ++i) qv[i] = *(const u32x4*)(l_q + 32 * i + 4 * j);
      }
      if constexpr (n >= WIN) asm volatile("s_waitcnt vmcnt(%0)" ::"i"(WIN - 1) : "memory");
      if constexpr (n == 9) TAG_READY();
      if constexpr (n >= 9 && n < 9 + 45 && (n - 9) % 5 == 0) FS(2 + (n - 9) / 5);
      if constexpr (PACING && n % 3 == 0) {
        constexpr int k = n / 3;
        if constexpr (PACING == 2 && k > 12) { const unsigned tv = __builtin_amdgcn_readfirstlane(tendv); tend = tv ? tv + stag : tend; }
        const unsigned now = pace_step<(k < 31 ? 65536 / (31 - k) : 0)>(tgt, tend);
        if constexpr (PACING == 2 && k >= 6 && k < 26) {
          unsigned vm;
          asm volatile("s_getreg_b32 %0, hwreg(HW_REG_IB_STS, 0, 4)" : "=s"(vm));
          l_est[sw] = (now + (((vm + 3 * (31 - k)) * tn5) >> 4)) | 1u;
        }
        if constexpr (PACING == 2 && k >= 12 && k < 30) tendv = *l_tend;
      }
      if constexpr (n == FB_AT) { if (lane == 0) l_f[FB + sw] = 1; }
      constexpr int m = n < 48 ? n : n - 48;
      buf[n] = ld_nt(n < 48 ? r0 : rv, voff + (m % 3) * 128, (m / 3) * KVT);
      SB();
      static_for<done_after(n > 0 ? n - 1 : 0), done_after(n)>(consume);
      SB();
    });
    STAMP(4);
    __builtin_amdgcn_s_setprio(3);
    wait_lds<0>(fbase + FE, 0x01010101u);
    float cq = l_c[lane >> 4];
    unsigned dst = (unsigned)(size_t)(lfloat*)(l_acc + (sw * 2 + (g & 1)) * 192 + 8 * j + 4 * ((lane >> 4) & 1));
    unsigned src = (unsigned)(size_t)(lfloat*)(l_acc + (lane >> 4) * 384 + 64 * sw + 8 * j + 4 * (g & 1));
    unsigned fla = fbase + FC + sw, one = 1u, flr = fbase + FC;
    int pvo = (lane & 16) ? OOB : (g & 1) * 128 + j * 16 + (lane >> 5) * 8;
    int pso = WS_OP + ((h * 2 + b) * 4 + s) * 768 + sw * 256;
    unsigned tagv = tag;
    asm volatile("" : "+v"(cq), "+v"(dst), "+v"(src), "+v"(fla), "+v"(flr), "+v"(one), "+v"(pvo), "+s"(pso), "+v"(tagv));
    const bool wr = (lane >> 5) == (g & 1);
    SB();
    static_for<done_after(95), 32>([&](auto G) { consume(G); SB(); });
    if (wr) {
#pragma unroll
      for (int i = 0; i < 3; ++i) *(lf32x4*)(size_t)(dst + 256 * i) = acc[i];
      MEMFENCE();
      *(lbyte*)(size_t)fla = (unsigned char)one;
    }
    STAMP(5);
    if (sw < 3) {
      f32x4 u0, u1;
      unsigned fv, fs;
      asm volatile("1:\n\tds_read_b32 %0, %4\n\tds_read_b128 %2, %5\n\tds_read_b128 %3, %5 offset:768\n\ts_waitcnt lgkmcnt(2)\n\tv_readfirstlane_b32 %1, %0\n\t"
                   "s_cmp_lg_u32 %1, 0x01010101\n\ts_waitcnt lgkmcnt(0)\n\ts_cbranch_scc1 1b"
                   : "=&v"(fv), "=&s"(fs), "=&v"(u0), "=&v"(u1) : "v"(flr), "v"(src) : "scc", "memory");
      float o[4];
#pragma unroll
      for (int e = 0; e < 4; ++e) o[e] = cq * (u0[e] + u1[e]);
      const auto s0 = __builtin_amdgcn_permlane32_swap(__float_as_uint(o[0]), __float_as_uint(o[2]), false, false);
      const auto s1 = __builtin_amdgcn_permlane32_swap(__float_as_uint(o[1]), __float_as_uint(o[3]), false, false);
      const float p0 = x16_sum(__uint_as_float(s0[0]) + __uint_as_float(s0[1])), p1 = x16_sum(__uint_as_float(s1[0]) + __uint_as_float(s1[1]));
      const u32x2 pv = {cvtpk(p0, p1), tagv};
      __builtin_amdgcn_raw_buffer_store_b64(pv, rws, pvo, pso, AUX_SC1);
    }
    MEMFENCE();
    *(lbyte*)(size_t)(fla + (FD - FC)) = (unsigned char)one;
    STAMP(6);
    STAMP_FLUSH();
    return;
  }

  __builtin_amdgcn_s_setprio(2);
  const int w = wave;
  const unsigned idx = w < 2 ? 2 * j8 + w : uq >> 1, ur = (idx * 11) >> 5, uc = idx - 3 * ur, up = uq & 1;
  const bool has_front = (w < 3) & (idx < 15);
  const unsigned m0 = e4 == 0 ? 0x0000ffffu : e4 == 1 ? 0xffff0000u : 0u;
  const unsigned m1 = e4 == 2 ? 0x0000ffffu : e4 == 3 ? 0xffff0000u : 0u;
  const int st_col = (8 * j + 4 * (lane >> 5) + 2 * (g & 1)) * 8;
  const rsrc_t rx = mk_rsrc(x, 2560);
  if (has_front) {
    const int xo = (lane >> 5) * 1280 + (lane & 31) * 16;
    const bool x3 = (lane & 16) == 0;
    const u32x4 xa = __builtin_amdgcn_raw_buffer_load_b128(rx, xo, 0, 0), xb = __builtin_amdgcn_raw_buffer_load_b128(rx, xo + 512, 0, 0);
    const u32x4 xc = __builtin_amdgcn_raw_buffer_load_b128(rx, x3 ? xo + 1024 : OOB, 0, 0);
    const u32x4 xr0 = __builtin_amdgcn_raw_buffer_load_b128(rx, bl * 1280 + g * 32, 256 * ur, 0);
    const u32x4 xr1 = __builtin_amdgcn_raw_buffer_load_b128(rx, bl * 1280 + g * 32 + 16, 256 * ur, 0);
    SB();
    FS(2);
    TAG_READY();
    float v[24];
#pragma unroll
    for (int e = 0; e < 4; ++e) { v[2 * e] = bflo(xa[e]); v[2 * e + 1] = bfhi(xa[e]); v[8 + 2 * e] = bflo(xb[e]); v[9 + 2 * e] = bfhi(xb[e]); v[16 + 2 * e] = bflo(xc[e]); v[17 + 2 * e] = bfhi(xc[e]); }
    float sum = 0.f;
#pragma unroll
    for (int e = 0; e < 24; ++e) sum += v[e];
    sum = x16_sum(row_sum(sum));
    const float mean = sum * (1.f / 640.f);
    float sq = 0.f, sq3 = 0.f;
#pragma unroll
    for (int e = 0; e < 16; ++e) { const float d = v[e] - mean; sq = fmaf(d, d, sq); }
#pragma unroll
    for (int e = 16; e < 24; ++e) { const float d = v[e] - mean; sq3 = fmaf(d, d, sq3); }
    sq = x16_sum(row_sum(sq + (x3 ? sq3 : 0.f)));
    const float mb = rbf(mean), den = rbf(__builtin_amdgcn_sqrtf(rbf(rbf(sq * (1.f / 640.f)) + 1e-5f)));
    const float inv = __builtin_amdgcn_rcpf(den);
    const float mb0 = __builtin_bit_cast(float, __builtin_amdgcn_readlane(__builtin_bit_cast(int, mb), 0));
    const float mb1 = __builtin_bit_cast(float, __builtin_amdgcn_readlane(__builtin_bit_cast(int, mb), 32));
    const float iv0 = __builtin_bit_cast(float, __builtin_amdgcn_readlane(__builtin_bit_cast(int, inv), 0));
    const float iv1 = __builtin_bit_cast(float, __builtin_amdgcn_readlane(__builtin_bit_cast(int, inv), 32));
    const float mm = bl ? mb1 : mb0, ii = bl ? iv1 : iv0;
    FS(3);
    asm volatile("s_waitcnt vmcnt(0)" : "+v"(fw[0]), "+v"(fw[1]), "+v"(fw[2]), "+v"(fw[3]), "+v"(fw[4]), "+v"(fw[5]), "+v"(fw[6]), "+v"(fw[7]),
                 "+v"(fw[8]), "+v"(fw[9]), "+v"(fw[10]), "+v"(fw[11]), "+v"(fw[12]), "+v"(fw[13]), "+v"(fw[14]), "+v"(fw[15]));
    f32x4 acc0 = {0.f, 0.f, 0.f, 0.f}, acc1 = {0.f, 0.f, 0.f, 0.f};
#pragma unroll
    for (int n = 0; n < 16; ++n) {
      const unsigned xw = n < 8 ? xr0[n >> 1] : xr1[(n - 8) >> 1];
      const float xv = (n & 1) ? bfhi(xw) : bflo(xw);
      const float xn = rbf(xv - mm) * ii;
      const unsigned d = cvtpk(xn, xn), a0 = d & m0, a1 = d & m1;
      acc0 = mfma16(a0, a1, fw[n][0], fw[n][1], acc0);
      acc1 = mfma16(a0, a1, fw[n][2], fw[n][3], acc1);
    }
    float v0, v1;
    proj_epilogue(acc0, acc1, g & 1, v0, v1);
    const int rec = (w < 2 ? WS_QP + (h * 5 + ur) * 3072 : WS_GU + ((xq * 5 + ur) * 2 + up) * 3072) + uc * 512;
    st_pair(rws, ((lane >> 4) & 1) * 1536 + st_col, rec, v0, v1, tag);
    STAMP(1); FS(4);
    asm volatile("s_waitcnt vmcnt(0)" ::: "memory");
    STAMP(11); FS(5);
  } else {
    asm volatile("s_waitcnt vmcnt(0)" ::"v"(fw[0]), "v"(fw[1]), "v"(fw[2]), "v"(fw[3]), "v"(fw[4]), "v"(fw[5]), "v"(fw[6]), "v"(fw[7]),
                 "v"(fw[8]), "v"(fw[9]), "v"(fw[10]), "v"(fw[11]), "v"(fw[12]), "v"(fw[13]), "v"(fw[14]), "v"(fw[15]));
    if (w == 3) {
      if (PRETOUCH) {
        wait_until(tstart + PRETOUCH);
        const unsigned kv1 = kv0 + 512 * 384 - 64, o0 = h * (192 * 1280), o1 = o0 + 192 * 1280 - 64, d0_ = h * (48 * 1280);
        unsigned t0, t1, t2, t3_, t4, t5, t6, t7, t8;
        asm volatile("s_load_dword %0, %9, %14\n\ts_load_dword %1, %10, %15\n\ts_load_dword %2, %10, %14\n\ts_load_dword %3, %11, %16\n\ts_load_dword %4, %11, %17\n\t"
                     "s_load_dword %5, %12, %18\n\ts_load_dword %6, %13, 0x0\n\ts_load_dword %7, %19, %20\n\ts_load_dword %8, %19, %21\n\ts_waitcnt lgkmcnt(0)"
                     : "=&s"(t0), "=&s"(t1), "=&s"(t2), "=&s"(t3_), "=&s"(t4), "=&s"(t5), "=&s"(t6), "=&s"(t7), "=&s"(t8)
                     : "s"(kc), "s"(vc), "s"(wo), "s"(wd), "s"(out), "s"(kv1), "s"(kv0), "s"(o0), "s"(o1), "s"(d0_), "s"(ws), "s"(WS_END), "s"(512));
      }
    }
    TAG_READY();
  }
  const int slot = w * 8 + j8;
  const bool has_back = slot < 30;
  const int t3 = slot / 10, cc = slot - 10 * t3;
  const int rb = bid / 80, rc = (bid - 80 * rb) >> 3, ro = bid & 7;
  if (w == 0) { const u32x2 pv = {AMP >= 9 ? ampx : amp0, tag}; __builtin_amdgcn_raw_buffer_store_b64(pv, rws, (lane == 0 && bid < 64) ? bid * 8 : OOB, WS_TS + 512, AUX_SC1); }
  const bool reducer = (w == 0) && (bid < 160);
  unsigned ha[2][2] = {{0u, 0u}, {0u, 0u}};
        unsigned tend = tstart + tn0;
        auto pace_round = [&](unsigned t_sample, unsigned t_read, int so, int bias) {
          wait_until(t_sample);
          u32x4 e;
          do { MEMFENCE(); e = *(const u32x4*)l_est; } while (!__builtin_amdgcn_readfirstlane(e[0] & e[1] & e[2] & e[3] & 1u));
          const u32x2 pv = {tstart + (unsigned)((int)((e[0] - tstart) + (e[1] - tstart) + (e[2] - tstart) + (e[3] - tstart)) >> 2), tag};
          __builtin_amdgcn_raw_buffer_store_b64(pv, rws, (lane == 0 && bid < 32) ? bid * 8 : OOB, so, AUX_SC1);
          wait_until(t_read);
          MEMFENCE();
          const u32x2 tv = __builtin_amdgcn_raw_buffer_load_b64(rws, (lane & 31) * 8, so, AUX_SC1);
          if (__all(tv[1] == tag)) {
            const float d = x16_sum(row_sum((float)(int)(tv[0] - tstart)));
            int di = (int)(d * (1.f / 32.f)) - bias;
            di = di < 0 ? 0 : (di > 2500 ? 2500 : di);
            tend = (tstart + __builtin_amdgcn_readfirstlane(di)) | 1u;
            *l_tend = tend;
          }
        };
        if (w < 3) {
          if (has_front) __builtin_amdgcn_s_sleep(QSLEEP); else wait_until(tstart + 420);
          u32x4 t[3];
          const int vo = (lane >> 5) * 3072 + (lane & 31) * 16, so = WS_QP + h * 5 * 3072 + b * 1536 + w * 512;
          for (unsigned spin = 0;; ++spin) {
            MEMFENCE();
            t[0] = ld_sc1(rws, vo, so); t[1] = ld_sc1(rws, vo, so + 6144); t[2] = ld_sc1(rws, lane < 32 ? vo : OOB, so + 12288);
            const bool ok = (t[0][1] == tag) & (t[0][3] == tag) & (t[1][1] == tag) & (t[1][3] == tag) & (((t[2][1] == tag) & (t[2][3] == tag)) | (lane >= 32));
            if (__all(ok) || spin > SPIN_CAP) { STAMPV(12, spin + 1); break; }
          }
          const float a = x32_sum((__uint_as_float(t[0][0]) + __uint_as_float(t[1][0])) + __uint_as_float(t[2][0]));
          const float c = x32_sum((__uint_as_float(t[0][2]) + __uint_as_float(t[1][2])) + __uint_as_float(t[2][2]));
          if (lane < 32) { l_q[32 * w + lane] = cvtpk(a, c); MEMFENCE(); l_f[FG + w] = 1; }
        } else if (PACING == 2) {
          pace_round(tstart + tn6, tstart + tn6 + tn7, WS_TS, tn1);
        } else {
          wait_until(tstart + 700);
        }
        STAMP(2); FS(6);
        if (has_back) {
          u32x4 t[3];
          const int so = WS_GU + xq * 5 * 6144 + (h & 3) * 384 + t3 * 128;
          const int vo = bl * 1536 + 16 * g;
          for (unsigned spin = 0;; ++spin) {
            MEMFENCE();
            t[0] = ld_sc1(rws, vo + e4 * 6144, so);
            t[1] = ld_sc1(rws, vo + e4 * 6144 + 3072, so);
            t[2] = ld_sc1(rws, vo + 4 * 6144 + (e4 & 1) * 3072, so);
            bool ok = true;
#pragma unroll
            for (int k = 0; k < 3; ++k) ok &= (t[k][1] == tag) & (t[k][3] == tag);
            if (__all(ok) || spin > SPIN_CAP) { STAMPV(13, spin + 1); break; }
          }
#pragma unroll
          for (int z = 0; z < 2; ++z) {
            const float ex = __uint_as_float(t[2][2 * z]);
            const float gg = rbf(quad_sum(__uint_as_float(t[0][2 * z]) + (e4 == 0 ? ex : 0.f)));
            const float uu = rbf(quad_sum(__uint_as_float(t[1][2 * z]) + (e4 == 1 ? ex : 0.f)));
            const float zz = 0.7978845608028654f * (gg + 0.044715f * gg * gg * gg);
            const float ge = rbf(gg * __builtin_amdgcn_rcpf(1.f + __builtin_amdgcn_exp2f(-2.8853900817779268f * zz)));
            const float hv = ge * uu;
            const unsigned d = cvtpk(hv, hv);
            ha[z][0] = d & m0; ha[z][1] = d & m1;
          }
        }
  const int oc = 4 * (lane & 1) + 2 * (lane >> 5) + ((lane >> 4) & 1);
  float xres = 0.f;
  if (reducer) xres = bflo(__builtin_amdgcn_raw_buffer_load_b16(rx, oc * 2, rb * 1280 + rc * 128 + ro * 16, 0));
  STAMP(3);
        if (w == 1) {
          wait_lds<4>(fbase + FA, 0x01010101u);
          const int so = WS_ML + (h * 2 + b) * 256;
          if (lane < 4) { const f32x2 ml = *(const f32x2*)(l_ml + 2 * lane); st_pair(rws, lane * 16, so + s * 64, ml[0], ml[1], tag); }
          asm volatile("s_waitcnt vmcnt(0)" ::: "memory");
          __builtin_amdgcn_s_sleep(MLSLEEP);
          u32x4 t;
          for (unsigned spin = 0;; ++spin) {
            MEMFENCE();
            t = ld_sc1(rws, (lane & 15) * 16, so);
            if (__all((t[1] == tag) & (t[3] == tag)) || spin > SPIN_CAP) break;
          }
          const float mv = __uint_as_float(t[0]);
          const float M = row_max(mv);
          const float f = __builtin_amdgcn_exp2f(mv - M);
          const float L = row_sum(f * __uint_as_float(t[2]));
          if ((lane >> 2) == s) { l_c[lane & 3] = f / L; MEMFENCE(); l_f[FE + (lane & 3)] = 1; }
        }
  if (PACING == 2 && w == 3 && tn8) {
    pace_round(tend - tn8, tend - tn8 + tn9, WS_TS + 256, tn10);
  }
  if (has_back) {
    const unsigned sLo = (e4 & 1) ? 0x01000c0cu : 0x0c0c0100u, sHi = (e4 & 1) ? 0x03020c0cu : 0x0c0c0302u;
    unsigned sel0 = e4 < 2 ? sLo : 0x0c0c0c0cu, sel1 = e4 < 2 ? 0x0c0c0c0cu : sLo, sel2 = e4 < 2 ? sHi : 0x0c0c0c0cu, sel3 = e4 < 2 ? 0x0c0c0c0cu : sHi;
    int op_so = WS_OP + h * 8 * 768 + t3 * 256, op_vo = (bl * 4 + e4) * 768 + 16 * g;
    int d_vo = ((48 * h + 16 * t3 + 2 * g) * 640 + 64 * cc) * 2 + 16 * j, o_vo = ((192 * h + 64 * t3 + (WOSEQ ? 1 : 8) * g) * 640 + 64 * cc) * 2 + 16 * j;
    int yvo = ((lane >> 4) & 1) * 256 + st_col / 2, yso = WS_YP + ((h * 3 + t3) * 10 + cc) * 512;
    int r_vo = (lane >> 1) * 5120 + (lane & 1) * 16 + WS_YP + rc * 512 + rb * 256 + ro * 32;
    int out_vo = (lane & 14) == 0 ? oc * 2 + rb * 1280 + rc * 128 + ro * 16 : OOB;
    unsigned tag2v = tag + 1u, tda = (unsigned)(size_t)(luint*)l_tend;
    asm volatile("" : "+v"(sel0), "+v"(sel1), "+v"(sel2), "+v"(sel3), "+s"(op_so), "+v"(op_vo), "+v"(d_vo), "+v"(o_vo), "+v"(yvo), "+s"(yso), "+v"(r_vo), "+v"(out_vo), "+v"(tag2v), "+v"(xres), "+v"(tda));
    int pass = DRYRUN ? (w + 3) >> 2 : 1;
    asm volatile("" : "+s"(pass));
#pragma nounroll
    for (; pass < 2; ++pass) {
      STAMP(4);
      const int live = pass ? -1 : 0;
      const rsrc_t rd = mk_rsrc(wd, 1536 * 1280 & live), ro_ = mk_rsrc(wo, 6144 * 1280 & live), rw2 = mk_rsrc(ws, WS_END & live), rout = mk_rsrc(out, 2560 & live);
      const unsigned want = tag & live, want2 = (tag + 1u) & live;
      unsigned fb = fbase + (pass ? FB : FF), fd = fbase + (pass ? FD : FF);
      asm volatile("" : "+v"(fb), "+v"(fd));
      wait_lds<1>(fb, 0x01010101u & live);
      const unsigned tfin = __builtin_amdgcn_readfirstlane(*(luint*)(size_t)tda) & live;
      delay_ticks(tn4 & live);
      u32x4 wb[10], t0, t1;
      unsigned fdv = 0;
#pragma unroll
      for (int n = 0; n < 10; ++n) {
        if (n == POLLPOS) {
          const rsrc_t rp = mk_rsrc(ws, __builtin_amdgcn_readfirstlane(fdv) == 0x01010101u ? (WS_END & live) : 0);
          t0 = ld_sc1(rp, op_vo, op_so); t1 = ld_sc1(rp, op_vo + 128, op_so);
          SB();
        }
        if (n < 2) wb[8 + n] = ld_nt(rd, d_vo + n * 1280, 0); else wb[n - 2] = ld_nt(ro_, o_vo, (n - 2) * (WOSEQ ? 10240 : 1280));
        if (n == POLLPOS - FDLOOK) { MEMFENCE(); fdv = *(luint*)(size_t)fd; }
        SB();
        if (n < 9 && BPACE) __builtin_amdgcn_s_sleep(BPACE);
        SB();
      }
      if (POLLPOS == 11) {
        if (pass) {
          wait_until(tfin ? tfin + tn12 - tn2 : tstart);
          wait_lds<1>(fd, 0x01010101u);
          delay_ticks(tn2);
        }
        t0 = ld_sc1(rw2, op_vo, op_so); t1 = ld_sc1(rw2, op_vo + 128, op_so);
        SB();
      }
      if (POLLPOS == 10) {
        const rsrc_t rp = mk_rsrc(ws, __builtin_amdgcn_readfirstlane(fdv) == 0x01010101u ? (WS_END & live) : 0);
        t0 = ld_sc1(rp, op_vo, op_so); t1 = ld_sc1(rp, op_vo + 128, op_so);
        SB();
      }
      STAMP(5);
      u32x4 bq[10];
#define PAIR(p) do { bq[2 * (p)] = u32x4{wb[2 * (p)][0], wb[2 * (p)][1], wb[2 * (p) + 1][0], wb[2 * (p) + 1][1]}; \
                     bq[2 * (p) + 1] = u32x4{wb[2 * (p)][2], wb[2 * (p)][3], wb[2 * (p) + 1][2], wb[2 * (p) + 1][3]}; } while (0)
      PAIR(4); PAIR(0); PAIR(1); PAIR(2);
      if (POLLPOS >= 10) PAIR(3);
      const u32x4 hA = {ha[0][0], ha[0][1], ha[1][0], ha[1][1]};
      f32x4 acc0 = mfma32(hA, bq[8], f32x4{0.f, 0.f, 0.f, 0.f}), acc1 = mfma32(hA, bq[9], f32x4{0.f, 0.f, 0.f, 0.f});
      asm volatile("" : "+v"(bq[0]), "+v"(bq[1]), "+v"(bq[2]), "+v"(bq[3]), "+v"(bq[4]), "+v"(bq[5]), "+v"(acc0), "+v"(acc1));
      if (POLLPOS >= 10) asm volatile("" : "+v"(bq[6]), "+v"(bq[7]));
      SB();
      unsigned np1 = 1;
      if (__builtin_expect(!__all((t0[1] == want) & (t0[3] == want) & (t1[1] == want) & (t1[3] == want)), 0)) {
        do {
          ++np1;
          wait_lds<1>(fd, 0x01010101u);
          delay_ticks(tn2);
          t0 = ld_sc1(rw2, op_vo, op_so); t1 = ld_sc1(rw2, op_vo + 128, op_so);
        } while (!__all((t0[1] == want) & (t0[3] == want) & (t1[1] == want) & (t1[3] == want)) && np1 < SPIN_CAP);
      }
      STAMPV(14, np1);
      STAMP(7);
      unsigned pk[4];
      {
        float u0, u1, u2, u3, u4, u5, u6, u7;
#define DPPADD(r, P) "v_add_f32_dpp " r ", " r ", " r " quad_perm:" P " row_mask:0xf bank_mask:0xf\n\t"
#define DPP8(P) DPPADD("%4", P) DPPADD("%5", P) DPPADD("%6", P) DPPADD("%7", P) DPPADD("%8", P) DPPADD("%9", P) DPPADD("%10", P) DPPADD("%11", P)
        asm volatile("v_lshlrev_b32 %4, 16, %12\n\tv_and_b32 %5, 0xffff0000, %12\n\tv_lshlrev_b32 %6, 16, %13\n\tv_and_b32 %7, 0xffff0000, %13\n\t"
                     "v_lshlrev_b32 %8, 16, %14\n\tv_and_b32 %9, 0xffff0000, %14\n\tv_lshlrev_b32 %10, 16, %15\n\tv_and_b32 %11, 0xffff0000, %15\n\t"
                     DPP8("[1,0,3,2]") DPP8("[2,3,0,1]")
                     "v_cvt_pk_bf16_f32 %0, %4, %5\n\tv_cvt_pk_bf16_f32 %1, %6, %7\n\tv_cvt_pk_bf16_f32 %2, %8, %9\n\tv_cvt_pk_bf16_f32 %3, %10, %11"
                     : "=&v"(pk[0]), "=&v"(pk[1]), "=&v"(pk[2]), "=&v"(pk[3]), "=&v"(u0), "=&v"(u1), "=&v"(u2), "=&v"(u3), "=&v"(u4), "=&v"(u5), "=&v"(u6), "=&v"(u7)
                     : "v"(t0[0]), "v"(t0[2]), "v"(t1[0]), "v"(t1[2]));
      }
      u32x4 aq[4];
#pragma unroll
      for (int p = 0; p < 4; ++p)
        aq[p] = u32x4{__builtin_amdgcn_perm(0u, pk[p], sel0), __builtin_amdgcn_perm(0u, pk[p], sel1), __builtin_amdgcn_perm(0u, pk[p], sel2), __builtin_amdgcn_perm(0u, pk[p], sel3)};
#pragma unroll
      for (int p = 0; p < 3; ++p) { acc0 = mfma32(aq[p], bq[2 * p], acc0); acc1 = mfma32(aq[p], bq[2 * p + 1], acc1); }
      if (POLLPOS < 10) { asm volatile("" : "+v"(aq[3]), "+v"(acc0), "+v"(acc1)); SB(); PAIR(3); }
      acc0 = mfma32(aq[3], bq[6], acc0); acc1 = mfma32(aq[3], bq[7], acc1);
      float v0, v1;
      proj_epilogue(acc0, acc1, g & 1, v0, v1);
      __builtin_amdgcn_raw_buffer_store_b64(u32x2{cvtpk(v0, v1), tag2v}, rw2, yvo, yso, AUX_SC1);
      STAMP(8);
      if (reducer) {
        if (pass) {
          const unsigned tn = (unsigned)__builtin_amdgcn_s_memrealtime();
          int wt = tfin ? (int)(tfin + tn13 - tn) : 40;
          asm("s_max_i32 %0, %0, %1\n\ts_min_i32 %0, %0, 0x64" : "+s"(wt) : "s"(tn3) : "scc");
          wait_until(tn + wt);
        }
        u32x4 t[3];
        unsigned np2 = 0;
        for (;;) {
          ++np2;
          MEMFENCE();
#pragma unroll
          for (int k = 0; k < 3; ++k) t[k] = ld_sc1(rw2, r_vo, k * 32 * 5120);
          bool ok = true;
#pragma unroll
          for (int k = 0; k < 3; ++k) ok &= (t[k][1] == want2) & (t[k][3] == want2);
          if (__all(ok) || np2 > SPIN_CAP) break;
        }
        STAMPV(15, np2);
        STAMP(9);
        const float y0 = (bflo(t[0][0]) + bflo(t[1][0])) + bflo(t[2][0]), y1 = (bfhi(t[0][0]) + bfhi(t[1][0])) + bfhi(t[2][0]);
        const float y2 = (bflo(t[0][2]) + bflo(t[1][2])) + bflo(t[2][2]), y3 = (bfhi(t[0][2]) + bfhi(t[1][2])) + bfhi(t[2][2]);
        const auto s0 = __builtin_amdgcn_permlane32_swap(__float_as_uint(y0), __float_as_uint(y2), false, false);
        const auto s1 = __builtin_amdgcn_permlane32_swap(__float_as_uint(y1), __float_as_uint(y3), false, false);
        const float z0 = __uint_as_float(s0[0]) + __uint_as_float(s0[1]), z1 = __uint_as_float(s1[0]) + __uint_as_float(s1[1]);
        const auto s2 = __builtin_amdgcn_permlane16_swap(__float_as_uint(z0), __float_as_uint(z1), false, false);
        float y = __uint_as_float(s2[0]) + __uint_as_float(s2[1]);
        y += dpp<ROR8>(y); y += dpp<ROR4>(y); y += dpp<XOR2>(y);
        __builtin_amdgcn_raw_buffer_store_b16((unsigned short)cvtpk(xres + y, 0.f), rout, out_vo, 0, AUX_OUT);
        if (bid == 0) __builtin_amdgcn_raw_buffer_store_b32(tag2v, rw2, lane == 0 ? WS_EPOCH : OOB, 0, AUX_SC1);
        STAMP(10);
        if (pass && bid == PBID) {
          if (AMP == 3) ampv = tfin - amp0 - 1500;
          AMPNOW(4, tfin + 300); AMPNOW(5, amp0 + 1800);
          if (AMP == 6) ampv = 10 * np2 + 40 * np1;
          if (AMP >= 7) {
            const u32x2 tv = __builtin_amdgcn_raw_buffer_load_b64(rws, lane * 8, WS_TS + 512, AUX_SC1);
            const float d = AMP >= 9 ? (float)tv[0] : (float)(int)(tv[0] - amp0);
            const float mx = x32_max(x16_max(row_max(d))), mn = -x32_max(x16_max(row_max(-d))), av = x32_sum(x16_sum(row_sum(d))) * (1.f / 64.f);
            int ai = (int)(AMP == 7 ? mx - mn : AMP == 8 ? av - mn : AMP == 9 ? mx : av);
            asm volatile("" : "+v"(ai));
            ampv = __builtin_amdgcn_readfirstlane(ai);
          }
          wait_until(amp0 + 4000 + 10 * ((int)ampv < 0 ? 0 : ampv));
        }
      }
    }
  }
  STAMP_FLUSH();
}

extern "C" void kernel_launch(void* const* d_in, const int* in_sizes, int n_in, void* d_out, int out_size, void* d_ws, size_t ws_size,
                              hipStream_t stream) {
  fused_decode<<<256, 512, 0, stream>>>((const u16*)d_in[0], (const u16*)d_in[1], (const u16*)d_in[2], (const u16*)d_in[3], (const u16*)d_in[4],
                                        (const u16*)d_in[6], (const u16*)d_in[7], (const u16*)d_in[8], (u16*)d_out, (unsigned char*)d_ws
                                        );
}
```

```cpp
#define PACING 2
#define CODEPF 16896
#define PRETOUCH 250
#define SCAT 0
#define STAG 11
#define POLLPOS 8
#define FDLOOK 1
#define H1DELAY 20
#define H2DELAY 40
#define SMP0 600
#define WT0 250
#define TFIN 0
#define SMP1 480
#define WT1 150
#define SLATE 1
#define TEND0 1490
#define TEND1 20
#define BIAS1 15
#define SDELAY 135
#define BDELAY 12
#define AMP 11
#include <hip/hip_runtime.h>
#include <hip/hip_bf16.h>
#include <stdint.h>
#include <type_traits>

typedef unsigned short u16;
typedef unsigned u32x4 __attribute__((ext_vector_type(4)));
typedef unsigned u32x2 __attribute__((ext_vector_type(2)));
typedef float f32x4 __attribute__((ext_vector_type(4)));
typedef float f32x2 __attribute__((ext_vector_type(2)));
typedef short s16x4 __attribute__((ext_vector_type(4)));
typedef __bf16 bf16x8 __attribute__((ext_vector_type(8)));
typedef __bf16 bf16x2 __attribute__((ext_vector_type(2)));
typedef __amdgpu_buffer_rsrc_t rsrc_t;

#define WIN 9
#define B1_AT 48
#define CATCH 3
#define QSLEEP 16
#define MLSLEEP 32
#define BPACE 5
#define H1SLEEP 0
#define KC 10
#define FB_AT 95
#define DRYRUN 1
#define RATE16 229
#define STOUCH 0
#define AUX_OUT 0
#define KVW 8
#define KVG 384
#define KVT 12288
#define WOSEQ 0
#define TPOLL 155
#define PBID 8

constexpr int AUX_NT = 2, AUX_SC1 = 16;
constexpr int RSRC_W3 = 0x00020000;
constexpr unsigned SPIN_CAP = 1u << 18;
constexpr int OOB = 0x40000000;

constexpr int WS_EPOCH = 0;
constexpr int WS_TS = 1024;
constexpr int WS_QP = 4096;
constexpr int WS_GU = WS_QP + 32 * 5 * 2 * 192 * 8;
constexpr int WS_ML = WS_GU + 8 * 5 * 2 * 2 * 192 * 8;
constexpr int WS_OP = WS_ML + 32 * 2 * 4 * 4 * 16;
constexpr int WS_YP = WS_QP;
constexpr int WS_END = WS_OP + 32 * 2 * 4 * 96 * 8;

template <int I, int N, class F> __device__ __forceinline__ void static_for(F&& f) {
  if constexpr (I < N) { f(std::integral_constant<int, I>{}); static_for<I + 1, N>(f); }
}
__device__ __forceinline__ rsrc_t mk_rsrc(const void* p, int bytes) {
  return __builtin_amdgcn_make_buffer_rsrc(const_cast<void*>(p), 0, bytes, RSRC_W3);
}
__device__ __forceinline__ u32x4 ld_nt(rsrc_t r, int voff, int soff) { return __builtin_amdgcn_raw_buffer_load_b128(r, voff, soff, AUX_NT); }
__device__ __forceinline__ u32x4 ld_sc1(rsrc_t r, int voff, int soff) { return __builtin_amdgcn_raw_buffer_load_b128(r, voff, soff, AUX_SC1); }
__device__ __forceinline__ void st_pair(rsrc_t r, int voff, int soff, float a, float b, unsigned tag) {
  u32x4 v = {__float_as_uint(a), tag, __float_as_uint(b), tag};
  __builtin_amdgcn_raw_buffer_store_b128(v, r, voff, soff, AUX_SC1);
}
__device__ __forceinline__ float bflo(unsigned v) { return __uint_as_float(v << 16); }
__device__ __forceinline__ float bfhi(unsigned v) { return __uint_as_float(v & 0xffff0000u); }
__device__ __forceinline__ unsigned cvtpk(float lo, float hi) {
  bf16x2 r = {(__bf16)lo, (__bf16)hi};
  return __builtin_bit_cast(unsigned, r);
}
__device__ __forceinline__ float rbf(float x) { return (float)(__bf16)x; }
template <int CTRL> __device__ __forceinline__ float dpp(float x) {
  return __builtin_bit_cast(float, __builtin_amdgcn_mov_dpp(__builtin_bit_cast(int, x), CTRL, 0xf, 0xf, true));
}
constexpr int XOR1 = 0xB1, XOR2 = 0x4E, HMIR = 0x141, ROR8 = 0x128, ROR4 = 0x124;
__device__ __forceinline__ float x16_sum(float x) { auto s = __builtin_amdgcn_permlane16_swap(__float_as_uint(x), __float_as_uint(x), false, false); return __uint_as_float(s[0]) + __uint_as_float(s[1]); }
__device__ __forceinline__ float x32_sum(float x) { auto s = __builtin_amdgcn_permlane32_swap(__float_as_uint(x), __float_as_uint(x), false, false); return __uint_as_float(s[0]) + __uint_as_float(s[1]); }
__device__ __forceinline__ float x16_max(float x) { auto s = __builtin_amdgcn_permlane16_swap(__float_as_uint(x), __float_as_uint(x), false, false); return fmaxf(__uint_as_float(s[0]), __uint_as_float(s[1])); }
__device__ __forceinline__ float x32_max(float x) { auto s = __builtin_amdgcn_permlane32_swap(__float_as_uint(x), __float_as_uint(x), false, false); return fmaxf(__uint_as_float(s[0]), __uint_as_float(s[1])); }
__device__ __forceinline__ float row_sum(float x) { x += dpp<XOR1>(x); x += dpp<XOR2>(x); x += dpp<HMIR>(x); x += dpp<ROR8>(x); return x; }
__device__ __forceinline__ float row_max(float x) { x = fmaxf(x, dpp<XOR1>(x)); x = fmaxf(x, dpp<XOR2>(x)); x = fmaxf(x, dpp<HMIR>(x)); x = fmaxf(x, dpp<ROR8>(x)); return x; }
__device__ __forceinline__ float quad_sum(float x) { x += dpp<XOR1>(x); x += dpp<XOR2>(x); return x; }
__device__ __forceinline__ float dot2(unsigned a, unsigned b, float c) {
  return __builtin_amdgcn_fdot2_f32_bf16(__builtin_bit_cast(bf16x2, a), __builtin_bit_cast(bf16x2, b), c, false);
}
__device__ __forceinline__ f32x4 mfma16(unsigned a0, unsigned a1, unsigned b0, unsigned b1, f32x4 c) {
  u32x2 a = {a0, a1}, b = {b0, b1};
  return __builtin_amdgcn_mfma_f32_16x16x16bf16_1k(__builtin_bit_cast(s16x4, a), __builtin_bit_cast(s16x4, b), c, 0, 0, 0);
}
__device__ __forceinline__ f32x4 mfma32(u32x4 a, u32x4 b, f32x4 c) {
  return __builtin_amdgcn_mfma_f32_16x16x32_bf16(__builtin_bit_cast(bf16x8, a), __builtin_bit_cast(bf16x8, b), c, 0, 0, 0);
}
template <int RECIP> __device__ __forceinline__ unsigned pace_step(unsigned& tgt, unsigned tend) {
  unsigned d, now;
  asm volatile("1:\n\ts_memrealtime vcc\n\ts_waitcnt lgkmcnt(0)\n\ts_sub_u32 %0, vcc_lo, %2\n\ts_cmp_lt_i32 %0, 0\n\ts_cbranch_scc1 1b\n\t"
               "s_sub_i32 %0, %3, vcc_lo\n\ts_max_i32 %0, %0, 0\n\ts_min_i32 %0, %0, 0x7d0\n\ts_mul_i32 %0, %0, %4\n\ts_lshr_b32 %0, %0, 16\n\t"
               "s_add_u32 %2, vcc_lo, %0\n\ts_mov_b32 %1, vcc_lo"
               : "=&s"(d), "=&s"(now), "+s"(tgt) : "s"(tend), "i"(RECIP) : "vcc", "scc");
  return now;
}
__device__ __forceinline__ void wait_until(unsigned tgt) {
  asm volatile("1:\n\ts_memrealtime vcc\n\ts_waitcnt lgkmcnt(0)\n\ts_sub_u32 vcc_lo, vcc_lo, %0\n\ts_cmp_lt_i32 vcc_lo, 0\n\ts_cbranch_scc1 1b" :: "s"(tgt) : "vcc", "scc", "memory");
}
#define SB() __builtin_amdgcn_sched_barrier(0)
#define WG_BARRIER() do { asm volatile("s_waitcnt lgkmcnt(0)\n\ts_barrier" ::: "memory"); } while (0)
#define MEMFENCE() asm volatile("" ::: "memory")

#define FS(i)
#define STAMP(i)
#define STAMPV(i, v)
#define STAMP_DECL
#define STAMP_FLUSH()

__device__ __forceinline__ void delay_ticks(int n) {
  if (n > 0) wait_until((unsigned)__builtin_amdgcn_s_memrealtime() + n);
}

typedef __attribute__((address_space(3))) float lfloat;
typedef __attribute__((address_space(3))) f32x4 lf32x4;
typedef __attribute__((address_space(3))) unsigned luint;
typedef __attribute__((address_space(3))) unsigned char lbyte;
template <int SLP> __device__ __forceinline__ void wait_lds(unsigned a, unsigned want) {
  unsigned v, sv;
  asm volatile("s_branch 2f\n1:\n\ts_sleep %4\n2:\n\tds_read_b32 %0, %2\n\ts_waitcnt lgkmcnt(0)\n\tv_readfirstlane_b32 %1, %0\n\ts_cmp_lg_u32 %1, %3\n\ts_cbranch_scc1 1b"
               : "=&v"(v), "=&s"(sv) : "v"(a), "s"(want), "i"(SLP) : "scc", "memory");
}

__device__ __forceinline__ void proj_epilogue(f32x4 acc0, f32x4 acc1, bool z, float& v0, float& v1) {
  float n0[4], n1[4];
#pragma unroll
  for (int r = 0; r < 4; ++r) {
    auto s = __builtin_amdgcn_permlane32_swap(__float_as_uint(acc0[r]), __float_as_uint(acc1[r]), false, false);
    n0[r] = __uint_as_float(s[0]); n1[r] = __uint_as_float(s[1]);
  }
  const float r0 = n0[0] + dpp<ROR8>(n1[0]), r1 = n0[1] + dpp<ROR8>(n1[1]);
  const float r2 = n1[2] + dpp<ROR8>(n0[2]), r3 = n1[3] + dpp<ROR8>(n0[3]);
  v0 = z ? r2 : r0; v1 = z ? r3 : r1;
}

__global__ void __launch_bounds__(512, 2)
fused_decode(const u16* x_, const u16* kc_, const u16* vc_, const u16* wq_, const u16* wo_, const u16* wg_, const u16* wu_, const u16* wd_, u16* out_, unsigned char* ws_
             ) {
  constexpr int tn0 = TEND0, tn1 = TEND1, tn2 = H1DELAY, tn3 = H2DELAY, tn4 = BDELAY, tn5 = RATE16, tn6 = SMP0, tn7 = WT0, tn8 = SMP1, tn9 = WT1, tn10 = BIAS1, tn11 = SDELAY, tn12 = TPOLL, tn13 = TFIN;
  __shared__ __attribute__((aligned(16))) float lds[96 + 8 + 4 + 4 + 20 + 8 * 192];
  unsigned* l_q = (unsigned*)lds;
  float* l_ml = lds + 96;
  float* l_c = lds + 104;
  unsigned* l_est = (unsigned*)(lds + 108);
  unsigned char* l_f = (unsigned char*)(lds + 112);
  constexpr int FA = 0, FB = 4, FC = 8, FD = 12, FE = 16, FF = 20, FG = 24;
  unsigned* l_tend = (unsigned*)(lds + 119);
  const unsigned fbase = (unsigned)(size_t)(lbyte*)l_f;
  float* l_acc = lds + 132;

  STAMP_DECL;
  STAMP(0);
  unsigned amp0, ampv = 0;
  { unsigned long long t_; asm volatile("s_memrealtime %0\n\ts_waitcnt lgkmcnt(0)" : "=s"(t_) :: "memory"); amp0 = (unsigned)t_; }
#define AMPNOW(k, base) do { if (AMP == (k)) { SB(); unsigned long long t_; asm volatile("s_memrealtime %0\n\ts_waitcnt lgkmcnt(0)" : "=s"(t_) :: "memory"); ampv = (unsigned)t_ - (base); SB(); } } while (0)
#define N8 "s_nop 0\n\ts_nop 0\n\ts_nop 0\n\ts_nop 0\n\ts_nop 0\n\ts_nop 0\n\ts_nop 0\n\ts_nop 0\n\t"
  asm volatile(N8 N8 N8 N8 N8 N8 N8 N8);
  AMPNOW(11, amp0);
  typedef unsigned long long u64;
  typedef u64 u64x8 __attribute__((ext_vector_type(8)));
  typedef u64 u64x2 __attribute__((ext_vector_type(2)));
  u64x8 ka; u64x2 kb; u64 tst;
  u32x4 fw[16];
  const int bid = blockIdx.x, tid = threadIdx.x;
  {
    unsigned v1, v2;
    asm volatile("s_load_dwordx16 s[36:51], %18, 0x0\n\ts_load_dwordx4 s[52:55], %18, 0x40\n\ts_memrealtime s[56:57]\n\t"
               "v_readfirstlane_b32 s14, %20\n\t"
               "s_lshr_b32 s15, %19, 3\n\t"
               "s_cmpk_ge_u32 s14, 0xc0\n\ts_cbranch_scc1 1f\n\t"
               "s_setprio 2\n\t"
               "s_and_b32 s16, %19, 7\n\t"
               "s_lshr_b32 s17, %19, 6\n\ts_lshl2_add_u32 s17, s16, s17\n\t"
               "s_and_b32 s18, s15, 7\n\ts_lshr_b32 s19, s14, 6\n\ts_lshl1_add_u32 s18, s18, s19\n\t"
               "s_lshr_b32 s19, %19, 4\n\t"
               "v_bfe_u32 %16, %20, 3, 3\n\tv_and_b32 %17, 7, %20\n\t"
               "s_movk_i32 s13, 0x3000\n\t"
               "s_cmpk_lt_u32 s14, 0x80\n\t"
               "s_cselect_b32 s18, s18, s19\n\ts_cselect_b32 s17, s17, s16\n\ts_cselect_b32 s13, s13, 0xc00\n\t"
               "s_mul_i32 s19, s18, 11\n\ts_lshr_b32 s19, s19, 5\n\t"
               "v_mad_u32_u24 %16, %16, s13, %17\n\t"
               "s_sub_u32 s17, s17, s19\n\ts_mul_i32 s17, s17, 3\n\ts_add_u32 s17, s17, s18\n\t"
               "s_mul_i32 s19, s19, s13\n\ts_add_u32 s12, s17, s19\n\ts_lshl_b32 s12, s12, 7\n\t"
               "v_lshlrev_b32 %16, 4, %16\n\t"
               "s_mul_i32 s10, s13, 0x280\n\ts_mov_b32 s11, 0x20000\n\ts_cmp_eq_u32 s18, 15\n\ts_cselect_b32 s10, 0, s10\n\t"
               "s_bitcmp1_b32 s15, 0\n\t"
               "s_waitcnt lgkmcnt(0)\n\t"
               "s_cselect_b64 s[8:9], s[48:49], s[46:47]\n\t"
               "s_cmpk_lt_u32 s14, 0x80\n\ts_cselect_b64 s[8:9], s[42:43], s[8:9]\n\t"
               "s_and_b32 s9, s9, 0xffff\n\t"
               "buffer_load_dwordx4 %0, %16, s[8:11], s12 offen nt\n\t" "s_add_u32 s12, s12, s13\n\t"
               "buffer_load_dwordx4 %1, %16, s[8:11], s12 offen nt\n\t" "s_add_u32 s12, s12, s13\n\t"
               "buffer_load_dwordx4 %2, %16, s[8:11], s12 offen nt\n\t" "s_add_u32 s12, s12, s13\n\t"
               "buffer_load_dwordx4 %3, %16, s[8:11], s12 offen nt\n\t" "s_add_u32 s12, s12, s13\n\t"
               "buffer_load_dwordx4 %4, %16, s[8:11], s12 offen nt\n\t" "s_add_u32 s12, s12, s13\n\t"
               "buffer_load_dwordx4 %5, %16, s[8:11], s12 offen nt\n\t" "s_add_u32 s12, s12, s13\n\t"
               "buffer_load_dwordx4 %6, %16, s[8:11], s12 offen nt\n\t" "s_add_u32 s12, s12, s13\n\t"
               "buffer_load_dwordx4 %7, %16, s[8:11], s12 offen nt\n\t" "s_add_u32 s12, s12, s13\n\t"
               "buffer_load_dwordx4 %8, %16, s[8:11], s12 offen nt\n\t" "s_add_u32 s12, s12, s13\n\t"
               "buffer_load_dwordx4 %9, %16, s[8:11], s12 offen nt\n\t" "s_add_u32 s12, s12, s13\n\t"
               "buffer_load_dwordx4 %10, %16, s[8:11], s12 offen nt\n\t" "s_add_u32 s12, s12, s13\n\t"
               "buffer_load_dwordx4 %11, %16, s[8:11], s12 offen nt\n\t" "s_add_u32 s12, s12, s13\n\t" "s_barrier\n\t"
               "buffer_load_dwordx4 %12, %16, s[8:11], s12 offen nt\n\t" "s_add_u32 s12, s12, s13\n\t"
               "buffer_load_dwordx4 %13, %16, s[8:11], s12 offen nt\n\t" "s_add_u32 s12, s12, s13\n\t"
               "buffer_load_dwordx4 %14, %16, s[8:11], s12 offen nt\n\t" "s_add_u32 s12, s12, s13\n\t"
               "buffer_load_dwordx4 %15, %16, s[8:11], s12 offen nt\n\t"
               "s_branch 2f\n"
               "1:\n\t"
               "v_and_b32 %16, 15, %20\n\tv_lshl_add_u32 %16, %16, 2, %21\n\tv_mov_b32 %17, 0\n\tds_write_b32 %16, %17\n\t"
               "s_waitcnt lgkmcnt(0)\n\ts_barrier\n"
               "2:"
               : "=&v"(fw[0]), "=&v"(fw[1]), "=&v"(fw[2]), "=&v"(fw[3]), "=&v"(fw[4]), "=&v"(fw[5]), "=&v"(fw[6]), "=&v"(fw[7]),
                 "=&v"(fw[8]), "=&v"(fw[9]), "=&v"(fw[10]), "=&v"(fw[11]), "=&v"(fw[12]), "=&v"(fw[13]), "=&v"(fw[14]), "=&v"(fw[15]),
                 "=&v"(v1), "=&v"(v2)
               : "s"(__builtin_amdgcn_kernarg_segment_ptr()), "s"(bid), "v"(tid), "s"(fbase - 16)
               : "s8", "s9", "s10", "s11", "s12", "s13", "s14", "s15", "s16", "s17", "s18", "s19", "scc", "memory",
                 "s36", "s37", "s38", "s39", "s40", "s41", "s42", "s43", "s44", "s45", "s46", "s47", "s48", "s49", "s50", "s51", "s52", "s53", "s54", "s55", "s56", "s57");
    asm volatile("" : "={s[36:51]}"(ka), "={s[52:55]}"(kb), "={s[56:57]}"(tst));
  }
  const int xq = bid & 7, uq = bid >> 3;
  const int h = xq * 4 + (uq >> 3), j8 = uq & 7, b = j8 >> 2, s = j8 & 3;
  const int lane = tid & 63, wave = __builtin_amdgcn_readfirstlane(tid >> 6);
  const int g = lane >> 3, j = lane & 7;
  const int bl = (lane >> 2) & 1, e4 = lane & 3;
  const bool isS = wave >= 4;
  const u16 *x = (const u16*)ka[0], *kc = (const u16*)ka[1], *vc = (const u16*)ka[2], *wo = (const u16*)ka[4], *wd = (const u16*)ka[7];
  u16* out = (u16*)kb[0];
  unsigned char* ws = (unsigned char*)kb[1];
  const unsigned tstart = (unsigned)tst;
  unsigned tag, ep;
  unsigned cp0, cp1;
  {
    u64 pc;
    asm volatile("s_getpc_b64 %0" : "=s"(pc));
    unsigned pco = (((wave + 5) & 7) * 32 + uq) * 128;
    pco = pco < CODEPF - 1024 ? pco : 0;
    asm volatile("s_load_dword %0, %3, 0x0\n\ts_load_dword %1, %4, %5\n\ts_load_dword %2, %4, %6" : "=&s"(ep), "=&s"(cp0), "=&s"(cp1) : "s"(ws), "s"(pc & ~127ull), "s"(pco), "s"(pco + 64));
  }
#define TAG_READY() asm volatile("s_waitcnt lgkmcnt(0)\n\ts_add_u32 %0, %1, 1" : "=s"(tag) : "s"(ep), "s"(cp0), "s"(cp1) : "scc")
  const unsigned ampx = ampv;
  FS(1);
  const rsrc_t rws = mk_rsrc(ws, WS_END);
  const unsigned kv0 = ((b * 32 + h) * 8192 + 6144 + 512 * s) * 384u;

  if (isS) {
    const int sw = wave - 4;
    const rsrc_t r0 = mk_rsrc(kc, 2 * 32 * 8192 * 384), rv = mk_rsrc(vc, 2 * 32 * 8192 * 384);
    const int voff = kv0 + KVW * sw * 384 + g * KVG + 16 * j;
    u32x4 buf[96];
    u32x4 qv[3];
    float sc[16];
    unsigned pd[16];
    f32x4 acc[3] = {{0.f, 0.f, 0.f, 0.f}, {0.f, 0.f, 0.f, 0.f}, {0.f, 0.f, 0.f, 0.f}};
    unsigned msk[4];
#pragma unroll
    for (int d = 0; d < 4; ++d) msk[d] = (j >> 1) == d ? ((j & 1) ? 0xffff0000u : 0x0000ffffu) : 0u;
    constexpr float C2 = 0.07216878364870322f * 1.4426950408889634f;
    unsigned tend = 0, tgt = 0, tendv = 0;
    if (SLATE) { unsigned kt; asm volatile("s_load_dword %0, %1, %2\n\ts_waitcnt lgkmcnt(0)" : "=&s"(kt) : "s"(kc), "s"(kv0)); }
    const int stag = ((2 * sw - 3) * STAG) >> 1;
    if (PACING) { tgt = SLATE ? tstart + tn11 + stag : tstart; tend = tstart + tn0 + stag; }

    auto consume = [&](auto G) {
      constexpr int t = decltype(G)::value;
      if constexpr (t < 16) {
        float a0 = 0.f, a1 = 0.f, a2 = 0.f;
#pragma unroll
        for (int e = 0; e < 4; ++e) { a0 = dot2(buf[3 * t][e], qv[0][e], a0); a1 = dot2(buf[3 * t + 1][e], qv[1][e], a1); a2 = dot2(buf[3 * t + 2][e], qv[2][e], a2); }
        float a = (a0 + a1) + a2;
        a += dpp<XOR1>(a); a += dpp<XOR2>(a); a += dpp<HMIR>(a);
        sc[t] = a;
        if constexpr (t == 15) {
          float m = sc[0];
#pragma unroll
          for (int u = 1; u < 16; ++u) m = fmaxf(m, sc[u]);
          m = fmaxf(m, dpp<ROR8>(m)); m = x16_max(m); m = x32_max(m);
          const float mc = m * C2;
          float l = 0.f;
#pragma unroll
          for (int u = 0; u < 16; ++u) { const float p = __builtin_amdgcn_exp2f(fmaf(sc[u], C2, -mc)); pd[u] = cvtpk(p, p); l += bfhi(pd[u]); }
          l += dpp<ROR8>(l); l = x16_sum(l); l = x32_sum(l);
          if (lane == 0) { *(f32x2*)(l_ml + 2 * sw) = f32x2{mc, l}; MEMFENCE(); l_f[FA + sw] = 1; }
          STAMP(3);
        }
      } else {
        constexpr int u = t - 16;
        const u32x4 a = {pd[u] & msk[0], pd[u] & msk[1], pd[u] & msk[2], pd[u] & msk[3]};
#pragma unroll
        for (int i = 0; i < 3; ++i) acc[i] = mfma32(a, buf[3 * t + i], acc[i]);
      }
    };
    constexpr auto done_after = [](int n) constexpr {
      if (n < B1_AT) return 0;
      const int landed = (n - WIN + 1) / 3;
      const int cu = 1 + (CATCH * (n - B1_AT)) / 3;
      return landed < cu ? landed : cu;
    };
    static_for<(SLATE ? 0 : 9), 96>([&](auto N) {
      constexpr int n = decltype(N)::value;
      if constexpr (n == B1_AT) {
        STAMP(1);
        wait_lds<0>(fbase + FG, 0x00010101u);
        STAMP(2);
#pragma unroll
        for (int i = 0; i < 3; ++i) qv[i] = *(const u32x4*)(l_q + 32 * i + 4 * j);
      }
      if constexpr (n >= WIN) asm volatile("s_waitcnt vmcnt(%0)" ::"i"(WIN - 1) : "memory");
      if constexpr (n == 9) TAG_READY();
      if constexpr (n >= 9 && n < 9 + 45 && (n - 9) % 5 == 0) FS(2 + (n - 9) / 5);
      if constexpr (PACING && n % 3 == 0) {
        constexpr int k = n / 3;
        if constexpr (PACING == 2 && k > 12) { const unsigned tv = __builtin_amdgcn_readfirstlane(tendv); tend = tv ? tv + stag : tend; }
        const unsigned now = pace_step<(k < 31 ? 65536 / (31 - k) : 0)>(tgt, tend);
        if constexpr (PACING == 2 && k >= 6 && k < 26) {
          unsigned vm;
          asm volatile("s_getreg_b32 %0, hwreg(HW_REG_IB_STS, 0, 4)" : "=s"(vm));
          l_est[sw] = (now + (((vm + 3 * (31 - k)) * tn5) >> 4)) | 1u;
        }
        if constexpr (PACING == 2 && k >= 12 && k < 30) tendv = *l_tend;
      }
      if constexpr (n == FB_AT) { if (lane == 0) l_f[FB + sw] = 1; }
      constexpr int m = n < 48 ? n : n - 48;
      buf[n] = ld_nt(n < 48 ? r0 : rv, voff + (m % 3) * 128, (m / 3) * KVT);
      SB();
      static_for<done_after(n > 0 ? n - 1 : 0), done_after(n)>(consume);
      SB();
    });
    STAMP(4);
    __builtin_amdgcn_s_setprio(3);
    wait_lds<0>(fbase + FE, 0x01010101u);
    float cq = l_c[lane >> 4];
    unsigned dst = (unsigned)(size_t)(lfloat*)(l_acc + (sw * 2 + (g & 1)) * 192 + 8 * j + 4 * ((lane >> 4) & 1));
    unsigned src = (unsigned)(size_t)(lfloat*)(l_acc + (lane >> 4) * 384 + 64 * sw + 8 * j + 4 * (g & 1));
    unsigned fla = fbase + FC + sw, one = 1u, flr = fbase + FC;
    int pvo = (lane & 16) ? OOB : (g & 1) * 128 + j * 16 + (lane >> 5) * 8;
    int pso = WS_OP + ((h * 2 + b) * 4 + s) * 768 + sw * 256;
    unsigned tagv = tag;
    asm volatile("" : "+v"(cq), "+v"(dst), "+v"(src), "+v"(fla), "+v"(flr), "+v"(one), "+v"(pvo), "+s"(pso), "+v"(tagv));
    const bool wr = (lane >> 5) == (g & 1);
    SB();
    static_for<done_after(95), 32>([&](auto G) { consume(G); SB(); });
    if (wr) {
#pragma unroll
      for (int i = 0; i < 3; ++i) *(lf32x4*)(size_t)(dst + 256 * i) = acc[i];
      MEMFENCE();
      *(lbyte*)(size_t)fla = (unsigned char)one;
    }
    STAMP(5);
    if (sw < 3) {
      f32x4 u0, u1;
      unsigned fv, fs;
      asm volatile("1:\n\tds_read_b32 %0, %4\n\tds_read_b128 %2, %5\n\tds_read_b128 %3, %5 offset:768\n\ts_waitcnt lgkmcnt(2)\n\tv_readfirstlane_b32 %1, %0\n\t"
                   "s_cmp_lg_u32 %1, 0x01010101\n\ts_waitcnt lgkmcnt(0)\n\ts_cbranch_scc1 1b"
                   : "=&v"(fv), "=&s"(fs), "=&v"(u0), "=&v"(u1) : "v"(flr), "v"(src) : "scc", "memory");
      float o[4];
#pragma unroll
      for (int e = 0; e < 4; ++e) o[e] = cq * (u0[e] + u1[e]);
      const auto s0 = __builtin_amdgcn_permlane32_swap(__float_as_uint(o[0]), __float_as_uint(o[2]), false, false);
      const auto s1 = __builtin_amdgcn_permlane32_swap(__float_as_uint(o[1]), __float_as_uint(o[3]), false, false);
      const float p0 = x16_sum(__uint_as_float(s0[0]) + __uint_as_float(s0[1])), p1 = x16_sum(__uint_as_float(s1[0]) + __uint_as_float(s1[1]));
      const u32x2 pv = {cvtpk(p0, p1), tagv};
      __builtin_amdgcn_raw_buffer_store_b64(pv, rws, pvo, pso, AUX_SC1);
    }
    MEMFENCE();
    *(lbyte*)(size_t)(fla + (FD - FC)) = (unsigned char)one;
    STAMP(6);
    STAMP_FLUSH();
    return;
  }

  __builtin_amdgcn_s_setprio(2);
  const int w = wave;
  const unsigned idx = w < 2 ? 2 * j8 + w : uq >> 1, ur = (idx * 11) >> 5, uc = idx - 3 * ur, up = uq & 1;
  const bool has_front = (w < 3) & (idx < 15);
  const unsigned m0 = e4 == 0 ? 0x0000ffffu : e4 == 1 ? 0xffff0000u : 0u;
  const unsigned m1 = e4 == 2 ? 0x0000ffffu : e4 == 3 ? 0xffff0000u : 0u;
  const int st_col = (8 * j + 4 * (lane >> 5) + 2 * (g & 1)) * 8;
  const rsrc_t rx = mk_rsrc(x, 2560);
  if (has_front) {
    const int xo = (lane >> 5) * 1280 + (lane & 31) * 16;
    const bool x3 = (lane & 16) == 0;
    const u32x4 xa = __builtin_amdgcn_raw_buffer_load_b128(rx, xo, 0, 0), xb = __builtin_amdgcn_raw_buffer_load_b128(rx, xo + 512, 0, 0);
    const u32x4 xc = __builtin_amdgcn_raw_buffer_load_b128(rx, x3 ? xo + 1024 : OOB, 0, 0);
    const u32x4 xr0 = __builtin_amdgcn_raw_buffer_load_b128(rx, bl * 1280 + g * 32, 256 * ur, 0);
    const u32x4 xr1 = __builtin_amdgcn_raw_buffer_load_b128(rx, bl * 1280 + g * 32 + 16, 256 * ur, 0);
    SB();
    FS(2);
    TAG_READY();
    float v[24];
#pragma unroll
    for (int e = 0; e < 4; ++e) { v[2 * e] = bflo(xa[e]); v[2 * e + 1] = bfhi(xa[e]); v[8 + 2 * e] = bflo(xb[e]); v[9 + 2 * e] = bfhi(xb[e]); v[16 + 2 * e] = bflo(xc[e]); v[17 + 2 * e] = bfhi(xc[e]); }
    float sum = 0.f;
#pragma unroll
    for (int e = 0; e < 24; ++e) sum += v[e];
    sum = x16_sum(row_sum(sum));
    const float mean = sum * (1.f / 640.f);
    float sq = 0.f, sq3 = 0.f;
#pragma unroll
    for (int e = 0; e < 16; ++e) { const float d = v[e] - mean; sq = fmaf(d, d, sq); }
#pragma unroll
    for (int e = 16; e < 24; ++e) { const float d = v[e] - mean; sq3 = fmaf(d, d, sq3); }
    sq = x16_sum(row_sum(sq + (x3 ? sq3 : 0.f)));
    const float mb = rbf(mean), den = rbf(__builtin_amdgcn_sqrtf(rbf(rbf(sq * (1.f / 640.f)) + 1e-5f)));
    const float inv = __builtin_amdgcn_rcpf(den);
    const float mb0 = __builtin_bit_cast(float, __builtin_amdgcn_readlane(__builtin_bit_cast(int, mb), 0));
    const float mb1 = __builtin_bit_cast(float, __builtin_amdgcn_readlane(__builtin_bit_cast(int, mb), 32));
    const float iv0 = __builtin_bit_cast(float, __builtin_amdgcn_readlane(__builtin_bit_cast(int, inv), 0));
    const float iv1 = __builtin_bit_cast(float, __builtin_amdgcn_readlane(__builtin_bit_cast(int, inv), 32));
    const float mm = bl ? mb1 : mb0, ii = bl ? iv1 : iv0;
    FS(3);
    asm volatile("s_waitcnt vmcnt(0)" : "+v"(fw[0]), "+v"(fw[1]), "+v"(fw[2]), "+v"(fw[3]), "+v"(fw[4]), "+v"(fw[5]), "+v"(fw[6]), "+v"(fw[7]),
                 "+v"(fw[8]), "+v"(fw[9]), "+v"(fw[10]), "+v"(fw[11]), "+v"(fw[12]), "+v"(fw[13]), "+v"(fw[14]), "+v"(fw[15]));
    f32x4 acc0 = {0.f, 0.f, 0.f, 0.f}, acc1 = {0.f, 0.f, 0.f, 0.f};
#pragma unroll
    for (int n = 0; n < 16; ++n) {
      const unsigned xw = n < 8 ? xr0[n >> 1] : xr1[(n - 8) >> 1];
      const float xv = (n & 1) ? bfhi(xw) : bflo(xw);
      const float xn = rbf(xv - mm) * ii;
      const unsigned d = cvtpk(xn, xn), a0 = d & m0, a1 = d & m1;
      acc0 = mfma16(a0, a1, fw[n][0], fw[n][1], acc0);
      acc1 = mfma16(a0, a1, fw[n][2], fw[n][3], acc1);
    }
    float v0, v1;
    proj_epilogue(acc0, acc1, g & 1, v0, v1);
    const int rec = (w < 2 ? WS_QP + (h * 5 + ur) * 3072 : WS_GU + ((xq * 5 + ur) * 2 + up) * 3072) + uc * 512;
    st_pair(rws, ((lane >> 4) & 1) * 1536 + st_col, rec, v0, v1, tag);
    STAMP(1); FS(4);
    asm volatile("s_waitcnt vmcnt(0)" ::: "memory");
    STAMP(11); FS(5);
  } else {
    asm volatile("s_waitcnt vmcnt(0)" ::"v"(fw[0]), "v"(fw[1]), "v"(fw[2]), "v"(fw[3]), "v"(fw[4]), "v"(fw[5]), "v"(fw[6]), "v"(fw[7]),
                 "v"(fw[8]), "v"(fw[9]), "v"(fw[10]), "v"(fw[11]), "v"(fw[12]), "v"(fw[13]), "v"(fw[14]), "v"(fw[15]));
    if (w == 3) {
      if (PRETOUCH) {
        wait_until(tstart + PRETOUCH);
        const unsigned kv1 = kv0 + 512 * 384 - 64, o0 = h * (192 * 1280), o1 = o0 + 192 * 1280 - 64, d0_ = h * (48 * 1280);
        unsigned t0, t1, t2, t3_, t4, t5, t6, t7, t8;
        asm volatile("s_load_dword %0, %9, %14\n\ts_load_dword %1, %10, %15\n\ts_load_dword %2, %10, %14\n\ts_load_dword %3, %11, %16\n\ts_load_dword %4, %11, %17\n\t"
                     "s_load_dword %5, %12, %18\n\ts_load_dword %6, %13, 0x0\n\ts_load_dword %7, %19, %20\n\ts_load_dword %8, %19, %21\n\ts_waitcnt lgkmcnt(0)"
                     : "=&s"(t0), "=&s"(t1), "=&s"(t2), "=&s"(t3_), "=&s"(t4), "=&s"(t5), "=&s"(t6), "=&s"(t7), "=&s"(t8)
                     : "s"(kc), "s"(vc), "s"(wo), "s"(wd), "s"(out), "s"(kv1), "s"(kv0), "s"(o0), "s"(o1), "s"(d0_), "s"(ws), "s"(WS_END), "s"(512));
      }
    }
    TAG_READY();
  }
  const int slot = w * 8 + j8;
  const bool has_back = slot < 30;
  const int t3 = slot / 10, cc = slot - 10 * t3;
  const int rb = bid / 80, rc = (bid - 80 * rb) >> 3, ro = bid & 7;
  if (w == 0) { const u32x2 pv = {AMP >= 9 ? ampx : amp0, tag}; __builtin_amdgcn_raw_buffer_store_b64(pv, rws, (lane == 0 && bid < 64) ? bid * 8 : OOB, WS_TS + 512, AUX_SC1); }
  const bool reducer = (w == 0) && (bid < 160);
  unsigned ha[2][2] = {{0u, 0u}, {0u, 0u}};
        unsigned tend = tstart + tn0;
        auto pace_round = [&](unsigned t_sample, unsigned t_read, int so, int bias) {
          wait_until(t_sample);
          u32x4 e;
          do { MEMFENCE(); e = *(const u32x4*)l_est; } while (!__builtin_amdgcn_readfirstlane(e[0] & e[1] & e[2] & e[3] & 1u));
          const u32x2 pv = {tstart + (unsigned)((int)((e[0] - tstart) + (e[1] - tstart) + (e[2] - tstart) + (e[3] - tstart)) >> 2), tag};
          __builtin_amdgcn_raw_buffer_store_b64(pv, rws, (lane == 0 && bid < 32) ? bid * 8 : OOB, so, AUX_SC1);
          wait_until(t_read);
          MEMFENCE();
          const u32x2 tv = __builtin_amdgcn_raw_buffer_load_b64(rws, (lane & 31) * 8, so, AUX_SC1);
          if (__all(tv[1] == tag)) {
            const float d = x16_sum(row_sum((float)(int)(tv[0] - tstart)));
            int di = (int)(d * (1.f / 32.f)) - bias;
            di = di < 0 ? 0 : (di > 2500 ? 2500 : di);
            tend = (tstart + __builtin_amdgcn_readfirstlane(di)) | 1u;
            *l_tend = tend;
          }
        };
        if (w < 3) {
          if (has_front) __builtin_amdgcn_s_sleep(QSLEEP); else wait_until(tstart + 420);
          u32x4 t[3];
          const int vo = (lane >> 5) * 3072 + (lane & 31) * 16, so = WS_QP + h * 5 * 3072 + b * 1536 + w * 512;
          for (unsigned spin = 0;; ++spin) {
            MEMFENCE();
            t[0] = ld_sc1(rws, vo, so); t[1] = ld_sc1(rws, vo, so + 6144); t[2] = ld_sc1(rws, lane < 32 ? vo : OOB, so + 12288);
            const bool ok = (t[0][1] == tag) & (t[0][3] == tag) & (t[1][1] == tag) & (t[1][3] == tag) & (((t[2][1] == tag) & (t[2][3] == tag)) | (lane >= 32));
            if (__all(ok) || spin > SPIN_CAP) { STAMPV(12, spin + 1); break; }
          }
          const float a = x32_sum((__uint_as_float(t[0][0]) + __uint_as_float(t[1][0])) + __uint_as_float(t[2][0]));
          const float c = x32_sum((__uint_as_float(t[0][2]) + __uint_as_float(t[1][2])) + __uint_as_float(t[2][2]));
          if (lane < 32) { l_q[32 * w + lane] = cvtpk(a, c); MEMFENCE(); l_f[FG + w] = 1; }
        } else if (PACING == 2) {
          pace_round(tstart + tn6, tstart + tn6 + tn7, WS_TS, tn1);
        } else {
          wait_until(tstart + 700);
        }
        STAMP(2); FS(6);
        if (has_back) {
          u32x4 t[3];
          const int so = WS_GU + xq * 5 * 6144 + (h & 3) * 384 + t3 * 128;
          const int vo = bl * 1536 + 16 * g;
          for (unsigned spin = 0;; ++spin) {
            MEMFENCE();
            t[0] = ld_sc1(rws, vo + e4 * 6144, so);
            t[1] = ld_sc1(rws, vo + e4 * 6144 + 3072, so);
            t[2] = ld_sc1(rws, vo + 4 * 6144 + (e4 & 1) * 3072, so);
            bool ok = true;
#pragma unroll
            for (int k = 0; k < 3; ++k) ok &= (t[k][1] == tag) & (t[k][3] == tag);
            if (__all(ok) || spin > SPIN_CAP) { STAMPV(13, spin + 1); break; }
          }
#pragma unroll
          for (int z = 0; z < 2; ++z) {
            const float ex = __uint_as_float(t[2][2 * z]);
            const float gg = rbf(quad_sum(__uint_as_float(t[0][2 * z]) + (e4 == 0 ? ex : 0.f)));
            const float uu = rbf(quad_sum(__uint_as_float(t[1][2 * z]) + (e4 == 1 ? ex : 0.f)));
            const float zz = 0.7978845608028654f * (gg + 0.044715f * gg * gg * gg);
            const float ge = rbf(gg * __builtin_amdgcn_rcpf(1.f + __builtin_amdgcn_exp2f(-2.8853900817779268f * zz)));
            const float hv = ge * uu;
            const unsigned d = cvtpk(hv, hv);
            ha[z][0] = d & m0; ha[z][1] = d & m1;
          }
        }
  const int oc = 4 * (lane & 1) + 2 * (lane >> 5) + ((lane >> 4) & 1);
  float xres = 0.f;
  if (reducer) xres = bflo(__builtin_amdgcn_raw_buffer_load_b16(rx, oc * 2, rb * 1280 + rc * 128 + ro * 16, 0));
  STAMP(3);
        if (w == 1) {
          wait_lds<4>(fbase + FA, 0x01010101u);
          const int so = WS_ML + (h * 2 + b) * 256;
          if (lane < 4) { const f32x2 ml = *(const f32x2*)(l_ml + 2 * lane); st_pair(rws, lane * 16, so + s * 64, ml[0], ml[1], tag); }
          asm volatile("s_waitcnt vmcnt(0)" ::: "memory");
          __builtin_amdgcn_s_sleep(MLSLEEP);
          u32x4 t;
          for (unsigned spin = 0;; ++spin) {
            MEMFENCE();
            t = ld_sc1(rws, (lane & 15) * 16, so);
            if (__all((t[1] == tag) & (t[3] == tag)) || spin > SPIN_CAP) break;
          }
          const float mv = __uint_as_float(t[0]);
          const float M = row_max(mv);
          const float f = __builtin_amdgcn_exp2f(mv - M);
          const float L = row_sum(f * __uint_as_float(t[2]));
          if ((lane >> 2) == s) { l_c[lane & 3] = f / L; MEMFENCE(); l_f[FE + (lane & 3)] = 1; }
        }
  if (PACING == 2 && w == 3 && tn8) {
    pace_round(tend - tn8, tend - tn8 + tn9, WS_TS + 256, tn10);
  }
  if (has_back) {
    const unsigned sLo = (e4 & 1) ? 0x01000c0cu : 0x0c0c0100u, sHi = (e4 & 1) ? 0x03020c0cu : 0x0c0c0302u;
    unsigned sel0 = e4 < 2 ? sLo : 0x0c0c0c0cu, sel1 = e4 < 2 ? 0x0c0c0c0cu : sLo, sel2 = e4 < 2 ? sHi : 0x0c0c0c0cu, sel3 = e4 < 2 ? 0x0c0c0c0cu : sHi;
    int op_so = WS_OP + h * 8 * 768 + t3 * 256, op_vo = (bl * 4 + e4) * 768 + 16 * g;
    int d_vo = ((48 * h + 16 * t3 + 2 * g) * 640 + 64 * cc) * 2 + 16 * j, o_vo = ((192 * h + 64 * t3 + (WOSEQ ? 1 : 8) * g) * 640 + 64 * cc) * 2 + 16 * j;
    int yvo = ((lane >> 4) & 1) * 256 + st_col / 2, yso = WS_YP + ((h * 3 + t3) * 10 + cc) * 512;
    int r_vo = (lane >> 1) * 5120 + (lane & 1) * 16 + WS_YP + rc * 512 + rb * 256 + ro * 32;
    int out_vo = (lane & 14) == 0 ? oc * 2 + rb * 1280 + rc * 128 + ro * 16 : OOB;
    unsigned tag2v = tag + 1u, tda = (unsigned)(size_t)(luint*)l_tend;
    asm volatile("" : "+v"(sel0), "+v"(sel1), "+v"(sel2), "+v"(sel3), "+s"(op_so), "+v"(op_vo), "+v"(d_vo), "+v"(o_vo), "+v"(yvo), "+s"(yso), "+v"(r_vo), "+v"(out_vo), "+v"(tag2v), "+v"(xres), "+v"(tda));
    int pass = DRYRUN ? (w + 3) >> 2 : 1;
    asm volatile("" : "+s"(pass));
#pragma nounroll
    for (; pass < 2; ++pass) {
      STAMP(4);
      const int live = pass ? -1 : 0;
      const rsrc_t rd = mk_rsrc(wd, 1536 * 1280 & live), ro_ = mk_rsrc(wo, 6144 * 1280 & live), rw2 = mk_rsrc(ws, WS_END & live), rout = mk_rsrc(out, 2560 & live);
      const unsigned want = tag & live, want2 = (tag + 1u) & live;
      unsigned fb = fbase + (pass ? FB : FF), fd = fbase + (pass ? FD : FF);
      asm volatile("" : "+v"(fb), "+v"(fd));
      wait_lds<1>(fb, 0x01010101u & live);
      const unsigned tfin = __builtin_amdgcn_readfirstlane(*(luint*)(size_t)tda) & live;
      delay_ticks(tn4 & live);
      u32x4 wb[10], t0, t1;
      unsigned fdv = 0;
#pragma unroll
      for (int n = 0; n < 10; ++n) {
        if (n == POLLPOS) {
          const rsrc_t rp = mk_rsrc(ws, __builtin_amdgcn_readfirstlane(fdv) == 0x01010101u ? (WS_END & live) : 0);
          t0 = ld_sc1(rp, op_vo, op_so); t1 = ld_sc1(rp, op_vo + 128, op_so);
          SB();
        }
        if (n < 2) wb[8 + n] = ld_nt(rd, d_vo + n * 1280, 0); else wb[n - 2] = ld_nt(ro_, o_vo, (n - 2) * (WOSEQ ? 10240 : 1280));
        if (n == POLLPOS - FDLOOK) { MEMFENCE(); fdv = *(luint*)(size_t)fd; }
        SB();
        if (n < 9 && BPACE) __builtin_amdgcn_s_sleep(BPACE);
        SB();
      }
      if (POLLPOS == 11) {
        if (pass) {
          wait_until(tfin ? tfin + tn12 - tn2 : tstart);
          wait_lds<1>(fd, 0x01010101u);
          delay_ticks(tn2);
        }
        t0 = ld_sc1(rw2, op_vo, op_so); t1 = ld_sc1(rw2, op_vo + 128, op_so);
        SB();
      }
      if (POLLPOS == 10) {
        const rsrc_t rp = mk_rsrc(ws, __builtin_amdgcn_readfirstlane(fdv) == 0x01010101u ? (WS_END & live) : 0);
        t0 = ld_sc1(rp, op_vo, op_so); t1 = ld_sc1(rp, op_vo + 128, op_so);
        SB();
      }
      STAMP(5);
      u32x4 bq[10];
#define PAIR(p) do { bq[2 * (p)] = u32x4{wb[2 * (p)][0], wb[2 * (p)][1], wb[2 * (p) + 1][0], wb[2 * (p) + 1][1]}; \
                     bq[2 * (p) + 1] = u32x4{wb[2 * (p)][2], wb[2 * (p)][3], wb[2 * (p) + 1][2], wb[2 * (p) + 1][3]}; } while (0)
      PAIR(4); PAIR(0); PAIR(1); PAIR(2);
      if (POLLPOS >= 10) PAIR(3);
      const u32x4 hA = {ha[0][0], ha[0][1], ha[1][0], ha[1][1]};
      f32x4 acc0 = mfma32(hA, bq[8], f32x4{0.f, 0.f, 0.f, 0.f}), acc1 = mfma32(hA, bq[9], f32x4{0.f, 0.f, 0.f, 0.f});
      asm volatile("" : "+v"(bq[0]), "+v"(bq[1]), "+v"(bq[2]), "+v"(bq[3]), "+v"(bq[4]), "+v"(bq[5]), "+v"(acc0), "+v"(acc1));
      if (POLLPOS >= 10) asm volatile("" : "+v"(bq[6]), "+v"(bq[7]));
      SB();
      unsigned np1 = 1;
      if (__builtin_expect(!__all((t0[1] == want) & (t0[3] == want) & (t1[1] == want) & (t1[3] == want)), 0)) {
        do {
          ++np1;
          wait_lds<1>(fd, 0x01010101u);
          delay_ticks(tn2);
          t0 = ld_sc1(rw2, op_vo, op_so); t1 = ld_sc1(rw2, op_vo + 128, op_so);
        } while (!__all((t0[1] == want) & (t0[3] == want) & (t1[1] == want) & (t1[3] == want)) && np1 < SPIN_CAP);
      }
      STAMPV(14, np1);
      STAMP(7);
      unsigned pk[4];
      {
        float u0, u1, u2, u3, u4, u5, u6, u7;
#define DPPADD(r, P) "v_add_f32_dpp " r ", " r ", " r " quad_perm:" P " row_mask:0xf bank_mask:0xf\n\t"
#define DPP8(P) DPPADD("%4", P) DPPADD("%5", P) DPPADD("%6", P) DPPADD("%7", P) DPPADD("%8", P) DPPADD("%9", P) DPPADD("%10", P) DPPADD("%11", P)
        asm volatile("v_lshlrev_b32 %4, 16, %12\n\tv_and_b32 %5, 0xffff0000, %12\n\tv_lshlrev_b32 %6, 16, %13\n\tv_and_b32 %7, 0xffff0000, %13\n\t"
                     "v_lshlrev_b32 %8, 16, %14\n\tv_and_b32 %9, 0xffff0000, %14\n\tv_lshlrev_b32 %10, 16, %15\n\tv_and_b32 %11, 0xffff0000, %15\n\t"
                     DPP8("[1,0,3,2]") DPP8("[2,3,0,1]")
                     "v_cvt_pk_bf16_f32 %0, %4, %5\n\tv_cvt_pk_bf16_f32 %1, %6, %7\n\tv_cvt_pk_bf16_f32 %2, %8, %9\n\tv_cvt_pk_bf16_f32 %3, %10, %11"
                     : "=&v"(pk[0]), "=&v"(pk[1]), "=&v"(pk[2]), "=&v"(pk[3]), "=&v"(u0), "=&v"(u1), "=&v"(u2), "=&v"(u3), "=&v"(u4), "=&v"(u5), "=&v"(u6), "=&v"(u7)
                     : "v"(t0[0]), "v"(t0[2]), "v"(t1[0]), "v"(t1[2]));
      }
      u32x4 aq[4];
#pragma unroll
      for (int p = 0; p < 4; ++p)
        aq[p] = u32x4{__builtin_amdgcn_perm(0u, pk[p], sel0), __builtin_amdgcn_perm(0u, pk[p], sel1), __builtin_amdgcn_perm(0u, pk[p], sel2), __builtin_amdgcn_perm(0u, pk[p], sel3)};
#pragma unroll
      for (int p = 0; p < 3; ++p) { acc0 = mfma32(aq[p], bq[2 * p], acc0); acc1 = mfma32(aq[p], bq[2 * p + 1], acc1); }
      if (POLLPOS < 10) { asm volatile("" : "+v"(aq[3]), "+v"(acc0), "+v"(acc1)); SB(); PAIR(3); }
      acc0 = mfma32(aq[3], bq[6], acc0); acc1 = mfma32(aq[3], bq[7], acc1);
      float v0, v1;
      proj_epilogue(acc0, acc1, g & 1, v0, v1);
      __builtin_amdgcn_raw_buffer_store_b64(u32x2{cvtpk(v0, v1), tag2v}, rw2, yvo, yso, AUX_SC1);
      STAMP(8);
      if (reducer) {
        if (pass) {
          const unsigned tn = (unsigned)__builtin_amdgcn_s_memrealtime();
          int wt = tfin ? (int)(tfin + tn13 - tn) : 40;
          asm("s_max_i32 %0, %0, %1\n\ts_min_i32 %0, %0, 0x64" : "+s"(wt) : "s"(tn3) : "scc");
          wait_until(tn + wt);
        }
        u32x4 t[3];
        unsigned np2 = 0;
        for (;;) {
          ++np2;
          MEMFENCE();
#pragma unroll
          for (int k = 0; k < 3; ++k) t[k] = ld_sc1(rw2, r_vo, k * 32 * 5120);
          bool ok = true;
#pragma unroll
          for (int k = 0; k < 3; ++k) ok &= (t[k][1] == want2) & (t[k][3] == want2);
          if (__all(ok) || np2 > SPIN_CAP) break;
        }
        STAMPV(15, np2);
        STAMP(9);
        const float y0 = (bflo(t[0][0]) + bflo(t[1][0])) + bflo(t[2][0]), y1 = (bfhi(t[0][0]) + bfhi(t[1][0])) + bfhi(t[2][0]);
        const float y2 = (bflo(t[0][2]) + bflo(t[1][2])) + bflo(t[2][2]), y3 = (bfhi(t[0][2]) + bfhi(t[1][2])) + bfhi(t[2][2]);
        const auto s0 = __builtin_amdgcn_permlane32_swap(__float_as_uint(y0), __float_as_uint(y2), false, false);
        const auto s1 = __builtin_amdgcn_permlane32_swap(__float_as_uint(y1), __float_as_uint(y3), false, false);
        const float z0 = __uint_as_float(s0[0]) + __uint_as_float(s0[1]), z1 = __uint_as_float(s1[0]) + __uint_as_float(s1[1]);
        const auto s2 = __builtin_amdgcn_permlane16_swap(__float_as_uint(z0), __float_as_uint(z1), false, false);
        float y = __uint_as_float(s2[0]) + __uint_as_float(s2[1]);
        y += dpp<ROR8>(y); y += dpp<ROR4>(y); y += dpp<XOR2>(y);
        __builtin_amdgcn_raw_buffer_store_b16((unsigned short)cvtpk(xres + y, 0.f), rout, out_vo, 0, AUX_OUT);
        if (bid == 0) __builtin_amdgcn_raw_buffer_store_b32(tag2v, rw2, lane == 0 ? WS_EPOCH : OOB, 0, AUX_SC1);
        STAMP(10);
        if (pass && bid == PBID) {
          if (AMP == 3) ampv = tfin - amp0 - 1500;
          AMPNOW(4, tfin + 300); AMPNOW(5, amp0 + 1800);
          if (AMP == 6) ampv = 10 * np2 + 40 * np1;
          if (AMP >= 7) {
            const u32x2 tv = __builtin_amdgcn_raw_buffer_load_b64(rws, lane * 8, WS_TS + 512, AUX_SC1);
            const float d = AMP >= 9 ? (float)tv[0] : (float)(int)(tv[0] - amp0);
            const float mx = x32_max(x16_max(row_max(d))), mn = -x32_max(x16_max(row_max(-d))), av = x32_sum(x16_sum(row_sum(d))) * (1.f / 64.f);
            int ai = (int)(AMP == 7 ? mx - mn : AMP == 8 ? av - mn : AMP == 9 ? mx : av);
            asm volatile("" : "+v"(ai));
            ampv = __builtin_amdgcn_readfirstlane(ai);
          }
          wait_until(amp0 + 4000 + 10 * ((int)ampv < 0 ? 0 : ampv));
        }
      }
    }
  }
  STAMP_FLUSH();
}

extern "C" void kernel_launch(void* const* d_in, const int* in_sizes, int n_in, void* d_out, int out_size, void* d_ws, size_t ws_size,
                              hipStream_t stream) {
  fused_decode<<<256, 512, 0, stream>>>((const u16*)d_in[0], (const u16*)d_in[1], (const u16*)d_in[2], (const u16*)d_in[3], (const u16*)d_in[4],
                                        (const u16*)d_in[6], (const u16*)d_in[7], (const u16*)d_in[8], (u16*)d_out, (unsigned char*)d_ws
                                        );
}
```

```cpp
#define P_B0 40
#define P_B1 40
#include <hip/hip_runtime.h>
#include <hip/hip_bf16.h>
#include <stdint.h>
#include <type_traits>

typedef unsigned short u16;
typedef unsigned u32x4 __attribute__((ext_vector_type(4)));
typedef unsigned u32x2 __attribute__((ext_vector_type(2)));
typedef float f32x4 __attribute__((ext_vector_type(4)));
typedef float f32x2 __attribute__((ext_vector_type(2)));
typedef short s16x4 __attribute__((ext_vector_type(4)));
typedef __bf16 bf16x8 __attribute__((ext_vector_type(8)));
typedef __bf16 bf16x2 __attribute__((ext_vector_type(2)));
typedef __amdgpu_buffer_rsrc_t rsrc_t;

#define WIN 9
#define Q_AT 39
#define BPACE 5
#define POLLPOS 8
#define STAG 11
#define HSTAG 0
#define CODE_BYTES 16640
#define P_KAP 34
#define P_FIN 42
constexpr int T_PLAN = 1480;
constexpr int S_DELAY = 110;
constexpr int RATE16 = 229;
constexpr int SMP0 = 480, WAIT0 = 220, BIAS0 = P_B0;
constexpr int SMP1 = 420, WAIT1 = 140, BIAS1 = P_B1;
constexpr int GAP1 = 200, KAPPA16 = P_KAP;
constexpr int BACK_DELAY = 16;
constexpr int LATE_DELAY = 20;
constexpr int FIN_DELAY = P_FIN;

constexpr int AUX_NT = 2, AUX_SC1 = 16;
constexpr int RSRC_W3 = 0x00020000;
constexpr unsigned SPIN_CAP = 1u << 18;
constexpr int OOB = 0x40000000;
constexpr unsigned ALL4 = 0x01010101u;

constexpr int WS_EPOCH = 0;
constexpr int WS_TS = 1024;
constexpr int WS_QP = 4096;
constexpr int WS_GU = WS_QP + 32 * 5 * 2 * 96 * 8;
constexpr int WS_ML = WS_GU + 8 * 5 * 2 * 2 * 96 * 8;
constexpr int WS_OP = WS_ML + 32 * 2 * 4 * 4 * 16;
constexpr int WS_YP = WS_OP + 32 * 2 * 4 * 96 * 8;
constexpr int WS_END = WS_YP + 96 * 10 * 2 * 32 * 8;

template <int I, int N, class F> __device__ __forceinline__ void static_for(F&& f) {
  if constexpr (I < N) { f(std::integral_constant<int, I>{}); static_for<I + 1, N>(f); }
}
__device__ __forceinline__ rsrc_t mk_rsrc(const void* p, int bytes) {
  return __builtin_amdgcn_make_buffer_rsrc(const_cast<void*>(p), 0, bytes, RSRC_W3);
}
__device__ __forceinline__ u32x4 ld_nt(rsrc_t r, int voff, int soff) { return __builtin_amdgcn_raw_buffer_load_b128(r, voff, soff, AUX_NT); }
__device__ __forceinline__ u32x4 ld_sc1(rsrc_t r, int voff, int soff) { return __builtin_amdgcn_raw_buffer_load_b128(r, voff, soff, AUX_SC1); }
__device__ __forceinline__ u32x2 ld2_sc1(rsrc_t r, int voff, int soff) { return __builtin_amdgcn_raw_buffer_load_b64(r, voff, soff, AUX_SC1); }
__device__ __forceinline__ void st_pair(rsrc_t r, int voff, int soff, float a, float b, unsigned tag) {
  u32x4 v = {__float_as_uint(a), tag, __float_as_uint(b), tag};
  __builtin_amdgcn_raw_buffer_store_b128(v, r, voff, soff, AUX_SC1);
}
__device__ __forceinline__ float bflo(unsigned v) { return __uint_as_float(v << 16); }
__device__ __forceinline__ float bfhi(unsigned v) { return __uint_as_float(v & 0xffff0000u); }
__device__ __forceinline__ unsigned cvtpk(float lo, float hi) {
  bf16x2 r = {(__bf16)lo, (__bf16)hi};
  return __builtin_bit_cast(unsigned, r);
}
__device__ __forceinline__ float rbf(float x) { return (float)(__bf16)x; }
template <int CTRL> __device__ __forceinline__ float dpp(float x) {
  return __builtin_bit_cast(float, __builtin_amdgcn_mov_dpp(__builtin_bit_cast(int, x), CTRL, 0xf, 0xf, true));
}
constexpr int XOR1 = 0xB1, XOR2 = 0x4E, HMIR = 0x141, ROR8 = 0x128, ROR4 = 0x124;
__device__ __forceinline__ float x16_sum(float x) { auto s = __builtin_amdgcn_permlane16_swap(__float_as_uint(x), __float_as_uint(x), false, false); return __uint_as_float(s[0]) + __uint_as_float(s[1]); }
__device__ __forceinline__ float x32_sum(float x) { auto s = __builtin_amdgcn_permlane32_swap(__float_as_uint(x), __float_as_uint(x), false, false); return __uint_as_float(s[0]) + __uint_as_float(s[1]); }
__device__ __forceinline__ float x16_max(float x) { auto s = __builtin_amdgcn_permlane16_swap(__float_as_uint(x), __float_as_uint(x), false, false); return fmaxf(__uint_as_float(s[0]), __uint_as_float(s[1])); }
__device__ __forceinline__ float x32_max(float x) { auto s = __builtin_amdgcn_permlane32_swap(__float_as_uint(x), __float_as_uint(x), false, false); return fmaxf(__uint_as_float(s[0]), __uint_as_float(s[1])); }
__device__ __forceinline__ float row_sum(float x) { x += dpp<XOR1>(x); x += dpp<XOR2>(x); x += dpp<HMIR>(x); x += dpp<ROR8>(x); return x; }
__device__ __forceinline__ float row_max(float x) { x = fmaxf(x, dpp<XOR1>(x)); x = fmaxf(x, dpp<XOR2>(x)); x = fmaxf(x, dpp<HMIR>(x)); x = fmaxf(x, dpp<ROR8>(x)); return x; }
__device__ __forceinline__ float quad_sum(float x) { x += dpp<XOR1>(x); x += dpp<XOR2>(x); return x; }
__device__ __forceinline__ float dot2(unsigned a, unsigned b, float c) {
  return __builtin_amdgcn_fdot2_f32_bf16(__builtin_bit_cast(bf16x2, a), __builtin_bit_cast(bf16x2, b), c, false);
}
__device__ __forceinline__ f32x4 mfma16(unsigned a0, unsigned a1, unsigned b0, unsigned b1, f32x4 c) {
  u32x2 a = {a0, a1}, b = {b0, b1};
  return __builtin_amdgcn_mfma_f32_16x16x16bf16_1k(__builtin_bit_cast(s16x4, a), __builtin_bit_cast(s16x4, b), c, 0, 0, 0);
}
__device__ __forceinline__ f32x4 mfma32(u32x4 a, u32x4 b, f32x4 c) {
  return __builtin_amdgcn_mfma_f32_16x16x32_bf16(__builtin_bit_cast(bf16x8, a), __builtin_bit_cast(bf16x8, b), c, 0, 0, 0);
}
template <int RECIP> __device__ __forceinline__ unsigned pace_step(unsigned& tgt, unsigned tend) {
  unsigned d, now;
  asm volatile("1:\n\ts_memrealtime vcc\n\ts_waitcnt lgkmcnt(0)\n\ts_sub_u32 %0, vcc_lo, %2\n\ts_cmp_lt_i32 %0, 0\n\ts_cbranch_scc1 1b\n\t"
               "s_sub_i32 %0, %3, vcc_lo\n\ts_max_i32 %0, %0, 0\n\ts_min_i32 %0, %0, 0x7d0\n\ts_mul_i32 %0, %0, %4\n\ts_lshr_b32 %0, %0, 16\n\t"
               "s_add_u32 %2, vcc_lo, %0\n\ts_mov_b32 %1, vcc_lo"
               : "=&s"(d), "=&s"(now), "+s"(tgt) : "s"(tend), "i"(RECIP) : "vcc", "scc");
  return now;
}
__device__ __forceinline__ void wait_until(unsigned tgt) {
  asm volatile("1:\n\ts_memrealtime vcc\n\ts_waitcnt lgkmcnt(0)\n\ts_sub_u32 vcc_lo, vcc_lo, %0\n\ts_cmp_lt_i32 vcc_lo, 0\n\ts_cbranch_scc1 1b" :: "s"(tgt) : "vcc", "scc", "memory");
}
#define SB() __builtin_amdgcn_sched_barrier(0)
#define MEMFENCE() asm volatile("" ::: "memory")

#define FS(i)
#define STAMP(i)
#define STAMPV(i, v)
#define STAMPT(i, v)
#define STAMP_DECL
#define STAMP_FLUSH()

__device__ __forceinline__ void delay_ticks(int n) {
  if (n > 0) wait_until((unsigned)__builtin_amdgcn_s_memrealtime() + n);
}

typedef __attribute__((address_space(3))) float lfloat;
typedef __attribute__((address_space(3))) f32x4 lf32x4;
typedef __attribute__((address_space(3))) unsigned luint;
typedef __attribute__((address_space(3))) unsigned char lbyte;
template <int SLP> __device__ __forceinline__ void wait_lds(unsigned a, unsigned want) {
  unsigned v, sv;
  asm volatile("s_branch 2f\n1:\n\ts_sleep %4\n2:\n\tds_read_b32 %0, %2\n\ts_waitcnt lgkmcnt(0)\n\tv_readfirstlane_b32 %1, %0\n\ts_cmp_lg_u32 %1, %3\n\ts_cbranch_scc1 1b"
               : "=&v"(v), "=&s"(sv) : "v"(a), "s"(want), "i"(SLP) : "scc", "memory");
}

__device__ __forceinline__ void proj_epilogue(f32x4 acc0, f32x4 acc1, bool z, float& v0, float& v1) {
  float n0[4], n1[4];
#pragma unroll
  for (int r = 0; r < 4; ++r) {
    auto s = __builtin_amdgcn_permlane32_swap(__float_as_uint(acc0[r]), __float_as_uint(acc1[r]), false, false);
    n0[r] = __uint_as_float(s[0]); n1[r] = __uint_as_float(s[1]);
  }
  const float r0 = n0[0] + dpp<ROR8>(n1[0]), r1 = n0[1] + dpp<ROR8>(n1[1]);
  const float r2 = n1[2] + dpp<ROR8>(n0[2]), r3 = n1[3] + dpp<ROR8>(n0[3]);
  v0 = z ? r2 : r0; v1 = z ? r3 : r1;
}

__global__ void __launch_bounds__(512, 2)
fused_decode(const u16* x_, const u16* kc_, const u16* vc_, const u16* wq_, const u16* wo_, const u16* wg_, const u16* wu_, const u16* wd_, u16* out_, unsigned char* ws_
             ) {
  __shared__ __attribute__((aligned(16))) float lds[96 + 8 + 4 + 16 + 8 * 192];
  unsigned* l_q = (unsigned*)lds;
  float* l_ml = lds + 96;
  float* l_c = lds + 104;
  unsigned* l_est = (unsigned*)(lds + 108);
  unsigned char* l_f = (unsigned char*)(lds + 112);
  constexpr int FA = 0, FB = 4, FC = 8, FD = 12, FE = 16, FG = 20;
  unsigned* l_tend = (unsigned*)(lds + 118);
  const unsigned fbase = (unsigned)(size_t)(lbyte*)l_f;
  float* l_acc = lds + 124;

  STAMP_DECL;
  STAMP(0);
  typedef unsigned long long u64;
  typedef u64 u64x8 __attribute__((ext_vector_type(8)));
  typedef u64 u64x2 __attribute__((ext_vector_type(2)));
  u64x8 ka; u64x2 kb;
  u32x4 fw[16];
  int bid = blockIdx.x, tid = threadIdx.x;
  {
    unsigned v1, v2;
    u64x8 ka0; u64x2 kb0;
    asm volatile("s_load_dwordx16 %0, %2, 0x0\n\ts_load_dwordx4 %1, %2, 0x40" : "=&{s[36:51]}"(ka0), "=&{s[52:55]}"(kb0) : "s"(__builtin_amdgcn_kernarg_segment_ptr()));
    asm volatile(
               "v_readfirstlane_b32 s14, %20\n\t"
               "s_lshr_b32 s15, %19, 3\n\t"
               "s_cmpk_ge_u32 s14, 0xc0\n\ts_cbranch_scc1 1f\n\t"
               "s_setprio 2\n\t"
               "s_and_b32 s16, %19, 7\n\t"
               "s_lshr_b32 s17, %19, 6\n\ts_lshl2_add_u32 s17, s16, s17\n\t"
               "s_and_b32 s18, s15, 7\n\ts_lshr_b32 s19, s14, 6\n\ts_lshl1_add_u32 s18, s18, s19\n\t"
               "s_lshr_b32 s19, %19, 4\n\t"
               "v_bfe_u32 %16, %20, 3, 3\n\tv_and_b32 %17, 7, %20\n\t"
               "s_movk_i32 s13, 0x3000\n\t"
               "s_cmpk_lt_u32 s14, 0x80\n\t"
               "s_cselect_b32 s18, s18, s19\n\ts_cselect_b32 s17, s17, s16\n\ts_cselect_b32 s13, s13, 0xc00\n\t"
               "s_mul_i32 s19, s18, 11\n\ts_lshr_b32 s19, s19, 5\n\t"
               "v_mad_u32_u24 %16, %16, s13, %17\n\t"
               "s_sub_u32 s17, s17, s19\n\ts_mul_i32 s17, s17, 3\n\ts_add_u32 s17, s17, s18\n\t"
               "s_mul_i32 s19, s19, s13\n\ts_add_u32 s12, s17, s19\n\ts_lshl_b32 s12, s12, 7\n\t"
               "v_lshlrev_b32 %16, 4, %16\n\t"
               "s_mul_i32 s10, s13, 0x280\n\ts_mov_b32 s11, 0x20000\n\ts_cmp_eq_u32 s18, 15\n\ts_cselect_b32 s10, 0, s10\n\t"
               "s_bitcmp1_b32 s15, 0\n\t"
               "s_waitcnt lgkmcnt(0)\n\t"
               "s_cselect_b64 s[8:9], s[48:49], s[46:47]\n\t"
               "s_cmpk_lt_u32 s14, 0x80\n\ts_cselect_b64 s[8:9], s[42:43], s[8:9]\n\t"
               "s_and_b32 s9, s9, 0xffff\n\t"
               "buffer_load_dwordx4 %0, %16, s[8:11], s12 offen nt\n\t" "s_add_u32 s12, s12, s13\n\t"
               "buffer_load_dwordx4 %1, %16, s[8:11], s12 offen nt\n\t" "s_add_u32 s12, s12, s13\n\t"
               "buffer_load_dwordx4 %2, %16, s[8:11], s12 offen nt\n\t" "s_add_u32 s12, s12, s13\n\t"
               "buffer_load_dwordx4 %3, %16, s[8:11], s12 offen nt\n\t" "s_add_u32 s12, s12, s13\n\t"
               "buffer_load_dwordx4 %4, %16, s[8:11], s12 offen nt\n\t" "s_add_u32 s12, s12, s13\n\t"
               "buffer_load_dwordx4 %5, %16, s[8:11], s12 offen nt\n\t" "s_add_u32 s12, s12, s13\n\t"
               "buffer_load_dwordx4 %6, %16, s[8:11], s12 offen nt\n\t" "s_add_u32 s12, s12, s13\n\t"
               "buffer_load_dwordx4 %7, %16, s[8:11], s12 offen nt\n\t" "s_add_u32 s12, s12, s13\n\t"
               "buffer_load_dwordx4 %8, %16, s[8:11], s12 offen nt\n\t" "s_add_u32 s12, s12, s13\n\t"
               "buffer_load_dwordx4 %9, %16, s[8:11], s12 offen nt\n\t" "s_add_u32 s12, s12, s13\n\t"
               "buffer_load_dwordx4 %10, %16, s[8:11], s12 offen nt\n\t" "s_add_u32 s12, s12, s13\n\t"
               "buffer_load_dwordx4 %11, %16, s[8:11], s12 offen nt\n\t" "s_add_u32 s12, s12, s13\n\t" "s_barrier\n\t"
               "buffer_load_dwordx4 %12, %16, s[8:11], s12 offen nt\n\t" "s_add_u32 s12, s12, s13\n\t"
               "buffer_load_dwordx4 %13, %16, s[8:11], s12 offen nt\n\t" "s_add_u32 s12, s12, s13\n\t"
               "buffer_load_dwordx4 %14, %16, s[8:11], s12 offen nt\n\t" "s_add_u32 s12, s12, s13\n\t"
               "buffer_load_dwordx4 %15, %16, s[8:11], s12 offen nt\n\t"
               "s_branch 2f\n"
               "1:\n\t"
               "v_and_b32 %16, 15, %20\n\tv_lshl_add_u32 %16, %16, 2, %21\n\tv_mov_b32 %17, 0\n\tds_write_b32 %16, %17\n\t"
               "s_waitcnt lgkmcnt(0)\n\ts_barrier\n"
               "2:"
               : "=&v"(fw[0]), "=&v"(fw[1]), "=&v"(fw[2]), "=&v"(fw[3]), "=&v"(fw[4]), "=&v"(fw[5]), "=&v"(fw[6]), "=&v"(fw[7]),
                 "=&v"(fw[8]), "=&v"(fw[9]), "=&v"(fw[10]), "=&v"(fw[11]), "=&v"(fw[12]), "=&v"(fw[13]), "=&v"(fw[14]), "=&v"(fw[15]),
                 "=&v"(v1), "=&v"(v2)
               : "{s[36:51]}"(ka0), "s"(bid), "v"(tid), "s"(fbase - 16), "{s[52:55]}"(kb0)
               : "s8", "s9", "s10", "s11", "s12", "s13", "s14", "s15", "s16", "s17", "s18", "s19", "scc", "memory");
    asm volatile("" : "={s[36:51]}"(ka), "={s[52:55]}"(kb), "+s"(bid) : "0"(ka0), "1"(kb0));
    asm volatile("" : "+v"(tid));
  }
  const int xq = bid & 7, uq = bid >> 3;
  const int h = xq * 4 + (uq >> 3), j8 = uq & 7, b = j8 >> 2, s = j8 & 3;
  const int lane = tid & 63, wave = __builtin_amdgcn_readfirstlane(tid >> 6);
  const int g = lane >> 3, j = lane & 7;
  const int bl = (lane >> 2) & 1, e4 = lane & 3;
  const bool isS = wave >= 4;
  const u16 *x = (const u16*)ka[0], *kc = (const u16*)ka[1], *vc = (const u16*)ka[2], *wo = (const u16*)ka[4], *wd = (const u16*)ka[7];
  u16* out = (u16*)kb[0];
  unsigned char* ws = (unsigned char*)kb[1];
  u64 tst;
  asm volatile("s_memrealtime %0\n\ts_waitcnt lgkmcnt(0)" : "=s"(tst));
  const unsigned tstart = (unsigned)tst;
  unsigned tag, ep;
  unsigned cp0, cp1;
  {
    u64 pc;
    asm volatile("s_getpc_b64 %0" : "=s"(pc));
    unsigned pco = (((wave + 5) & 7) * 32 + uq) * 128;
    pco = pco < CODE_BYTES - 1024 ? pco : 0;
    asm volatile("s_load_dword %0, %3, 0x0\n\ts_load_dword %1, %4, %5\n\ts_load_dword %2, %4, %6" : "=&s"(ep), "=&s"(cp0), "=&s"(cp1) : "s"(ws), "s"(pc & ~127ull), "s"(pco), "s"(pco + 64));
  }
#define TAG_READY() asm volatile("s_waitcnt lgkmcnt(0)\n\ts_add_u32 %0, %1, 1\n\ts_addc_u32 %0, %0, 0" : "=&s"(tag) : "s"(ep), "s"(cp0), "s"(cp1) : "scc")
  FS(1);
  const rsrc_t rws = mk_rsrc(ws, WS_END);

  if (isS) {
    const int sw = wave - 4;
    const rsrc_t r0 = mk_rsrc(kc, 2 * 32 * 8192 * 384), rv = mk_rsrc(vc, 2 * 32 * 8192 * 384);
#define SCAT 2
    const int voff = ((b * 32 + h) * 8192 + 6144 + 512 * s + (SCAT == 1 ? 16 * (8 * sw + g) : SCAT == 2 ? 128 * sw + g : SCAT == 3 ? 4 * (8 * sw + g) : 8 * sw + g)) * 384 + 16 * j;
    u32x4 buf[96];
    u32x4 qv[3];
    float sc[16];
    unsigned pd[16];
    f32x4 acc[3] = {{0.f, 0.f, 0.f, 0.f}, {0.f, 0.f, 0.f, 0.f}, {0.f, 0.f, 0.f, 0.f}};
    unsigned msk[4];
#pragma unroll
    for (int d = 0; d < 4; ++d) msk[d] = (j >> 1) == d ? ((j & 1) ? 0xffff0000u : 0x0000ffffu) : 0u;
    constexpr float C2 = 0.07216878364870322f * 1.4426950408889634f;
    const int stag = ((2 * sw - 3) * STAG) >> 1;
    unsigned tgt = tstart + S_DELAY + stag, tend = tstart + T_PLAN + stag, tendv = 0;

    auto consume = [&](auto G) {
      constexpr int t = decltype(G)::value;
      if constexpr (t < 16) {
        float a0 = 0.f, a1 = 0.f, a2 = 0.f;
#pragma unroll
        for (int e = 0; e < 4; ++e) { a0 = dot2(buf[3 * t][e], qv[0][e], a0); a1 = dot2(buf[3 * t + 1][e], qv[1][e], a1); a2 = dot2(buf[3 * t + 2][e], qv[2][e], a2); }
        float a = (a0 + a1) + a2;
        a += dpp<XOR1>(a); a += dpp<XOR2>(a); a += dpp<HMIR>(a);
        sc[t] = a;
        if constexpr (t == 15) {
          float m = sc[0];
#pragma unroll
          for (int u = 1; u < 16; ++u) m = fmaxf(m, sc[u]);
          m = fmaxf(m, dpp<ROR8>(m)); m = x16_max(m); m = x32_max(m);
          const float mc = m * C2;
          float l = 0.f;
#pragma unroll
          for (int u = 0; u < 16; ++u) { const float p = __builtin_amdgcn_exp2f(fmaf(sc[u], C2, -mc)); pd[u] = cvtpk(p, p); l += bfhi(pd[u]); }
          l += dpp<ROR8>(l); l = x16_sum(l); l = x32_sum(l);
          if (lane == 0) { *(f32x2*)(l_ml + 2 * sw) = f32x2{mc, l}; MEMFENCE(); l_f[FA + sw] = 1; }
          STAMP(3);
        }
      } else {
        constexpr int u = t - 16;
        const u32x4 a = {pd[u] & msk[0], pd[u] & msk[1], pd[u] & msk[2], pd[u] & msk[3]};
#pragma unroll
        for (int i = 0; i < 3; ++i) acc[i] = mfma32(a, buf[3 * t + i], acc[i]);
      }
    };
    constexpr auto done_after = [](int n) constexpr {
      if (n < Q_AT) return 0;
      const int landed = (n - WIN + 1) / 3;
      const int cu = 1 + n - Q_AT;
      return landed < cu ? landed : cu;
    };
    static_for<0, 96>([&](auto N) {
      constexpr int n = decltype(N)::value;
      if constexpr (n == Q_AT) {
        STAMP(1);
        wait_lds<0>(fbase + FG, ALL4);
        STAMP(2);
#pragma unroll
        for (int i = 0; i < 3; ++i) qv[i] = *(const u32x4*)(l_q + 32 * i + 4 * j);
      }
      if constexpr (n >= WIN) asm volatile("s_waitcnt vmcnt(%0)" ::"i"(WIN - 1) : "memory");
      if constexpr (n == 9) TAG_READY();
      if constexpr (n >= 9 && n < 9 + 45 && (n - 9) % 5 == 0) FS(2 + (n - 9) / 5);
      if constexpr (n % 3 == 0) {
        constexpr int k = n / 3;
        if constexpr (k > 12) { const unsigned tv = __builtin_amdgcn_readfirstlane(tendv); tend = tv ? tv + stag : tend; }
        const unsigned now = pace_step<(k < 31 ? 65536 / (31 - k) : 0)>(tgt, tend);
        if constexpr (k >= 6 && k < 26) {
          unsigned vm;
          asm volatile("s_getreg_b32 %0, hwreg(HW_REG_IB_STS, 0, 4)" : "=s"(vm));
          l_est[sw] = (now + (((vm + 3 * (31 - k)) * RATE16) >> 4)) | 1u;
        }
        if constexpr (k >= 12 && k < 30) tendv = *l_tend;
      }
      if constexpr (n == 95) { if (lane == 0) l_f[FB + sw] = 1; }
      constexpr int m = n < 48 ? n : n - 48;
      buf[n] = SCAT == 1 ? ld_nt(n < 48 ? r0 : rv, voff + (m % 24) * 128, (m / 24) * 3072)
             : SCAT == 3 ? ld_nt(n < 48 ? r0 : rv, voff + (m % 12) * 128, (m / 12) * 49152)
                         : ld_nt(n < 48 ? r0 : rv, voff + (m % 3) * 128, (m / 3) * (SCAT == 2 ? 3072 : 12288));
      SB();
      static_for<done_after(n > 0 ? n - 1 : 0), done_after(n)>(consume);
      SB();
    });
    STAMP(4);
    __builtin_amdgcn_s_setprio(3);
    wait_lds<0>(fbase + FE, ALL4);
    float cq = l_c[lane >> 4];
    unsigned dst = (unsigned)(size_t)(lfloat*)(l_acc + (sw * 2 + (g & 1)) * 192 + 8 * j + 4 * ((lane >> 4) & 1));
    unsigned src = (unsigned)(size_t)(lfloat*)(l_acc + (lane >> 4) * 384 + 64 * sw + 8 * j + 4 * (g & 1));
    unsigned fla = fbase + FC + sw, one = 1u, flr = fbase + FC;
    int pvo = (lane & 16) ? OOB : (g & 1) * 128 + j * 16 + (lane >> 5) * 8;
    int pso = WS_OP + ((h * 2 + b) * 4 + s) * 768 + sw * 256;
    unsigned tagv = tag;
    asm volatile("" : "+v"(cq), "+v"(dst), "+v"(src), "+v"(fla), "+v"(flr), "+v"(one), "+v"(pvo), "+s"(pso), "+v"(tagv));
    const bool wr = (lane >> 5) == (g & 1);
    SB();
    static_for<done_after(95), 32>([&](auto G) { consume(G); SB(); });
    if (wr) {
#pragma unroll
      for (int i = 0; i < 3; ++i) *(lf32x4*)(size_t)(dst + 256 * i) = acc[i];
      MEMFENCE();
      *(lbyte*)(size_t)fla = (unsigned char)one;
    }
    STAMP(5);
    if (sw < 3) {
      f32x4 u0, u1;
      unsigned fv, fs;
      asm volatile("1:\n\tds_read_b32 %0, %4\n\tds_read_b128 %2, %5\n\tds_read_b128 %3, %5 offset:768\n\ts_waitcnt lgkmcnt(2)\n\tv_readfirstlane_b32 %1, %0\n\t"
                   "s_cmp_lg_u32 %1, 0x01010101\n\ts_waitcnt lgkmcnt(0)\n\ts_cbranch_scc1 1b"
                   : "=&v"(fv), "=&s"(fs), "=&v"(u0), "=&v"(u1) : "v"(flr), "v"(src) : "scc", "memory");
      float o[4];
#pragma unroll
      for (int e = 0; e < 4; ++e) o[e] = cq * (u0[e] + u1[e]);
      const auto s0 = __builtin_amdgcn_permlane32_swap(__float_as_uint(o[0]), __float_as_uint(o[2]), false, false);
      const auto s1 = __builtin_amdgcn_permlane32_swap(__float_as_uint(o[1]), __float_as_uint(o[3]), false, false);
      const float p0 = x16_sum(__uint_as_float(s0[0]) + __uint_as_float(s0[1])), p1 = x16_sum(__uint_as_float(s1[0]) + __uint_as_float(s1[1]));
      const u32x2 pv = {cvtpk(p0, p1), tagv};
      __builtin_amdgcn_raw_buffer_store_b64(pv, rws, pvo, pso, AUX_SC1);
    }
    MEMFENCE();
    *(lbyte*)(size_t)(fla + (FD - FC)) = (unsigned char)one;
    STAMP(6);
    STAMP_FLUSH();
    return;
  }

  __builtin_amdgcn_s_setprio(2);
  const int w = wave;
  const unsigned idx = w < 2 ? 2 * j8 + w : uq >> 1, ur = (idx * 11) >> 5, uc = idx - 3 * ur, up = uq & 1;
  const bool has_front = (w < 3) & (idx < 15);
  const unsigned m0 = e4 == 0 ? 0x0000ffffu : e4 == 1 ? 0xffff0000u : 0u;
  const unsigned m1 = e4 == 2 ? 0x0000ffffu : e4 == 3 ? 0xffff0000u : 0u;
  const int st_col = (8 * j + 4 * (lane >> 5) + 2 * (g & 1)) * 8;
  const rsrc_t rx = mk_rsrc(x, 2560);
  if (has_front) {
    const int xo = (lane >> 5) * 1280 + (lane & 31) * 16;
    const bool x3 = (lane & 16) == 0;
    const u32x4 xa = __builtin_amdgcn_raw_buffer_load_b128(rx, xo, 0, 0), xb = __builtin_amdgcn_raw_buffer_load_b128(rx, xo + 512, 0, 0);
    const u32x4 xc = __builtin_amdgcn_raw_buffer_load_b128(rx, x3 ? xo + 1024 : OOB, 0, 0);
    const u32x4 xr0 = __builtin_amdgcn_raw_buffer_load_b128(rx, bl * 1280 + g * 32, 256 * ur, 0);
    const u32x4 xr1 = __builtin_amdgcn_raw_buffer_load_b128(rx, bl * 1280 + g * 32 + 16, 256 * ur, 0);
    SB();
    FS(2);
    TAG_READY();
    float v[24];
#pragma unroll
    for (int e = 0; e < 4; ++e) { v[2 * e] = bflo(xa[e]); v[2 * e + 1] = bfhi(xa[e]); v[8 + 2 * e] = bflo(xb[e]); v[9 + 2 * e] = bfhi(xb[e]); v[16 + 2 * e] = bflo(xc[e]); v[17 + 2 * e] = bfhi(xc[e]); }
    float sum = 0.f;
#pragma unroll
    for (int e = 0; e < 24; ++e) sum += v[e];
    sum = x16_sum(row_sum(sum));
    const float mean = sum * (1.f / 640.f);
    float sq = 0.f, sq3 = 0.f;
#pragma unroll
    for (int e = 0; e < 16; ++e) { const float d = v[e] - mean; sq = fmaf(d, d, sq); }
#pragma unroll
    for (int e = 16; e < 24; ++e) { const float d = v[e] - mean; sq3 = fmaf(d, d, sq3); }
    sq = x16_sum(row_sum(sq + (x3 ? sq3 : 0.f)));
    const float mb = rbf(mean), den = rbf(__builtin_amdgcn_sqrtf(rbf(rbf(sq * (1.f / 640.f)) + 1e-5f)));
    const float inv = __builtin_amdgcn_rcpf(den);
    const float mb0 = __builtin_bit_cast(float, __builtin_amdgcn_readlane(__builtin_bit_cast(int, mb), 0));
    const float mb1 = __builtin_bit_cast(float, __builtin_amdgcn_readlane(__builtin_bit_cast(int, mb), 32));
    const float iv0 = __builtin_bit_cast(float, __builtin_amdgcn_readlane(__builtin_bit_cast(int, inv), 0));
    const float iv1 = __builtin_bit_cast(float, __builtin_amdgcn_readlane(__builtin_bit_cast(int, inv), 32));
    const float mm = bl ? mb1 : mb0, ii = bl ? iv1 : iv0;
    FS(3);
    asm volatile("s_waitcnt vmcnt(0)" : "+v"(fw[0]), "+v"(fw[1]), "+v"(fw[2]), "+v"(fw[3]), "+v"(fw[4]), "+v"(fw[5]), "+v"(fw[6]), "+v"(fw[7]),
                 "+v"(fw[8]), "+v"(fw[9]), "+v"(fw[10]), "+v"(fw[11]), "+v"(fw[12]), "+v"(fw[13]), "+v"(fw[14]), "+v"(fw[15]));
    f32x4 acc0 = {0.f, 0.f, 0.f, 0.f}, acc1 = {0.f, 0.f, 0.f, 0.f};
#pragma unroll
    for (int n = 0; n < 16; ++n) {
      const unsigned xw = n < 8 ? xr0[n >> 1] : xr1[(n - 8) >> 1];
      const float xv = (n & 1) ? bfhi(xw) : bflo(xw);
      const float xn = rbf(xv - mm) * ii;
      const unsigned d = cvtpk(xn, xn), a0 = d & m0, a1 = d & m1;
      acc0 = mfma16(a0, a1, fw[n][0], fw[n][1], acc0);
      acc1 = mfma16(a0, a1, fw[n][2], fw[n][3], acc1);
    }
    float v0, v1;
    proj_epilogue(acc0, acc1, g & 1, v0, v1);
    const int rec = (w < 2 ? WS_QP + (h * 5 + ur) * 1536 : WS_GU + ((xq * 5 + ur) * 2 + up) * 1536) + uc * 256;
    __builtin_amdgcn_raw_buffer_store_b64(u32x2{cvtpk(v0, v1), tag}, rws, ((lane >> 4) & 1) * 768 + st_col / 2, rec, AUX_SC1);
    STAMP(1); FS(4);
    asm volatile("s_waitcnt vmcnt(0)" ::: "memory");
    STAMP(11); FS(5);
  } else {
    asm volatile("s_waitcnt vmcnt(0)" ::"v"(fw[0]), "v"(fw[1]), "v"(fw[2]), "v"(fw[3]), "v"(fw[4]), "v"(fw[5]), "v"(fw[6]), "v"(fw[7]),
                 "v"(fw[8]), "v"(fw[9]), "v"(fw[10]), "v"(fw[11]), "v"(fw[12]), "v"(fw[13]), "v"(fw[14]), "v"(fw[15]));
    TAG_READY();
  }
  const int slot = w * 8 + j8;
  const bool has_back = slot < 30;
  const int t3 = slot / 10, cc = slot - 10 * t3;
  const int rb = bid / 80, rc = (bid - 80 * rb) >> 3, ro = bid & 7;
  const bool reducer = (w == 0) && (bid < 160);
  unsigned ha[2][2] = {{0u, 0u}, {0u, 0u}};
  unsigned tend = tstart + T_PLAN;
  auto sample = [&](unsigned t) {
    wait_until(t);
    u32x4 e;
    do { MEMFENCE(); e = *(const u32x4*)l_est; } while (!__builtin_amdgcn_readfirstlane(e[0] & e[1] & e[2] & e[3] & 1u));
    return (int)((e[0] - tstart) + (e[1] - tstart) + (e[2] - tstart) + (e[3] - tstart));
  };
  auto pace_round = [&](int est4, unsigned t_read, int so, int bias) {
    const u32x2 pv = {tstart + (unsigned)(est4 >> 2), tag};
    __builtin_amdgcn_raw_buffer_store_b64(pv, rws, (lane == 0 && bid < 32) ? bid * 8 : OOB, so, AUX_SC1);
    wait_until(t_read);
    MEMFENCE();
    const u32x2 tv = __builtin_amdgcn_raw_buffer_load_b64(rws, (lane & 31) * 8, so, AUX_SC1);
    if (__all(tv[1] == tag)) {
      const float d = x16_sum(row_sum((float)(int)(tv[0] - tstart)));
      int di = (int)(d * (1.f / 32.f)) - bias;
      di = di < 0 ? 0 : (di > 2500 ? 2500 : di);
      tend = (tstart + __builtin_amdgcn_readfirstlane(di)) | 1u;
      *l_tend = tend;
    }
  };
  int e1 = 0;
  if (w < 3) {
    if (has_front) __builtin_amdgcn_s_sleep(16); else wait_until(tstart + 360);
    u32x2 t[3];
    const int vo = (lane >> 5) * 1536 + (lane & 31) * 8, so = WS_QP + h * 5 * 1536 + b * 768 + w * 256;
    for (unsigned spin = 0;; ++spin) {
      MEMFENCE();
      t[0] = ld2_sc1(rws, vo, so); t[1] = ld2_sc1(rws, vo, so + 3072); t[2] = ld2_sc1(rws, lane < 32 ? vo : OOB, so + 6144);
      const bool ok = (t[0][1] == tag) & (t[1][1] == tag) & ((t[2][1] == tag) | (lane >= 32));
      if (__all(ok) || spin > SPIN_CAP) { STAMPV(12, spin + 1); break; }
    }
    const float a = x32_sum((bflo(t[0][0]) + bflo(t[1][0])) + bflo(t[2][0]));
    const float c = x32_sum((bfhi(t[0][0]) + bfhi(t[1][0])) + bfhi(t[2][0]));
    if (lane < 32) { l_q[32 * w + lane] = cvtpk(a, c); MEMFENCE(); l_f[FG + w] = 1; }
  } else {
    l_f[FG + 3] = 1;
    pace_round(sample(tstart + SMP0), tstart + SMP0 + WAIT0, WS_TS, BIAS0);
    STAMPT(1, tend);
    STAMP(9);
    e1 = sample(tend - SMP1 - GAP1);
  }
  STAMP(2); FS(6);
  if (has_back) {
    u32x2 t[3];
    const int so = WS_GU + xq * 5 * 3072 + (h & 3) * 192 + t3 * 64;
    const int vo = bl * 768 + 8 * g;
    for (unsigned spin = 0;; ++spin) {
      MEMFENCE();
      t[0] = ld2_sc1(rws, vo + e4 * 3072, so);
      t[1] = ld2_sc1(rws, vo + e4 * 3072 + 1536, so);
      t[2] = ld2_sc1(rws, vo + 4 * 3072 + (e4 & 1) * 1536, so);
      if (__all((t[0][1] == tag) & (t[1][1] == tag) & (t[2][1] == tag)) || spin > SPIN_CAP) { STAMPV(13, spin + 1); break; }
    }
#pragma unroll
    for (int z = 0; z < 2; ++z) {
      const float ex = z ? bfhi(t[2][0]) : bflo(t[2][0]);
      const float gg = rbf(quad_sum((z ? bfhi(t[0][0]) : bflo(t[0][0])) + (e4 == 0 ? ex : 0.f)));
      const float uu = rbf(quad_sum((z ? bfhi(t[1][0]) : bflo(t[1][0])) + (e4 == 1 ? ex : 0.f)));
      const float zz = 0.7978845608028654f * (gg + 0.044715f * gg * gg * gg);
      const float ge = rbf(gg * __builtin_amdgcn_rcpf(1.f + __builtin_amdgcn_exp2f(-2.8853900817779268f * zz)));
      const float hv = ge * uu;
      const unsigned d = cvtpk(hv, hv);
      ha[z][0] = d & m0; ha[z][1] = d & m1;
    }
  }
  const int oc = 4 * (lane & 1) + 2 * (lane >> 5) + ((lane >> 4) & 1);
  const rsrc_t rout = mk_rsrc(out, 2560);
  int out_vo = (lane & 14) == 0 ? oc * 2 + rb * 1280 + rc * 128 + ro * 16 : OOB;
  float xres = 0.f;
  if (reducer) {
    xres = bflo(__builtin_amdgcn_raw_buffer_load_b16(rx, oc * 2, rb * 1280 + rc * 128 + ro * 16, 0));
    __builtin_amdgcn_raw_buffer_store_b16((unsigned short)0, rout, out_vo, 0, AUX_SC1);
  }
  STAMP(3);
  if (w == 1) {
    wait_lds<4>(fbase + FA, ALL4);
    const int so = WS_ML + (h * 2 + b) * 256;
    if (lane < 4) { const f32x2 ml = *(const f32x2*)(l_ml + 2 * lane); st_pair(rws, lane * 16, so + s * 64, ml[0], ml[1], tag); }
    asm volatile("s_waitcnt vmcnt(0)" ::: "memory");
    __builtin_amdgcn_s_sleep(32);
    u32x4 t;
    for (unsigned spin = 0;; ++spin) {
      MEMFENCE();
      t = ld_sc1(rws, (lane & 15) * 16, so);
      if (__all((t[1] == tag) & (t[3] == tag)) || spin > SPIN_CAP) break;
    }
    const float mv = __uint_as_float(t[0]);
    const float M = row_max(mv);
    const float f = __builtin_amdgcn_exp2f(mv - M);
    const float L = row_sum(f * __uint_as_float(t[2]));
    if ((lane >> 2) == s) { l_c[lane & 3] = f / L; MEMFENCE(); l_f[FE + (lane & 3)] = 1; }
  }
  if (w == 3) {
    const int e2 = sample(tend - SMP1);
    pace_round(e2 + (((e2 - e1) * KAPPA16) >> 4), tend - SMP1 + WAIT1, WS_TS + 256, BIAS1);
    STAMPT(11, tend);
  }
  if (has_back) {
    const unsigned sLo = (e4 & 1) ? 0x01000c0cu : 0x0c0c0100u, sHi = (e4 & 1) ? 0x03020c0cu : 0x0c0c0302u;
    unsigned sel0 = e4 < 2 ? sLo : 0x0c0c0c0cu, sel1 = e4 < 2 ? 0x0c0c0c0cu : sLo, sel2 = e4 < 2 ? sHi : 0x0c0c0c0cu, sel3 = e4 < 2 ? 0x0c0c0c0cu : sHi;
    int op_so = WS_OP + h * 8 * 768 + t3 * 256, op_vo = (bl * 4 + e4) * 768 + 16 * g;
    int d_vo = ((48 * h + 16 * t3 + 2 * g) * 640 + 64 * cc) * 2 + 16 * j, o_vo = ((192 * h + 64 * t3 + 8 * g) * 640 + 64 * cc) * 2 + 16 * j;
    int yvo = ((lane >> 4) & 1) * 256 + st_col / 2, yso = WS_YP + ((h * 3 + t3) * 10 + cc) * 512;
    int r_vo = (lane >> 1) * 5120 + (lane & 1) * 16 + WS_YP + rc * 512 + rb * 256 + ro * 32;
    unsigned tagv = tag, fd = fbase + FD;
    asm volatile("" : "+v"(sel0), "+v"(sel1), "+v"(sel2), "+v"(sel3), "+s"(op_so), "+v"(op_vo), "+v"(d_vo), "+v"(o_vo), "+v"(yvo), "+s"(yso), "+v"(r_vo), "+v"(out_vo), "+v"(tagv), "+v"(xres), "+v"(fd));
    const rsrc_t rd = mk_rsrc(wd, 1536 * 1280), ro_ = mk_rsrc(wo, 6144 * 1280);
    STAMP(4);
    wait_lds<1>(fbase + FB, ALL4);
    delay_ticks(BACK_DELAY + HSTAG * w);
    u32x4 wb[10], t0, t1;
    unsigned fdv = 0;
#pragma unroll
    for (int n = 0; n < 10; ++n) {
      if (n == POLLPOS) {
        const rsrc_t rp = mk_rsrc(ws, __builtin_amdgcn_readfirstlane(fdv) == ALL4 ? WS_END : 0);
        t0 = ld_sc1(rp, op_vo, op_so); t1 = ld_sc1(rp, op_vo + 128, op_so);
        SB();
      }
      if (n < 2) wb[8 + n] = ld_nt(rd, d_vo + n * 1280, 0); else wb[n - 2] = ld_nt(ro_, o_vo, (n - 2) * 1280);
      if (n == POLLPOS - 1) { MEMFENCE(); fdv = *(luint*)(size_t)fd; }
      SB();
      if (n < 9) __builtin_amdgcn_s_sleep(BPACE);
      SB();
    }
    STAMP(5);
    u32x4 bq[10];
#define PAIR(p) do { bq[2 * (p)] = u32x4{wb[2 * (p)][0], wb[2 * (p)][1], wb[2 * (p) + 1][0], wb[2 * (p) + 1][1]}; \
                     bq[2 * (p) + 1] = u32x4{wb[2 * (p)][2], wb[2 * (p)][3], wb[2 * (p) + 1][2], wb[2 * (p) + 1][3]}; } while (0)
    PAIR(4); PAIR(0); PAIR(1); PAIR(2);
    const u32x4 hA = {ha[0][0], ha[0][1], ha[1][0], ha[1][1]};
    f32x4 acc0 = mfma32(hA, bq[8], f32x4{0.f, 0.f, 0.f, 0.f}), acc1 = mfma32(hA, bq[9], f32x4{0.f, 0.f, 0.f, 0.f});
    asm volatile("" : "+v"(bq[0]), "+v"(bq[1]), "+v"(bq[2]), "+v"(bq[3]), "+v"(bq[4]), "+v"(bq[5]), "+v"(acc0), "+v"(acc1));
    SB();
    unsigned np1 = 1;
    if (__builtin_expect(__builtin_amdgcn_ballot_w64((((t0[1] ^ tagv) | (t0[3] ^ tagv)) | ((t1[1] ^ tagv) | (t1[3] ^ tagv))) != 0u) != 0, 0)) {
      do {
        ++np1;
        MEMFENCE();
        if (__builtin_amdgcn_readfirstlane(*(luint*)(size_t)fd) != ALL4) { wait_lds<1>(fd, ALL4); delay_ticks(LATE_DELAY); }
        t0 = ld_sc1(rws, op_vo, op_so); t1 = ld_sc1(rws, op_vo + 128, op_so);
      } while (!__all((t0[1] == tag) & (t0[3] == tag) & (t1[1] == tag) & (t1[3] == tag)) && np1 < SPIN_CAP);
    }
    STAMPV(14, np1);
    STAMP(7);
    unsigned pk[4];
    {
      float u0, u1, u2, u3, u4, u5, u6, u7;
#define DPPADD(r, P) "v_add_f32_dpp " r ", " r ", " r " quad_perm:" P " row_mask:0xf bank_mask:0xf\n\t"
#define DPP8(P) DPPADD("%4", P) DPPADD("%5", P) DPPADD("%6", P) DPPADD("%7", P) DPPADD("%8", P) DPPADD("%9", P) DPPADD("%10", P) DPPADD("%11", P)
      asm volatile("v_lshlrev_b32 %4, 16, %12\n\tv_and_b32 %5, 0xffff0000, %12\n\tv_lshlrev_b32 %6, 16, %13\n\tv_and_b32 %7, 0xffff0000, %13\n\t"
                   "v_lshlrev_b32 %8, 16, %14\n\tv_and_b32 %9, 0xffff0000, %14\n\tv_lshlrev_b32 %10, 16, %15\n\tv_and_b32 %11, 0xffff0000, %15\n\t"
                   DPP8("[1,0,3,2]") DPP8("[2,3,0,1]")
                   "v_cvt_pk_bf16_f32 %0, %4, %5\n\tv_cvt_pk_bf16_f32 %1, %6, %7\n\tv_cvt_pk_bf16_f32 %2, %8, %9\n\tv_cvt_pk_bf16_f32 %3, %10, %11"
                   : "=&v"(pk[0]), "=&v"(pk[1]), "=&v"(pk[2]), "=&v"(pk[3]), "=&v"(u0), "=&v"(u1), "=&v"(u2), "=&v"(u3), "=&v"(u4), "=&v"(u5), "=&v"(u6), "=&v"(u7)
                   : "v"(t0[0]), "v"(t0[2]), "v"(t1[0]), "v"(t1[2]));
    }
    u32x4 aq[4];
#pragma unroll
    for (int p = 0; p < 4; ++p)
      aq[p] = u32x4{__builtin_amdgcn_perm(0u, pk[p], sel0), __builtin_amdgcn_perm(0u, pk[p], sel1), __builtin_amdgcn_perm(0u, pk[p], sel2), __builtin_amdgcn_perm(0u, pk[p], sel3)};
#pragma unroll
    for (int p = 0; p < 3; ++p) { acc0 = mfma32(aq[p], bq[2 * p], acc0); acc1 = mfma32(aq[p], bq[2 * p + 1], acc1); }
    asm volatile("" : "+v"(aq[3]), "+v"(acc0), "+v"(acc1));
    SB();
    acc0 = mfma16(aq[3][0], aq[3][1], wb[6][0], wb[6][1], acc0); acc1 = mfma16(aq[3][0], aq[3][1], wb[6][2], wb[6][3], acc1);
    acc0 = mfma16(aq[3][2], aq[3][3], wb[7][0], wb[7][1], acc0); acc1 = mfma16(aq[3][2], aq[3][3], wb[7][2], wb[7][3], acc1);
    float v0, v1;
    proj_epilogue(acc0, acc1, g & 1, v0, v1);
    __builtin_amdgcn_raw_buffer_store_b64(u32x2{cvtpk(v0, v1), tagv}, rws, yvo, yso, AUX_SC1);
    STAMP(8);
    if (reducer) {
      delay_ticks(FIN_DELAY);
      float y0, y1, y2, y3;
      unsigned np2 = 0;
      for (;;) {
        MEMFENCE();
        const u32x4 a = ld_sc1(rws, r_vo, 0), c = ld_sc1(rws, r_vo, 32 * 5120), d = ld_sc1(rws, r_vo, 64 * 5120);
        unsigned bad = ((a[1] ^ tagv) | (a[3] ^ tagv)) | ((c[1] ^ tagv) | (c[3] ^ tagv));
        y0 = bflo(a[0]) + bflo(c[0]); y1 = bfhi(a[0]) + bfhi(c[0]); y2 = bflo(a[2]) + bflo(c[2]); y3 = bfhi(a[2]) + bfhi(c[2]);
        asm volatile("" : "+v"(bad), "+v"(y0), "+v"(y1), "+v"(y2), "+v"(y3));
        bad |= (d[1] ^ tagv) | (d[3] ^ tagv);
        y0 += bflo(d[0]); y1 += bfhi(d[0]); y2 += bflo(d[2]); y3 += bfhi(d[2]);
        if (__builtin_expect(__builtin_amdgcn_ballot_w64(bad != 0u) == 0, 1)) break;
        if (++np2 > SPIN_CAP) break;
      }
      STAMPV(15, np2 + 1);
      STAMP(9);
      const auto s0 = __builtin_amdgcn_permlane32_swap(__float_as_uint(y0), __float_as_uint(y2), false, false);
      const auto s1 = __builtin_amdgcn_permlane32_swap(__float_as_uint(y1), __float_as_uint(y3), false, false);
      const float z0 = __uint_as_float(s0[0]) + __uint_as_float(s0[1]), z1 = __uint_as_float(s1[0]) + __uint_as_float(s1[1]);
      const auto s2 = __builtin_amdgcn_permlane16_swap(__float_as_uint(z0), __float_as_uint(z1), false, false);
      float y = __uint_as_float(s2[0]) + __uint_as_float(s2[1]);
      y += dpp<ROR8>(y); y += dpp<ROR4>(y); y += dpp<XOR2>(y);
      __builtin_amdgcn_raw_buffer_store_b16((unsigned short)cvtpk(xres + y, 0.f), rout, out_vo, 0, AUX_SC1);
      if (bid == 0) __builtin_amdgcn_raw_buffer_store_b32(tagv, rws, lane == 0 ? WS_EPOCH : OOB, 0, AUX_SC1);
      STAMP(10);
    }
  }
  STAMP_FLUSH();
}

extern "C" void kernel_launch(void* const* d_in, const int* in_sizes, int n_in, void* d_out, int out_size, void* d_ws, size_t ws_size,
                              hipStream_t stream) {
  fused_decode<<<256, 512, 0, stream>>>((const u16*)d_in[0], (const u16*)d_in[1], (const u16*)d_in[2], (const u16*)d_in[3], (const u16*)d_in[4],
                                        (const u16*)d_in[6], (const u16*)d_in[7], (const u16*)d_in[8], (u16*)d_out, (unsigned char*)d_ws
                                        );
}
```

```cpp
#define AMP 0
#include <hip/hip_runtime.h>
#include <hip/hip_bf16.h>
#include <stdint.h>
#include <type_traits>

typedef unsigned short u16;
typedef unsigned u32x4 __attribute__((ext_vector_type(4)));
typedef unsigned u32x2 __attribute__((ext_vector_type(2)));
typedef float f32x4 __attribute__((ext_vector_type(4)));
typedef float f32x2 __attribute__((ext_vector_type(2)));
typedef short s16x4 __attribute__((ext_vector_type(4)));
typedef __bf16 bf16x8 __attribute__((ext_vector_type(8)));
typedef __bf16 bf16x2 __attribute__((ext_vector_type(2)));
typedef __amdgpu_buffer_rsrc_t rsrc_t;

#define WIN 9
#define Q_AT 39
#define BPACE 5
#define POLLPOS 8
#define STAG 11
#define HSTAG 0
#define CODE_BYTES 16640
#define P_B0 40
#define P_B1 30
#define P_KAP 34
#define P_FIN 42
constexpr int T_PLAN = 1480;
constexpr int S_DELAY = 110;
constexpr int RATE16 = 229;
constexpr int SMP0 = 480, WAIT0 = 220, BIAS0 = P_B0;
constexpr int SMP1 = 420, WAIT1 = 140, BIAS1 = P_B1;
constexpr int GAP1 = 200, KAPPA16 = P_KAP;
constexpr int BACK_DELAY = 16;
constexpr int LATE_DELAY = 20;
constexpr int FIN_DELAY = P_FIN;

constexpr int AUX_NT = 2, AUX_SC1 = 16;
constexpr int RSRC_W3 = 0x00020000;
constexpr unsigned SPIN_CAP = 1u << 18;
constexpr int OOB = 0x40000000;
constexpr unsigned ALL4 = 0x01010101u;

constexpr int WS_EPOCH = 0;
constexpr int WS_TS = 1024;
constexpr int WS_QP = 4096;
constexpr int WS_GU = WS_QP + 32 * 5 * 2 * 96 * 8;
constexpr int WS_ML = WS_GU + 8 * 5 * 2 * 2 * 96 * 8;
constexpr int WS_OP = WS_ML + 32 * 2 * 4 * 4 * 16;
constexpr int WS_YP = WS_OP + 32 * 2 * 4 * 96 * 8;
constexpr int WS_END = WS_YP + 96 * 10 * 2 * 32 * 8;

template <int I, int N, class F> __device__ __forceinline__ void static_for(F&& f) {
  if constexpr (I < N) { f(std::integral_constant<int, I>{}); static_for<I + 1, N>(f); }
}
__device__ __forceinline__ rsrc_t mk_rsrc(const void* p, int bytes) {
  return __builtin_amdgcn_make_buffer_rsrc(const_cast<void*>(p), 0, bytes, RSRC_W3);
}
__device__ __forceinline__ u32x4 ld_nt(rsrc_t r, int voff, int soff) { return __builtin_amdgcn_raw_buffer_load_b128(r, voff, soff, AUX_NT); }
__device__ __forceinline__ u32x4 ld_sc1(rsrc_t r, int voff, int soff) { return __builtin_amdgcn_raw_buffer_load_b128(r, voff, soff, AUX_SC1); }
__device__ __forceinline__ u32x2 ld2_sc1(rsrc_t r, int voff, int soff) { return __builtin_amdgcn_raw_buffer_load_b64(r, voff, soff, AUX_SC1); }
__device__ __forceinline__ void st_pair(rsrc_t r, int voff, int soff, float a, float b, unsigned tag) {
  u32x4 v = {__float_as_uint(a), tag, __float_as_uint(b), tag};
  __builtin_amdgcn_raw_buffer_store_b128(v, r, voff, soff, AUX_SC1);
}
__device__ __forceinline__ float bflo(unsigned v) { return __uint_as_float(v << 16); }
__device__ __forceinline__ float bfhi(unsigned v) { return __uint_as_float(v & 0xffff0000u); }
__device__ __forceinline__ unsigned cvtpk(float lo, float hi) {
  bf16x2 r = {(__bf16)lo, (__bf16)hi};
  return __builtin_bit_cast(unsigned, r);
}
__device__ __forceinline__ float rbf(float x) { return (float)(__bf16)x; }
template <int CTRL> __device__ __forceinline__ float dpp(float x) {
  return __builtin_bit_cast(float, __builtin_amdgcn_mov_dpp(__builtin_bit_cast(int, x), CTRL, 0xf, 0xf, true));
}
constexpr int XOR1 = 0xB1, XOR2 = 0x4E, HMIR = 0x141, ROR8 = 0x128, ROR4 = 0x124;
__device__ __forceinline__ float x16_sum(float x) { auto s = __builtin_amdgcn_permlane16_swap(__float_as_uint(x), __float_as_uint(x), false, false); return __uint_as_float(s[0]) + __uint_as_float(s[1]); }
__device__ __forceinline__ float x32_sum(float x) { auto s = __builtin_amdgcn_permlane32_swap(__float_as_uint(x), __float_as_uint(x), false, false); return __uint_as_float(s[0]) + __uint_as_float(s[1]); }
__device__ __forceinline__ float x16_max(float x) { auto s = __builtin_amdgcn_permlane16_swap(__float_as_uint(x), __float_as_uint(x), false, false); return fmaxf(__uint_as_float(s[0]), __uint_as_float(s[1])); }
__device__ __forceinline__ float x32_max(float x) { auto s = __builtin_amdgcn_permlane32_swap(__float_as_uint(x), __float_as_uint(x), false, false); return fmaxf(__uint_as_float(s[0]), __uint_as_float(s[1])); }
__device__ __forceinline__ float row_sum(float x) { x += dpp<XOR1>(x); x += dpp<XOR2>(x); x += dpp<HMIR>(x); x += dpp<ROR8>(x); return x; }
__device__ __forceinline__ float row_max(float x) { x = fmaxf(x, dpp<XOR1>(x)); x = fmaxf(x, dpp<XOR2>(x)); x = fmaxf(x, dpp<HMIR>(x)); x = fmaxf(x, dpp<ROR8>(x)); return x; }
__device__ __forceinline__ float quad_sum(float x) { x += dpp<XOR1>(x); x += dpp<XOR2>(x); return x; }
__device__ __forceinline__ float dot2(unsigned a, unsigned b, float c) {
  return __builtin_amdgcn_fdot2_f32_bf16(__builtin_bit_cast(bf16x2, a), __builtin_bit_cast(bf16x2, b), c, false);
}
__device__ __forceinline__ f32x4 mfma16(unsigned a0, unsigned a1, unsigned b0, unsigned b1, f32x4 c) {
  u32x2 a = {a0, a1}, b = {b0, b1};
  return __builtin_amdgcn_mfma_f32_16x16x16bf16_1k(__builtin_bit_cast(s16x4, a), __builtin_bit_cast(s16x4, b), c, 0, 0, 0);
}
__device__ __forceinline__ f32x4 mfma32(u32x4 a, u32x4 b, f32x4 c) {
  return __builtin_amdgcn_mfma_f32_16x16x32_bf16(__builtin_bit_cast(bf16x8, a), __builtin_bit_cast(bf16x8, b), c, 0, 0, 0);
}
template <int RECIP> __device__ __forceinline__ unsigned pace_step(unsigned& tgt, unsigned tend) {
  unsigned d, now;
  asm volatile("1:\n\ts_memrealtime vcc\n\ts_waitcnt lgkmcnt(0)\n\ts_sub_u32 %0, vcc_lo, %2\n\ts_cmp_lt_i32 %0, 0\n\ts_cbranch_scc1 1b\n\t"
               "s_sub_i32 %0, %3, vcc_lo\n\ts_max_i32 %0, %0, 0\n\ts_min_i32 %0, %0, 0x7d0\n\ts_mul_i32 %0, %0, %4\n\ts_lshr_b32 %0, %0, 16\n\t"
               "s_add_u32 %2, vcc_lo, %0\n\ts_mov_b32 %1, vcc_lo"
               : "=&s"(d), "=&s"(now), "+s"(tgt) : "s"(tend), "i"(RECIP) : "vcc", "scc");
  return now;
}
__device__ __forceinline__ void wait_until(unsigned tgt) {
  asm volatile("1:\n\ts_memrealtime vcc\n\ts_waitcnt lgkmcnt(0)\n\ts_sub_u32 vcc_lo, vcc_lo, %0\n\ts_cmp_lt_i32 vcc_lo, 0\n\ts_cbranch_scc1 1b" :: "s"(tgt) : "vcc", "scc", "memory");
}
#define SB() __builtin_amdgcn_sched_barrier(0)
#define MEMFENCE() asm volatile("" ::: "memory")

#define FS(i)
#define STAMP(i)
#define STAMPV(i, v)
#define STAMPT(i, v)
#define STAMP_DECL
#define STAMP_FLUSH()

__device__ __forceinline__ void delay_ticks(int n) {
  if (n > 0) wait_until((unsigned)__builtin_amdgcn_s_memrealtime() + n);
}

typedef __attribute__((address_space(3))) float lfloat;
typedef __attribute__((address_space(3))) f32x4 lf32x4;
typedef __attribute__((address_space(3))) unsigned luint;
typedef __attribute__((address_space(3))) unsigned char lbyte;
template <int SLP> __device__ __forceinline__ void wait_lds(unsigned a, unsigned want) {
  unsigned v, sv;
  asm volatile("s_branch 2f\n1:\n\ts_sleep %4\n2:\n\tds_read_b32 %0, %2\n\ts_waitcnt lgkmcnt(0)\n\tv_readfirstlane_b32 %1, %0\n\ts_cmp_lg_u32 %1, %3\n\ts_cbranch_scc1 1b"
               : "=&v"(v), "=&s"(sv) : "v"(a), "s"(want), "i"(SLP) : "scc", "memory");
}

__device__ __forceinline__ void proj_epilogue(f32x4 acc0, f32x4 acc1, bool z, float& v0, float& v1) {
  float n0[4], n1[4];
#pragma unroll
  for (int r = 0; r < 4; ++r) {
    auto s = __builtin_amdgcn_permlane32_swap(__float_as_uint(acc0[r]), __float_as_uint(acc1[r]), false, false);
    n0[r] = __uint_as_float(s[0]); n1[r] = __uint_as_float(s[1]);
  }
  const float r0 = n0[0] + dpp<ROR8>(n1[0]), r1 = n0[1] + dpp<ROR8>(n1[1]);
  const float r2 = n1[2] + dpp<ROR8>(n0[2]), r3 = n1[3] + dpp<ROR8>(n0[3]);
  v0 = z ? r2 : r0; v1 = z ? r3 : r1;
}

__global__ void __launch_bounds__(512, 2)
fused_decode(const u16* x_, const u16* kc_, const u16* vc_, const u16* wq_, const u16* wo_, const u16* wg_, const u16* wu_, const u16* wd_, u16* out_, unsigned char* ws_
             ) {
  __shared__ __attribute__((aligned(16))) float lds[96 + 8 + 4 + 16 + 8 * 192];
  unsigned* l_q = (unsigned*)lds;
  float* l_ml = lds + 96;
  float* l_c = lds + 104;
  unsigned* l_est = (unsigned*)(lds + 108);
  unsigned char* l_f = (unsigned char*)(lds + 112);
  constexpr int FA = 0, FB = 4, FC = 8, FD = 12, FE = 16, FG = 20;
  unsigned* l_tend = (unsigned*)(lds + 118);
  const unsigned fbase = (unsigned)(size_t)(lbyte*)l_f;
  float* l_acc = lds + 124;

  STAMP_DECL;
  STAMP(0);
  unsigned amp0;
  { unsigned long long t_; asm volatile("s_memrealtime %0\n\ts_waitcnt lgkmcnt(0)" : "=s"(t_) :: "memory"); amp0 = (unsigned)t_; }
  typedef unsigned long long u64;
  typedef u64 u64x8 __attribute__((ext_vector_type(8)));
  typedef u64 u64x2 __attribute__((ext_vector_type(2)));
  u64x8 ka; u64x2 kb;
  u32x4 fw[16];
  int bid = blockIdx.x, tid = threadIdx.x;
  {
    unsigned v1, v2;
    u64x8 ka0; u64x2 kb0;
    asm volatile("s_load_dwordx16 %0, %2, 0x0\n\ts_load_dwordx4 %1, %2, 0x40" : "=&{s[36:51]}"(ka0), "=&{s[52:55]}"(kb0) : "s"(__builtin_amdgcn_kernarg_segment_ptr()));
    asm volatile(
               "v_readfirstlane_b32 s14, %20\n\t"
               "s_lshr_b32 s15, %19, 3\n\t"
               "s_cmpk_ge_u32 s14, 0xc0\n\ts_cbranch_scc1 1f\n\t"
               "s_setprio 2\n\t"
               "s_and_b32 s16, %19, 7\n\t"
               "s_lshr_b32 s17, %19, 6\n\ts_lshl2_add_u32 s17, s16, s17\n\t"
               "s_and_b32 s18, s15, 7\n\ts_lshr_b32 s19, s14, 6\n\ts_lshl1_add_u32 s18, s18, s19\n\t"
               "s_lshr_b32 s19, %19, 4\n\t"
               "v_bfe_u32 %16, %20, 3, 3\n\tv_and_b32 %17, 7, %20\n\t"
               "s_movk_i32 s13, 0x3000\n\t"
               "s_cmpk_lt_u32 s14, 0x80\n\t"
               "s_cselect_b32 s18, s18, s19\n\ts_cselect_b32 s17, s17, s16\n\ts_cselect_b32 s13, s13, 0xc00\n\t"
               "s_mul_i32 s19, s18, 11\n\ts_lshr_b32 s19, s19, 5\n\t"
               "v_mad_u32_u24 %16, %16, s13, %17\n\t"
               "s_sub_u32 s17, s17, s19\n\ts_mul_i32 s17, s17, 3\n\ts_add_u32 s17, s17, s18\n\t"
               "s_mul_i32 s19, s19, s13\n\ts_add_u32 s12, s17, s19\n\ts_lshl_b32 s12, s12, 7\n\t"
               "v_lshlrev_b32 %16, 4, %16\n\t"
               "s_mul_i32 s10, s13, 0x280\n\ts_mov_b32 s11, 0x20000\n\ts_cmp_eq_u32 s18, 15\n\ts_cselect_b32 s10, 0, s10\n\t"
               "s_bitcmp1_b32 s15, 0\n\t"
               "s_waitcnt lgkmcnt(0)\n\t"
               "s_cselect_b64 s[8:9], s[48:49], s[46:47]\n\t"
               "s_cmpk_lt_u32 s14, 0x80\n\ts_cselect_b64 s[8:9], s[42:43], s[8:9]\n\t"
               "s_and_b32 s9, s9, 0xffff\n\t"
               "buffer_load_dwordx4 %0, %16, s[8:11], s12 offen nt\n\t" "s_add_u32 s12, s12, s13\n\t"
               "buffer_load_dwordx4 %1, %16, s[8:11], s12 offen nt\n\t" "s_add_u32 s12, s12, s13\n\t"
               "buffer_load_dwordx4 %2, %16, s[8:11], s12 offen nt\n\t" "s_add_u32 s12, s12, s13\n\t"
               "buffer_load_dwordx4 %3, %16, s[8:11], s12 offen nt\n\t" "s_add_u32 s12, s12, s13\n\t"
               "buffer_load_dwordx4 %4, %16, s[8:11], s12 offen nt\n\t" "s_add_u32 s12, s12, s13\n\t"
               "buffer_load_dwordx4 %5, %16, s[8:11], s12 offen nt\n\t" "s_add_u32 s12, s12, s13\n\t"
               "buffer_load_dwordx4 %6, %16, s[8:11], s12 offen nt\n\t" "s_add_u32 s12, s12, s13\n\t"
               "buffer_load_dwordx4 %7, %16, s[8:11], s12 offen nt\n\t" "s_add_u32 s12, s12, s13\n\t"
               "buffer_load_dwordx4 %8, %16, s[8:11], s12 offen nt\n\t" "s_add_u32 s12, s12, s13\n\t"
               "buffer_load_dwordx4 %9, %16, s[8:11], s12 offen nt\n\t" "s_add_u32 s12, s12, s13\n\t"
               "buffer_load_dwordx4 %10, %16, s[8:11], s12 offen nt\n\t" "s_add_u32 s12, s12, s13\n\t"
               "buffer_load_dwordx4 %11, %16, s[8:11], s12 offen nt\n\t" "s_add_u32 s12, s12, s13\n\t" "s_barrier\n\t"
               "buffer_load_dwordx4 %12, %16, s[8:11], s12 offen nt\n\t" "s_add_u32 s12, s12, s13\n\t"
               "buffer_load_dwordx4 %13, %16, s[8:11], s12 offen nt\n\t" "s_add_u32 s12, s12, s13\n\t"
               "buffer_load_dwordx4 %14, %16, s[8:11], s12 offen nt\n\t" "s_add_u32 s12, s12, s13\n\t"
               "buffer_load_dwordx4 %15, %16, s[8:11], s12 offen nt\n\t"
               "s_branch 2f\n"
               "1:\n\t"
               "v_and_b32 %16, 15, %20\n\tv_lshl_add_u32 %16, %16, 2, %21\n\tv_mov_b32 %17, 0\n\tds_write_b32 %16, %17\n\t"
               "s_waitcnt lgkmcnt(0)\n\ts_barrier\n"
               "2:"
               : "=&v"(fw[0]), "=&v"(fw[1]), "=&v"(fw[2]), "=&v"(fw[3]), "=&v"(fw[4]), "=&v"(fw[5]), "=&v"(fw[6]), "=&v"(fw[7]),
                 "=&v"(fw[8]), "=&v"(fw[9]), "=&v"(fw[10]), "=&v"(fw[11]), "=&v"(fw[12]), "=&v"(fw[13]), "=&v"(fw[14]), "=&v"(fw[15]),
                 "=&v"(v1), "=&v"(v2)
               : "{s[36:51]}"(ka0), "s"(bid), "v"(tid), "s"(fbase - 16), "{s[52:55]}"(kb0)
               : "s8", "s9", "s10", "s11", "s12", "s13", "s14", "s15", "s16", "s17", "s18", "s19", "scc", "memory");
    asm volatile("" : "={s[36:51]}"(ka), "={s[52:55]}"(kb), "+s"(bid) : "0"(ka0), "1"(kb0));
    asm volatile("" : "+v"(tid));
  }
  const int xq = bid & 7, uq = bid >> 3;
  const int h = xq * 4 + (uq >> 3), j8 = uq & 7, b = j8 >> 2, s = j8 & 3;
  const int lane = tid & 63, wave = __builtin_amdgcn_readfirstlane(tid >> 6);
  const int g = lane >> 3, j = lane & 7;
  const int bl = (lane >> 2) & 1, e4 = lane & 3;
  const bool isS = wave >= 4;
  const u16 *x = (const u16*)ka[0], *kc = (const u16*)ka[1], *vc = (const u16*)ka[2], *wo = (const u16*)ka[4], *wd = (const u16*)ka[7];
  u16* out = (u16*)kb[0];
  unsigned char* ws = (unsigned char*)kb[1];
  u64 tst;
  asm volatile("s_memrealtime %0\n\ts_waitcnt lgkmcnt(0)" : "=s"(tst));
  const unsigned tstart = (unsigned)tst;
  unsigned tag, ep;
  unsigned cp0, cp1;
  {
    u64 pc;
    asm volatile("s_getpc_b64 %0" : "=s"(pc));
    unsigned pco = (((wave + 5) & 7) * 32 + uq) * 128;
    pco = pco < CODE_BYTES - 1024 ? pco : 0;
    asm volatile("s_load_dword %0, %3, 0x0\n\ts_load_dword %1, %4, %5\n\ts_load_dword %2, %4, %6" : "=&s"(ep), "=&s"(cp0), "=&s"(cp1) : "s"(ws), "s"(pc & ~127ull), "s"(pco), "s"(pco + 64));
  }
#define TAG_READY() asm volatile("s_waitcnt lgkmcnt(0)\n\ts_add_u32 %0, %1, 1\n\ts_addc_u32 %0, %0, 0" : "=&s"(tag) : "s"(ep), "s"(cp0), "s"(cp1) : "scc")
  FS(1);
  const rsrc_t rws = mk_rsrc(ws, WS_END);

  if (isS) {
    const int sw = wave - 4;
    const rsrc_t r0 = mk_rsrc(kc, 2 * 32 * 8192 * 384), rv = mk_rsrc(vc, 2 * 32 * 8192 * 384);
#define SCAT 2
    const int voff = ((b * 32 + h) * 8192 + 6144 + 512 * s + (SCAT == 1 ? 16 * (8 * sw + g) : SCAT == 2 ? 128 * sw + g : SCAT == 3 ? 4 * (8 * sw + g) : 8 * sw + g)) * 384 + 16 * j;
    u32x4 buf[96];
    u32x4 qv[3];
    float sc[16];
    unsigned pd[16];
    f32x4 acc[3] = {{0.f, 0.f, 0.f, 0.f}, {0.f, 0.f, 0.f, 0.f}, {0.f, 0.f, 0.f, 0.f}};
    unsigned msk[4];
#pragma unroll
    for (int d = 0; d < 4; ++d) msk[d] = (j >> 1) == d ? ((j & 1) ? 0xffff0000u : 0x0000ffffu) : 0u;
    constexpr float C2 = 0.07216878364870322f * 1.4426950408889634f;
    const int stag = ((2 * sw - 3) * STAG) >> 1;
    unsigned tgt = tstart + S_DELAY + stag, tend = tstart + T_PLAN + stag, tendv = 0;

    auto consume = [&](auto G) {
      constexpr int t = decltype(G)::value;
      if constexpr (t < 16) {
        float a0 = 0.f, a1 = 0.f, a2 = 0.f;
#pragma unroll
        for (int e = 0; e < 4; ++e) { a0 = dot2(buf[3 * t][e], qv[0][e], a0); a1 = dot2(buf[3 * t + 1][e], qv[1][e], a1); a2 = dot2(buf[3 * t + 2][e], qv[2][e], a2); }
        float a = (a0 + a1) + a2;
        a += dpp<XOR1>(a); a += dpp<XOR2>(a); a += dpp<HMIR>(a);
        sc[t] = a;
        if constexpr (t == 15) {
          float m = sc[0];
#pragma unroll
          for (int u = 1; u < 16; ++u) m = fmaxf(m, sc[u]);
          m = fmaxf(m, dpp<ROR8>(m)); m = x16_max(m); m = x32_max(m);
          const float mc = m * C2;
          float l = 0.f;
#pragma unroll
          for (int u = 0; u < 16; ++u) { const float p = __builtin_amdgcn_exp2f(fmaf(sc[u], C2, -mc)); pd[u] = cvtpk(p, p); l += bfhi(pd[u]); }
          l += dpp<ROR8>(l); l = x16_sum(l); l = x32_sum(l);
          if (lane == 0) { *(f32x2*)(l_ml + 2 * sw) = f32x2{mc, l}; MEMFENCE(); l_f[FA + sw] = 1; }
          STAMP(3);
        }
      } else {
        constexpr int u = t - 16;
        const u32x4 a = {pd[u] & msk[0], pd[u] & msk[1], pd[u] & msk[2], pd[u] & msk[3]};
#pragma unroll
        for (int i = 0; i < 3; ++i) acc[i] = mfma32(a, buf[3 * t + i], acc[i]);
      }
    };
    constexpr auto done_after = [](int n) constexpr {
      if (n < Q_AT) return 0;
      const int landed = (n - WIN + 1) / 3;
      const int cu = 1 + n - Q_AT;
      return landed < cu ? landed : cu;
    };
    static_for<0, 96>([&](auto N) {
      constexpr int n = decltype(N)::value;
      if constexpr (n == Q_AT) {
        STAMP(1);
        wait_lds<0>(fbase + FG, ALL4);
        STAMP(2);
#pragma unroll
        for (int i = 0; i < 3; ++i) qv[i] = *(const u32x4*)(l_q + 32 * i + 4 * j);
      }
      if constexpr (n >= WIN) asm volatile("s_waitcnt vmcnt(%0)" ::"i"(WIN - 1) : "memory");
      if constexpr (n == 9) TAG_READY();
      if constexpr (n >= 9 && n < 9 + 45 && (n - 9) % 5 == 0) FS(2 + (n - 9) / 5);
      if constexpr (n % 3 == 0) {
        constexpr int k = n / 3;
        if constexpr (k > 12) { const unsigned tv = __builtin_amdgcn_readfirstlane(tendv); tend = tv ? tv + stag : tend; }
        const unsigned now = pace_step<(k < 31 ? 65536 / (31 - k) : 0)>(tgt, tend);
        if constexpr (k >= 6 && k < 26) {
          unsigned vm;
          asm volatile("s_getreg_b32 %0, hwreg(HW_REG_IB_STS, 0, 4)" : "=s"(vm));
          l_est[sw] = (now + (((vm + 3 * (31 - k)) * RATE16) >> 4)) | 1u;
        }
        if constexpr (k >= 12 && k < 30) tendv = *l_tend;
      }
      if constexpr (n == 95) { if (lane == 0) l_f[FB + sw] = 1; }
      constexpr int m = n < 48 ? n : n - 48;
      buf[n] = SCAT == 1 ? ld_nt(n < 48 ? r0 : rv, voff + (m % 24) * 128, (m / 24) * 3072)
             : SCAT == 3 ? ld_nt(n < 48 ? r0 : rv, voff + (m % 12) * 128, (m / 12) * 49152)
                         : ld_nt(n < 48 ? r0 : rv, voff + (m % 3) * 128, (m / 3) * (SCAT == 2 ? 3072 : 12288));
      SB();
      static_for<done_after(n > 0 ? n - 1 : 0), done_after(n)>(consume);
      SB();
    });
    STAMP(4);
    __builtin_amdgcn_s_setprio(3);
    wait_lds<0>(fbase + FE, ALL4);
    float cq = l_c[lane >> 4];
    unsigned dst = (unsigned)(size_t)(lfloat*)(l_acc + (sw * 2 + (g & 1)) * 192 + 8 * j + 4 * ((lane >> 4) & 1));
    unsigned src = (unsigned)(size_t)(lfloat*)(l_acc + (lane >> 4) * 384 + 64 * sw + 8 * j + 4 * (g & 1));
    unsigned fla = fbase + FC + sw, one = 1u, flr = fbase + FC;
    int pvo = (lane & 16) ? OOB : (g & 1) * 128 + j * 16 + (lane >> 5) * 8;
    int pso = WS_OP + ((h * 2 + b) * 4 + s) * 768 + sw * 256;
    unsigned tagv = tag;
    asm volatile("" : "+v"(cq), "+v"(dst), "+v"(src), "+v"(fla), "+v"(flr), "+v"(one), "+v"(pvo), "+s"(pso), "+v"(tagv));
    const bool wr = (lane >> 5) == (g & 1);
    SB();
    static_for<done_after(95), 32>([&](auto G) { consume(G); SB(); });
    if (wr) {
#pragma unroll
      for (int i = 0; i < 3; ++i) *(lf32x4*)(size_t)(dst + 256 * i) = acc[i];
      MEMFENCE();
      *(lbyte*)(size_t)fla = (unsigned char)one;
    }
    STAMP(5);
    if (sw < 3) {
      f32x4 u0, u1;
      unsigned fv, fs;
      asm volatile("1:\n\tds_read_b32 %0, %4\n\tds_read_b128 %2, %5\n\tds_read_b128 %3, %5 offset:768\n\ts_waitcnt lgkmcnt(2)\n\tv_readfirstlane_b32 %1, %0\n\t"
                   "s_cmp_lg_u32 %1, 0x01010101\n\ts_waitcnt lgkmcnt(0)\n\ts_cbranch_scc1 1b"
                   : "=&v"(fv), "=&s"(fs), "=&v"(u0), "=&v"(u1) : "v"(flr), "v"(src) : "scc", "memory");
      float o[4];
#pragma unroll
      for (int e = 0; e < 4; ++e) o[e] = cq * (u0[e] + u1[e]);
      const auto s0 = __builtin_amdgcn_permlane32_swap(__float_as_uint(o[0]), __float_as_uint(o[2]), false, false);
      const auto s1 = __builtin_amdgcn_permlane32_swap(__float_as_uint(o[1]), __float_as_uint(o[3]), false, false);
      const float p0 = x16_sum(__uint_as_float(s0[0]) + __uint_as_float(s0[1])), p1 = x16_sum(__uint_as_float(s1[0]) + __uint_as_float(s1[1]));
      const u32x2 pv = {cvtpk(p0, p1), tagv};
      __builtin_amdgcn_raw_buffer_store_b64(pv, rws, pvo, pso, AUX_SC1);
    }
    MEMFENCE();
    *(lbyte*)(size_t)(fla + (FD - FC)) = (unsigned char)one;
    STAMP(6);
    STAMP_FLUSH();
    return;
  }

  __builtin_amdgcn_s_setprio(2);
  const int w = wave;
  const unsigned idx = w < 2 ? 2 * j8 + w : uq >> 1, ur = (idx * 11) >> 5, uc = idx - 3 * ur, up = uq & 1;
  const bool has_front = (w < 3) & (idx < 15);
  const unsigned m0 = e4 == 0 ? 0x0000ffffu : e4 == 1 ? 0xffff0000u : 0u;
  const unsigned m1 = e4 == 2 ? 0x0000ffffu : e4 == 3 ? 0xffff0000u : 0u;
  const int st_col = (8 * j + 4 * (lane >> 5) + 2 * (g & 1)) * 8;
  const rsrc_t rx = mk_rsrc(x, 2560);
  if (has_front) {
    const int xo = (lane >> 5) * 1280 + (lane & 31) * 16;
    const bool x3 = (lane & 16) == 0;
    const u32x4 xa = __builtin_amdgcn_raw_buffer_load_b128(rx, xo, 0, 0), xb = __builtin_amdgcn_raw_buffer_load_b128(rx, xo + 512, 0, 0);
    const u32x4 xc = __builtin_amdgcn_raw_buffer_load_b128(rx, x3 ? xo + 1024 : OOB, 0, 0);
    const u32x4 xr0 = __builtin_amdgcn_raw_buffer_load_b128(rx, bl * 1280 + g * 32, 256 * ur, 0);
    const u32x4 xr1 = __builtin_amdgcn_raw_buffer_load_b128(rx, bl * 1280 + g * 32 + 16, 256 * ur, 0);
    SB();
    FS(2);
    TAG_READY();
    float v[24];
#pragma unroll
    for (int e = 0; e < 4; ++e) { v[2 * e] = bflo(xa[e]); v[2 * e + 1] = bfhi(xa[e]); v[8 + 2 * e] = bflo(xb[e]); v[9 + 2 * e] = bfhi(xb[e]); v[16 + 2 * e] = bflo(xc[e]); v[17 + 2 * e] = bfhi(xc[e]); }
    float sum = 0.f;
#pragma unroll
    for (int e = 0; e < 24; ++e) sum += v[e];
    sum = x16_sum(row_sum(sum));
    const float mean = sum * (1.f / 640.f);
    float sq = 0.f, sq3 = 0.f;
#pragma unroll
    for (int e = 0; e < 16; ++e) { const float d = v[e] - mean; sq = fmaf(d, d, sq); }
#pragma unroll
    for (int e = 16; e < 24; ++e) { const float d = v[e] - mean; sq3 = fmaf(d, d, sq3); }
    sq = x16_sum(row_sum(sq + (x3 ? sq3 : 0.f)));
    const float mb = rbf(mean), den = rbf(__builtin_amdgcn_sqrtf(rbf(rbf(sq * (1.f / 640.f)) + 1e-5f)));
    const float inv = __builtin_amdgcn_rcpf(den);
    const float mb0 = __builtin_bit_cast(float, __builtin_amdgcn_readlane(__builtin_bit_cast(int, mb), 0));
    const float mb1 = __builtin_bit_cast(float, __builtin_amdgcn_readlane(__builtin_bit_cast(int, mb), 32));
    const float iv0 = __builtin_bit_cast(float, __builtin_amdgcn_readlane(__builtin_bit_cast(int, inv), 0));
    const float iv1 = __builtin_bit_cast(float, __builtin_amdgcn_readlane(__builtin_bit_cast(int, inv), 32));
    const float mm = bl ? mb1 : mb0, ii = bl ? iv1 : iv0;
    FS(3);
    asm volatile("s_waitcnt vmcnt(0)" : "+v"(fw[0]), "+v"(fw[1]), "+v"(fw[2]), "+v"(fw[3]), "+v"(fw[4]), "+v"(fw[5]), "+v"(fw[6]), "+v"(fw[7]),
                 "+v"(fw[8]), "+v"(fw[9]), "+v"(fw[10]), "+v"(fw[11]), "+v"(fw[12]), "+v"(fw[13]), "+v"(fw[14]), "+v"(fw[15]));
    f32x4 acc0 = {0.f, 0.f, 0.f, 0.f}, acc1 = {0.f, 0.f, 0.f, 0.f};
#pragma unroll
    for (int n = 0; n < 16; ++n) {
      const unsigned xw = n < 8 ? xr0[n >> 1] : xr1[(n - 8) >> 1];
      const float xv = (n & 1) ? bfhi(xw) : bflo(xw);
      const float xn = rbf(xv - mm) * ii;
      const unsigned d = cvtpk(xn, xn), a0 = d & m0, a1 = d & m1;
      acc0 = mfma16(a0, a1, fw[n][0], fw[n][1], acc0);
      acc1 = mfma16(a0, a1, fw[n][2], fw[n][3], acc1);
    }
    float v0, v1;
    proj_epilogue(acc0, acc1, g & 1, v0, v1);
    const int rec = (w < 2 ? WS_QP + (h * 5 + ur) * 1536 : WS_GU + ((xq * 5 + ur) * 2 + up) * 1536) + uc * 256;
    __builtin_amdgcn_raw_buffer_store_b64(u32x2{cvtpk(v0, v1), tag}, rws, ((lane >> 4) & 1) * 768 + st_col / 2, rec, AUX_SC1);
    STAMP(1); FS(4);
    asm volatile("s_waitcnt vmcnt(0)" ::: "memory");
    STAMP(11); FS(5);
  } else {
    asm volatile("s_waitcnt vmcnt(0)" ::"v"(fw[0]), "v"(fw[1]), "v"(fw[2]), "v"(fw[3]), "v"(fw[4]), "v"(fw[5]), "v"(fw[6]), "v"(fw[7]),
                 "v"(fw[8]), "v"(fw[9]), "v"(fw[10]), "v"(fw[11]), "v"(fw[12]), "v"(fw[13]), "v"(fw[14]), "v"(fw[15]));
    TAG_READY();
  }
  const int slot = w * 8 + j8;
  const bool has_back = slot < 30;
  const int t3 = slot / 10, cc = slot - 10 * t3;
  const int rb = bid / 80, rc = (bid - 80 * rb) >> 3, ro = bid & 7;
  const bool reducer = (w == 0) && (bid < 160);
  unsigned ha[2][2] = {{0u, 0u}, {0u, 0u}};
  unsigned tend = tstart + T_PLAN;
  auto sample = [&](unsigned t) {
    wait_until(t);
    u32x4 e;
    do { MEMFENCE(); e = *(const u32x4*)l_est; } while (!__builtin_amdgcn_readfirstlane(e[0] & e[1] & e[2] & e[3] & 1u));
    return (int)((e[0] - tstart) + (e[1] - tstart) + (e[2] - tstart) + (e[3] - tstart));
  };
  auto pace_round = [&](int est4, unsigned t_read, int so, int bias) {
    const u32x2 pv = {tstart + (unsigned)(est4 >> 2), tag};
    __builtin_amdgcn_raw_buffer_store_b64(pv, rws, (lane == 0 && bid < 32) ? bid * 8 : OOB, so, AUX_SC1);
    wait_until(t_read);
    MEMFENCE();
    const u32x2 tv = __builtin_amdgcn_raw_buffer_load_b64(rws, (lane & 31) * 8, so, AUX_SC1);
    if (__all(tv[1] == tag)) {
      const float d = x16_sum(row_sum((float)(int)(tv[0] - tstart)));
      int di = (int)(d * (1.f / 32.f)) - bias;
      di = di < 0 ? 0 : (di > 2500 ? 2500 : di);
      tend = (tstart + __builtin_amdgcn_readfirstlane(di)) | 1u;
      *l_tend = tend;
    }
  };
  int e1 = 0;
  if (w < 3) {
    if (has_front) __builtin_amdgcn_s_sleep(16); else wait_until(tstart + 360);
    u32x2 t[3];
    const int vo = (lane >> 5) * 1536 + (lane & 31) * 8, so = WS_QP + h * 5 * 1536 + b * 768 + w * 256;
    for (unsigned spin = 0;; ++spin) {
      MEMFENCE();
      t[0] = ld2_sc1(rws, vo, so); t[1] = ld2_sc1(rws, vo, so + 3072); t[2] = ld2_sc1(rws, lane < 32 ? vo : OOB, so + 6144);
      const bool ok = (t[0][1] == tag) & (t[1][1] == tag) & ((t[2][1] == tag) | (lane >= 32));
      if (__all(ok) || spin > SPIN_CAP) { STAMPV(12, spin + 1); break; }
    }
    const float a = x32_sum((bflo(t[0][0]) + bflo(t[1][0])) + bflo(t[2][0]));
    const float c = x32_sum((bfhi(t[0][0]) + bfhi(t[1][0])) + bfhi(t[2][0]));
    if (lane < 32) { l_q[32 * w + lane] = cvtpk(a, c); MEMFENCE(); l_f[FG + w] = 1; }
  } else {
    l_f[FG + 3] = 1;
    pace_round(sample(tstart + SMP0), tstart + SMP0 + WAIT0, WS_TS, BIAS0);
    STAMPT(1, tend);
    STAMP(9);
    e1 = sample(tend - SMP1 - GAP1);
  }
  STAMP(2); FS(6);
  if (has_back) {
    u32x2 t[3];
    const int so = WS_GU + xq * 5 * 3072 + (h & 3) * 192 + t3 * 64;
    const int vo = bl * 768 + 8 * g;
    for (unsigned spin = 0;; ++spin) {
      MEMFENCE();
      t[0] = ld2_sc1(rws, vo + e4 * 3072, so);
      t[1] = ld2_sc1(rws, vo + e4 * 3072 + 1536, so);
      t[2] = ld2_sc1(rws, vo + 4 * 3072 + (e4 & 1) * 1536, so);
      if (__all((t[0][1] == tag) & (t[1][1] == tag) & (t[2][1] == tag)) || spin > SPIN_CAP) { STAMPV(13, spin + 1); break; }
    }
#pragma unroll
    for (int z = 0; z < 2; ++z) {
      const float ex = z ? bfhi(t[2][0]) : bflo(t[2][0]);
      const float gg = rbf(quad_sum((z ? bfhi(t[0][0]) : bflo(t[0][0])) + (e4 == 0 ? ex : 0.f)));
      const float uu = rbf(quad_sum((z ? bfhi(t[1][0]) : bflo(t[1][0])) + (e4 == 1 ? ex : 0.f)));
      const float zz = 0.7978845608028654f * (gg + 0.044715f * gg * gg * gg);
      const float ge = rbf(gg * __builtin_amdgcn_rcpf(1.f + __builtin_amdgcn_exp2f(-2.8853900817779268f * zz)));
      const float hv = ge * uu;
      const unsigned d = cvtpk(hv, hv);
      ha[z][0] = d & m0; ha[z][1] = d & m1;
    }
  }
  const int oc = 4 * (lane & 1) + 2 * (lane >> 5) + ((lane >> 4) & 1);
  const rsrc_t rout = mk_rsrc(out, 2560);
  int out_vo = (lane & 14) == 0 ? oc * 2 + rb * 1280 + rc * 128 + ro * 16 : OOB;
  float xres = 0.f;
  if (reducer) {
    xres = bflo(__builtin_amdgcn_raw_buffer_load_b16(rx, oc * 2, rb * 1280 + rc * 128 + ro * 16, 0));
    __builtin_amdgcn_raw_buffer_store_b16((unsigned short)0, rout, out_vo, 0, AUX_SC1);
  }
  STAMP(3);
  if (w == 1) {
    wait_lds<4>(fbase + FA, ALL4);
    const int so = WS_ML + (h * 2 + b) * 256;
    if (lane < 4) { const f32x2 ml = *(const f32x2*)(l_ml + 2 * lane); st_pair(rws, lane * 16, so + s * 64, ml[0], ml[1], tag); }
    asm volatile("s_waitcnt vmcnt(0)" ::: "memory");
    __builtin_amdgcn_s_sleep(32);
    u32x4 t;
    for (unsigned spin = 0;; ++spin) {
      MEMFENCE();
      t = ld_sc1(rws, (lane & 15) * 16, so);
      if (__all((t[1] == tag) & (t[3] == tag)) || spin > SPIN_CAP) break;
    }
    const float mv = __uint_as_float(t[0]);
    const float M = row_max(mv);
    const float f = __builtin_amdgcn_exp2f(mv - M);
    const float L = row_sum(f * __uint_as_float(t[2]));
    if ((lane >> 2) == s) { l_c[lane & 3] = f / L; MEMFENCE(); l_f[FE + (lane & 3)] = 1; }
  }
  if (w == 3) {
    const int e2 = sample(tend - SMP1);
    pace_round(e2 + (((e2 - e1) * KAPPA16) >> 4), tend - SMP1 + WAIT1, WS_TS + 256, BIAS1);
    STAMPT(11, tend);
  }
  if (has_back) {
    const unsigned sLo = (e4 & 1) ? 0x01000c0cu : 0x0c0c0100u, sHi = (e4 & 1) ? 0x03020c0cu : 0x0c0c0302u;
    unsigned sel0 = e4 < 2 ? sLo : 0x0c0c0c0cu, sel1 = e4 < 2 ? 0x0c0c0c0cu : sLo, sel2 = e4 < 2 ? sHi : 0x0c0c0c0cu, sel3 = e4 < 2 ? 0x0c0c0c0cu : sHi;
    int op_so = WS_OP + h * 8 * 768 + t3 * 256, op_vo = (bl * 4 + e4) * 768 + 16 * g;
    int d_vo = ((48 * h + 16 * t3 + 2 * g) * 640 + 64 * cc) * 2 + 16 * j, o_vo = ((192 * h + 64 * t3 + 8 * g) * 640 + 64 * cc) * 2 + 16 * j;
    int yvo = ((lane >> 4) & 1) * 256 + st_col / 2, yso = WS_YP + ((h * 3 + t3) * 10 + cc) * 512;
    int r_vo = (lane >> 1) * 5120 + (lane & 1) * 16 + WS_YP + rc * 512 + rb * 256 + ro * 32;
    unsigned tagv = tag, fd = fbase + FD;
    asm volatile("" : "+v"(sel0), "+v"(sel1), "+v"(sel2), "+v"(sel3), "+s"(op_so), "+v"(op_vo), "+v"(d_vo), "+v"(o_vo), "+v"(yvo), "+s"(yso), "+v"(r_vo), "+v"(out_vo), "+v"(tagv), "+v"(xres), "+v"(fd));
    const rsrc_t rd = mk_rsrc(wd, 1536 * 1280), ro_ = mk_rsrc(wo, 6144 * 1280);
    STAMP(4);
    wait_lds<1>(fbase + FB, ALL4);
    delay_ticks(BACK_DELAY + HSTAG * w);
    u32x4 wb[10], t0, t1;
    unsigned fdv = 0;
#pragma unroll
    for (int n = 0; n < 10; ++n) {
      if (n == POLLPOS) {
        const rsrc_t rp = mk_rsrc(ws, __builtin_amdgcn_readfirstlane(fdv) == ALL4 ? WS_END : 0);
        t0 = ld_sc1(rp, op_vo, op_so); t1 = ld_sc1(rp, op_vo + 128, op_so);
        SB();
      }
      if (n < 2) wb[8 + n] = ld_nt(rd, d_vo + n * 1280, 0); else wb[n - 2] = ld_nt(ro_, o_vo, (n - 2) * 1280);
      if (n == POLLPOS - 1) { MEMFENCE(); fdv = *(luint*)(size_t)fd; }
      SB();
      if (n < 9) __builtin_amdgcn_s_sleep(BPACE);
      SB();
    }
    STAMP(5);
    u32x4 bq[10];
#define PAIR(p) do { bq[2 * (p)] = u32x4{wb[2 * (p)][0], wb[2 * (p)][1], wb[2 * (p) + 1][0], wb[2 * (p) + 1][1]}; \
                     bq[2 * (p) + 1] = u32x4{wb[2 * (p)][2], wb[2 * (p)][3], wb[2 * (p) + 1][2], wb[2 * (p) + 1][3]}; } while (0)
    PAIR(4); PAIR(0); PAIR(1); PAIR(2);
    const u32x4 hA = {ha[0][0], ha[0][1], ha[1][0], ha[1][1]};
    f32x4 acc0 = mfma32(hA, bq[8], f32x4{0.f, 0.f, 0.f, 0.f}), acc1 = mfma32(hA, bq[9], f32x4{0.f, 0.f, 0.f, 0.f});
    asm volatile("" : "+v"(bq[0]), "+v"(bq[1]), "+v"(bq[2]), "+v"(bq[3]), "+v"(bq[4]), "+v"(bq[5]), "+v"(acc0), "+v"(acc1));
    SB();
    unsigned np1 = 1;
    if (__builtin_expect(__builtin_amdgcn_ballot_w64((((t0[1] ^ tagv) | (t0[3] ^ tagv)) | ((t1[1] ^ tagv) | (t1[3] ^ tagv))) != 0u) != 0, 0)) {
      do {
        ++np1;
        MEMFENCE();
        if (__builtin_amdgcn_readfirstlane(*(luint*)(size_t)fd) != ALL4) { wait_lds<1>(fd, ALL4); delay_ticks(LATE_DELAY); }
        t0 = ld_sc1(rws, op_vo, op_so); t1 = ld_sc1(rws, op_vo + 128, op_so);
      } while (!__all((t0[1] == tag) & (t0[3] == tag) & (t1[1] == tag) & (t1[3] == tag)) && np1 < SPIN_CAP);
    }
    STAMPV(14, np1);
    STAMP(7);
    unsigned pk[4];
    {
      float u0, u1, u2, u3, u4, u5, u6, u7;
#define DPPADD(r, P) "v_add_f32_dpp " r ", " r ", " r " quad_perm:" P " row_mask:0xf bank_mask:0xf\n\t"
#define DPP8(P) DPPADD("%4", P) DPPADD("%5", P) DPPADD("%6", P) DPPADD("%7", P) DPPADD("%8", P) DPPADD("%9", P) DPPADD("%10", P) DPPADD("%11", P)
      asm volatile("v_lshlrev_b32 %4, 16, %12\n\tv_and_b32 %5, 0xffff0000, %12\n\tv_lshlrev_b32 %6, 16, %13\n\tv_and_b32 %7, 0xffff0000, %13\n\t"
                   "v_lshlrev_b32 %8, 16, %14\n\tv_and_b32 %9, 0xffff0000, %14\n\tv_lshlrev_b32 %10, 16, %15\n\tv_and_b32 %11, 0xffff0000, %15\n\t"
                   DPP8("[1,0,3,2]") DPP8("[2,3,0,1]")
                   "v_cvt_pk_bf16_f32 %0, %4, %5\n\tv_cvt_pk_bf16_f32 %1, %6, %7\n\tv_cvt_pk_bf16_f32 %2, %8, %9\n\tv_cvt_pk_bf16_f32 %3, %10, %11"
                   : "=&v"(pk[0]), "=&v"(pk[1]), "=&v"(pk[2]), "=&v"(pk[3]), "=&v"(u0), "=&v"(u1), "=&v"(u2), "=&v"(u3), "=&v"(u4), "=&v"(u5), "=&v"(u6), "=&v"(u7)
                   : "v"(t0[0]), "v"(t0[2]), "v"(t1[0]), "v"(t1[2]));
    }
    u32x4 aq[4];
#pragma unroll
    for (int p = 0; p < 4; ++p)
      aq[p] = u32x4{__builtin_amdgcn_perm(0u, pk[p], sel0), __builtin_amdgcn_perm(0u, pk[p], sel1), __builtin_amdgcn_perm(0u, pk[p], sel2), __builtin_amdgcn_perm(0u, pk[p], sel3)};
#pragma unroll
    for (int p = 0; p < 3; ++p) { acc0 = mfma32(aq[p], bq[2 * p], acc0); acc1 = mfma32(aq[p], bq[2 * p + 1], acc1); }
    asm volatile("" : "+v"(aq[3]), "+v"(acc0), "+v"(acc1));
    SB();
    acc0 = mfma16(aq[3][0], aq[3][1], wb[6][0], wb[6][1], acc0); acc1 = mfma16(aq[3][0], aq[3][1], wb[6][2], wb[6][3], acc1);
    acc0 = mfma16(aq[3][2], aq[3][3], wb[7][0], wb[7][1], acc0); acc1 = mfma16(aq[3][2], aq[3][3], wb[7][2], wb[7][3], acc1);
    float v0, v1;
    proj_epilogue(acc0, acc1, g & 1, v0, v1);
    __builtin_amdgcn_raw_buffer_store_b64(u32x2{cvtpk(v0, v1), tagv}, rws, yvo, yso, AUX_SC1);
    STAMP(8);
    if (reducer) {
      delay_ticks(FIN_DELAY);
      float y0, y1, y2, y3;
      unsigned np2 = 0;
      for (;;) {
        MEMFENCE();
        const u32x4 a = ld_sc1(rws, r_vo, 0), c = ld_sc1(rws, r_vo, 32 * 5120), d = ld_sc1(rws, r_vo, 64 * 5120);
        unsigned bad = ((a[1] ^ tagv) | (a[3] ^ tagv)) | ((c[1] ^ tagv) | (c[3] ^ tagv));
        y0 = bflo(a[0]) + bflo(c[0]); y1 = bfhi(a[0]) + bfhi(c[0]); y2 = bflo(a[2]) + bflo(c[2]); y3 = bfhi(a[2]) + bfhi(c[2]);
        asm volatile("" : "+v"(bad), "+v"(y0), "+v"(y1), "+v"(y2), "+v"(y3));
        bad |= (d[1] ^ tagv) | (d[3] ^ tagv);
        y0 += bflo(d[0]); y1 += bfhi(d[0]); y2 += bflo(d[2]); y3 += bfhi(d[2]);
        if (__builtin_expect(__builtin_amdgcn_ballot_w64(bad != 0u) == 0, 1)) break;
        if (++np2 > SPIN_CAP) break;
      }
      STAMPV(15, np2 + 1);
      STAMP(9);
      const auto s0 = __builtin_amdgcn_permlane32_swap(__float_as_uint(y0), __float_as_uint(y2), false, false);
      const auto s1 = __builtin_amdgcn_permlane32_swap(__float_as_uint(y1), __float_as_uint(y3), false, false);
      const float z0 = __uint_as_float(s0[0]) + __uint_as_float(s0[1]), z1 = __uint_as_float(s1[0]) + __uint_as_float(s1[1]);
      const auto s2 = __builtin_amdgcn_permlane16_swap(__float_as_uint(z0), __float_as_uint(z1), false, false);
      float y = __uint_as_float(s2[0]) + __uint_as_float(s2[1]);
      y += dpp<ROR8>(y); y += dpp<ROR4>(y); y += dpp<XOR2>(y);
      __builtin_amdgcn_raw_buffer_store_b16((unsigned short)cvtpk(xres + y, 0.f), rout, out_vo, 0, AUX_SC1);
      if (bid == 0) __builtin_amdgcn_raw_buffer_store_b32(tagv, rws, lane == 0 ? WS_EPOCH : OOB, 0, AUX_SC1);
      STAMP(10);
      if (bid == 8) {
        unsigned long long t_; asm volatile("s_memrealtime %0\n\ts_waitcnt lgkmcnt(0)" : "=s"(t_) :: "memory");
        unsigned tf = *l_tend; asm volatile("" : "+v"(tf)); tf = __builtin_amdgcn_readfirstlane(tf);
        int v = AMP == 0 ? 0 : AMP == 1 ? (int)(tf - tstart) - 1400 : AMP == 2 ? (int)((unsigned)t_ - tf) - 300 : (int)((unsigned)t_ - tstart) - 1700;
        asm volatile("s_max_i32 %0, %0, 0\n\ts_min_i32 %0, %0, 0x190" : "+s"(v) :: "scc");
        wait_until(amp0 + 4000 + 10 * v + ((tstart - amp0) > 95u ? 5000 : 0));
      }
    }
  }
  STAMP_FLUSH();
}

extern "C" void kernel_launch(void* const* d_in, const int* in_sizes, int n_in, void* d_out, int out_size, void* d_ws, size_t ws_size,
                              hipStream_t stream) {
  fused_decode<<<256, 512, 0, stream>>>((const u16*)d_in[0], (const u16*)d_in[1], (const u16*)d_in[2], (const u16*)d_in[3], (const u16*)d_in[4],
                                        (const u16*)d_in[6], (const u16*)d_in[7], (const u16*)d_in[8], (u16*)d_out, (unsigned char*)d_ws
                                        );
}
```

```cpp
#define P_SD 162
#include <hip/hip_runtime.h>
#include <hip/hip_bf16.h>
#include <stdint.h>
#include <type_traits>

typedef unsigned short u16;
typedef unsigned u32x4 __attribute__((ext_vector_type(4)));
typedef unsigned u32x2 __attribute__((ext_vector_type(2)));
typedef float f32x4 __attribute__((ext_vector_type(4)));
typedef float f32x2 __attribute__((ext_vector_type(2)));
typedef short s16x4 __attribute__((ext_vector_type(4)));
typedef __bf16 bf16x8 __attribute__((ext_vector_type(8)));
typedef __bf16 bf16x2 __attribute__((ext_vector_type(2)));
typedef __amdgpu_buffer_rsrc_t rsrc_t;

#define QK 7
#define BPACE 5
#define POLLPOS 8
#define STAG 10
#define WOSEQ 0
#define AUX_OP 16
#define OPSTR 1048576
#define OPB 768
#define OPC 256
#define OPS 0
#define OPM 1048576
#define OP_REC(r) (((r) >> OPS) * OPB + ((r) & ((1 << OPS) - 1)) * OPM)
#define DEAD 2
#define VMW 8
#define KEEP1 10
#define HSTAG 0
#define CODE_BYTES 15488
#define P_RATE 214
#define P_G0 18
#define P_G1 56
#define P_BAT 76
constexpr int S_DELAY = P_SD;
constexpr int T_PLAN = S_DELAY + 1240;
constexpr int RATE16 = P_RATE;
constexpr int LAT0 = 73;
constexpr int SMP0 = 500, WAIT0 = 220, BIAS0 = 0, KAPPA0 = P_G0;
constexpr int SMP1 = 400, WAIT1 = 140, BIAS1 = 0, KAPPA16 = P_G1;
constexpr int BACK_AT = P_BAT;
constexpr int BACK_DELAY = 12;
constexpr int LATE_DELAY = 20;
constexpr int FIN_DELAY = 46;

constexpr int AUX_NT = 2, AUX_SC1 = 16;
constexpr int RSRC_W3 = 0x00020000;
constexpr unsigned SPIN_CAP = 1u << 18;
constexpr int OOB = 0x40000000;
constexpr unsigned ALL4 = 0x01010101u;

constexpr int WS_EPOCH = 0;
constexpr int WS_TS = 1024;
constexpr int WS_QP = 4096;
constexpr int WS_GU = WS_QP + 32 * 5 * 2 * 96 * 8;
constexpr int WS_ML = WS_GU + 8 * 5 * 2 * 2 * 96 * 8;
constexpr int WS_OP = WS_ML + 32 * 2 * 4 * 4 * 16;
constexpr int WS_YP = WS_OP + 32 * 2 * 4 * 96 * 8;
constexpr int WS_END = OPSTR == 6144 ? WS_YP + 96 * 10 * 2 * 32 * 8 : (34 << 20);

constexpr int WS_POLL = WS_END;
#define OP_BASE(h) (OPSTR == 6144 ? WS_OP + (h) * 6144 : (1 << 21) + (h) * OPSTR)
#define QP_BASE(h) (WS_QP + (h) * 7680)
#define GU_BASE(x) (WS_GU + (x) * 15360)
#define GU_REC(r) ((r) * 3072)
template <int I, int N, class F> __device__ __forceinline__ void static_for(F&& f) {
  if constexpr (I < N) { f(std::integral_constant<int, I>{}); static_for<I + 1, N>(f); }
}
__device__ __forceinline__ rsrc_t mk_rsrc(const void* p, int bytes) {
  return __builtin_amdgcn_make_buffer_rsrc(const_cast<void*>(p), 0, bytes, RSRC_W3);
}
__device__ __forceinline__ u32x4 ld_nt(rsrc_t r, int voff, int soff) { return __builtin_amdgcn_raw_buffer_load_b128(r, voff, soff, AUX_NT); }
__device__ __forceinline__ u32x4 ld_sc1(rsrc_t r, int voff, int soff) { return __builtin_amdgcn_raw_buffer_load_b128(r, voff, soff, AUX_SC1); }
__device__ __forceinline__ u32x2 ld2_sc1(rsrc_t r, int voff, int soff) { return __builtin_amdgcn_raw_buffer_load_b64(r, voff, soff, AUX_SC1); }
__device__ __forceinline__ void st_pair(rsrc_t r, int voff, int soff, float a, float b, unsigned tag) {
  u32x4 v = {__float_as_uint(a), tag, __float_as_uint(b), tag};
  __builtin_amdgcn_raw_buffer_store_b128(v, r, voff, soff, AUX_SC1);
}
__device__ __forceinline__ float bflo(unsigned v) { return __uint_as_float(v << 16); }
__device__ __forceinline__ float bfhi(unsigned v) { return __uint_as_float(v & 0xffff0000u); }
__device__ __forceinline__ unsigned cvtpk(float lo, float hi) {
  bf16x2 r = {(__bf16)lo, (__bf16)hi};
  return __builtin_bit_cast(unsigned, r);
}
__device__ __forceinline__ float rbf(float x) { return (float)(__bf16)x; }
template <int CTRL> __device__ __forceinline__ float dpp(float x) {
  return __builtin_bit_cast(float, __builtin_amdgcn_mov_dpp(__builtin_bit_cast(int, x), CTRL, 0xf, 0xf, true));
}
constexpr int XOR1 = 0xB1, XOR2 = 0x4E, HMIR = 0x141, ROR8 = 0x128, ROR4 = 0x124;
__device__ __forceinline__ float x16_sum(float x) { auto s = __builtin_amdgcn_permlane16_swap(__float_as_uint(x), __float_as_uint(x), false, false); return __uint_as_float(s[0]) + __uint_as_float(s[1]); }
__device__ __forceinline__ float x32_sum(float x) { auto s = __builtin_amdgcn_permlane32_swap(__float_as_uint(x), __float_as_uint(x), false, false); return __uint_as_float(s[0]) + __uint_as_float(s[1]); }
__device__ __forceinline__ float x16_max(float x) { auto s = __builtin_amdgcn_permlane16_swap(__float_as_uint(x), __float_as_uint(x), false, false); return fmaxf(__uint_as_float(s[0]), __uint_as_float(s[1])); }
__device__ __forceinline__ float x32_max(float x) { auto s = __builtin_amdgcn_permlane32_swap(__float_as_uint(x), __float_as_uint(x), false, false); return fmaxf(__uint_as_float(s[0]), __uint_as_float(s[1])); }
__device__ __forceinline__ float row_sum(float x) { x += dpp<XOR1>(x); x += dpp<XOR2>(x); x += dpp<HMIR>(x); x += dpp<ROR8>(x); return x; }
__device__ __forceinline__ float row_max(float x) { x = fmaxf(x, dpp<XOR1>(x)); x = fmaxf(x, dpp<XOR2>(x)); x = fmaxf(x, dpp<HMIR>(x)); x = fmaxf(x, dpp<ROR8>(x)); return x; }
__device__ __forceinline__ float quad_sum(float x) { x += dpp<XOR1>(x); x += dpp<XOR2>(x); return x; }
__device__ __forceinline__ float dot2(unsigned a, unsigned b, float c) {
  return __builtin_amdgcn_fdot2_f32_bf16(__builtin_bit_cast(bf16x2, a), __builtin_bit_cast(bf16x2, b), c, false);
}
__device__ __forceinline__ f32x4 mfma16(unsigned a0, unsigned a1, unsigned b0, unsigned b1, f32x4 c) {
  u32x2 a = {a0, a1}, b = {b0, b1};
  return __builtin_amdgcn_mfma_f32_16x16x16bf16_1k(__builtin_bit_cast(s16x4, a), __builtin_bit_cast(s16x4, b), c, 0, 0, 0);
}
__device__ __forceinline__ f32x4 mfma32(u32x4 a, u32x4 b, f32x4 c) {
  return __builtin_amdgcn_mfma_f32_16x16x32_bf16(__builtin_bit_cast(bf16x8, a), __builtin_bit_cast(bf16x8, b), c, 0, 0, 0);
}
template <int RECIP> __device__ __forceinline__ unsigned pace_step(unsigned& tgt, unsigned tend, unsigned& tdone, unsigned& vm) {
  unsigned d, now;
  asm volatile("1:\n\ts_memrealtime vcc\n\ts_getreg_b32 %4, hwreg(HW_REG_IB_STS, 0, 4)\n\ts_waitcnt lgkmcnt(0)\n\ts_cmp_lg_u32 %4, 0\n\ts_cselect_b32 %3, vcc_lo, %3\n\t"
               "s_sub_u32 %0, vcc_lo, %2\n\ts_cmp_lt_i32 %0, 0\n\ts_cbranch_scc1 1b\n\t"
               "s_sub_i32 %0, %5, vcc_lo\n\ts_max_i32 %0, %0, 0\n\ts_min_i32 %0, %0, 0x7d0\n\ts_mul_i32 %0, %0, %6\n\ts_lshr_b32 %0, %0, 16\n\t"
               "s_add_u32 %2, vcc_lo, %0\n\ts_mov_b32 %1, vcc_lo"
               : "=&s"(d), "=&s"(now), "+s"(tgt), "+s"(tdone), "=&s"(vm) : "s"(tend), "i"(RECIP) : "vcc", "scc");
  return now;
}
__device__ __forceinline__ void wait_until(unsigned tgt) {
  asm volatile("1:\n\ts_memrealtime vcc\n\ts_waitcnt lgkmcnt(0)\n\ts_sub_u32 vcc_lo, vcc_lo, %0\n\ts_cmp_lt_i32 vcc_lo, 0\n\ts_cbranch_scc1 1b" :: "s"(tgt) : "vcc", "scc", "memory");
}
#define SB() __builtin_amdgcn_sched_barrier(0)
#define MEMFENCE() asm volatile("" ::: "memory")

#define FS(i)
#define STAMP(i)
#define STAMPV(i, v)
#define STAMPT(i, v)
#define STAMP_DECL
#define STAMP_FLUSH()

__device__ __forceinline__ void delay_ticks(int n) {
  if (n > 0) wait_until((unsigned)__builtin_amdgcn_s_memrealtime() + n);
}

typedef __attribute__((address_space(3))) float lfloat;
typedef __attribute__((address_space(3))) f32x4 lf32x4;
typedef __attribute__((address_space(3))) unsigned luint;
typedef __attribute__((address_space(3))) unsigned char lbyte;
template <int SLP> __device__ __forceinline__ void wait_lds(unsigned a, unsigned want) {
  unsigned v, sv;
  asm volatile("s_branch 2f\n1:\n\ts_sleep %4\n2:\n\tds_read_b32 %0, %2\n\ts_waitcnt lgkmcnt(0)\n\tv_readfirstlane_b32 %1, %0\n\ts_cmp_lg_u32 %1, %3\n\ts_cbranch_scc1 1b"
               : "=&v"(v), "=&s"(sv) : "v"(a), "s"(want), "i"(SLP) : "scc", "memory");
}

__device__ __forceinline__ u32x4 proj_epilogue(f32x4 acc0, f32x4 acc1, unsigned tag) {
  float r[4];
#pragma unroll
  for (int i = 0; i < 4; ++i) {
    auto s = __builtin_amdgcn_permlane32_swap(__float_as_uint(acc0[i]), __float_as_uint(acc1[i]), false, false);
    r[i] = __uint_as_float(s[0]) + dpp<ROR8>(__uint_as_float(s[1]));
  }
  return u32x4{cvtpk(r[0], r[1]), tag, cvtpk(r[2], r[3]), tag};
}

__global__ void __launch_bounds__(512, 2)
fused_decode(const u16* x_, const u16* kc_, const u16* vc_, const u16* wq_, const u16* wo_, const u16* wg_, const u16* wu_, const u16* wd_, u16* out_, unsigned char* ws_
             ) {
  __shared__ __attribute__((aligned(16))) float lds[96 + 8 + 4 + 16 + 8 * 192];
  unsigned* l_q = (unsigned*)lds;
  float* l_ml = lds + 96;
  float* l_c = lds + 104;
  unsigned* l_est = (unsigned*)(lds + 108);
  unsigned char* l_f = (unsigned char*)(lds + 116);
  constexpr int FA = 0, FB = 4, FC = 8, FD = 12, FE = 16, FG = 20;
  unsigned* l_tend = (unsigned*)(lds + 122);
  const unsigned fbase = (unsigned)(size_t)(lbyte*)l_f;
  float* l_acc = lds + 124;

  STAMP_DECL;
  STAMP(0);
  typedef unsigned long long u64;
  typedef u64 u64x8 __attribute__((ext_vector_type(8)));
  typedef u64 u64x2 __attribute__((ext_vector_type(2)));
  u64x8 ka; u64x2 kb;
  u32x4 fw[16];
  int bid = blockIdx.x, tid = threadIdx.x;
  {
    unsigned v1, v2;
    u64x8 ka0; u64x2 kb0;
    asm volatile("s_load_dwordx16 %0, %2, 0x0\n\ts_load_dwordx4 %1, %2, 0x40" : "=&{s[36:51]}"(ka0), "=&{s[52:55]}"(kb0) : "s"(__builtin_amdgcn_kernarg_segment_ptr()));
    asm volatile(
               "v_readfirstlane_b32 s14, %20\n\t"
               "s_lshr_b32 s15, %19, 3\n\t"
               "s_cmpk_ge_u32 s14, 0xc0\n\ts_cbranch_scc1 1f\n\t"
               "s_setprio 2\n\t"
               "s_and_b32 s16, %19, 7\n\t"
               "s_lshr_b32 s17, %19, 6\n\ts_lshl2_add_u32 s17, s16, s17\n\t"
               "s_and_b32 s18, s15, 7\n\ts_lshr_b32 s19, s14, 6\n\ts_lshl1_add_u32 s18, s18, s19\n\t"
               "s_lshr_b32 s19, %19, 4\n\t"
               "v_bfe_u32 %16, %20, 3, 3\n\tv_and_b32 %17, 7, %20\n\t"
               "s_movk_i32 s13, 0x3000\n\t"
               "s_cmpk_lt_u32 s14, 0x80\n\t"
               "s_cselect_b32 s18, s18, s19\n\ts_cselect_b32 s17, s17, s16\n\ts_cselect_b32 s13, s13, 0xc00\n\t"
               "s_mul_i32 s19, s18, 11\n\ts_lshr_b32 s19, s19, 5\n\t"
               "v_mad_u32_u24 %16, %16, s13, %17\n\t"
               "s_sub_u32 s17, s17, s19\n\ts_mul_i32 s17, s17, 3\n\ts_add_u32 s17, s17, s18\n\t"
               "s_mul_i32 s19, s19, s13\n\ts_add_u32 s12, s17, s19\n\ts_lshl_b32 s12, s12, 7\n\t"
               "v_lshlrev_b32 %16, 4, %16\n\t"
               "s_mul_i32 s10, s13, 0x280\n\ts_mov_b32 s11, 0x20000\n\ts_cmp_eq_u32 s18, 15\n\ts_cselect_b32 s10, 0, s10\n\t"
               "s_bitcmp1_b32 s15, 0\n\t"
               "s_waitcnt lgkmcnt(0)\n\t"
               "s_cselect_b64 s[8:9], s[48:49], s[46:47]\n\t"
               "s_cmpk_lt_u32 s14, 0x80\n\ts_cselect_b64 s[8:9], s[42:43], s[8:9]\n\t"
               "s_and_b32 s9, s9, 0xffff\n\t"
               "buffer_load_dwordx4 %0, %16, s[8:11], s12 offen nt\n\t" "s_add_u32 s12, s12, s13\n\t"
               "buffer_load_dwordx4 %1, %16, s[8:11], s12 offen nt\n\t" "s_add_u32 s12, s12, s13\n\t"
               "buffer_load_dwordx4 %2, %16, s[8:11], s12 offen nt\n\t" "s_add_u32 s12, s12, s13\n\t"
               "buffer_load_dwordx4 %3, %16, s[8:11], s12 offen nt\n\t" "s_add_u32 s12, s12, s13\n\t"
               "buffer_load_dwordx4 %4, %16, s[8:11], s12 offen nt\n\t" "s_add_u32 s12, s12, s13\n\t"
               "buffer_load_dwordx4 %5, %16, s[8:11], s12 offen nt\n\t" "s_add_u32 s12, s12, s13\n\t"
               "buffer_load_dwordx4 %6, %16, s[8:11], s12 offen nt\n\t" "s_add_u32 s12, s12, s13\n\t"
               "buffer_load_dwordx4 %7, %16, s[8:11], s12 offen nt\n\t" "s_add_u32 s12, s12, s13\n\t"
               "buffer_load_dwordx4 %8, %16, s[8:11], s12 offen nt\n\t" "s_add_u32 s12, s12, s13\n\t"
               "buffer_load_dwordx4 %9, %16, s[8:11], s12 offen nt\n\t" "s_add_u32 s12, s12, s13\n\t"
               "buffer_load_dwordx4 %10, %16, s[8:11], s12 offen nt\n\t" "s_add_u32 s12, s12, s13\n\t"
               "buffer_load_dwordx4 %11, %16, s[8:11], s12 offen nt\n\t" "s_add_u32 s12, s12, s13\n\t" "s_barrier\n\t"
               "buffer_load_dwordx4 %12, %16, s[8:11], s12 offen nt\n\t" "s_add_u32 s12, s12, s13\n\t"
               "buffer_load_dwordx4 %13, %16, s[8:11], s12 offen nt\n\t" "s_add_u32 s12, s12, s13\n\t"
               "buffer_load_dwordx4 %14, %16, s[8:11], s12 offen nt\n\t" "s_add_u32 s12, s12, s13\n\t"
               "buffer_load_dwordx4 %15, %16, s[8:11], s12 offen nt\n\t"
               "s_branch 2f\n"
               "1:\n\t"
               "v_and_b32 %16, 15, %20\n\tv_lshl_add_u32 %16, %16, 2, %21\n\tv_mov_b32 %17, 0\n\tds_write_b32 %16, %17\n\t"
               "s_waitcnt lgkmcnt(0)\n\ts_barrier\n"
               "2:"
               : "=&v"(fw[0]), "=&v"(fw[1]), "=&v"(fw[2]), "=&v"(fw[3]), "=&v"(fw[4]), "=&v"(fw[5]), "=&v"(fw[6]), "=&v"(fw[7]),
                 "=&v"(fw[8]), "=&v"(fw[9]), "=&v"(fw[10]), "=&v"(fw[11]), "=&v"(fw[12]), "=&v"(fw[13]), "=&v"(fw[14]), "=&v"(fw[15]),
                 "=&v"(v1), "=&v"(v2)
               : "{s[36:51]}"(ka0), "s"(bid), "v"(tid), "s"(fbase - 32), "{s[52:55]}"(kb0)
               : "s8", "s9", "s10", "s11", "s12", "s13", "s14", "s15", "s16", "s17", "s18", "s19", "scc", "memory");
    asm volatile("" : "={s[36:51]}"(ka), "={s[52:55]}"(kb), "+s"(bid) : "0"(ka0), "1"(kb0));
    asm volatile("" : "+v"(tid));
  }
  const int xq = bid & 7, uq = bid >> 3;
  const int h = xq * 4 + (uq >> 3), j8 = uq & 7, b = j8 >> 2, s = j8 & 3;
  const int lane = tid & 63, wave = __builtin_amdgcn_readfirstlane(tid >> 6);
  const int g = lane >> 3, j = lane & 7;
  const int bl = (lane >> 2) & 1, e4 = lane & 3;
  const bool isS = wave >= 4;
  const u16 *x = (const u16*)ka[0], *kc = (const u16*)ka[1], *vc = (const u16*)ka[2], *wo = (const u16*)ka[4], *wd = (const u16*)ka[7];
  u16* out = (u16*)kb[0];
  unsigned char* ws = (unsigned char*)kb[1];
  u64 tst;
  asm volatile("s_memrealtime %0\n\ts_waitcnt lgkmcnt(0)" : "=s"(tst));
  const unsigned tstart = (unsigned)tst;
  unsigned tag, ep;
  unsigned cp0, cp1;
  {
    u64 pc;
    asm volatile("s_getpc_b64 %0" : "=s"(pc));
    unsigned pco = (((wave + 5) & 7) * 32 + uq) * 128;
    pco = pco < CODE_BYTES - 1024 ? pco : 0;
    asm volatile("s_load_dword %0, %3, 0x0\n\ts_load_dword %1, %4, %5\n\ts_load_dword %2, %4, %6" : "=&s"(ep), "=&s"(cp0), "=&s"(cp1) : "s"(ws), "s"(pc & ~127ull), "s"(pco), "s"(pco + 64));
  }
#define TAG_READY() asm volatile("s_waitcnt lgkmcnt(0)\n\ts_add_u32 %0, %1, 1\n\ts_addc_u32 %0, %0, 0" : "=&s"(tag) : "s"(ep), "s"(cp0), "s"(cp1) : "scc")
  FS(1);
  const rsrc_t rws = mk_rsrc(ws, WS_END);

  if (isS) {
    const int sw = wave - 4;
    const rsrc_t r0 = mk_rsrc(kc, 2 * 32 * 8192 * 384), rv = mk_rsrc(vc, 2 * 32 * 8192 * 384);
    const int voff = ((b * 32 + h) * 8192 + 6144 + 32 * s + 8 * sw + g) * 384 + 16 * j;
    u32x4 buf[96];
    u32x4 qv[3];
    float sc[16];
    unsigned pd[16];
    f32x4 acc[3] = {{0.f, 0.f, 0.f, 0.f}, {0.f, 0.f, 0.f, 0.f}, {0.f, 0.f, 0.f, 0.f}};
    unsigned msk[4];
#pragma unroll
    for (int d = 0; d < 4; ++d) msk[d] = (j >> 1) == d ? ((j & 1) ? 0xffff0000u : 0x0000ffffu) : 0u;
    constexpr float C2 = 0.07216878364870322f * 1.4426950408889634f;
    const int stag = ((2 * (uq >> 2) - 7) * STAG) >> 1;
    unsigned tgt = tstart + S_DELAY + stag, tend = tstart + T_PLAN + stag, tendv = 0, tprev = tgt;

    auto consume = [&](auto G) {
      constexpr int t = decltype(G)::value;
      if constexpr (t < 16) {
        float a0 = 0.f, a1 = 0.f, a2 = 0.f;
#pragma unroll
        for (int e = 0; e < 4; ++e) { a0 = dot2(buf[3 * t][e], qv[0][e], a0); a1 = dot2(buf[3 * t + 1][e], qv[1][e], a1); a2 = dot2(buf[3 * t + 2][e], qv[2][e], a2); }
        float a = (a0 + a1) + a2;
        a += dpp<XOR1>(a); a += dpp<XOR2>(a); a += dpp<HMIR>(a);
        sc[t] = a;
        if constexpr (t == 15) {
          float m = sc[0];
#pragma unroll
          for (int u = 1; u < 16; ++u) m = fmaxf(m, sc[u]);
          m = fmaxf(m, dpp<ROR8>(m)); m = x16_max(m); m = x32_max(m);
          const float mc = m * C2;
          float l = 0.f;
#pragma unroll
          for (int u = 0; u < 16; ++u) { const float p = __builtin_amdgcn_exp2f(fmaf(sc[u], C2, -mc)); pd[u] = cvtpk(p, p); l += bfhi(pd[u]); }
          l += dpp<ROR8>(l); l = x16_sum(l); l = x32_sum(l);
          if (lane == 0) { *(f32x2*)(l_ml + 2 * sw) = f32x2{mc, l}; MEMFENCE(); l_f[FA + sw] = 1; }
          STAMP(3);
        }
      } else {
        constexpr int u = t - 16;
        const u32x4 a = {pd[u] & msk[0], pd[u] & msk[1], pd[u] & msk[2], pd[u] & msk[3]};
#pragma unroll
        for (int i = 0; i < 3; ++i) acc[i] = mfma32(a, buf[3 * t + i], acc[i]);
      }
    };
    static_for<0, 16>([&](auto K) {
      constexpr int k = decltype(K)::value;
      if constexpr (k > 6) { const unsigned tv = __builtin_amdgcn_readfirstlane(tendv); tend = tv ? tv + stag : tend; }
      unsigned vm, tdone = tprev + LAT0;
      const unsigned now = pace_step<(k < 15 ? 65536 / (15 - k) : 0)>(tgt, tend, tdone, vm);
      SB();
#pragma unroll
      for (int i = 0; i < 6; ++i) { buf[6 * k + i] = ld_nt(k < 8 ? r0 : rv, voff + (i % 3) * 128, ((2 * k + i / 3) & 15) * 49152); SB(); }
      if constexpr (k == 15) { if (lane == 0) l_f[FB + sw] = 1; }
      if constexpr (k >= 2 && k < 13) *(u32x2*)(l_est + 2 * sw) = u32x2{(now - stag + ((6 * (15 - k) * RATE16) >> 4)) | 1u, tdone - tprev + VMW * vm};
      tprev = now;
      if constexpr (k == 1) TAG_READY();
      if constexpr (k >= 1 && k < 10) FS(1 + k);
      if constexpr (k >= 1 && k < 15) asm volatile("s_waitcnt vmcnt(6)" ::: "memory");
      if constexpr (k == QK) {
        STAMP(1);
        wait_lds<0>(fbase + FG, ALL4);
        STAMP(2);
#pragma unroll
        for (int i = 0; i < 3; ++i) qv[i] = *(const u32x4*)(l_q + 32 * i + 4 * j);
      }
      SB();
      constexpr int lo = k <= QK ? 0 : (k == QK + 1 ? QK + 1 : 2 * k - 2), hi = k < QK ? 0 : (k == QK ? QK + 1 : (k < 15 ? 2 * k : 2 * k - 2));
      static_for<lo, hi>([&](auto G) { consume(G); SB(); });
      if constexpr (k >= 6 && k < 14) tendv = *l_tend;
      SB();
    });
    STAMP(4);
    __builtin_amdgcn_s_setprio(3);
    wait_lds<0>(fbase + FE, ALL4);
    float cq = l_c[lane >> 4];
    unsigned dst = (unsigned)(size_t)(lfloat*)(l_acc + (sw * 2 + (g & 1)) * 192 + 8 * j + 4 * ((lane >> 4) & 1));
    unsigned src = (unsigned)(size_t)(lfloat*)(l_acc + (lane >> 4) * 384 + 64 * sw + 8 * j + 4 * (g & 1));
    unsigned fla = fbase + FC + sw, one = 1u, flr = fbase + FC;
    int pvo = lane < 16 ? lane * 16 : OOB;
    int pso = OP_BASE(h) + OP_REC(b * 4 + s) + sw * OPC;
    unsigned tagv = tag;
    asm volatile("" : "+v"(cq), "+v"(dst), "+v"(src), "+v"(fla), "+v"(flr), "+v"(one), "+v"(pvo), "+s"(pso), "+v"(tagv));
    const bool wr = (lane >> 5) == (g & 1);
    SB();
    static_for<28, 32>([&](auto G) { consume(G); SB(); });
    if (wr) {
#pragma unroll
      for (int i = 0; i < 3; ++i) *(lf32x4*)(size_t)(dst + 256 * i) = acc[i];
      MEMFENCE();
      *(lbyte*)(size_t)fla = (unsigned char)one;
    }
    STAMP(5);
#define OPW 3
    if (sw < OPW) {
      f32x4 u0, u1;
      unsigned fv, fs;
      asm volatile("1:\n\tds_read_b32 %0, %4\n\tds_read_b128 %2, %5\n\tds_read_b128 %3, %5 offset:768\n\ts_waitcnt lgkmcnt(2)\n\tv_readfirstlane_b32 %1, %0\n\t"
                   "s_cmp_lg_u32 %1, 0x01010101\n\ts_waitcnt lgkmcnt(0)\n\ts_cbranch_scc1 1b"
                   : "=&v"(fv), "=&s"(fs), "=&v"(u0), "=&v"(u1) : "v"(flr), "v"(src) : "scc", "memory");
      float o[4];
#pragma unroll
      for (int e = 0; e < 4; ++e) o[e] = x32_sum(x16_sum(cq * (u0[e] + u1[e])));
      const u32x4 pv = {cvtpk(o[0], o[1]), tagv, cvtpk(o[2], o[3]), tagv};
      __builtin_amdgcn_raw_buffer_store_b128(pv, rws, pvo, pso, AUX_OP);
    }
    MEMFENCE();
    *(lbyte*)(size_t)(fla + (FD - FC)) = (unsigned char)one;
    STAMP(6);
    STAMP_FLUSH();
    return;
  }

  __builtin_amdgcn_s_setprio(2);
  const int w = wave;
  const unsigned idx = w < 2 ? 2 * j8 + w : uq >> 1, ur = (idx * 11) >> 5, uc = idx - 3 * ur, up = uq & 1;
  const bool has_front = (w < 3) & (idx < 15);
  const unsigned m0 = e4 == 0 ? 0x0000ffffu : e4 == 1 ? 0xffff0000u : 0u;
  const unsigned m1 = e4 == 2 ? 0x0000ffffu : e4 == 3 ? 0xffff0000u : 0u;
  const int st_col = (g & 1) ? OOB : (lane >> 5) * 128 + j * 16;
  const rsrc_t rx = mk_rsrc(x, 2560);
  if (has_front) {
    const int xo = (lane >> 5) * 1280 + (lane & 31) * 16;
    const bool x3 = (lane & 16) == 0;
    const u32x4 xa = __builtin_amdgcn_raw_buffer_load_b128(rx, xo, 0, 0), xb = __builtin_amdgcn_raw_buffer_load_b128(rx, xo + 512, 0, 0);
    const u32x4 xc = __builtin_amdgcn_raw_buffer_load_b128(rx, x3 ? xo + 1024 : OOB, 0, 0);
    const u32x4 xr0 = __builtin_amdgcn_raw_buffer_load_b128(rx, bl * 1280 + g * 32, 256 * ur, 0);
    const u32x4 xr1 = __builtin_amdgcn_raw_buffer_load_b128(rx, bl * 1280 + g * 32 + 16, 256 * ur, 0);
    SB();
    FS(2);
    TAG_READY();
    float v[24];
#pragma unroll
    for (int e = 0; e < 4; ++e) { v[2 * e] = bflo(xa[e]); v[2 * e + 1] = bfhi(xa[e]); v[8 + 2 * e] = bflo(xb[e]); v[9 + 2 * e] = bfhi(xb[e]); v[16 + 2 * e] = bflo(xc[e]); v[17 + 2 * e] = bfhi(xc[e]); }
    float sum = 0.f;
#pragma unroll
    for (int e = 0; e < 24; ++e) sum += v[e];
    sum = x16_sum(row_sum(sum));
    const float mean = sum * (1.f / 640.f);
    float sq = 0.f, sq3 = 0.f;
#pragma unroll
    for (int e = 0; e < 16; ++e) { const float d = v[e] - mean; sq = fmaf(d, d, sq); }
#pragma unroll
    for (int e = 16; e < 24; ++e) { const float d = v[e] - mean; sq3 = fmaf(d, d, sq3); }
    sq = x16_sum(row_sum(sq + (x3 ? sq3 : 0.f)));
    const float mb = rbf(mean), den = rbf(__builtin_amdgcn_sqrtf(rbf(rbf(sq * (1.f / 640.f)) + 1e-5f)));
    const float inv = __builtin_amdgcn_rcpf(den);
    const float mb0 = __builtin_bit_cast(float, __builtin_amdgcn_readlane(__builtin_bit_cast(int, mb), 0));
    const float mb1 = __builtin_bit_cast(float, __builtin_amdgcn_readlane(__builtin_bit_cast(int, mb), 32));
    const float iv0 = __builtin_bit_cast(float, __builtin_amdgcn_readlane(__builtin_bit_cast(int, inv), 0));
    const float iv1 = __builtin_bit_cast(float, __builtin_amdgcn_readlane(__builtin_bit_cast(int, inv), 32));
    const float mm = bl ? mb1 : mb0, ii = bl ? iv1 : iv0;
    FS(3);
    asm volatile("s_waitcnt vmcnt(0)" : "+v"(fw[0]), "+v"(fw[1]), "+v"(fw[2]), "+v"(fw[3]), "+v"(fw[4]), "+v"(fw[5]), "+v"(fw[6]), "+v"(fw[7]),
                 "+v"(fw[8]), "+v"(fw[9]), "+v"(fw[10]), "+v"(fw[11]), "+v"(fw[12]), "+v"(fw[13]), "+v"(fw[14]), "+v"(fw[15]));
    f32x4 acc0 = {0.f, 0.f, 0.f, 0.f}, acc1 = {0.f, 0.f, 0.f, 0.f};
#pragma unroll
    for (int n = 0; n < 16; ++n) {
      const unsigned xw = n < 8 ? xr0[n >> 1] : xr1[(n - 8) >> 1];
      const float xv = (n & 1) ? bfhi(xw) : bflo(xw);
      const float xn = rbf(xv - mm) * ii;
      const unsigned d = cvtpk(xn, xn), a0 = d & m0, a1 = d & m1;
      acc0 = mfma16(a0, a1, fw[n][0], fw[n][1], acc0);
      acc1 = mfma16(a0, a1, fw[n][2], fw[n][3], acc1);
    }
    const int rec = (w < 2 ? QP_BASE(h) + ur * 1536 : GU_BASE(xq) + GU_REC(ur) + up * 1536) + uc * 256;
    __builtin_amdgcn_raw_buffer_store_b128(proj_epilogue(acc0, acc1, tag), rws, ((lane >> 4) & 1) * 768 + st_col, rec, AUX_SC1);
    STAMP(1); FS(4);
    asm volatile("s_waitcnt vmcnt(0)" ::: "memory");
    STAMP(11); FS(5);
  } else {
    asm volatile("s_waitcnt vmcnt(0)" ::"v"(fw[0]), "v"(fw[1]), "v"(fw[2]), "v"(fw[3]), "v"(fw[4]), "v"(fw[5]), "v"(fw[6]), "v"(fw[7]),
                 "v"(fw[8]), "v"(fw[9]), "v"(fw[10]), "v"(fw[11]), "v"(fw[12]), "v"(fw[13]), "v"(fw[14]), "v"(fw[15]));
    TAG_READY();
  }
  const int slot = w * 8 + j8;
  const bool has_back = slot < 30;
  const int t3 = slot / 10, cc = slot - 10 * t3;
  const int rid = xq * 20 + uq;
  const int rb = rid / 80, rc = (rid - 80 * rb) >> 3, ro = rid & 7;
  const bool reducer = (w == 0) && (bid < 160);
  unsigned ha[2][2] = {{0u, 0u}, {0u, 0u}};
  unsigned tend = tstart + T_PLAN;
  auto pace_round = [&](unsigned t_sample, unsigned t_read, int so, int bias, int gain16, int keep16) {
    wait_until(t_sample);
    u32x4 e, f;
    do { MEMFENCE(); e = *(const u32x4*)l_est; f = *(const u32x4*)(l_est + 4); } while (!__builtin_amdgcn_readfirstlane(e[0] & e[2] & f[0] & f[2] & 1u));
    const u32x4 pv = {tstart + (unsigned)((int)((e[0] - tstart) + (e[2] - tstart) + (f[0] - tstart) + (f[2] - tstart)) >> 2), tag, e[1] + e[3] + f[1] + f[3], tag};
    __builtin_amdgcn_raw_buffer_store_b128(pv, rws, (lane == 0 && uq % 9 == 0) ? (uq / 9 * 8 + xq) * 16 : OOB, so, AUX_SC1);
    wait_until(t_read);
    MEMFENCE();
    const u32x4 tv = ld_sc1(rws, (lane & 31) * 16, so);
    if (__all((tv[1] == tag) & (tv[3] == tag))) {
      const float d = x16_sum(row_sum((float)(int)(tv[0] - tstart))) * (1.f / 32.f);
      const float dr = x16_sum(row_sum((float)(int)tv[2])) * (1.f / 128.f) - (float)LAT0;
      int di = (int)(d + fmaxf(dr, 0.f) * (float)gain16 * (1.f / 16.f)) - bias;
      const int dold = (int)(tend - tstart);
      if (keep16 && di < dold) di += ((dold - di) * keep16) >> 4;
      di = di < 0 ? 0 : (di > 2500 ? 2500 : di);
      tend = (tstart + __builtin_amdgcn_readfirstlane(di)) | 1u;
      *l_tend = tend;
      STAMPT(so == WS_TS ? 12 : 15, stv[0] + 100 * (50 + __builtin_amdgcn_readfirstlane((int)dr)));
    }
  };
  if (w < 3) {
    if (has_front) __builtin_amdgcn_s_sleep(16); else wait_until(tstart + 360);
    u32x2 t[3];
    const int vo = (lane >> 5) * 1536 + (lane & 31) * 8, so = QP_BASE(h) + b * 768 + w * 256;
    for (unsigned spin = 0;; ++spin) {
      MEMFENCE();
      t[0] = ld2_sc1(rws, vo, so); t[1] = ld2_sc1(rws, vo, so + 3072); t[2] = ld2_sc1(rws, lane < 32 ? vo : OOB, so + 6144);
      const bool ok = (t[0][1] == tag) & (t[1][1] == tag) & ((t[2][1] == tag) | (lane >= 32));
      if (__all(ok) || spin > SPIN_CAP) { STAMPV(12, spin + 1); break; }
    }
    const float a = x32_sum((bflo(t[0][0]) + bflo(t[1][0])) + bflo(t[2][0]));
    const float c = x32_sum((bfhi(t[0][0]) + bfhi(t[1][0])) + bfhi(t[2][0]));
    if (lane < 32) { l_q[32 * w + ((lane >> 1) & 7) * 4 + (lane >> 4) * 2 + (lane & 1)] = cvtpk(a, c); MEMFENCE(); l_f[FG + w] = 1; }
  } else {
    l_f[FG + 3] = 1;
    pace_round(tstart + SMP0, tstart + SMP0 + WAIT0, WS_TS, BIAS0, KAPPA0, 0);
    STAMPT(1, tend);
    STAMP(9);
  }
  STAMP(2); FS(6);
  if (has_back) {
    u32x2 t[3];
    const int f16 = (h & 3) * 3 + t3;
    const int so = GU_BASE(xq) + (f16 >> 2) * 256 + (f16 & 3) * 32;
    const int vo = bl * 768 + ((g >> 1) & 1) * 128 + (g >> 2) * 16 + (g & 1) * 8;
    for (unsigned spin = 0;; ++spin) {
      MEMFENCE();
      t[0] = ld2_sc1(rws, vo + GU_REC(e4), so);
      t[1] = ld2_sc1(rws, vo + GU_REC(e4) + 1536, so);
      t[2] = ld2_sc1(rws, vo + GU_REC(4) + (e4 & 1) * 1536, so);
      if (__all((t[0][1] == tag) & (t[1][1] == tag) & (t[2][1] == tag)) || spin > SPIN_CAP) { STAMPV(13, spin + 1); break; }
    }
#pragma unroll
    for (int z = 0; z < 2; ++z) {
      const float ex = z ? bfhi(t[2][0]) : bflo(t[2][0]);
      const float gg = rbf(quad_sum((z ? bfhi(t[0][0]) : bflo(t[0][0])) + (e4 == 0 ? ex : 0.f)));
      const float uu = rbf(quad_sum((z ? bfhi(t[1][0]) : bflo(t[1][0])) + (e4 == 1 ? ex : 0.f)));
      const float zz = 0.7978845608028654f * (gg + 0.044715f * gg * gg * gg);
      const float ge = rbf(gg * __builtin_amdgcn_rcpf(1.f + __builtin_amdgcn_exp2f(-2.8853900817779268f * zz)));
      const float hv = ge * uu;
      const unsigned d = cvtpk(hv, hv);
      ha[z][0] = d & m0; ha[z][1] = d & m1;
    }
  }
  const int oc = 16 * (ro & 3) + 8 * (lane & 1) + 4 * (ro >> 2) + 2 * (lane >> 5) + ((lane >> 4) & 1);
  const rsrc_t rout = mk_rsrc(out, 2560);
  int out_vo = (lane & 14) == 0 ? oc * 2 + rb * 1280 + rc * 128 : OOB;
  float xres = 0.f;
  if (reducer) {
    xres = bflo(__builtin_amdgcn_raw_buffer_load_b16(rx, oc * 2, rb * 1280 + rc * 128, 0));
    __builtin_amdgcn_raw_buffer_store_b16((unsigned short)0, rout, out_vo, 0, AUX_SC1);
  }
  STAMP(3);
  if (w == 1) {
    wait_lds<4>(fbase + FA, ALL4);
    const int so = WS_ML + (h * 2 + b) * 256;
    if (lane < 4) { const f32x2 ml = *(const f32x2*)(l_ml + 2 * lane); st_pair(rws, lane * 16, so + s * 64, ml[0], ml[1], tag); }
    asm volatile("s_waitcnt vmcnt(0)" ::: "memory");
    __builtin_amdgcn_s_sleep(32);
    u32x4 t;
    for (unsigned spin = 0;; ++spin) {
      MEMFENCE();
      t = ld_sc1(rws, (lane & 15) * 16, so);
      if (__all((t[1] == tag) & (t[3] == tag)) || spin > SPIN_CAP) break;
    }
    const float mv = __uint_as_float(t[0]);
    const float M = row_max(mv);
    const float f = __builtin_amdgcn_exp2f(mv - M);
    const float L = row_sum(f * __uint_as_float(t[2]));
    if ((lane >> 2) == s) { l_c[lane & 3] = f / L; MEMFENCE(); l_f[FE + (lane & 3)] = 1; }
  }
  if (w == 3) {
    pace_round(tend - SMP1, tend - SMP1 + WAIT1, WS_TS + 512, BIAS1, KAPPA16, KEEP1);
    STAMPT(11, tend);
  }
  if (has_back) {
    const unsigned sLo = (e4 & 1) ? 0x01000c0cu : 0x0c0c0100u, sHi = (e4 & 1) ? 0x03020c0cu : 0x0c0c0302u;
    unsigned sel0 = e4 < 2 ? sLo : 0x0c0c0c0cu, sel1 = e4 < 2 ? 0x0c0c0c0cu : sLo, sel2 = e4 < 2 ? sHi : 0x0c0c0c0cu, sel3 = e4 < 2 ? 0x0c0c0c0cu : sHi;
    int op_so = OP_BASE(h) + t3 * OPC, op_vo = OP_REC(bl * 4 + e4) + 16 * g;
    int d_vo = ((48 * h + 16 * t3 + 2 * g) * 640 + 64 * cc) * 2 + 16 * j, o_vo = ((192 * h + 64 * t3 + (WOSEQ ? 1 : 8) * g) * 640 + 64 * cc) * 2 + 16 * j;
    int yvo = ((lane >> 4) & 1) * 256 + st_col, yso = WS_YP + ((h * 3 + t3) * 10 + cc) * 512;
    int r_vo = (lane >> 1) * 5120 + (lane & 1) * 16 + WS_YP + rc * 512 + rb * 256 + ro * 32;
    unsigned tagv = tag, fd = fbase + FD;
    asm volatile("" : "+v"(sel0), "+v"(sel1), "+v"(sel2), "+v"(sel3), "+s"(op_so), "+v"(op_vo), "+v"(d_vo), "+v"(o_vo), "+v"(yvo), "+s"(yso), "+v"(r_vo), "+v"(out_vo), "+v"(tagv), "+v"(xres), "+v"(fd));
    const rsrc_t rd = mk_rsrc(wd, 1536 * 1280), ro_ = mk_rsrc(wo, 6144 * 1280);
    STAMP(4);
    wait_lds<1>(fbase + FB, ALL4);
    {
      unsigned tf = *l_tend;
      asm volatile("" : "+v"(tf));
      tf = __builtin_amdgcn_readfirstlane(tf);
      const unsigned tn = (unsigned)__builtin_amdgcn_s_memrealtime();
      int wt = tf ? (int)(tf + BACK_AT - tn) : 0;
      asm("s_max_i32 %0, %0, %1\n\ts_min_i32 %0, %0, 0xc8" : "+s"(wt) : "s"(BACK_DELAY) : "scc");
      wait_until(tn + wt);
    }
    u32x4 wb[10], t0, t1;
    unsigned fdv = 0;
#pragma unroll
    for (int n = 0; n < 10; ++n) {
      if (n == POLLPOS) {
        const rsrc_t rp = mk_rsrc(ws, __builtin_amdgcn_readfirstlane(fdv) == ALL4 ? WS_POLL : 0);
        t0 = ld_sc1(rp, op_vo, op_so); t1 = ld_sc1(rp, op_vo + 128, op_so);
        SB();
      }
      if (n < 2) wb[8 + n] = ld_nt(rd, d_vo + n * 1280, 0); else wb[n - 2] = ld_nt(ro_, o_vo, (n - 2) * (WOSEQ ? 10240 : 1280));
      if (n == POLLPOS - 1) { MEMFENCE(); fdv = *(luint*)(size_t)fd; }
      SB();
      if (n < 9) __builtin_amdgcn_s_sleep(BPACE);
      SB();
    }
    STAMP(5);
    u32x4 bq[10];
#define PAIR(p) do { bq[2 * (p)] = u32x4{wb[2 * (p)][0], wb[2 * (p)][1], wb[2 * (p) + 1][0], wb[2 * (p) + 1][1]}; \
                     bq[2 * (p) + 1] = u32x4{wb[2 * (p)][2], wb[2 * (p)][3], wb[2 * (p) + 1][2], wb[2 * (p) + 1][3]}; } while (0)
    PAIR(4); PAIR(0); PAIR(1); PAIR(2);
    const u32x4 hA = {ha[0][0], ha[0][1], ha[1][0], ha[1][1]};
    f32x4 acc0 = mfma32(hA, bq[8], f32x4{0.f, 0.f, 0.f, 0.f}), acc1 = mfma32(hA, bq[9], f32x4{0.f, 0.f, 0.f, 0.f});
    asm volatile("" : "+v"(bq[0]), "+v"(bq[1]), "+v"(bq[2]), "+v"(bq[3]), "+v"(bq[4]), "+v"(bq[5]), "+v"(acc0), "+v"(acc1));
    SB();
    unsigned np1 = 1;
    if (__builtin_expect(__builtin_amdgcn_ballot_w64((((t0[1] ^ tagv) | (t0[3] ^ tagv)) | ((t1[1] ^ tagv) | (t1[3] ^ tagv))) != 0u) != 0, 0)) {
      do {
        ++np1;
        MEMFENCE();
        if (__builtin_amdgcn_readfirstlane(*(luint*)(size_t)fd) != ALL4) { wait_lds<1>(fd, ALL4); delay_ticks(LATE_DELAY); }
        t0 = ld_sc1(rws, op_vo, op_so); t1 = ld_sc1(rws, op_vo + 128, op_so);
      } while (!__all((t0[1] == tag) & (t0[3] == tag) & (t1[1] == tag) & (t1[3] == tag)) && np1 < SPIN_CAP);
    }
    STAMPV(14, np1);
    STAMP(7);
    unsigned pk[4];
    {
      float u0, u1, u2, u3, u4, u5, u6, u7;
#define DPPADD(r, P) "v_add_f32_dpp " r ", " r ", " r " quad_perm:" P " row_mask:0xf bank_mask:0xf\n\t"
#define DPP8(P) DPPADD("%4", P) DPPADD("%5", P) DPPADD("%6", P) DPPADD("%7", P) DPPADD("%8", P) DPPADD("%9", P) DPPADD("%10", P) DPPADD("%11", P)
      asm volatile("v_lshlrev_b32 %4, 16, %12\n\tv_and_b32 %5, 0xffff0000, %12\n\tv_lshlrev_b32 %6, 16, %13\n\tv_and_b32 %7, 0xffff0000, %13\n\t"
                   "v_lshlrev_b32 %8, 16, %14\n\tv_and_b32 %9, 0xffff0000, %14\n\tv_lshlrev_b32 %10, 16, %15\n\tv_and_b32 %11, 0xffff0000, %15\n\t"
                   DPP8("[1,0,3,2]") DPP8("[2,3,0,1]")
                   "v_cvt_pk_bf16_f32 %0, %4, %5\n\tv_cvt_pk_bf16_f32 %1, %6, %7\n\tv_cvt_pk_bf16_f32 %2, %8, %9\n\tv_cvt_pk_bf16_f32 %3, %10, %11"
                   : "=&v"(pk[0]), "=&v"(pk[1]), "=&v"(pk[2]), "=&v"(pk[3]), "=&v"(u0), "=&v"(u1), "=&v"(u2), "=&v"(u3), "=&v"(u4), "=&v"(u5), "=&v"(u6), "=&v"(u7)
                   : "v"(t0[0]), "v"(t0[2]), "v"(t1[0]), "v"(t1[2]));
    }
    u32x4 aq[4];
#pragma unroll
    for (int p = 0; p < 4; ++p)
      aq[p] = u32x4{__builtin_amdgcn_perm(0u, pk[p], sel0), __builtin_amdgcn_perm(0u, pk[p], sel1), __builtin_amdgcn_perm(0u, pk[p], sel2), __builtin_amdgcn_perm(0u, pk[p], sel3)};
#pragma unroll
    for (int p = 0; p < 3; ++p) { acc0 = mfma32(aq[p], bq[2 * p], acc0); acc1 = mfma32(aq[p], bq[2 * p + 1], acc1); }
    asm volatile("" : "+v"(aq[3]), "+v"(acc0), "+v"(acc1));
    SB();
    acc0 = mfma16(aq[3][0], aq[3][1], wb[6][0], wb[6][1], acc0); acc1 = mfma16(aq[3][0], aq[3][1], wb[6][2], wb[6][3], acc1);
    acc0 = mfma16(aq[3][2], aq[3][3], wb[7][0], wb[7][1], acc0); acc1 = mfma16(aq[3][2], aq[3][3], wb[7][2], wb[7][3], acc1);
    __builtin_amdgcn_raw_buffer_store_b128(proj_epilogue(acc0, acc1, tagv), rws, yvo, yso, AUX_SC1);
    STAMP(8);
    if (reducer) {
      delay_ticks(FIN_DELAY);
      float y0, y1, y2, y3;
      unsigned np2 = 0;
      for (;;) {
        MEMFENCE();
        const u32x4 a = ld_sc1(rws, r_vo, 0), c = ld_sc1(rws, r_vo, 32 * 5120), d = ld_sc1(rws, r_vo, 64 * 5120);
        unsigned bad = ((a[1] ^ tagv) | (a[3] ^ tagv)) | ((c[1] ^ tagv) | (c[3] ^ tagv));
        y0 = bflo(a[0]) + bflo(c[0]); y1 = bfhi(a[0]) + bfhi(c[0]); y2 = bflo(a[2]) + bflo(c[2]); y3 = bfhi(a[2]) + bfhi(c[2]);
        asm volatile("" : "+v"(bad), "+v"(y0), "+v"(y1), "+v"(y2), "+v"(y3));
        bad |= (d[1] ^ tagv) | (d[3] ^ tagv);
        y0 += bflo(d[0]); y1 += bfhi(d[0]); y2 += bflo(d[2]); y3 += bfhi(d[2]);
        if (__builtin_expect(__builtin_amdgcn_ballot_w64(bad != 0u) == 0, 1)) break;
        if (++np2 > SPIN_CAP) break;
      }
      STAMPV(15, np2 + 1);
      STAMP(9);
      const auto s0 = __builtin_amdgcn_permlane32_swap(__float_as_uint(y0), __float_as_uint(y2), false, false);
      const auto s1 = __builtin_amdgcn_permlane32_swap(__float_as_uint(y1), __float_as_uint(y3), false, false);
      const float z0 = __uint_as_float(s0[0]) + __uint_as_float(s0[1]), z1 = __uint_as_float(s1[0]) + __uint_as_float(s1[1]);
      const auto s2 = __builtin_amdgcn_permlane16_swap(__float_as_uint(z0), __float_as_uint(z1), false, false);
      float y = __uint_as_float(s2[0]) + __uint_as_float(s2[1]);
      y += dpp<ROR8>(y); y += dpp<ROR4>(y); y += dpp<XOR2>(y);
      __builtin_amdgcn_raw_buffer_store_b16((unsigned short)cvtpk(xres + y, 0.f), rout, out_vo, 0, AUX_SC1);
      if (bid == 0) __builtin_amdgcn_raw_buffer_store_b32(tagv, rws, lane == 0 ? WS_EPOCH : OOB, 0, AUX_SC1);
      STAMP(10);
    }
  }
  STAMP_FLUSH();
}

extern "C" void kernel_launch(void* const* d_in, const int* in_sizes, int n_in, void* d_out, int out_size, void* d_ws, size_t ws_size,
                              hipStream_t stream) {
  fused_decode<<<256, 512, 0, stream>>>((const u16*)d_in[0], (const u16*)d_in[1], (const u16*)d_in[2], (const u16*)d_in[3], (const u16*)d_in[4],
                                        (const u16*)d_in[6], (const u16*)d_in[7], (const u16*)d_in[8], (u16*)d_out, (unsigned char*)d_ws
                                        );
}
```

```cpp
#include <hip/hip_runtime.h>
#include <hip/hip_bf16.h>
#include <stdint.h>
#include <type_traits>

typedef unsigned short u16;
typedef unsigned u32x4 __attribute__((ext_vector_type(4)));
typedef unsigned u32x2 __attribute__((ext_vector_type(2)));
typedef float f32x4 __attribute__((ext_vector_type(4)));
typedef float f32x2 __attribute__((ext_vector_type(2)));
typedef short s16x4 __attribute__((ext_vector_type(4)));
typedef __bf16 bf16x8 __attribute__((ext_vector_type(8)));
typedef __bf16 bf16x2 __attribute__((ext_vector_type(2)));
typedef __amdgpu_buffer_rsrc_t rsrc_t;

constexpr int QK = 6, CU = 6;
constexpr int STAG = 10;
constexpr int BPACE = 5;
constexpr int POLLPOS = 8;
constexpr int KEEP1 = 10;
constexpr int S_DELAY = 152;
constexpr int T_PLAN = 1392;
constexpr int RATE16 = 215;
constexpr int LAT0 = 73;
constexpr int SMP0 = 500, WAIT0 = 220, DEAD0 = 20, GAIN0 = 48;
constexpr int SMP1 = 385, WAIT1 = 130, DEAD1 = 13, GAIN1 = 52;
constexpr int BACK_AT = 66;
constexpr int BACK_DELAY = 12;
constexpr int LATE_DELAY = 20;
constexpr int FIN_DELAY = 48;
constexpr int CODE_BYTES = 16000;

constexpr int AUX_NT = 2, AUX_SC1 = 16;
constexpr int RSRC_W3 = 0x00020000;
constexpr unsigned SPIN_CAP = 1u << 18;
constexpr int OOB = 0x40000000;
constexpr unsigned ALL4 = 0x01010101u;

constexpr int WS_EPOCH = 0;
constexpr int WS_TS = 1024;
constexpr int WS_QP = 4096;
constexpr int WS_GU = WS_QP + 32 * 5 * 2 * 96 * 8;
constexpr int WS_ML = WS_GU + 8 * 5 * 2 * 2 * 96 * 8;
constexpr int WS_YP = WS_ML + 32 * 2 * 4 * 4 * 16;
constexpr int WS_OP = 2 << 20, OP_STRIDE = 1 << 20;
constexpr int WS_END = WS_OP + 32 * OP_STRIDE;
static_assert(WS_YP + 96 * 10 * 2 * 32 * 8 <= WS_OP, "workspace map");

template <int I, int N, class F> __device__ __forceinline__ void static_for(F&& f) {
  if constexpr (I < N) { f(std::integral_constant<int, I>{}); static_for<I + 1, N>(f); }
}
__device__ __forceinline__ rsrc_t mk_rsrc(const void* p, int bytes) {
  return __builtin_amdgcn_make_buffer_rsrc(const_cast<void*>(p), 0, bytes, RSRC_W3);
}
__device__ __forceinline__ u32x4 ld_nt(rsrc_t r, int voff, int soff) { return __builtin_amdgcn_raw_buffer_load_b128(r, voff, soff, AUX_NT); }
__device__ __forceinline__ u32x4 ld_sc1(rsrc_t r, int voff, int soff) { return __builtin_amdgcn_raw_buffer_load_b128(r, voff, soff, AUX_SC1); }
__device__ __forceinline__ u32x2 ld2_sc1(rsrc_t r, int voff, int soff) { return __builtin_amdgcn_raw_buffer_load_b64(r, voff, soff, AUX_SC1); }
__device__ __forceinline__ void st_pair(rsrc_t r, int voff, int soff, float a, float b, unsigned tag) {
  u32x4 v = {__float_as_uint(a), tag, __float_as_uint(b), tag};
  __builtin_amdgcn_raw_buffer_store_b128(v, r, voff, soff, AUX_SC1);
}
__device__ __forceinline__ float bflo(unsigned v) { return __uint_as_float(v << 16); }
__device__ __forceinline__ float bfhi(unsigned v) { return __uint_as_float(v & 0xffff0000u); }
__device__ __forceinline__ unsigned cvtpk(float lo, float hi) {
  bf16x2 r = {(__bf16)lo, (__bf16)hi};
  return __builtin_bit_cast(unsigned, r);
}
__device__ __forceinline__ float rbf(float x) { return (float)(__bf16)x; }
template <int CTRL> __device__ __forceinline__ float dpp(float x) {
  return __builtin_bit_cast(float, __builtin_amdgcn_mov_dpp(__builtin_bit_cast(int, x), CTRL, 0xf, 0xf, true));
}
constexpr int XOR1 = 0xB1, XOR2 = 0x4E, HMIR = 0x141, ROR8 = 0x128, ROR4 = 0x124;
__device__ __forceinline__ float x16_sum(float x) { auto s = __builtin_amdgcn_permlane16_swap(__float_as_uint(x), __float_as_uint(x), false, false); return __uint_as_float(s[0]) + __uint_as_float(s[1]); }
__device__ __forceinline__ float x32_sum(float x) { auto s = __builtin_amdgcn_permlane32_swap(__float_as_uint(x), __float_as_uint(x), false, false); return __uint_as_float(s[0]) + __uint_as_float(s[1]); }
__device__ __forceinline__ float x16_max(float x) { auto s = __builtin_amdgcn_permlane16_swap(__float_as_uint(x), __float_as_uint(x), false, false); return fmaxf(__uint_as_float(s[0]), __uint_as_float(s[1])); }
__device__ __forceinline__ float x32_max(float x) { auto s = __builtin_amdgcn_permlane32_swap(__float_as_uint(x), __float_as_uint(x), false, false); return fmaxf(__uint_as_float(s[0]), __uint_as_float(s[1])); }
__device__ __forceinline__ float row_sum(float x) { x += dpp<XOR1>(x); x += dpp<XOR2>(x); x += dpp<HMIR>(x); x += dpp<ROR8>(x); return x; }
__device__ __forceinline__ float row_max(float x) { x = fmaxf(x, dpp<XOR1>(x)); x = fmaxf(x, dpp<XOR2>(x)); x = fmaxf(x, dpp<HMIR>(x)); x = fmaxf(x, dpp<ROR8>(x)); return x; }
__device__ __forceinline__ float quad_sum(float x) { x += dpp<XOR1>(x); x += dpp<XOR2>(x); return x; }
__device__ __forceinline__ float dot2(unsigned a, unsigned b, float c) {
  return __builtin_amdgcn_fdot2_f32_bf16(__builtin_bit_cast(bf16x2, a), __builtin_bit_cast(bf16x2, b), c, false);
}
__device__ __forceinline__ f32x4 mfma16(unsigned a0, unsigned a1, unsigned b0, unsigned b1, f32x4 c) {
  u32x2 a = {a0, a1}, b = {b0, b1};
  return __builtin_amdgcn_mfma_f32_16x16x16bf16_1k(__builtin_bit_cast(s16x4, a), __builtin_bit_cast(s16x4, b), c, 0, 0, 0);
}
__device__ __forceinline__ f32x4 mfma32(u32x4 a, u32x4 b, f32x4 c) {
  return __builtin_amdgcn_mfma_f32_16x16x32_bf16(__builtin_bit_cast(bf16x8, a), __builtin_bit_cast(bf16x8, b), c, 0, 0, 0);
}
template <int RECIP> __device__ __forceinline__ unsigned pace_step(unsigned tprev, unsigned tend, unsigned& tdone) {
  unsigned d, now, tgt;
  asm volatile("s_sub_i32 %0, %5, %4\n\ts_max_i32 %0, %0, 0\n\ts_min_i32 %0, %0, 0x7d0\n\ts_mul_i32 %0, %0, %6\n\ts_lshr_b32 %0, %0, 16\n\ts_add_u32 %2, %4, %0\n"
               "1:\n\ts_memrealtime vcc\n\ts_getreg_b32 %0, hwreg(HW_REG_IB_STS, 0, 4)\n\ts_waitcnt lgkmcnt(0)\n\ts_cmp_lg_u32 %0, 0\n\ts_cselect_b32 %3, vcc_lo, %3\n\t"
               "s_sub_u32 %0, vcc_lo, %2\n\ts_cmp_lt_i32 %0, 0\n\ts_cbranch_scc1 1b\n\ts_mov_b32 %1, vcc_lo"
               : "=&s"(d), "=&s"(now), "=&s"(tgt), "+s"(tdone) : "s"(tprev), "s"(tend), "i"(RECIP) : "vcc", "scc");
  return now;
}
__device__ __forceinline__ void wait_until(unsigned tgt) {
  asm volatile("1:\n\ts_memrealtime vcc\n\ts_waitcnt lgkmcnt(0)\n\ts_sub_u32 vcc_lo, vcc_lo, %0\n\ts_cmp_lt_i32 vcc_lo, 0\n\ts_cbranch_scc1 1b" :: "s"(tgt) : "vcc", "scc", "memory");
}
#define SB() __builtin_amdgcn_sched_barrier(0)
#define MEMFENCE() asm volatile("" ::: "memory")

__device__ __forceinline__ void delay_ticks(int n) {
  if (n > 0) wait_until((unsigned)__builtin_amdgcn_s_memrealtime() + n);
}

typedef __attribute__((address_space(3))) float lfloat;
typedef __attribute__((address_space(3))) f32x4 lf32x4;
typedef __attribute__((address_space(3))) unsigned luint;
typedef __attribute__((address_space(3))) unsigned char lbyte;
template <int SLP> __device__ __forceinline__ void wait_lds(unsigned a, unsigned want) {
  unsigned v, sv;
  asm volatile("s_branch 2f\n1:\n\ts_sleep %4\n2:\n\tds_read_b32 %0, %2\n\ts_waitcnt lgkmcnt(0)\n\tv_readfirstlane_b32 %1, %0\n\ts_cmp_lg_u32 %1, %3\n\ts_cbranch_scc1 1b"
               : "=&v"(v), "=&s"(sv) : "v"(a), "s"(want), "i"(SLP) : "scc", "memory");
}

__device__ __forceinline__ u32x4 proj_epilogue(f32x4 acc0, f32x4 acc1, unsigned tag) {
  float r[4];
#pragma unroll
  for (int i = 0; i < 4; ++i) {
    auto s = __builtin_amdgcn_permlane32_swap(__float_as_uint(acc0[i]), __float_as_uint(acc1[i]), false, false);
    r[i] = __uint_as_float(s[0]) + dpp<ROR8>(__uint_as_float(s[1]));
  }
  return u32x4{cvtpk(r[0], r[1]), tag, cvtpk(r[2], r[3]), tag};
}

__global__ void __launch_bounds__(512, 2)
fused_decode(const u16* x_, const u16* kc_, const u16* vc_, const u16* wq_, const u16* wo_, const u16* wg_, const u16* wu_, const u16* wd_, u16* out_, unsigned char* ws_) {
  __shared__ __attribute__((aligned(16))) float lds[96 + 8 + 4 + 16 + 8 * 192];
  unsigned* l_q = (unsigned*)lds;
  float* l_ml = lds + 96;
  float* l_c = lds + 104;
  unsigned* l_est = (unsigned*)(lds + 108);
  unsigned char* l_f = (unsigned char*)(lds + 116);
  constexpr int FA = 0, FB = 4, FC = 8, FD = 12, FE = 16, FG = 20;
  unsigned* l_tend = (unsigned*)(lds + 122);
  const unsigned fbase = (unsigned)(size_t)(lbyte*)l_f;
  float* l_acc = lds + 124;

  typedef unsigned long long u64;
  typedef u64 u64x8 __attribute__((ext_vector_type(8)));
  typedef u64 u64x2 __attribute__((ext_vector_type(2)));
  u64x8 ka; u64x2 kb; u64 pc;
  u32x4 fw[16];
  int bid = blockIdx.x, tid = threadIdx.x;
  {
    unsigned v1, v2;
    u64x8 ka0; u64x2 kb0;
    asm volatile("s_load_dwordx16 %0, %3, 0x0\n\ts_load_dwordx4 %1, %3, 0x40\n\ts_getpc_b64 %2" : "=&{s[36:51]}"(ka0), "=&{s[52:55]}"(kb0), "=&s"(pc) : "s"(__builtin_amdgcn_kernarg_segment_ptr()));
    asm volatile(
               "v_readfirstlane_b32 s14, %20\n\t"
               "s_lshr_b32 s15, %19, 3\n\t"
               "s_cmpk_ge_u32 s14, 0xc0\n\ts_cbranch_scc1 1f\n\t"
               "s_setprio 2\n\t"
               "s_and_b32 s16, %19, 7\n\t"
               "s_lshr_b32 s17, %19, 6\n\ts_lshl2_add_u32 s17, s16, s17\n\t"
               "s_and_b32 s18, s15, 7\n\ts_lshr_b32 s19, s14, 6\n\ts_lshl1_add_u32 s18, s18, s19\n\t"
               "s_lshr_b32 s19, %19, 4\n\t"
               "v_bfe_u32 %16, %20, 3, 3\n\tv_and_b32 %17, 7, %20\n\t"
               "s_movk_i32 s13, 0x3000\n\t"
               "s_cmpk_lt_u32 s14, 0x80\n\t"
               "s_cselect_b32 s18, s18, s19\n\ts_cselect_b32 s17, s17, s16\n\ts_cselect_b32 s13, s13, 0xc00\n\t"
               "s_mul_i32 s19, s18, 11\n\ts_lshr_b32 s19, s19, 5\n\t"
               "v_mad_u32_u24 %16, %16, s13, %17\n\t"
               "s_sub_u32 s17, s17, s19\n\ts_mul_i32 s17, s17, 3\n\ts_add_u32 s17, s17, s18\n\t"
               "s_mul_i32 s19, s19, s13\n\ts_add_u32 s12, s17, s19\n\ts_lshl_b32 s12, s12, 7\n\t"
               "v_lshlrev_b32 %16, 4, %16\n\t"
               "s_mul_i32 s10, s13, 0x280\n\ts_mov_b32 s11, 0x20000\n\ts_cmp_eq_u32 s18, 15\n\ts_cselect_b32 s10, 0, s10\n\t"
               "s_bitcmp1_b32 s15, 0\n\t"
               "s_waitcnt lgkmcnt(0)\n\t"
               "s_cselect_b64 s[8:9], s[48:49], s[46:47]\n\t"
               "s_cmpk_lt_u32 s14, 0x80\n\ts_cselect_b64 s[8:9], s[42:43], s[8:9]\n\t"
               "s_and_b32 s9, s9, 0xffff\n\t"
               "buffer_load_dwordx4 %0, %16, s[8:11], s12 offen nt\n\t" "s_add_u32 s12, s12, s13\n\t"
               "buffer_load_dwordx4 %1, %16, s[8:11], s12 offen nt\n\t" "s_add_u32 s12, s12, s13\n\t"
               "buffer_load_dwordx4 %2, %16, s[8:11], s12 offen nt\n\t" "s_add_u32 s12, s12, s13\n\t"
               "buffer_load_dwordx4 %3, %16, s[8:11], s12 offen nt\n\t" "s_add_u32 s12, s12, s13\n\t"
               "buffer_load_dwordx4 %4, %16, s[8:11], s12 offen nt\n\t" "s_add_u32 s12, s12, s13\n\t"
               "buffer_load_dwordx4 %5, %16, s[8:11], s12 offen nt\n\t" "s_add_u32 s12, s12, s13\n\t"
               "buffer_load_dwordx4 %6, %16, s[8:11], s12 offen nt\n\t" "s_add_u32 s12, s12, s13\n\t"
               "buffer_load_dwordx4 %7, %16, s[8:11], s12 offen nt\n\t" "s_add_u32 s12, s12, s13\n\t"
               "buffer_load_dwordx4 %8, %16, s[8:11], s12 offen nt\n\t" "s_add_u32 s12, s12, s13\n\t"
               "buffer_load_dwordx4 %9, %16, s[8:11], s12 offen nt\n\t" "s_add_u32 s12, s12, s13\n\t"
               "buffer_load_dwordx4 %10, %16, s[8:11], s12 offen nt\n\t" "s_add_u32 s12, s12, s13\n\t"
               "buffer_load_dwordx4 %11, %16, s[8:11], s12 offen nt\n\t" "s_add_u32 s12, s12, s13\n\t" "s_barrier\n\t"
               "buffer_load_dwordx4 %12, %16, s[8:11], s12 offen nt\n\t" "s_add_u32 s12, s12, s13\n\t"
               "buffer_load_dwordx4 %13, %16, s[8:11], s12 offen nt\n\t" "s_add_u32 s12, s12, s13\n\t"
               "buffer_load_dwordx4 %14, %16, s[8:11], s12 offen nt\n\t" "s_add_u32 s12, s12, s13\n\t"
               "buffer_load_dwordx4 %15, %16, s[8:11], s12 offen nt\n\t"
               "s_branch 2f\n"
               "1:\n\t"
               "v_and_b32 %16, 15, %20\n\tv_lshl_add_u32 %16, %16, 2, %21\n\tv_mov_b32 %17, 0\n\tds_write_b32 %16, %17\n\t"
               "s_waitcnt lgkmcnt(0)\n\ts_barrier\n"
               "2:"
               : "=&v"(fw[0]), "=&v"(fw[1]), "=&v"(fw[2]), "=&v"(fw[3]), "=&v"(fw[4]), "=&v"(fw[5]), "=&v"(fw[6]), "=&v"(fw[7]),
                 "=&v"(fw[8]), "=&v"(fw[9]), "=&v"(fw[10]), "=&v"(fw[11]), "=&v"(fw[12]), "=&v"(fw[13]), "=&v"(fw[14]), "=&v"(fw[15]),
                 "=&v"(v1), "=&v"(v2)
               : "{s[36:51]}"(ka0), "s"(bid), "v"(tid), "s"(fbase - 32), "{s[52:55]}"(kb0)
               : "s8", "s9", "s10", "s11", "s12", "s13", "s14", "s15", "s16", "s17", "s18", "s19", "scc", "memory");
    asm volatile("" : "={s[36:51]}"(ka), "={s[52:55]}"(kb), "+s"(bid), "+s"(pc) : "0"(ka0), "1"(kb0));
    asm volatile("" : "+v"(tid));
  }
  const int xq = bid & 7, uq = bid >> 3;
  const int h = xq * 4 + (uq >> 3), j8 = uq & 7, b = j8 >> 2, s = j8 & 3;
  const int lane = tid & 63, wave = __builtin_amdgcn_readfirstlane(tid >> 6);
  const int g = lane >> 3, j = lane & 7;
  const int bl = (lane >> 2) & 1, e4 = lane & 3;
  const bool isS = wave >= 4;
  const u16 *x = (const u16*)ka[0], *kc = (const u16*)ka[1], *vc = (const u16*)ka[2], *wo = (const u16*)ka[4], *wd = (const u16*)ka[7];
  u16* out = (u16*)kb[0];
  unsigned char* ws = (unsigned char*)kb[1];
  u64 tst;
  asm volatile("s_memrealtime %0\n\ts_waitcnt lgkmcnt(0)" : "=s"(tst));
  const unsigned tstart = (unsigned)tst;
  unsigned tag, ep;
  unsigned cp0, cp1;
  {
    unsigned pco = (((wave + 5) & 7) * 32 + uq) * 128;
    pco = pco < CODE_BYTES ? pco : 0;
    asm volatile("s_load_dword %0, %3, 0x0\n\ts_load_dword %1, %4, %5\n\ts_load_dword %2, %4, %6" : "=&s"(ep), "=&s"(cp0), "=&s"(cp1) : "s"(ws), "s"(pc & ~255ull), "s"(pco), "s"(pco + 64));
  }
#define TAG_READY() asm volatile("s_waitcnt lgkmcnt(0)\n\ts_add_u32 %0, %1, 1\n\ts_addc_u32 %0, %0, 0" : "=&s"(tag) : "s"(ep), "s"(cp0), "s"(cp1) : "scc", "memory")
  const rsrc_t rws = mk_rsrc(ws, WS_END);

  if (isS) {
    const int sw = wave - 4;
    const rsrc_t r0 = mk_rsrc(kc, 2 * 32 * 8192 * 384), rv = mk_rsrc(vc, 2 * 32 * 8192 * 384);
    const int voff = ((b * 32 + h) * 8192 + 6144 + 32 * s + 8 * sw + g) * 384 + 16 * j;
    u32x4 buf[96];
    u32x4 qv[3];
    float sc[16];
    unsigned pd[16];
    f32x4 acc[3] = {{0.f, 0.f, 0.f, 0.f}, {0.f, 0.f, 0.f, 0.f}, {0.f, 0.f, 0.f, 0.f}};
    unsigned msk[4];
#pragma unroll
    for (int d = 0; d < 4; ++d) msk[d] = (j >> 1) == d ? ((j & 1) ? 0xffff0000u : 0x0000ffffu) : 0u;
    constexpr float C2 = 0.07216878364870322f * 1.4426950408889634f;
    const int stag = ((2 * (uq >> 2) - 7) * STAG) >> 1;
    unsigned tprev = tstart + S_DELAY + stag, tend = tstart + T_PLAN + stag;

    auto consume = [&](auto G) {
      constexpr int t = decltype(G)::value;
      if constexpr (t < 16) {
        float a0 = 0.f, a1 = 0.f, a2 = 0.f;
#pragma unroll
        for (int e = 0; e < 4; ++e) { a0 = dot2(buf[3 * t][e], qv[0][e], a0); a1 = dot2(buf[3 * t + 1][e], qv[1][e], a1); a2 = dot2(buf[3 * t + 2][e], qv[2][e], a2); }
        float a = (a0 + a1) + a2;
        a += dpp<XOR1>(a); a += dpp<XOR2>(a); a += dpp<HMIR>(a);
        sc[t] = a;
        if constexpr (t == 15) {
          float m = sc[0];
#pragma unroll
          for (int u = 1; u < 16; ++u) m = fmaxf(m, sc[u]);
          m = fmaxf(m, dpp<ROR8>(m)); m = x16_max(m); m = x32_max(m);
          const float mc = m * C2;
          float l = 0.f;
#pragma unroll
          for (int u = 0; u < 16; ++u) { const float p = __builtin_amdgcn_exp2f(fmaf(sc[u], C2, -mc)); pd[u] = cvtpk(p, p); l += bfhi(pd[u]); }
          l += dpp<ROR8>(l); l = x16_sum(l); l = x32_sum(l);
          if (lane == 0) { *(f32x2*)(l_ml + 2 * sw) = f32x2{mc, l}; MEMFENCE(); l_f[FA + sw] = 1; }
        }
      } else {
        constexpr int u = t - 16;
        const u32x4 a = {pd[u] & msk[0], pd[u] & msk[1], pd[u] & msk[2], pd[u] & msk[3]};
#pragma unroll
        for (int i = 0; i < 3; ++i) acc[i] = mfma32(a, buf[3 * t + i], acc[i]);
      }
    };
    static_for<0, 16>([&](auto K) {
      constexpr int k = decltype(K)::value;
      if constexpr (k > 6) {
        MEMFENCE();
        unsigned tv = *l_tend;
        asm volatile("" : "+v"(tv));
        tv = __builtin_amdgcn_readfirstlane(tv);
        tend = tv ? tv + stag : tend;
      }
      unsigned tdone = tprev + LAT0;
      const unsigned now = pace_step<(k ? 65536 / (16 - k) : 0)>(tprev, tend, tdone);
      SB();
#pragma unroll
      for (int i = 0; i < 6; ++i) { buf[6 * k + i] = ld_nt(k < 8 ? r0 : rv, voff + (i % 3) * 128, ((2 * k + i / 3) & 15) * 49152); SB(); }
      if constexpr (k == 15) { if (lane == 0) l_f[FB + sw] = 1; }
      if constexpr (k == 1) TAG_READY();
      if constexpr (k >= 1 && k < 15) asm volatile("s_waitcnt vmcnt(6)" ::: "memory");
      if constexpr (k >= 2 && k < 13) {
        if (tdone == now) tdone = (unsigned)__builtin_amdgcn_s_memrealtime();
        *(u32x2*)(l_est + 2 * sw) = u32x2{(now - stag + ((6 * (15 - k) * RATE16) >> 4)) | 1u, tdone - tprev};
      }
      tprev = now;
      if constexpr (k == QK) {
        wait_lds<0>(fbase + FG, ALL4);
#pragma unroll
        for (int i = 0; i < 3; ++i) qv[i] = *(const u32x4*)(l_q + 32 * i + 4 * j);
      }
      SB();
      constexpr auto upto = [](int kk) constexpr { if (kk < QK) return 0; const int c = CU * (kk - QK + 1), l = kk < 15 ? 2 * kk : 28; return c < l ? c : l; };
      static_for<upto(k - 1), upto(k)>([&](auto G) { consume(G); SB(); });
      SB();
    });
    __builtin_amdgcn_s_setprio(3);
    wait_lds<0>(fbase + FE, ALL4);
    float cq = l_c[lane >> 4];
    unsigned dst = (unsigned)(size_t)(lfloat*)(l_acc + (sw * 2 + (g & 1)) * 192 + 8 * j + 4 * ((lane >> 4) & 1));
    unsigned src = (unsigned)(size_t)(lfloat*)(l_acc + (lane >> 4) * 384 + 64 * sw + 8 * j + 4 * (g & 1));
    unsigned fla = fbase + FC + sw, one = 1u, flr = fbase + FC;
    int pvo = (lane & 16) ? OOB : (g & 1) * 128 + j * 16 + (lane >> 5) * 8;
    int pso = WS_OP + h * OP_STRIDE + (b * 4 + s) * 768 + sw * 256;
    unsigned tagv = tag;
    asm volatile("" : "+v"(cq), "+v"(dst), "+v"(src), "+v"(fla), "+v"(flr), "+v"(one), "+v"(pvo), "+s"(pso), "+v"(tagv));
    const bool wr = (lane >> 5) == (g & 1);
    SB();
    static_for<28, 32>([&](auto G) { consume(G); SB(); });
    if (wr) {
#pragma unroll
      for (int i = 0; i < 3; ++i) *(lf32x4*)(size_t)(dst + 256 * i) = acc[i];
      MEMFENCE();
      *(lbyte*)(size_t)fla = (unsigned char)one;
    }
    if (sw < 3) {
      f32x4 u0, u1;
      unsigned fv, fs;
      asm volatile("1:\n\tds_read_b32 %0, %4\n\tds_read_b128 %2, %5\n\tds_read_b128 %3, %5 offset:768\n\ts_waitcnt lgkmcnt(2)\n\tv_readfirstlane_b32 %1, %0\n\t"
                   "s_cmp_lg_u32 %1, 0x01010101\n\ts_waitcnt lgkmcnt(0)\n\ts_cbranch_scc1 1b"
                   : "=&v"(fv), "=&s"(fs), "=&v"(u0), "=&v"(u1) : "v"(flr), "v"(src) : "scc", "memory");
      float o[4];
#pragma unroll
      for (int e = 0; e < 4; ++e) o[e] = cq * (u0[e] + u1[e]);
      const auto s0 = __builtin_amdgcn_permlane32_swap(__float_as_uint(o[0]), __float_as_uint(o[2]), false, false);
      const auto s1 = __builtin_amdgcn_permlane32_swap(__float_as_uint(o[1]), __float_as_uint(o[3]), false, false);
      const float p0 = x16_sum(__uint_as_float(s0[0]) + __uint_as_float(s0[1])), p1 = x16_sum(__uint_as_float(s1[0]) + __uint_as_float(s1[1]));
      __builtin_amdgcn_raw_buffer_store_b64(u32x2{cvtpk(p0, p1), tagv}, rws, pvo, pso, AUX_SC1);
    }
    MEMFENCE();
    *(lbyte*)(size_t)(fla + (FD - FC)) = (unsigned char)one;
    return;
  }

  __builtin_amdgcn_s_setprio(2);
  const int w = wave;
  const unsigned idx = w < 2 ? 2 * j8 + w : uq >> 1, ur = (idx * 11) >> 5, uc = idx - 3 * ur, up = uq & 1;
  const bool has_front = (w < 3) & (idx < 15);
  const unsigned m0 = e4 == 0 ? 0x0000ffffu : e4 == 1 ? 0xffff0000u : 0u;
  const unsigned m1 = e4 == 2 ? 0x0000ffffu : e4 == 3 ? 0xffff0000u : 0u;
  const int st_col = (g & 1) ? OOB : (lane >> 5) * 128 + j * 16;
  const rsrc_t rx = mk_rsrc(x, 2560);
  if (has_front) {
    const int xo = (lane >> 5) * 1280 + (lane & 31) * 16;
    const bool x3 = (lane & 16) == 0;
    const u32x4 xa = __builtin_amdgcn_raw_buffer_load_b128(rx, xo, 0, 0), xb = __builtin_amdgcn_raw_buffer_load_b128(rx, xo + 512, 0, 0);
    const u32x4 xc = __builtin_amdgcn_raw_buffer_load_b128(rx, x3 ? xo + 1024 : OOB, 0, 0);
    const u32x4 xr0 = __builtin_amdgcn_raw_buffer_load_b128(rx, bl * 1280 + g * 32, 256 * ur, 0);
    const u32x4 xr1 = __builtin_amdgcn_raw_buffer_load_b128(rx, bl * 1280 + g * 32 + 16, 256 * ur, 0);
    SB();
    TAG_READY();
    float v[24];
#pragma unroll
    for (int e = 0; e < 4; ++e) { v[2 * e] = bflo(xa[e]); v[2 * e + 1] = bfhi(xa[e]); v[8 + 2 * e] = bflo(xb[e]); v[9 + 2 * e] = bfhi(xb[e]); v[16 + 2 * e] = bflo(xc[e]); v[17 + 2 * e] = bfhi(xc[e]); }
    float sum = 0.f;
#pragma unroll
    for (int e = 0; e < 24; ++e) sum += v[e];
    sum = x16_sum(row_sum(sum));
    const float mean = sum * (1.f / 640.f);
    float sq = 0.f, sq3 = 0.f;
#pragma unroll
    for (int e = 0; e < 16; ++e) { const float d = v[e] - mean; sq = fmaf(d, d, sq); }
#pragma unroll
    for (int e = 16; e < 24; ++e) { const float d = v[e] - mean; sq3 = fmaf(d, d, sq3); }
    sq = x16_sum(row_sum(sq + (x3 ? sq3 : 0.f)));
    const float mb = rbf(mean), den = rbf(__builtin_amdgcn_sqrtf(rbf(rbf(sq * (1.f / 640.f)) + 1e-5f)));
    const float inv = __builtin_amdgcn_rcpf(den);
    const float mb0 = __builtin_bit_cast(float, __builtin_amdgcn_readlane(__builtin_bit_cast(int, mb), 0));
    const float mb1 = __builtin_bit_cast(float, __builtin_amdgcn_readlane(__builtin_bit_cast(int, mb), 32));
    const float iv0 = __builtin_bit_cast(float, __builtin_amdgcn_readlane(__builtin_bit_cast(int, inv), 0));
    const float iv1 = __builtin_bit_cast(float, __builtin_amdgcn_readlane(__builtin_bit_cast(int, inv), 32));
    const float mm = bl ? mb1 : mb0, ii = bl ? iv1 : iv0;
    asm volatile("s_waitcnt vmcnt(0)" : "+v"(fw[0]), "+v"(fw[1]), "+v"(fw[2]), "+v"(fw[3]), "+v"(fw[4]), "+v"(fw[5]), "+v"(fw[6]), "+v"(fw[7]),
                 "+v"(fw[8]), "+v"(fw[9]), "+v"(fw[10]), "+v"(fw[11]), "+v"(fw[12]), "+v"(fw[13]), "+v"(fw[14]), "+v"(fw[15]));
    f32x4 acc0 = {0.f, 0.f, 0.f, 0.f}, acc1 = {0.f, 0.f, 0.f, 0.f};
#pragma unroll
    for (int n = 0; n < 16; ++n) {
      const unsigned xw = n < 8 ? xr0[n >> 1] : xr1[(n - 8) >> 1];
      const float xv = (n & 1) ? bfhi(xw) : bflo(xw);
      const float xn = rbf(xv - mm) * ii;
      const unsigned d = cvtpk(xn, xn), a0 = d & m0, a1 = d & m1;
      acc0 = mfma16(a0, a1, fw[n][0], fw[n][1], acc0);
      acc1 = mfma16(a0, a1, fw[n][2], fw[n][3], acc1);
    }
    const int rec = (w < 2 ? WS_QP + (h * 5 + ur) * 1536 : WS_GU + ((xq * 5 + ur) * 2 + up) * 1536) + uc * 256;
    __builtin_amdgcn_raw_buffer_store_b128(proj_epilogue(acc0, acc1, tag), rws, ((lane >> 4) & 1) * 768 + st_col, rec, AUX_SC1);
    asm volatile("s_waitcnt vmcnt(0)" ::: "memory");
  } else {
    asm volatile("s_waitcnt vmcnt(0)" ::"v"(fw[0]), "v"(fw[1]), "v"(fw[2]), "v"(fw[3]), "v"(fw[4]), "v"(fw[5]), "v"(fw[6]), "v"(fw[7]),
                 "v"(fw[8]), "v"(fw[9]), "v"(fw[10]), "v"(fw[11]), "v"(fw[12]), "v"(fw[13]), "v"(fw[14]), "v"(fw[15]));
    TAG_READY();
  }
  const int slot = w * 8 + j8;
  const bool has_back = slot < 30;
  const int t3 = slot / 10, cc = slot - 10 * t3;
  const int rid = xq * 20 + uq;
  const int rb = rid / 80, rc = (rid - 80 * rb) >> 3, ro = rid & 7;
  const bool reducer = (w == 0) && (bid < 160);
  unsigned ha[2][2] = {{0u, 0u}, {0u, 0u}};
  unsigned tend = tstart + T_PLAN;
  auto pace_round = [&](unsigned t_sample, unsigned t_read, int so, int dead, int gain16, int keep16) {
    wait_until(t_sample);
    u32x4 e, f;
    do { MEMFENCE(); e = *(const u32x4*)l_est; f = *(const u32x4*)(l_est + 4); } while (!__builtin_amdgcn_readfirstlane(e[0] & e[2] & f[0] & f[2] & 1u));
    const u32x4 pv = {tstart + (unsigned)((int)((e[0] - tstart) + (e[2] - tstart) + (f[0] - tstart) + (f[2] - tstart)) >> 2), tag, e[1] + e[3] + f[1] + f[3], tag};
    __builtin_amdgcn_raw_buffer_store_b128(pv, rws, (lane == 0 && uq % 9 == 0) ? (uq / 9 * 8 + xq) * 16 : OOB, so, AUX_SC1);
    wait_until(t_read);
    MEMFENCE();
    const u32x4 tv = ld_sc1(rws, (lane & 31) * 16, so);
    if (__all((tv[1] == tag) & (tv[3] == tag))) {
      const float d = x16_sum(row_sum((float)(int)(tv[0] - tstart))) * (1.f / 32.f);
      const float dr = x16_sum(row_sum((float)(int)tv[2])) * (1.f / 128.f) - (float)LAT0;
      int di = (int)(d + fmaxf(dr, 0.f) + fmaxf(dr - (float)dead, 0.f) * (float)gain16 * (1.f / 16.f));
      const int dold = (int)(tend - tstart);
      if (keep16 && di < dold) di += ((dold - di) * keep16) >> 4;
      di = di < 0 ? 0 : (di > 2500 ? 2500 : di);
      tend = (tstart + __builtin_amdgcn_readfirstlane(di)) | 1u;
      *l_tend = tend;
    }
  };
  if (w < 3) {
    if (has_front) __builtin_amdgcn_s_sleep(16); else wait_until(tstart + 360);
    u32x2 t[3];
    const int vo = (lane >> 5) * 1536 + (lane & 31) * 8, so = WS_QP + h * 7680 + b * 768 + w * 256;
    for (unsigned spin = 0;; ++spin) {
      MEMFENCE();
      t[0] = ld2_sc1(rws, vo, so); t[1] = ld2_sc1(rws, vo, so + 3072); t[2] = ld2_sc1(rws, lane < 32 ? vo : OOB, so + 6144);
      const bool ok = (t[0][1] == tag) & (t[1][1] == tag) & ((t[2][1] == tag) | (lane >= 32));
      if (__all(ok) || spin > SPIN_CAP) break;
    }
    const float a = x32_sum((bflo(t[0][0]) + bflo(t[1][0])) + bflo(t[2][0]));
    const float c = x32_sum((bfhi(t[0][0]) + bfhi(t[1][0])) + bfhi(t[2][0]));
    if (lane < 32) { l_q[32 * w + ((lane >> 1) & 7) * 4 + (lane >> 4) * 2 + (lane & 1)] = cvtpk(a, c); MEMFENCE(); l_f[FG + w] = 1; }
  } else {
    l_f[FG + 3] = 1;
    pace_round(tstart + SMP0, tstart + SMP0 + WAIT0, WS_TS, DEAD0, GAIN0, 0);
  }
  if (has_back) {
    u32x2 t[3];
    const int f16 = (h & 3) * 3 + t3;
    const int so = WS_GU + xq * 15360 + (f16 >> 2) * 256 + (f16 & 3) * 32;
    const int vo = bl * 768 + ((g >> 1) & 1) * 128 + (g >> 2) * 16 + (g & 1) * 8;
    for (unsigned spin = 0;; ++spin) {
      MEMFENCE();
      t[0] = ld2_sc1(rws, vo + e4 * 3072, so);
      t[1] = ld2_sc1(rws, vo + e4 * 3072 + 1536, so);
      t[2] = ld2_sc1(rws, vo + 4 * 3072 + (e4 & 1) * 1536, so);
      if (__all((t[0][1] == tag) & (t[1][1] == tag) & (t[2][1] == tag)) || spin > SPIN_CAP) break;
    }
#pragma unroll
    for (int z = 0; z < 2; ++z) {
      const float ex = z ? bfhi(t[2][0]) : bflo(t[2][0]);
      const float gg = rbf(quad_sum((z ? bfhi(t[0][0]) : bflo(t[0][0])) + (e4 == 0 ? ex : 0.f)));
      const float uu = rbf(quad_sum((z ? bfhi(t[1][0]) : bflo(t[1][0])) + (e4 == 1 ? ex : 0.f)));
      const float zz = 0.7978845608028654f * (gg + 0.044715f * gg * gg * gg);
      const float ge = rbf(gg * __builtin_amdgcn_rcpf(1.f + __builtin_amdgcn_exp2f(-2.8853900817779268f * zz)));
      const float hv = ge * uu;
      const unsigned d = cvtpk(hv, hv);
      ha[z][0] = d & m0; ha[z][1] = d & m1;
    }
  }
  const int oc = 16 * (ro & 3) + 8 * (lane & 1) + 4 * (ro >> 2) + 2 * (lane >> 5) + ((lane >> 4) & 1);
  const rsrc_t rout = mk_rsrc(out, 2560);
  int out_vo = (lane & 14) == 0 ? oc * 2 + rb * 1280 + rc * 128 : OOB;
  float xres = 0.f;
  if (reducer) {
    xres = bflo(__builtin_amdgcn_raw_buffer_load_b16(rx, oc * 2, rb * 1280 + rc * 128, 0));
    __builtin_amdgcn_raw_buffer_store_b16((unsigned short)0, rout, out_vo, 0, AUX_SC1);
  }
  if (w == 1) {
    wait_lds<4>(fbase + FA, ALL4);
    const int so = WS_ML + (h * 2 + b) * 256;
    if (lane < 4) { const f32x2 ml = *(const f32x2*)(l_ml + 2 * lane); st_pair(rws, lane * 16, so + s * 64, ml[0], ml[1], tag); }
    asm volatile("s_waitcnt vmcnt(0)" ::: "memory");
    __builtin_amdgcn_s_sleep(32);
    u32x4 t;
    for (unsigned spin = 0;; ++spin) {
      MEMFENCE();
      t = ld_sc1(rws, (lane & 15) * 16, so);
      if (__all((t[1] == tag) & (t[3] == tag)) || spin > SPIN_CAP) break;
    }
    const float mv = __uint_as_float(t[0]);
    const float M = row_max(mv);
    const float f = __builtin_amdgcn_exp2f(mv - M);
    const float L = row_sum(f * __uint_as_float(t[2]));
    if ((lane >> 2) == s) { l_c[lane & 3] = f / L; MEMFENCE(); l_f[FE + (lane & 3)] = 1; }
  }
  if (w == 3) {
    pace_round(tend - SMP1, tend - SMP1 + WAIT1, WS_TS + 512, DEAD1, GAIN1, KEEP1);
  }
  if (has_back) {
    const unsigned sLo = (e4 & 1) ? 0x01000c0cu : 0x0c0c0100u, sHi = (e4 & 1) ? 0x03020c0cu : 0x0c0c0302u;
    unsigned sel0 = e4 < 2 ? sLo : 0x0c0c0c0cu, sel1 = e4 < 2 ? 0x0c0c0c0cu : sLo, sel2 = e4 < 2 ? sHi : 0x0c0c0c0cu, sel3 = e4 < 2 ? 0x0c0c0c0cu : sHi;
    int op_so = WS_OP + h * OP_STRIDE + t3 * 256, op_vo = (bl * 4 + e4) * 768 + 16 * g;
    int d_vo = ((48 * h + 16 * t3 + 2 * g) * 640 + 64 * cc) * 2 + 16 * j, o_vo = ((192 * h + 64 * t3 + 8 * g) * 640 + 64 * cc) * 2 + 16 * j;
    int yvo = ((lane >> 4) & 1) * 256 + st_col, yso = WS_YP + ((h * 3 + t3) * 10 + cc) * 512;
    int r_vo = (lane >> 1) * 5120 + (lane & 1) * 16 + WS_YP + rc * 512 + rb * 256 + ro * 32;
    unsigned tagv = tag, fd = fbase + FD;
    asm volatile("" : "+v"(sel0), "+v"(sel1), "+v"(sel2), "+v"(sel3), "+s"(op_so), "+v"(op_vo), "+v"(d_vo), "+v"(o_vo), "+v"(yvo), "+s"(yso), "+v"(r_vo), "+v"(out_vo), "+v"(tagv), "+v"(xres), "+v"(fd));
    const rsrc_t rd = mk_rsrc(wd, 1536 * 1280), ro_ = mk_rsrc(wo, 6144 * 1280);
    wait_lds<1>(fbase + FB, ALL4);
    {
      unsigned tf = *l_tend;
      asm volatile("" : "+v"(tf));
      tf = __builtin_amdgcn_readfirstlane(tf);
      const unsigned tn = (unsigned)__builtin_amdgcn_s_memrealtime();
      int wt = tf ? (int)(tf + BACK_AT - tn) : 0;
      asm("s_max_i32 %0, %0, %1\n\ts_min_i32 %0, %0, 0xc8" : "+s"(wt) : "s"(BACK_DELAY) : "scc");
      wait_until(tn + wt);
    }
    u32x4 wb[10], t0, t1;
    unsigned fdv = 0;
#pragma unroll
    for (int n = 0; n < 10; ++n) {
      if (n == POLLPOS) {
        const rsrc_t rp = mk_rsrc(ws, __builtin_amdgcn_readfirstlane(fdv) == ALL4 ? WS_END : 0);
        t0 = ld_sc1(rp, op_vo, op_so); t1 = ld_sc1(rp, op_vo + 128, op_so);
        SB();
      }
      if (n < 2) wb[8 + n] = ld_nt(rd, d_vo + n * 1280, 0); else wb[n - 2] = ld_nt(ro_, o_vo, (n - 2) * 1280);
      if (n == POLLPOS - 1) { MEMFENCE(); fdv = *(luint*)(size_t)fd; }
      SB();
      if (n < 9) __builtin_amdgcn_s_sleep(BPACE);
      SB();
    }
    u32x4 bq[10];
#define PAIR(p) do { bq[2 * (p)] = u32x4{wb[2 * (p)][0], wb[2 * (p)][1], wb[2 * (p) + 1][0], wb[2 * (p) + 1][1]}; \
                     bq[2 * (p) + 1] = u32x4{wb[2 * (p)][2], wb[2 * (p)][3], wb[2 * (p) + 1][2], wb[2 * (p) + 1][3]}; } while (0)
    PAIR(4); PAIR(0); PAIR(1); PAIR(2);
    const u32x4 hA = {ha[0][0], ha[0][1], ha[1][0], ha[1][1]};
    f32x4 acc0 = mfma32(hA, bq[8], f32x4{0.f, 0.f, 0.f, 0.f}), acc1 = mfma32(hA, bq[9], f32x4{0.f, 0.f, 0.f, 0.f});
    asm volatile("" : "+v"(bq[0]), "+v"(bq[1]), "+v"(bq[2]), "+v"(bq[3]), "+v"(bq[4]), "+v"(bq[5]), "+v"(acc0), "+v"(acc1));
    SB();
    unsigned np1 = 1;
    if (__builtin_expect(__builtin_amdgcn_ballot_w64((((t0[1] ^ tagv) | (t0[3] ^ tagv)) | ((t1[1] ^ tagv) | (t1[3] ^ tagv))) != 0u) != 0, 0)) {
      do {
        ++np1;
        MEMFENCE();
        if (__builtin_amdgcn_readfirstlane(*(luint*)(size_t)fd) != ALL4) { wait_lds<1>(fd, ALL4); delay_ticks(LATE_DELAY); }
        t0 = ld_sc1(rws, op_vo, op_so); t1 = ld_sc1(rws, op_vo + 128, op_so);
      } while (!__all((t0[1] == tag) & (t0[3] == tag) & (t1[1] == tag) & (t1[3] == tag)) && np1 < SPIN_CAP);
    }
    unsigned pk[4];
    {
      float u0, u1, u2, u3, u4, u5, u6, u7;
#define DPPADD(r, P) "v_add_f32_dpp " r ", " r ", " r " quad_perm:" P " row_mask:0xf bank_mask:0xf\n\t"
#define DPP8(P) DPPADD("%4", P) DPPADD("%5", P) DPPADD("%6", P) DPPADD("%7", P) DPPADD("%8", P) DPPADD("%9", P) DPPADD("%10", P) DPPADD("%11", P)
      asm volatile("v_lshlrev_b32 %4, 16, %12\n\tv_and_b32 %5, 0xffff0000, %12\n\tv_lshlrev_b32 %6, 16, %13\n\tv_and_b32 %7, 0xffff0000, %13\n\t"
                   "v_lshlrev_b32 %8, 16, %14\n\tv_and_b32 %9, 0xffff0000, %14\n\tv_lshlrev_b32 %10, 16, %15\n\tv_and_b32 %11, 0xffff0000, %15\n\t"
                   DPP8("[1,0,3,2]") DPP8("[2,3,0,1]")
                   "v_cvt_pk_bf16_f32 %0, %4, %5\n\tv_cvt_pk_bf16_f32 %1, %6, %7\n\tv_cvt_pk_bf16_f32 %2, %8, %9\n\tv_cvt_pk_bf16_f32 %3, %10, %11"
                   : "=&v"(pk[0]), "=&v"(pk[1]), "=&v"(pk[2]), "=&v"(pk[3]), "=&v"(u0), "=&v"(u1), "=&v"(u2), "=&v"(u3), "=&v"(u4), "=&v"(u5), "=&v"(u6), "=&v"(u7)
                   : "v"(t0[0]), "v"(t0[2]), "v"(t1[0]), "v"(t1[2]));
    }
    u32x4 aq[4];
#pragma unroll
    for (int p = 0; p < 4; ++p)
      aq[p] = u32x4{__builtin_amdgcn_perm(0u, pk[p], sel0), __builtin_amdgcn_perm(0u, pk[p], sel1), __builtin_amdgcn_perm(0u, pk[p], sel2), __builtin_amdgcn_perm(0u, pk[p], sel3)};
#pragma unroll
    for (int p = 0; p < 3; ++p) { acc0 = mfma32(aq[p], bq[2 * p], acc0); acc1 = mfma32(aq[p], bq[2 * p + 1], acc1); }
    asm volatile("" : "+v"(aq[3]), "+v"(acc0), "+v"(acc1));
    SB();
    acc0 = mfma16(aq[3][0], aq[3][1], wb[6][0], wb[6][1], acc0); acc1 = mfma16(aq[3][0], aq[3][1], wb[6][2], wb[6][3], acc1);
    acc0 = mfma16(aq[3][2], aq[3][3], wb[7][0], wb[7][1], acc0); acc1 = mfma16(aq[3][2], aq[3][3], wb[7][2], wb[7][3], acc1);
    __builtin_amdgcn_raw_buffer_store_b128(proj_epilogue(acc0, acc1, tagv), rws, yvo, yso, AUX_SC1);
    if (reducer) {
      delay_ticks(FIN_DELAY);
      float y0, y1, y2, y3;
      unsigned np2 = 0;
      for (;;) {
        MEMFENCE();
        const u32x4 a = ld_sc1(rws, r_vo, 0), c = ld_sc1(rws, r_vo, 32 * 5120), d = ld_sc1(rws, r_vo, 64 * 5120);
        unsigned bad = ((a[1] ^ tagv) | (a[3] ^ tagv)) | ((c[1] ^ tagv) | (c[3] ^ tagv));
        y0 = bflo(a[0]) + bflo(c[0]); y1 = bfhi(a[0]) + bfhi(c[0]); y2 = bflo(a[2]) + bflo(c[2]); y3 = bfhi(a[2]) + bfhi(c[2]);
        asm volatile("" : "+v"(bad), "+v"(y0), "+v"(y1), "+v"(y2), "+v"(y3));
        bad |= (d[1] ^ tagv) | (d[3] ^ tagv);
        y0 += bflo(d[0]); y1 += bfhi(d[0]); y2 += bflo(d[2]); y3 += bfhi(d[2]);
        if (__builtin_expect(__builtin_amdgcn_ballot_w64(bad != 0u) == 0, 1)) break;
        if (++np2 > SPIN_CAP) break;
      }
      const auto s0 = __builtin_amdgcn_permlane32_swap(__float_as_uint(y0), __float_as_uint(y2), false, false);
      const auto s1 = __builtin_amdgcn_permlane32_swap(__float_as_uint(y1), __float_as_uint(y3), false, false);
      const float z0 = __uint_as_float(s0[0]) + __uint_as_float(s0[1]), z1 = __uint_as_float(s1[0]) + __uint_as_float(s1[1]);
      const auto s2 = __builtin_amdgcn_permlane16_swap(__float_as_uint(z0), __float_as_uint(z1), false, false);
      float y = __uint_as_float(s2[0]) + __uint_as_float(s2[1]);
      y += dpp<ROR8>(y); y += dpp<ROR4>(y); y += dpp<XOR2>(y);
      __builtin_amdgcn_raw_buffer_store_b16((unsigned short)cvtpk(xres + y, 0.f), rout, out_vo, 0, AUX_SC1);
      if (bid == 0) __builtin_amdgcn_raw_buffer_store_b32(tagv, rws, lane == 0 ? WS_EPOCH : OOB, 0, AUX_SC1);
    }
  }
}

extern "C" void kernel_launch(void* const* d_in, const int* in_sizes, int n_in, void* d_out, int out_size, void* d_ws, size_t ws_size,
                              hipStream_t stream) {
  fused_decode<<<256, 512, 0, stream>>>((const u16*)d_in[0], (const u16*)d_in[1], (const u16*)d_in[2], (const u16*)d_in[3], (const u16*)d_in[4],
                                        (const u16*)d_in[6], (const u16*)d_in[7], (const u16*)d_in[8], (u16*)d_out, (unsigned char*)d_ws);
}
```

```cpp
#define AMP 1
#include <hip/hip_runtime.h>
#include <hip/hip_bf16.h>
#include <stdint.h>
#include <type_traits>

typedef unsigned short u16;
typedef unsigned u32x4 __attribute__((ext_vector_type(4)));
typedef unsigned u32x2 __attribute__((ext_vector_type(2)));
typedef float f32x4 __attribute__((ext_vector_type(4)));
typedef float f32x2 __attribute__((ext_vector_type(2)));
typedef short s16x4 __attribute__((ext_vector_type(4)));
typedef __bf16 bf16x8 __attribute__((ext_vector_type(8)));
typedef __bf16 bf16x2 __attribute__((ext_vector_type(2)));
typedef __amdgpu_buffer_rsrc_t rsrc_t;

constexpr int QK = 6, CU = 6;
constexpr int STAG = 10;
constexpr int BPACE = 5;
constexpr int POLLPOS = 8;
constexpr int KEEP1 = 10;
constexpr int S_DELAY = 152;
constexpr int T_PLAN = 1392;
constexpr int RATE16 = 215;
constexpr int LAT0 = 73;
constexpr int SMP0 = 500, WAIT0 = 220, DEAD0 = 20, GAIN0 = 48;
constexpr int SMP1 = 385, WAIT1 = 130, DEAD1 = 13, GAIN1 = 52;
constexpr int BACK_AT = 66;
constexpr int BACK_DELAY = 12;
constexpr int LATE_DELAY = 20;
constexpr int FIN_DELAY = 48;
constexpr int CODE_BYTES = 16000;

constexpr int AUX_NT = 2, AUX_SC1 = 16;
constexpr int RSRC_W3 = 0x00020000;
constexpr unsigned SPIN_CAP = 1u << 18;
constexpr int OOB = 0x40000000;
constexpr unsigned ALL4 = 0x01010101u;

constexpr int WS_EPOCH = 0;
constexpr int WS_TS = 1024;
constexpr int WS_QP = 4096;
constexpr int WS_GU = WS_QP + 32 * 5 * 2 * 96 * 8;
constexpr int WS_ML = WS_GU + 8 * 5 * 2 * 2 * 96 * 8;
constexpr int WS_YP = WS_ML + 32 * 2 * 4 * 4 * 16;
constexpr int WS_OP = 2 << 20, OP_STRIDE = 1 << 20;
constexpr int WS_END = WS_OP + 32 * OP_STRIDE;
static_assert(WS_YP + 96 * 10 * 2 * 32 * 8 <= WS_OP, "workspace map");

template <int I, int N, class F> __device__ __forceinline__ void static_for(F&& f) {
  if constexpr (I < N) { f(std::integral_constant<int, I>{}); static_for<I + 1, N>(f); }
}
__device__ __forceinline__ rsrc_t mk_rsrc(const void* p, int bytes) {
  return __builtin_amdgcn_make_buffer_rsrc(const_cast<void*>(p), 0, bytes, RSRC_W3);
}
__device__ __forceinline__ u32x4 ld_nt(rsrc_t r, int voff, int soff) { return __builtin_amdgcn_raw_buffer_load_b128(r, voff, soff, AUX_NT); }
__device__ __forceinline__ u32x4 ld_sc1(rsrc_t r, int voff, int soff) { return __builtin_amdgcn_raw_buffer_load_b128(r, voff, soff, AUX_SC1); }
__device__ __forceinline__ u32x2 ld2_sc1(rsrc_t r, int voff, int soff) { return __builtin_amdgcn_raw_buffer_load_b64(r, voff, soff, AUX_SC1); }
__device__ __forceinline__ void st_pair(rsrc_t r, int voff, int soff, float a, float b, unsigned tag) {
  u32x4 v = {__float_as_uint(a), tag, __float_as_uint(b), tag};
  __builtin_amdgcn_raw_buffer_store_b128(v, r, voff, soff, AUX_SC1);
}
__device__ __forceinline__ float bflo(unsigned v) { return __uint_as_float(v << 16); }
__device__ __forceinline__ float bfhi(unsigned v) { return __uint_as_float(v & 0xffff0000u); }
__device__ __forceinline__ unsigned cvtpk(float lo, float hi) {
  bf16x2 r = {(__bf16)lo, (__bf16)hi};
  return __builtin_bit_cast(unsigned, r);
}
__device__ __forceinline__ float rbf(float x) { return (float)(__bf16)x; }
template <int CTRL> __device__ __forceinline__ float dpp(float x) {
  return __builtin_bit_cast(float, __builtin_amdgcn_mov_dpp(__builtin_bit_cast(int, x), CTRL, 0xf, 0xf, true));
}
constexpr int XOR1 = 0xB1, XOR2 = 0x4E, HMIR = 0x141, ROR8 = 0x128, ROR4 = 0x124;
__device__ __forceinline__ float x16_sum(float x) { auto s = __builtin_amdgcn_permlane16_swap(__float_as_uint(x), __float_as_uint(x), false, false); return __uint_as_float(s[0]) + __uint_as_float(s[1]); }
__device__ __forceinline__ float x32_sum(float x) { auto s = __builtin_amdgcn_permlane32_swap(__float_as_uint(x), __float_as_uint(x), false, false); return __uint_as_float(s[0]) + __uint_as_float(s[1]); }
__device__ __forceinline__ float x16_max(float x) { auto s = __builtin_amdgcn_permlane16_swap(__float_as_uint(x), __float_as_uint(x), false, false); return fmaxf(__uint_as_float(s[0]), __uint_as_float(s[1])); }
__device__ __forceinline__ float x32_max(float x) { auto s = __builtin_amdgcn_permlane32_swap(__float_as_uint(x), __float_as_uint(x), false, false); return fmaxf(__uint_as_float(s[0]), __uint_as_float(s[1])); }
__device__ __forceinline__ float row_sum(float x) { x += dpp<XOR1>(x); x += dpp<XOR2>(x); x += dpp<HMIR>(x); x += dpp<ROR8>(x); return x; }
__device__ __forceinline__ float row_max(float x) { x = fmaxf(x, dpp<XOR1>(x)); x = fmaxf(x, dpp<XOR2>(x)); x = fmaxf(x, dpp<HMIR>(x)); x = fmaxf(x, dpp<ROR8>(x)); return x; }
__device__ __forceinline__ float quad_sum(float x) { x += dpp<XOR1>(x); x += dpp<XOR2>(x); return x; }
__device__ __forceinline__ float dot2(unsigned a, unsigned b, float c) {
  return __builtin_amdgcn_fdot2_f32_bf16(__builtin_bit_cast(bf16x2, a), __builtin_bit_cast(bf16x2, b), c, false);
}
__device__ __forceinline__ f32x4 mfma16(unsigned a0, unsigned a1, unsigned b0, unsigned b1, f32x4 c) {
  u32x2 a = {a0, a1}, b = {b0, b1};
  return __builtin_amdgcn_mfma_f32_16x16x16bf16_1k(__builtin_bit_cast(s16x4, a), __builtin_bit_cast(s16x4, b), c, 0, 0, 0);
}
__device__ __forceinline__ f32x4 mfma32(u32x4 a, u32x4 b, f32x4 c) {
  return __builtin_amdgcn_mfma_f32_16x16x32_bf16(__builtin_bit_cast(bf16x8, a), __builtin_bit_cast(bf16x8, b), c, 0, 0, 0);
}
template <int RECIP> __device__ __forceinline__ unsigned pace_step(unsigned tprev, unsigned tend, unsigned& tdone) {
  unsigned d, now, tgt;
  asm volatile("s_sub_i32 %0, %5, %4\n\ts_max_i32 %0, %0, 0\n\ts_min_i32 %0, %0, 0x7d0\n\ts_mul_i32 %0, %0, %6\n\ts_lshr_b32 %0, %0, 16\n\ts_add_u32 %2, %4, %0\n"
               "1:\n\ts_memrealtime vcc\n\ts_getreg_b32 %0, hwreg(HW_REG_IB_STS, 0, 4)\n\ts_waitcnt lgkmcnt(0)\n\ts_cmp_lg_u32 %0, 0\n\ts_cselect_b32 %3, vcc_lo, %3\n\t"
               "s_sub_u32 %0, vcc_lo, %2\n\ts_cmp_lt_i32 %0, 0\n\ts_cbranch_scc1 1b\n\ts_mov_b32 %1, vcc_lo"
               : "=&s"(d), "=&s"(now), "=&s"(tgt), "+s"(tdone) : "s"(tprev), "s"(tend), "i"(RECIP) : "vcc", "scc");
  return now;
}
__device__ __forceinline__ void wait_until(unsigned tgt) {
  asm volatile("1:\n\ts_memrealtime vcc\n\ts_waitcnt lgkmcnt(0)\n\ts_sub_u32 vcc_lo, vcc_lo, %0\n\ts_cmp_lt_i32 vcc_lo, 0\n\ts_cbranch_scc1 1b" :: "s"(tgt) : "vcc", "scc", "memory");
}
#define SB() __builtin_amdgcn_sched_barrier(0)
#define MEMFENCE() asm volatile("" ::: "memory")

__device__ __forceinline__ void delay_ticks(int n) {
  if (n > 0) wait_until((unsigned)__builtin_amdgcn_s_memrealtime() + n);
}

typedef __attribute__((address_space(3))) float lfloat;
typedef __attribute__((address_space(3))) f32x4 lf32x4;
typedef __attribute__((address_space(3))) unsigned luint;
typedef __attribute__((address_space(3))) unsigned char lbyte;
template <int SLP> __device__ __forceinline__ void wait_lds(unsigned a, unsigned want) {
  unsigned v, sv;
  asm volatile("s_branch 2f\n1:\n\ts_sleep %4\n2:\n\tds_read_b32 %0, %2\n\ts_waitcnt lgkmcnt(0)\n\tv_readfirstlane_b32 %1, %0\n\ts_cmp_lg_u32 %1, %3\n\ts_cbranch_scc1 1b"
               : "=&v"(v), "=&s"(sv) : "v"(a), "s"(want), "i"(SLP) : "scc", "memory");
}

__device__ __forceinline__ u32x4 proj_epilogue(f32x4 acc0, f32x4 acc1, unsigned tag) {
  float r[4];
#pragma unroll
  for (int i = 0; i < 4; ++i) {
    auto s = __builtin_amdgcn_permlane32_swap(__float_as_uint(acc0[i]), __float_as_uint(acc1[i]), false, false);
    r[i] = __uint_as_float(s[0]) + dpp<ROR8>(__uint_as_float(s[1]));
  }
  return u32x4{cvtpk(r[0], r[1]), tag, cvtpk(r[2], r[3]), tag};
}

__global__ void __launch_bounds__(512, 2)
fused_decode(const u16* x_, const u16* kc_, const u16* vc_, const u16* wq_, const u16* wo_, const u16* wg_, const u16* wu_, const u16* wd_, u16* out_, unsigned char* ws_) {
  __shared__ __attribute__((aligned(16))) float lds[96 + 8 + 4 + 16 + 8 * 192];
  unsigned* l_q = (unsigned*)lds;
  float* l_ml = lds + 96;
  float* l_c = lds + 104;
  unsigned* l_est = (unsigned*)(lds + 108);
  unsigned char* l_f = (unsigned char*)(lds + 116);
  constexpr int FA = 0, FB = 4, FC = 8, FD = 12, FE = 16, FG = 20;
  unsigned* l_tend = (unsigned*)(lds + 122);
  const unsigned fbase = (unsigned)(size_t)(lbyte*)l_f;
  float* l_acc = lds + 124;

  unsigned amp0;
  { unsigned long long t_; asm volatile("s_memrealtime %0\n\ts_waitcnt lgkmcnt(0)" : "=s"(t_) :: "memory"); amp0 = (unsigned)t_; }
  typedef unsigned long long u64;
  typedef u64 u64x8 __attribute__((ext_vector_type(8)));
  typedef u64 u64x2 __attribute__((ext_vector_type(2)));
  u64x8 ka; u64x2 kb; u64 pc;
  u32x4 fw[16];
  int bid = blockIdx.x, tid = threadIdx.x;
  {
    unsigned v1, v2;
    u64x8 ka0; u64x2 kb0;
    asm volatile("s_load_dwordx16 %0, %3, 0x0\n\ts_load_dwordx4 %1, %3, 0x40\n\ts_getpc_b64 %2" : "=&{s[36:51]}"(ka0), "=&{s[52:55]}"(kb0), "=&s"(pc) : "s"(__builtin_amdgcn_kernarg_segment_ptr()));
    asm volatile(
               "v_readfirstlane_b32 s14, %20\n\t"
               "s_lshr_b32 s15, %19, 3\n\t"
               "s_cmpk_ge_u32 s14, 0xc0\n\ts_cbranch_scc1 1f\n\t"
               "s_setprio 2\n\t"
               "s_and_b32 s16, %19, 7\n\t"
               "s_lshr_b32 s17, %19, 6\n\ts_lshl2_add_u32 s17, s16, s17\n\t"
               "s_and_b32 s18, s15, 7\n\ts_lshr_b32 s19, s14, 6\n\ts_lshl1_add_u32 s18, s18, s19\n\t"
               "s_lshr_b32 s19, %19, 4\n\t"
               "v_bfe_u32 %16, %20, 3, 3\n\tv_and_b32 %17, 7, %20\n\t"
               "s_movk_i32 s13, 0x3000\n\t"
               "s_cmpk_lt_u32 s14, 0x80\n\t"
               "s_cselect_b32 s18, s18, s19\n\ts_cselect_b32 s17, s17, s16\n\ts_cselect_b32 s13, s13, 0xc00\n\t"
               "s_mul_i32 s19, s18, 11\n\ts_lshr_b32 s19, s19, 5\n\t"
               "v_mad_u32_u24 %16, %16, s13, %17\n\t"
               "s_sub_u32 s17, s17, s19\n\ts_mul_i32 s17, s17, 3\n\ts_add_u32 s17, s17, s18\n\t"
               "s_mul_i32 s19, s19, s13\n\ts_add_u32 s12, s17, s19\n\ts_lshl_b32 s12, s12, 7\n\t"
               "v_lshlrev_b32 %16, 4, %16\n\t"
               "s_mul_i32 s10, s13, 0x280\n\ts_mov_b32 s11, 0x20000\n\ts_cmp_eq_u32 s18, 15\n\ts_cselect_b32 s10, 0, s10\n\t"
               "s_bitcmp1_b32 s15, 0\n\t"
               "s_waitcnt lgkmcnt(0)\n\t"
               "s_cselect_b64 s[8:9], s[48:49], s[46:47]\n\t"
               "s_cmpk_lt_u32 s14, 0x80\n\ts_cselect_b64 s[8:9], s[42:43], s[8:9]\n\t"
               "s_and_b32 s9, s9, 0xffff\n\t"
               "buffer_load_dwordx4 %0, %16, s[8:11], s12 offen nt\n\t" "s_add_u32 s12, s12, s13\n\t"
               "buffer_load_dwordx4 %1, %16, s[8:11], s12 offen nt\n\t" "s_add_u32 s12, s12, s13\n\t"
               "buffer_load_dwordx4 %2, %16, s[8:11], s12 offen nt\n\t" "s_add_u32 s12, s12, s13\n\t"
               "buffer_load_dwordx4 %3, %16, s[8:11], s12 offen nt\n\t" "s_add_u32 s12, s12, s13\n\t"
               "buffer_load_dwordx4 %4, %16, s[8:11], s12 offen nt\n\t" "s_add_u32 s12, s12, s13\n\t"
               "buffer_load_dwordx4 %5, %16, s[8:11], s12 offen nt\n\t" "s_add_u32 s12, s12, s13\n\t"
               "buffer_load_dwordx4 %6, %16, s[8:11], s12 offen nt\n\t" "s_add_u32 s12, s12, s13\n\t"
               "buffer_load_dwordx4 %7, %16, s[8:11], s12 offen nt\n\t" "s_add_u32 s12, s12, s13\n\t"
               "buffer_load_dwordx4 %8, %16, s[8:11], s12 offen nt\n\t" "s_add_u32 s12, s12, s13\n\t"
               "buffer_load_dwordx4 %9, %16, s[8:11], s12 offen nt\n\t" "s_add_u32 s12, s12, s13\n\t"
               "buffer_load_dwordx4 %10, %16, s[8:11], s12 offen nt\n\t" "s_add_u32 s12, s12, s13\n\t"
               "buffer_load_dwordx4 %11, %16, s[8:11], s12 offen nt\n\t" "s_add_u32 s12, s12, s13\n\t" "s_barrier\n\t"
               "buffer_load_dwordx4 %12, %16, s[8:11], s12 offen nt\n\t" "s_add_u32 s12, s12, s13\n\t"
               "buffer_load_dwordx4 %13, %16, s[8:11], s12 offen nt\n\t" "s_add_u32 s12, s12, s13\n\t"
               "buffer_load_dwordx4 %14, %16, s[8:11], s12 offen nt\n\t" "s_add_u32 s12, s12, s13\n\t"
               "buffer_load_dwordx4 %15, %16, s[8:11], s12 offen nt\n\t"
               "s_branch 2f\n"
               "1:\n\t"
               "v_and_b32 %16, 15, %20\n\tv_lshl_add_u32 %16, %16, 2, %21\n\tv_mov_b32 %17, 0\n\tds_write_b32 %16, %17\n\t"
               "s_waitcnt lgkmcnt(0)\n\ts_barrier\n"
               "2:"
               : "=&v"(fw[0]), "=&v"(fw[1]), "=&v"(fw[2]), "=&v"(fw[3]), "=&v"(fw[4]), "=&v"(fw[5]), "=&v"(fw[6]), "=&v"(fw[7]),
                 "=&v"(fw[8]), "=&v"(fw[9]), "=&v"(fw[10]), "=&v"(fw[11]), "=&v"(fw[12]), "=&v"(fw[13]), "=&v"(fw[14]), "=&v"(fw[15]),
                 "=&v"(v1), "=&v"(v2)
               : "{s[36:51]}"(ka0), "s"(bid), "v"(tid), "s"(fbase - 32), "{s[52:55]}"(kb0)
               : "s8", "s9", "s10", "s11", "s12", "s13", "s14", "s15", "s16", "s17", "s18", "s19", "scc", "memory");
    asm volatile("" : "={s[36:51]}"(ka), "={s[52:55]}"(kb), "+s"(bid), "+s"(pc) : "0"(ka0), "1"(kb0));
    asm volatile("" : "+v"(tid));
  }
  const int xq = bid & 7, uq = bid >> 3;
  const int h = xq * 4 + (uq >> 3), j8 = uq & 7, b = j8 >> 2, s = j8 & 3;
  const int lane = tid & 63, wave = __builtin_amdgcn_readfirstlane(tid >> 6);
  const int g = lane >> 3, j = lane & 7;
  const int bl = (lane >> 2) & 1, e4 = lane & 3;
  const bool isS = wave >= 4;
  const u16 *x = (const u16*)ka[0], *kc = (const u16*)ka[1], *vc = (const u16*)ka[2], *wo = (const u16*)ka[4], *wd = (const u16*)ka[7];
  u16* out = (u16*)kb[0];
  unsigned char* ws = (unsigned char*)kb[1];
  u64 tst;
  asm volatile("s_memrealtime %0\n\ts_waitcnt lgkmcnt(0)" : "=s"(tst));
  const unsigned tstart = (unsigned)tst;
  unsigned tag, ep;
  unsigned cp0, cp1;
  {
    unsigned pco = (((wave + 5) & 7) * 32 + uq) * 128;
    pco = pco < CODE_BYTES ? pco : 0;
    asm volatile("s_load_dword %0, %3, 0x0\n\ts_load_dword %1, %4, %5\n\ts_load_dword %2, %4, %6" : "=&s"(ep), "=&s"(cp0), "=&s"(cp1) : "s"(ws), "s"(pc & ~255ull), "s"(pco), "s"(pco + 64));
  }
#define TAG_READY() asm volatile("s_waitcnt lgkmcnt(0)\n\ts_add_u32 %0, %1, 1\n\ts_addc_u32 %0, %0, 0" : "=&s"(tag) : "s"(ep), "s"(cp0), "s"(cp1) : "scc", "memory")
  const rsrc_t rws = mk_rsrc(ws, WS_END);

  if (isS) {
    const int sw = wave - 4;
    const rsrc_t r0 = mk_rsrc(kc, 2 * 32 * 8192 * 384), rv = mk_rsrc(vc, 2 * 32 * 8192 * 384);
    const int voff = ((b * 32 + h) * 8192 + 6144 + 32 * s + 8 * sw + g) * 384 + 16 * j;
    u32x4 buf[96];
    u32x4 qv[3];
    float sc[16];
    unsigned pd[16];
    f32x4 acc[3] = {{0.f, 0.f, 0.f, 0.f}, {0.f, 0.f, 0.f, 0.f}, {0.f, 0.f, 0.f, 0.f}};
    unsigned msk[4];
#pragma unroll
    for (int d = 0; d < 4; ++d) msk[d] = (j >> 1) == d ? ((j & 1) ? 0xffff0000u : 0x0000ffffu) : 0u;
    constexpr float C2 = 0.07216878364870322f * 1.4426950408889634f;
    const int stag = ((2 * (uq >> 2) - 7) * STAG) >> 1;
    unsigned tprev = tstart + S_DELAY + stag, tend = tstart + T_PLAN + stag;

    auto consume = [&](auto G) {
      constexpr int t = decltype(G)::value;
      if constexpr (t < 16) {
        float a0 = 0.f, a1 = 0.f, a2 = 0.f;
#pragma unroll
        for (int e = 0; e < 4; ++e) { a0 = dot2(buf[3 * t][e], qv[0][e], a0); a1 = dot2(buf[3 * t + 1][e], qv[1][e], a1); a2 = dot2(buf[3 * t + 2][e], qv[2][e], a2); }
        float a = (a0 + a1) + a2;
        a += dpp<XOR1>(a); a += dpp<XOR2>(a); a += dpp<HMIR>(a);
        sc[t] = a;
        if constexpr (t == 15) {
          float m = sc[0];
#pragma unroll
          for (int u = 1; u < 16; ++u) m = fmaxf(m, sc[u]);
          m = fmaxf(m, dpp<ROR8>(m)); m = x16_max(m); m = x32_max(m);
          const float mc = m * C2;
          float l = 0.f;
#pragma unroll
          for (int u = 0; u < 16; ++u) { const float p = __builtin_amdgcn_exp2f(fmaf(sc[u], C2, -mc)); pd[u] = cvtpk(p, p); l += bfhi(pd[u]); }
          l += dpp<ROR8>(l); l = x16_sum(l); l = x32_sum(l);
          if (lane == 0) { *(f32x2*)(l_ml + 2 * sw) = f32x2{mc, l}; MEMFENCE(); l_f[FA + sw] = 1; }
        }
      } else {
        constexpr int u = t - 16;
        const u32x4 a = {pd[u] & msk[0], pd[u] & msk[1], pd[u] & msk[2], pd[u] & msk[3]};
#pragma unroll
        for (int i = 0; i < 3; ++i) acc[i] = mfma32(a, buf[3 * t + i], acc[i]);
      }
    };
    static_for<0, 16>([&](auto K) {
      constexpr int k = decltype(K)::value;
      if constexpr (k > 6) {
        MEMFENCE();
        unsigned tv = *l_tend;
        asm volatile("" : "+v"(tv));
        tv = __builtin_amdgcn_readfirstlane(tv);
        tend = tv ? tv + stag : tend;
      }
      unsigned tdone = tprev + LAT0;
      const unsigned now = pace_step<(k ? 65536 / (16 - k) : 0)>(tprev, tend, tdone);
      SB();
#pragma unroll
      for (int i = 0; i < 6; ++i) { buf[6 * k + i] = ld_nt(k < 8 ? r0 : rv, voff + (i % 3) * 128, ((2 * k + i / 3) & 15) * 49152); SB(); }
      if constexpr (k == 15) { if (lane == 0) l_f[FB + sw] = 1; }
      if constexpr (k == 1) TAG_READY();
      if constexpr (k >= 1 && k < 15) asm volatile("s_waitcnt vmcnt(6)" ::: "memory");
      if constexpr (k >= 2 && k < 13) {
        if (tdone == now) tdone = (unsigned)__builtin_amdgcn_s_memrealtime();
        *(u32x2*)(l_est + 2 * sw) = u32x2{(now - stag + ((6 * (15 - k) * RATE16) >> 4)) | 1u, tdone - tprev};
      }
      tprev = now;
      if constexpr (k == QK) {
        wait_lds<0>(fbase + FG, ALL4);
#pragma unroll
        for (int i = 0; i < 3; ++i) qv[i] = *(const u32x4*)(l_q + 32 * i + 4 * j);
      }
      SB();
      constexpr auto upto = [](int kk) constexpr { if (kk < QK) return 0; const int c = CU * (kk - QK + 1), l = kk < 15 ? 2 * kk : 28; return c < l ? c : l; };
      static_for<upto(k - 1), upto(k)>([&](auto G) { consume(G); SB(); });
      SB();
    });
    __builtin_amdgcn_s_setprio(3);
    wait_lds<0>(fbase + FE, ALL4);
    float cq = l_c[lane >> 4];
    unsigned dst = (unsigned)(size_t)(lfloat*)(l_acc + (sw * 2 + (g & 1)) * 192 + 8 * j + 4 * ((lane >> 4) & 1));
    unsigned src = (unsigned)(size_t)(lfloat*)(l_acc + (lane >> 4) * 384 + 64 * sw + 8 * j + 4 * (g & 1));
    unsigned fla = fbase + FC + sw, one = 1u, flr = fbase + FC;
    int pvo = (lane & 16) ? OOB : (g & 1) * 128 + j * 16 + (lane >> 5) * 8;
    int pso = WS_OP + h * OP_STRIDE + (b * 4 + s) * 768 + sw * 256;
    unsigned tagv = tag;
    asm volatile("" : "+v"(cq), "+v"(dst), "+v"(src), "+v"(fla), "+v"(flr), "+v"(one), "+v"(pvo), "+s"(pso), "+v"(tagv));
    const bool wr = (lane >> 5) == (g & 1);
    SB();
    static_for<28, 32>([&](auto G) { consume(G); SB(); });
    if (wr) {
#pragma unroll
      for (int i = 0; i < 3; ++i) *(lf32x4*)(size_t)(dst + 256 * i) = acc[i];
      MEMFENCE();
      *(lbyte*)(size_t)fla = (unsigned char)one;
    }
    if (sw < 3) {
      f32x4 u0, u1;
      unsigned fv, fs;
      asm volatile("1:\n\tds_read_b32 %0, %4\n\tds_read_b128 %2, %5\n\tds_read_b128 %3, %5 offset:768\n\ts_waitcnt lgkmcnt(2)\n\tv_readfirstlane_b32 %1, %0\n\t"
                   "s_cmp_lg_u32 %1, 0x01010101\n\ts_waitcnt lgkmcnt(0)\n\ts_cbranch_scc1 1b"
                   : "=&v"(fv), "=&s"(fs), "=&v"(u0), "=&v"(u1) : "v"(flr), "v"(src) : "scc", "memory");
      float o[4];
#pragma unroll
      for (int e = 0; e < 4; ++e) o[e] = cq * (u0[e] + u1[e]);
      const auto s0 = __builtin_amdgcn_permlane32_swap(__float_as_uint(o[0]), __float_as_uint(o[2]), false, false);
      const auto s1 = __builtin_amdgcn_permlane32_swap(__float_as_uint(o[1]), __float_as_uint(o[3]), false, false);
      const float p0 = x16_sum(__uint_as_float(s0[0]) + __uint_as_float(s0[1])), p1 = x16_sum(__uint_as_float(s1[0]) + __uint_as_float(s1[1]));
      __builtin_amdgcn_raw_buffer_store_b64(u32x2{cvtpk(p0, p1), tagv}, rws, pvo, pso, AUX_SC1);
    }
    MEMFENCE();
    *(lbyte*)(size_t)(fla + (FD - FC)) = (unsigned char)one;
    return;
  }

  __builtin_amdgcn_s_setprio(2);
  const int w = wave;
  const unsigned idx = w < 2 ? 2 * j8 + w : uq >> 1, ur = (idx * 11) >> 5, uc = idx - 3 * ur, up = uq & 1;
  const bool has_front = (w < 3) & (idx < 15);
  const unsigned m0 = e4 == 0 ? 0x0000ffffu : e4 == 1 ? 0xffff0000u : 0u;
  const unsigned m1 = e4 == 2 ? 0x0000ffffu : e4 == 3 ? 0xffff0000u : 0u;
  const int st_col = (g & 1) ? OOB : (lane >> 5) * 128 + j * 16;
  const rsrc_t rx = mk_rsrc(x, 2560);
  if (has_front) {
    const int xo = (lane >> 5) * 1280 + (lane & 31) * 16;
    const bool x3 = (lane & 16) == 0;
    const u32x4 xa = __builtin_amdgcn_raw_buffer_load_b128(rx, xo, 0, 0), xb = __builtin_amdgcn_raw_buffer_load_b128(rx, xo + 512, 0, 0);
    const u32x4 xc = __builtin_amdgcn_raw_buffer_load_b128(rx, x3 ? xo + 1024 : OOB, 0, 0);
    const u32x4 xr0 = __builtin_amdgcn_raw_buffer_load_b128(rx, bl * 1280 + g * 32, 256 * ur, 0);
    const u32x4 xr1 = __builtin_amdgcn_raw_buffer_load_b128(rx, bl * 1280 + g * 32 + 16, 256 * ur, 0);
    SB();
    TAG_READY();
    float v[24];
#pragma unroll
    for (int e = 0; e < 4; ++e) { v[2 * e] = bflo(xa[e]); v[2 * e + 1] = bfhi(xa[e]); v[8 + 2 * e] = bflo(xb[e]); v[9 + 2 * e] = bfhi(xb[e]); v[16 + 2 * e] = bflo(xc[e]); v[17 + 2 * e] = bfhi(xc[e]); }
    float sum = 0.f;
#pragma unroll
    for (int e = 0; e < 24; ++e) sum += v[e];
    sum = x16_sum(row_sum(sum));
    const float mean = sum * (1.f / 640.f);
    float sq = 0.f, sq3 = 0.f;
#pragma unroll
    for (int e = 0; e < 16; ++e) { const float d = v[e] - mean; sq = fmaf(d, d, sq); }
#pragma unroll
    for (int e = 16; e < 24; ++e) { const float d = v[e] - mean; sq3 = fmaf(d, d, sq3); }
    sq = x16_sum(row_sum(sq + (x3 ? sq3 : 0.f)));
    const float mb = rbf(mean), den = rbf(__builtin_amdgcn_sqrtf(rbf(rbf(sq * (1.f / 640.f)) + 1e-5f)));
    const float inv = __builtin_amdgcn_rcpf(den);
    const float mb0 = __builtin_bit_cast(float, __builtin_amdgcn_readlane(__builtin_bit_cast(int, mb), 0));
    const float mb1 = __builtin_bit_cast(float, __builtin_amdgcn_readlane(__builtin_bit_cast(int, mb), 32));
    const float iv0 = __builtin_bit_cast(float, __builtin_amdgcn_readlane(__builtin_bit_cast(int, inv), 0));
    const float iv1 = __builtin_bit_cast(float, __builtin_amdgcn_readlane(__builtin_bit_cast(int, inv), 32));
    const float mm = bl ? mb1 : mb0, ii = bl ? iv1 : iv0;
    asm volatile("s_waitcnt vmcnt(0)" : "+v"(fw[0]), "+v"(fw[1]), "+v"(fw[2]), "+v"(fw[3]), "+v"(fw[4]), "+v"(fw[5]), "+v"(fw[6]), "+v"(fw[7]),
                 "+v"(fw[8]), "+v"(fw[9]), "+v"(fw[10]), "+v"(fw[11]), "+v"(fw[12]), "+v"(fw[13]), "+v"(fw[14]), "+v"(fw[15]));
    f32x4 acc0 = {0.f, 0.f, 0.f, 0.f}, acc1 = {0.f, 0.f, 0.f, 0.f};
#pragma unroll
    for (int n = 0; n < 16; ++n) {
      const unsigned xw = n < 8 ? xr0[n >> 1] : xr1[(n - 8) >> 1];
      const float xv = (n & 1) ? bfhi(xw) : bflo(xw);
      const float xn = rbf(xv - mm) * ii;
      const unsigned d = cvtpk(xn, xn), a0 = d & m0, a1 = d & m1;
      acc0 = mfma16(a0, a1, fw[n][0], fw[n][1], acc0);
      acc1 = mfma16(a0, a1, fw[n][2], fw[n][3], acc1);
    }
    const int rec = (w < 2 ? WS_QP + (h * 5 + ur) * 1536 : WS_GU + ((xq * 5 + ur) * 2 + up) * 1536) + uc * 256;
    __builtin_amdgcn_raw_buffer_store_b128(proj_epilogue(acc0, acc1, tag), rws, ((lane >> 4) & 1) * 768 + st_col, rec, AUX_SC1);
    asm volatile("s_waitcnt vmcnt(0)" ::: "memory");
  } else {
    asm volatile("s_waitcnt vmcnt(0)" ::"v"(fw[0]), "v"(fw[1]), "v"(fw[2]), "v"(fw[3]), "v"(fw[4]), "v"(fw[5]), "v"(fw[6]), "v"(fw[7]),
                 "v"(fw[8]), "v"(fw[9]), "v"(fw[10]), "v"(fw[11]), "v"(fw[12]), "v"(fw[13]), "v"(fw[14]), "v"(fw[15]));
    TAG_READY();
  }
  const int slot = w * 8 + j8;
  const bool has_back = slot < 30;
  const int t3 = slot / 10, cc = slot - 10 * t3;
  const int rid = xq * 20 + uq;
  const int rb = rid / 80, rc = (rid - 80 * rb) >> 3, ro = rid & 7;
  const bool reducer = (w == 0) && (bid < 160);
  unsigned ha[2][2] = {{0u, 0u}, {0u, 0u}};
  unsigned tend = tstart + T_PLAN;
  auto pace_round = [&](unsigned t_sample, unsigned t_read, int so, int dead, int gain16, int keep16) {
    wait_until(t_sample);
    u32x4 e, f;
    do { MEMFENCE(); e = *(const u32x4*)l_est; f = *(const u32x4*)(l_est + 4); } while (!__builtin_amdgcn_readfirstlane(e[0] & e[2] & f[0] & f[2] & 1u));
    const u32x4 pv = {tstart + (unsigned)((int)((e[0] - tstart) + (e[2] - tstart) + (f[0] - tstart) + (f[2] - tstart)) >> 2), tag, e[1] + e[3] + f[1] + f[3], tag};
    __builtin_amdgcn_raw_buffer_store_b128(pv, rws, (lane == 0 && uq % 9 == 0) ? (uq / 9 * 8 + xq) * 16 : OOB, so, AUX_SC1);
    wait_until(t_read);
    MEMFENCE();
    const u32x4 tv = ld_sc1(rws, (lane & 31) * 16, so);
    if (__all((tv[1] == tag) & (tv[3] == tag))) {
      const float d = x16_sum(row_sum((float)(int)(tv[0] - tstart))) * (1.f / 32.f);
      const float dr = x16_sum(row_sum((float)(int)tv[2])) * (1.f / 128.f) - (float)LAT0;
      int di = (int)(d + fmaxf(dr, 0.f) + fmaxf(dr - (float)dead, 0.f) * (float)gain16 * (1.f / 16.f));
      const int dold = (int)(tend - tstart);
      if (keep16 && di < dold) di += ((dold - di) * keep16) >> 4;
      di = di < 0 ? 0 : (di > 2500 ? 2500 : di);
      tend = (tstart + __builtin_amdgcn_readfirstlane(di)) | 1u;
      *l_tend = tend;
    }
  };
  if (w < 3) {
    if (has_front) __builtin_amdgcn_s_sleep(16); else wait_until(tstart + 360);
    u32x2 t[3];
    const int vo = (lane >> 5) * 1536 + (lane & 31) * 8, so = WS_QP + h * 7680 + b * 768 + w * 256;
    for (unsigned spin = 0;; ++spin) {
      MEMFENCE();
      t[0] = ld2_sc1(rws, vo, so); t[1] = ld2_sc1(rws, vo, so + 3072); t[2] = ld2_sc1(rws, lane < 32 ? vo : OOB, so + 6144);
      const bool ok = (t[0][1] == tag) & (t[1][1] == tag) & ((t[2][1] == tag) | (lane >= 32));
      if (__all(ok) || spin > SPIN_CAP) break;
    }
    const float a = x32_sum((bflo(t[0][0]) + bflo(t[1][0])) + bflo(t[2][0]));
    const float c = x32_sum((bfhi(t[0][0]) + bfhi(t[1][0])) + bfhi(t[2][0]));
    if (lane < 32) { l_q[32 * w + ((lane >> 1) & 7) * 4 + (lane >> 4) * 2 + (lane & 1)] = cvtpk(a, c); MEMFENCE(); l_f[FG + w] = 1; }
  } else {
    l_f[FG + 3] = 1;
    if (AMP >= 4) { unsigned long long t_; asm volatile("s_memrealtime %0\n\ts_waitcnt lgkmcnt(0)" : "=s"(t_) :: "memory");
      __builtin_amdgcn_raw_buffer_store_b64(u32x2{AMP == 4 ? tstart : (unsigned)t_ - tstart, tag}, rws, (lane == 0 && bid < 64) ? bid * 8 : OOB, 3072, AUX_SC1); }
    pace_round(tstart + SMP0, tstart + SMP0 + WAIT0, WS_TS, DEAD0, GAIN0, 0);
  }
  if (has_back) {
    u32x2 t[3];
    const int f16 = (h & 3) * 3 + t3;
    const int so = WS_GU + xq * 15360 + (f16 >> 2) * 256 + (f16 & 3) * 32;
    const int vo = bl * 768 + ((g >> 1) & 1) * 128 + (g >> 2) * 16 + (g & 1) * 8;
    for (unsigned spin = 0;; ++spin) {
      MEMFENCE();
      t[0] = ld2_sc1(rws, vo + e4 * 3072, so);
      t[1] = ld2_sc1(rws, vo + e4 * 3072 + 1536, so);
      t[2] = ld2_sc1(rws, vo + 4 * 3072 + (e4 & 1) * 1536, so);
      if (__all((t[0][1] == tag) & (t[1][1] == tag) & (t[2][1] == tag)) || spin > SPIN_CAP) break;
    }
#pragma unroll
    for (int z = 0; z < 2; ++z) {
      const float ex = z ? bfhi(t[2][0]) : bflo(t[2][0]);
      const float gg = rbf(quad_sum((z ? bfhi(t[0][0]) : bflo(t[0][0])) + (e4 == 0 ? ex : 0.f)));
      const float uu = rbf(quad_sum((z ? bfhi(t[1][0]) : bflo(t[1][0])) + (e4 == 1 ? ex : 0.f)));
      const float zz = 0.7978845608028654f * (gg + 0.044715f * gg * gg * gg);
      const float ge = rbf(gg * __builtin_amdgcn_rcpf(1.f + __builtin_amdgcn_exp2f(-2.8853900817779268f * zz)));
      const float hv = ge * uu;
      const unsigned d = cvtpk(hv, hv);
      ha[z][0] = d & m0; ha[z][1] = d & m1;
    }
  }
  const int oc = 16 * (ro & 3) + 8 * (lane & 1) + 4 * (ro >> 2) + 2 * (lane >> 5) + ((lane >> 4) & 1);
  const rsrc_t rout = mk_rsrc(out, 2560);
  int out_vo = (lane & 14) == 0 ? oc * 2 + rb * 1280 + rc * 128 : OOB;
  float xres = 0.f;
  if (reducer) {
    xres = bflo(__builtin_amdgcn_raw_buffer_load_b16(rx, oc * 2, rb * 1280 + rc * 128, 0));
    __builtin_amdgcn_raw_buffer_store_b16((unsigned short)0, rout, out_vo, 0, AUX_SC1);
  }
  if (w == 1) {
    wait_lds<4>(fbase + FA, ALL4);
    const int so = WS_ML + (h * 2 + b) * 256;
    if (lane < 4) { const f32x2 ml = *(const f32x2*)(l_ml + 2 * lane); st_pair(rws, lane * 16, so + s * 64, ml[0], ml[1], tag); }
    asm volatile("s_waitcnt vmcnt(0)" ::: "memory");
    __builtin_amdgcn_s_sleep(32);
    u32x4 t;
    for (unsigned spin = 0;; ++spin) {
      MEMFENCE();
      t = ld_sc1(rws, (lane & 15) * 16, so);
      if (__all((t[1] == tag) & (t[3] == tag)) || spin > SPIN_CAP) break;
    }
    const float mv = __uint_as_float(t[0]);
    const float M = row_max(mv);
    const float f = __builtin_amdgcn_exp2f(mv - M);
    const float L = row_sum(f * __uint_as_float(t[2]));
    if ((lane >> 2) == s) { l_c[lane & 3] = f / L; MEMFENCE(); l_f[FE + (lane & 3)] = 1; }
  }
  if (w == 3) {
    pace_round(tend - SMP1, tend - SMP1 + WAIT1, WS_TS + 512, DEAD1, GAIN1, KEEP1);
  }
  if (has_back) {
    const unsigned sLo = (e4 & 1) ? 0x01000c0cu : 0x0c0c0100u, sHi = (e4 & 1) ? 0x03020c0cu : 0x0c0c0302u;
    unsigned sel0 = e4 < 2 ? sLo : 0x0c0c0c0cu, sel1 = e4 < 2 ? 0x0c0c0c0cu : sLo, sel2 = e4 < 2 ? sHi : 0x0c0c0c0cu, sel3 = e4 < 2 ? 0x0c0c0c0cu : sHi;
    int op_so = WS_OP + h * OP_STRIDE + t3 * 256, op_vo = (bl * 4 + e4) * 768 + 16 * g;
    int d_vo = ((48 * h + 16 * t3 + 2 * g) * 640 + 64 * cc) * 2 + 16 * j, o_vo = ((192 * h + 64 * t3 + 8 * g) * 640 + 64 * cc) * 2 + 16 * j;
    int yvo = ((lane >> 4) & 1) * 256 + st_col, yso = WS_YP + ((h * 3 + t3) * 10 + cc) * 512;
    int r_vo = (lane >> 1) * 5120 + (lane & 1) * 16 + WS_YP + rc * 512 + rb * 256 + ro * 32;
    unsigned tagv = tag, fd = fbase + FD;
    asm volatile("" : "+v"(sel0), "+v"(sel1), "+v"(sel2), "+v"(sel3), "+s"(op_so), "+v"(op_vo), "+v"(d_vo), "+v"(o_vo), "+v"(yvo), "+s"(yso), "+v"(r_vo), "+v"(out_vo), "+v"(tagv), "+v"(xres), "+v"(fd));
    const rsrc_t rd = mk_rsrc(wd, 1536 * 1280), ro_ = mk_rsrc(wo, 6144 * 1280);
    wait_lds<1>(fbase + FB, ALL4);
    {
      unsigned tf = *l_tend;
      asm volatile("" : "+v"(tf));
      tf = __builtin_amdgcn_readfirstlane(tf);
      const unsigned tn = (unsigned)__builtin_amdgcn_s_memrealtime();
      int wt = tf ? (int)(tf + BACK_AT - tn) : 0;
      asm("s_max_i32 %0, %0, %1\n\ts_min_i32 %0, %0, 0xc8" : "+s"(wt) : "s"(BACK_DELAY) : "scc");
      wait_until(tn + wt);
    }
    u32x4 wb[10], t0, t1;
    unsigned fdv = 0;
#pragma unroll
    for (int n = 0; n < 10; ++n) {
      if (n == POLLPOS) {
        const rsrc_t rp = mk_rsrc(ws, __builtin_amdgcn_readfirstlane(fdv) == ALL4 ? WS_END : 0);
        t0 = ld_sc1(rp, op_vo, op_so); t1 = ld_sc1(rp, op_vo + 128, op_so);
        SB();
      }
      if (n < 2) wb[8 + n] = ld_nt(rd, d_vo + n * 1280, 0); else wb[n - 2] = ld_nt(ro_, o_vo, (n - 2) * 1280);
      if (n == POLLPOS - 1) { MEMFENCE(); fdv = *(luint*)(size_t)fd; }
      SB();
      if (n < 9) __builtin_amdgcn_s_sleep(BPACE);
      SB();
    }
    u32x4 bq[10];
#define PAIR(p) do { bq[2 * (p)] = u32x4{wb[2 * (p)][0], wb[2 * (p)][1], wb[2 * (p) + 1][0], wb[2 * (p) + 1][1]}; \
                     bq[2 * (p) + 1] = u32x4{wb[2 * (p)][2], wb[2 * (p)][3], wb[2 * (p) + 1][2], wb[2 * (p) + 1][3]}; } while (0)
    PAIR(4); PAIR(0); PAIR(1); PAIR(2);
    const u32x4 hA = {ha[0][0], ha[0][1], ha[1][0], ha[1][1]};
    f32x4 acc0 = mfma32(hA, bq[8], f32x4{0.f, 0.f, 0.f, 0.f}), acc1 = mfma32(hA, bq[9], f32x4{0.f, 0.f, 0.f, 0.f});
    asm volatile("" : "+v"(bq[0]), "+v"(bq[1]), "+v"(bq[2]), "+v"(bq[3]), "+v"(bq[4]), "+v"(bq[5]), "+v"(acc0), "+v"(acc1));
    SB();
    unsigned np1 = 1;
    if (__builtin_expect(__builtin_amdgcn_ballot_w64((((t0[1] ^ tagv) | (t0[3] ^ tagv)) | ((t1[1] ^ tagv) | (t1[3] ^ tagv))) != 0u) != 0, 0)) {
      do {
        ++np1;
        MEMFENCE();
        if (__builtin_amdgcn_readfirstlane(*(luint*)(size_t)fd) != ALL4) { wait_lds<1>(fd, ALL4); delay_ticks(LATE_DELAY); }
        t0 = ld_sc1(rws, op_vo, op_so); t1 = ld_sc1(rws, op_vo + 128, op_so);
      } while (!__all((t0[1] == tag) & (t0[3] == tag) & (t1[1] == tag) & (t1[3] == tag)) && np1 < SPIN_CAP);
    }
    unsigned pk[4];
    {
      float u0, u1, u2, u3, u4, u5, u6, u7;
#define DPPADD(r, P) "v_add_f32_dpp " r ", " r ", " r " quad_perm:" P " row_mask:0xf bank_mask:0xf\n\t"
#define DPP8(P) DPPADD("%4", P) DPPADD("%5", P) DPPADD("%6", P) DPPADD("%7", P) DPPADD("%8", P) DPPADD("%9", P) DPPADD("%10", P) DPPADD("%11", P)
      asm volatile("v_lshlrev_b32 %4, 16, %12\n\tv_and_b32 %5, 0xffff0000, %12\n\tv_lshlrev_b32 %6, 16, %13\n\tv_and_b32 %7, 0xffff0000, %13\n\t"
                   "v_lshlrev_b32 %8, 16, %14\n\tv_and_b32 %9, 0xffff0000, %14\n\tv_lshlrev_b32 %10, 16, %15\n\tv_and_b32 %11, 0xffff0000, %15\n\t"
                   DPP8("[1,0,3,2]") DPP8("[2,3,0,1]")
                   "v_cvt_pk_bf16_f32 %0, %4, %5\n\tv_cvt_pk_bf16_f32 %1, %6, %7\n\tv_cvt_pk_bf16_f32 %2, %8, %9\n\tv_cvt_pk_bf16_f32 %3, %10, %11"
                   : "=&v"(pk[0]), "=&v"(pk[1]), "=&v"(pk[2]), "=&v"(pk[3]), "=&v"(u0), "=&v"(u1), "=&v"(u2), "=&v"(u3), "=&v"(u4), "=&v"(u5), "=&v"(u6), "=&v"(u7)
                   : "v"(t0[0]), "v"(t0[2]), "v"(t1[0]), "v"(t1[2]));
    }
    u32x4 aq[4];
#pragma unroll
    for (int p = 0; p < 4; ++p)
      aq[p] = u32x4{__builtin_amdgcn_perm(0u, pk[p], sel0), __builtin_amdgcn_perm(0u, pk[p], sel1), __builtin_amdgcn_perm(0u, pk[p], sel2), __builtin_amdgcn_perm(0u, pk[p], sel3)};
#pragma unroll
    for (int p = 0; p < 3; ++p) { acc0 = mfma32(aq[p], bq[2 * p], acc0); acc1 = mfma32(aq[p], bq[2 * p + 1], acc1); }
    asm volatile("" : "+v"(aq[3]), "+v"(acc0), "+v"(acc1));
    SB();
    acc0 = mfma16(aq[3][0], aq[3][1], wb[6][0], wb[6][1], acc0); acc1 = mfma16(aq[3][0], aq[3][1], wb[6][2], wb[6][3], acc1);
    acc0 = mfma16(aq[3][2], aq[3][3], wb[7][0], wb[7][1], acc0); acc1 = mfma16(aq[3][2], aq[3][3], wb[7][2], wb[7][3], acc1);
    __builtin_amdgcn_raw_buffer_store_b128(proj_epilogue(acc0, acc1, tagv), rws, yvo, yso, AUX_SC1);
    if (reducer) {
      delay_ticks(FIN_DELAY);
      float y0, y1, y2, y3;
      unsigned np2 = 0;
      for (;;) {
        MEMFENCE();
        const u32x4 a = ld_sc1(rws, r_vo, 0), c = ld_sc1(rws, r_vo, 32 * 5120), d = ld_sc1(rws, r_vo, 64 * 5120);
        unsigned bad = ((a[1] ^ tagv) | (a[3] ^ tagv)) | ((c[1] ^ tagv) | (c[3] ^ tagv));
        y0 = bflo(a[0]) + bflo(c[0]); y1 = bfhi(a[0]) + bfhi(c[0]); y2 = bflo(a[2]) + bflo(c[2]); y3 = bfhi(a[2]) + bfhi(c[2]);
        asm volatile("" : "+v"(bad), "+v"(y0), "+v"(y1), "+v"(y2), "+v"(y3));
        bad |= (d[1] ^ tagv) | (d[3] ^ tagv);
        y0 += bflo(d[0]); y1 += bfhi(d[0]); y2 += bflo(d[2]); y3 += bfhi(d[2]);
        if (__builtin_expect(__builtin_amdgcn_ballot_w64(bad != 0u) == 0, 1)) break;
        if (++np2 > SPIN_CAP) break;
      }
      const auto s0 = __builtin_amdgcn_permlane32_swap(__float_as_uint(y0), __float_as_uint(y2), false, false);
      const auto s1 = __builtin_amdgcn_permlane32_swap(__float_as_uint(y1), __float_as_uint(y3), false, false);
      const float z0 = __uint_as_float(s0[0]) + __uint_as_float(s0[1]), z1 = __uint_as_float(s1[0]) + __uint_as_float(s1[1]);
      const auto s2 = __builtin_amdgcn_permlane16_swap(__float_as_uint(z0), __float_as_uint(z1), false, false);
      float y = __uint_as_float(s2[0]) + __uint_as_float(s2[1]);
      y += dpp<ROR8>(y); y += dpp<ROR4>(y); y += dpp<XOR2>(y);
      __builtin_amdgcn_raw_buffer_store_b16((unsigned short)cvtpk(xres + y, 0.f), rout, out_vo, 0, AUX_SC1);
      if (bid == 0) __builtin_amdgcn_raw_buffer_store_b32(tagv, rws, lane == 0 ? WS_EPOCH : OOB, 0, AUX_SC1);
      if (bid == 8) {
        unsigned long long t_; asm volatile("s_memrealtime %0\n\ts_waitcnt lgkmcnt(0)" : "=s"(t_) :: "memory");
        unsigned tf = *l_tend; asm volatile("" : "+v"(tf)); tf = __builtin_amdgcn_readfirstlane(tf);
        int v = AMP == 1 ? (int)(tf - tstart) - 1300 : AMP == 2 ? (int)((unsigned)t_ - tf) - 300 : (int)((unsigned)t_ - tstart) - 1650;
        if (AMP >= 4) {
          const u32x2 tv = ld2_sc1(rws, lane * 8, 3072);
          const float d = AMP == 4 ? (float)(int)(tv[0] - amp0) : (float)tv[0];
          const float mx = x32_max(x16_max(row_max(d))), mn = -x32_max(x16_max(row_max(-d)));
          int ai = (int)(AMP == 4 ? mx - mn : mx); asm volatile("" : "+v"(ai)); v = __builtin_amdgcn_readfirstlane(ai);
        }
        asm volatile("s_max_i32 %0, %0, 0\n\ts_min_i32 %0, %0, 0x190" : "+s"(v) :: "scc");
        wait_until(amp0 + 4000 + 10 * v + ((tstart - amp0) > 95u ? 5000 : 0));
      }
    }
  }
}

extern "C" void kernel_launch(void* const* d_in, const int* in_sizes, int n_in, void* d_out, int out_size, void* d_ws, size_t ws_size,
                              hipStream_t stream) {
  fused_decode<<<256, 512, 0, stream>>>((const u16*)d_in[0], (const u16*)d_in[1], (const u16*)d_in[2], (const u16*)d_in[3], (const u16*)d_in[4],
                                        (const u16*)d_in[6], (const u16*)d_in[7], (const u16*)d_in[8], (u16*)d_out, (unsigned char*)d_ws);
}
```

```cpp
#include <hip/hip_runtime.h>
#include <hip/hip_bf16.h>
#include <stdint.h>
#include <type_traits>

typedef unsigned short u16;
typedef unsigned u32x4 __attribute__((ext_vector_type(4)));
typedef unsigned u32x2 __attribute__((ext_vector_type(2)));
typedef float f32x4 __attribute__((ext_vector_type(4)));
typedef float f32x2 __attribute__((ext_vector_type(2)));
typedef short s16x4 __attribute__((ext_vector_type(4)));
typedef __bf16 bf16x8 __attribute__((ext_vector_type(8)));
typedef __bf16 bf16x2 __attribute__((ext_vector_type(2)));
typedef __amdgpu_buffer_rsrc_t rsrc_t;

constexpr int QK = 6, CU = 6;
constexpr int STAG = 10;
constexpr int BPACE = 5;
constexpr int POLLPOS = 8;
constexpr int KEEP1 = 10;
constexpr int S_DELAY = 156;
constexpr int T_PLAN = 1396;
constexpr int RATE16 = 215;
constexpr int LAT0 = 73;
constexpr int SMP0 = 520, WAIT0 = 200, DEAD0 = 20, GAIN0 = 54;
constexpr int SMP1 = 385, WAIT1 = 130, DEAD1 = 13, GAIN1 = 52;
constexpr int TDEAD0 = 0, TREND0 = 0, TDEAD1 = 1, TREND1 = 11;
constexpr int BACK_AT = 66;
constexpr int BACK_DELAY = 12;
constexpr int LATE_DELAY = 20;
constexpr int FIN_DELAY = 48;
constexpr int CODE_BYTES = 16256;

constexpr int AUX_NT = 2, AUX_SC1 = 16;
constexpr int RSRC_W3 = 0x00020000;
constexpr unsigned SPIN_CAP = 1u << 18;
constexpr int OOB = 0x40000000;
constexpr unsigned ALL4 = 0x01010101u;

constexpr int WS_EPOCH = 0;
constexpr int WS_TS = 1024;
constexpr int WS_QP = 4096;
constexpr int WS_GU = WS_QP + 32 * 5 * 2 * 96 * 8;
constexpr int WS_ML = WS_GU + 8 * 5 * 2 * 2 * 96 * 8;
constexpr int WS_YP = WS_ML + 32 * 2 * 4 * 4 * 16;
constexpr int WS_OP = 2 << 20, OP_STRIDE = 1 << 20;
constexpr int WS_END = WS_OP + 32 * OP_STRIDE;
static_assert(WS_YP + 96 * 10 * 2 * 32 * 8 <= WS_OP, "workspace map");

template <int I, int N, class F> __device__ __forceinline__ void static_for(F&& f) {
  if constexpr (I < N) { f(std::integral_constant<int, I>{}); static_for<I + 1, N>(f); }
}
__device__ __forceinline__ rsrc_t mk_rsrc(const void* p, int bytes) {
  return __builtin_amdgcn_make_buffer_rsrc(const_cast<void*>(p), 0, bytes, RSRC_W3);
}
__device__ __forceinline__ u32x4 ld_nt(rsrc_t r, int voff, int soff) { return __builtin_amdgcn_raw_buffer_load_b128(r, voff, soff, AUX_NT); }
__device__ __forceinline__ u32x4 ld_sc1(rsrc_t r, int voff, int soff) { return __builtin_amdgcn_raw_buffer_load_b128(r, voff, soff, AUX_SC1); }
__device__ __forceinline__ u32x2 ld2_sc1(rsrc_t r, int voff, int soff) { return __builtin_amdgcn_raw_buffer_load_b64(r, voff, soff, AUX_SC1); }
__device__ __forceinline__ void st_pair(rsrc_t r, int voff, int soff, float a, float b, unsigned tag) {
  u32x4 v = {__float_as_uint(a), tag, __float_as_uint(b), tag};
  __builtin_amdgcn_raw_buffer_store_b128(v, r, voff, soff, AUX_SC1);
}
__device__ __forceinline__ float bflo(unsigned v) { return __uint_as_float(v << 16); }
__device__ __forceinline__ float bfhi(unsigned v) { return __uint_as_float(v & 0xffff0000u); }
__device__ __forceinline__ unsigned cvtpk(float lo, float hi) {
  bf16x2 r = {(__bf16)lo, (__bf16)hi};
  return __builtin_bit_cast(unsigned, r);
}
__device__ __forceinline__ float rbf(float x) { return (float)(__bf16)x; }
template <int CTRL> __device__ __forceinline__ float dpp(float x) {
  return __builtin_bit_cast(float, __builtin_amdgcn_mov_dpp(__builtin_bit_cast(int, x), CTRL, 0xf, 0xf, true));
}
constexpr int XOR1 = 0xB1, XOR2 = 0x4E, HMIR = 0x141, ROR8 = 0x128, ROR4 = 0x124;
__device__ __forceinline__ float x16_sum(float x) { auto s = __builtin_amdgcn_permlane16_swap(__float_as_uint(x), __float_as_uint(x), false, false); return __uint_as_float(s[0]) + __uint_as_float(s[1]); }
__device__ __forceinline__ float x32_sum(float x) { auto s = __builtin_amdgcn_permlane32_swap(__float_as_uint(x), __float_as_uint(x), false, false); return __uint_as_float(s[0]) + __uint_as_float(s[1]); }
__device__ __forceinline__ float x16_max(float x) { auto s = __builtin_amdgcn_permlane16_swap(__float_as_uint(x), __float_as_uint(x), false, false); return fmaxf(__uint_as_float(s[0]), __uint_as_float(s[1])); }
__device__ __forceinline__ float x32_max(float x) { auto s = __builtin_amdgcn_permlane32_swap(__float_as_uint(x), __float_as_uint(x), false, false); return fmaxf(__uint_as_float(s[0]), __uint_as_float(s[1])); }
__device__ __forceinline__ float row_sum(float x) { x += dpp<XOR1>(x); x += dpp<XOR2>(x); x += dpp<HMIR>(x); x += dpp<ROR8>(x); return x; }
__device__ __forceinline__ float row_max(float x) { x = fmaxf(x, dpp<XOR1>(x)); x = fmaxf(x, dpp<XOR2>(x)); x = fmaxf(x, dpp<HMIR>(x)); x = fmaxf(x, dpp<ROR8>(x)); return x; }
__device__ __forceinline__ float quad_sum(float x) { x += dpp<XOR1>(x); x += dpp<XOR2>(x); return x; }
__device__ __forceinline__ float dot2(unsigned a, unsigned b, float c) {
  return __builtin_amdgcn_fdot2_f32_bf16(__builtin_bit_cast(bf16x2, a), __builtin_bit_cast(bf16x2, b), c, false);
}
__device__ __forceinline__ f32x4 mfma16(unsigned a0, unsigned a1, unsigned b0, unsigned b1, f32x4 c) {
  u32x2 a = {a0, a1}, b = {b0, b1};
  return __builtin_amdgcn_mfma_f32_16x16x16bf16_1k(__builtin_bit_cast(s16x4, a), __builtin_bit_cast(s16x4, b), c, 0, 0, 0);
}
__device__ __forceinline__ f32x4 mfma32(u32x4 a, u32x4 b, f32x4 c) {
  return __builtin_amdgcn_mfma_f32_16x16x32_bf16(__builtin_bit_cast(bf16x8, a), __builtin_bit_cast(bf16x8, b), c, 0, 0, 0);
}
template <int RECIP> __device__ __forceinline__ unsigned pace_step(unsigned tprev, unsigned tend, unsigned& tdone) {
  unsigned d, now, tgt;
  asm volatile("s_sub_i32 %0, %5, %4\n\ts_max_i32 %0, %0, 0\n\ts_min_i32 %0, %0, 0x7d0\n\ts_mul_i32 %0, %0, %6\n\ts_lshr_b32 %0, %0, 16\n\ts_add_u32 %2, %4, %0\n"
               "1:\n\ts_memrealtime vcc\n\ts_getreg_b32 %0, hwreg(HW_REG_IB_STS, 0, 4)\n\ts_waitcnt lgkmcnt(0)\n\ts_cmp_lg_u32 %0, 0\n\ts_cselect_b32 %3, vcc_lo, %3\n\t"
               "s_sub_u32 %0, vcc_lo, %2\n\ts_cmp_lt_i32 %0, 0\n\ts_cbranch_scc1 1b\n\ts_mov_b32 %1, vcc_lo"
               : "=&s"(d), "=&s"(now), "=&s"(tgt), "+s"(tdone) : "s"(tprev), "s"(tend), "i"(RECIP) : "vcc", "scc");
  return now;
}
__device__ __forceinline__ void wait_until(unsigned tgt) {
  asm volatile("1:\n\ts_memrealtime vcc\n\ts_waitcnt lgkmcnt(0)\n\ts_sub_u32 vcc_lo, vcc_lo, %0\n\ts_cmp_lt_i32 vcc_lo, 0\n\ts_cbranch_scc1 1b" :: "s"(tgt) : "vcc", "scc", "memory");
}
#define SB() __builtin_amdgcn_sched_barrier(0)
#define MEMFENCE() asm volatile("" ::: "memory")

__device__ __forceinline__ void delay_ticks(int n) {
  if (n > 0) wait_until((unsigned)__builtin_amdgcn_s_memrealtime() + n);
}

typedef __attribute__((address_space(3))) float lfloat;
typedef __attribute__((address_space(3))) f32x4 lf32x4;
typedef __attribute__((address_space(3))) unsigned luint;
typedef __attribute__((address_space(3))) unsigned char lbyte;
template <int SLP> __device__ __forceinline__ void wait_lds(unsigned a, unsigned want) {
  unsigned v, sv;
  asm volatile("s_branch 2f\n1:\n\ts_sleep %4\n2:\n\tds_read_b32 %0, %2\n\ts_waitcnt lgkmcnt(0)\n\tv_readfirstlane_b32 %1, %0\n\ts_cmp_lg_u32 %1, %3\n\ts_cbranch_scc1 1b"
               : "=&v"(v), "=&s"(sv) : "v"(a), "s"(want), "i"(SLP) : "scc", "memory");
}

__device__ __forceinline__ u32x4 proj_epilogue(f32x4 acc0, f32x4 acc1, unsigned tag) {
  float r[4];
#pragma unroll
  for (int i = 0; i < 4; ++i) {
    auto s = __builtin_amdgcn_permlane32_swap(__float_as_uint(acc0[i]), __float_as_uint(acc1[i]), false, false);
    r[i] = __uint_as_float(s[0]) + dpp<ROR8>(__uint_as_float(s[1]));
  }
  return u32x4{cvtpk(r[0], r[1]), tag, cvtpk(r[2], r[3]), tag};
}

__global__ void __launch_bounds__(512, 2)
fused_decode(const u16* x_, const u16* kc_, const u16* vc_, const u16* wq_, const u16* wo_, const u16* wg_, const u16* wu_, const u16* wd_, u16* out_, unsigned char* ws_) {
  __shared__ __attribute__((aligned(16))) float lds[96 + 8 + 4 + 16 + 8 * 192];
  unsigned* l_q = (unsigned*)lds;
  float* l_ml = lds + 96;
  float* l_c = lds + 104;
  unsigned* l_est = (unsigned*)(lds + 108);
  unsigned char* l_f = (unsigned char*)(lds + 116);
  constexpr int FA = 0, FB = 4, FC = 8, FD = 12, FE = 16, FG = 20;
  unsigned* l_tend = (unsigned*)(lds + 122);
  unsigned* l_tag = (unsigned*)(lds + 123);
  const unsigned fbase = (unsigned)(size_t)(lbyte*)l_f;
  float* l_acc = lds + 124;

  typedef unsigned long long u64;
  typedef u64 u64x8 __attribute__((ext_vector_type(8)));
  typedef u64 u64x2 __attribute__((ext_vector_type(2)));
  u64x8 ka; u64x2 kb; u64 pc;
  u32x4 fw[16];
  int bid = blockIdx.x, tid = threadIdx.x;
  {
    unsigned v1, v2;
    u64x8 ka0; u64x2 kb0;
    asm volatile("s_load_dwordx16 %0, %3, 0x0\n\ts_load_dwordx4 %1, %3, 0x40\n\ts_getpc_b64 %2" : "=&{s[36:51]}"(ka0), "=&{s[52:55]}"(kb0), "=&s"(pc) : "s"(__builtin_amdgcn_kernarg_segment_ptr()));
    asm volatile(
               "v_readfirstlane_b32 s14, %20\n\t"
               "s_lshr_b32 s15, %19, 3\n\t"
               "s_cmpk_ge_u32 s14, 0xc0\n\ts_cbranch_scc1 1f\n\t"
               "s_setprio 2\n\t"
               "s_and_b32 s16, %19, 7\n\t"
               "s_lshr_b32 s17, %19, 6\n\ts_lshl2_add_u32 s17, s16, s17\n\t"
               "s_and_b32 s18, s15, 7\n\ts_lshr_b32 s19, s14, 6\n\ts_lshl1_add_u32 s18, s18, s19\n\t"
               "s_lshr_b32 s19, %19, 4\n\t"
               "v_bfe_u32 %16, %20, 3, 3\n\tv_and_b32 %17, 7, %20\n\t"
               "s_movk_i32 s13, 0x3000\n\t"
               "s_cmpk_lt_u32 s14, 0x80\n\t"
               "s_cselect_b32 s18, s18, s19\n\ts_cselect_b32 s17, s17, s16\n\ts_cselect_b32 s13, s13, 0xc00\n\t"
               "s_mul_i32 s19, s18, 11\n\ts_lshr_b32 s19, s19, 5\n\t"
               "v_mad_u32_u24 %16, %16, s13, %17\n\t"
               "s_sub_u32 s17, s17, s19\n\ts_mul_i32 s17, s17, 3\n\ts_add_u32 s17, s17, s18\n\t"
               "s_mul_i32 s19, s19, s13\n\ts_add_u32 s12, s17, s19\n\ts_lshl_b32 s12, s12, 7\n\t"
               "v_lshlrev_b32 %16, 4, %16\n\t"
               "s_mul_i32 s10, s13, 0x280\n\ts_mov_b32 s11, 0x20000\n\ts_cmp_eq_u32 s18, 15\n\ts_cselect_b32 s10, 0, s10\n\t"
               "s_bitcmp1_b32 s15, 0\n\t"
               "s_waitcnt lgkmcnt(0)\n\t"
               "s_cselect_b64 s[8:9], s[48:49], s[46:47]\n\t"
               "s_cmpk_lt_u32 s14, 0x80\n\ts_cselect_b64 s[8:9], s[42:43], s[8:9]\n\t"
               "s_and_b32 s9, s9, 0xffff\n\t"
               "buffer_load_dwordx4 %0, %16, s[8:11], s12 offen nt\n\t" "s_add_u32 s12, s12, s13\n\t"
               "buffer_load_dwordx4 %1, %16, s[8:11], s12 offen nt\n\t" "s_add_u32 s12, s12, s13\n\t"
               "buffer_load_dwordx4 %2, %16, s[8:11], s12 offen nt\n\t" "s_add_u32 s12, s12, s13\n\t"
               "buffer_load_dwordx4 %3, %16, s[8:11], s12 offen nt\n\t" "s_add_u32 s12, s12, s13\n\t"
               "buffer_load_dwordx4 %4, %16, s[8:11], s12 offen nt\n\t" "s_add_u32 s12, s12, s13\n\t"
               "buffer_load_dwordx4 %5, %16, s[8:11], s12 offen nt\n\t" "s_add_u32 s12, s12, s13\n\t"
               "buffer_load_dwordx4 %6, %16, s[8:11], s12 offen nt\n\t" "s_add_u32 s12, s12, s13\n\t"
               "buffer_load_dwordx4 %7, %16, s[8:11], s12 offen nt\n\t" "s_add_u32 s12, s12, s13\n\t"
               "buffer_load_dwordx4 %8, %16, s[8:11], s12 offen nt\n\t" "s_add_u32 s12, s12, s13\n\t"
               "buffer_load_dwordx4 %9, %16, s[8:11], s12 offen nt\n\t" "s_add_u32 s12, s12, s13\n\t"
               "buffer_load_dwordx4 %10, %16, s[8:11], s12 offen nt\n\t" "s_add_u32 s12, s12, s13\n\t"
               "buffer_load_dwordx4 %11, %16, s[8:11], s12 offen nt\n\t" "s_add_u32 s12, s12, s13\n\t" "s_barrier\n\t"
               "buffer_load_dwordx4 %12, %16, s[8:11], s12 offen nt\n\t" "s_add_u32 s12, s12, s13\n\t"
               "buffer_load_dwordx4 %13, %16, s[8:11], s12 offen nt\n\t" "s_add_u32 s12, s12, s13\n\t"
               "buffer_load_dwordx4 %14, %16, s[8:11], s12 offen nt\n\t" "s_add_u32 s12, s12, s13\n\t"
               "buffer_load_dwordx4 %15, %16, s[8:11], s12 offen nt\n\t"
               "s_branch 2f\n"
               "1:\n\t"
               "s_cmpk_lg_u32 s14, 0xc0\n\ts_cbranch_scc1 3f\n\t"
               "v_and_b32 %16, 3, %20\n\tv_lshl_add_u32 %16, %16, 5, s15\n\tv_lshlrev_b32 %16, 7, %16\n\tv_min_u32 %16, %24, %16\n\t"
               "global_load_dwordx4 %15, %16, %23\n"
               "3:\n\t"
               "v_and_b32 %16, 15, %20\n\tv_lshl_add_u32 %16, %16, 2, %21\n\tv_mov_b32 %17, 0\n\tds_write_b32 %16, %17\n\t"
               "s_waitcnt lgkmcnt(0)\n\ts_barrier\n"
               "2:"
               : "=&v"(fw[0]), "=&v"(fw[1]), "=&v"(fw[2]), "=&v"(fw[3]), "=&v"(fw[4]), "=&v"(fw[5]), "=&v"(fw[6]), "=&v"(fw[7]),
                 "=&v"(fw[8]), "=&v"(fw[9]), "=&v"(fw[10]), "=&v"(fw[11]), "=&v"(fw[12]), "=&v"(fw[13]), "=&v"(fw[14]), "=&v"(fw[15]),
                 "=&v"(v1), "=&v"(v2)
               : "{s[36:51]}"(ka0), "s"(bid), "v"(tid), "s"(fbase - 32), "{s[52:55]}"(kb0), "s"(pc), "i"(CODE_BYTES - 128)
               : "s8", "s9", "s10", "s11", "s12", "s13", "s14", "s15", "s16", "s17", "s18", "s19", "scc", "memory");
    asm volatile("" : "={s[36:51]}"(ka), "={s[52:55]}"(kb), "+s"(bid) : "0"(ka0), "1"(kb0));
    asm volatile("" : "+v"(tid));
  }
  const int xq = bid & 7, uq = bid >> 3;
  const int h = xq * 4 + (uq >> 3), j8 = uq & 7, b = j8 >> 2, s = j8 & 3;
  const int lane = tid & 63, wave = __builtin_amdgcn_readfirstlane(tid >> 6);
  const int g = lane >> 3, j = lane & 7;
  const int bl = (lane >> 2) & 1, e4 = lane & 3;
  const bool isS = wave >= 4;
  const u16 *x = (const u16*)ka[0], *kc = (const u16*)ka[1], *vc = (const u16*)ka[2], *wo = (const u16*)ka[4], *wd = (const u16*)ka[7];
  u16* out = (u16*)kb[0];
  unsigned char* ws = (unsigned char*)kb[1];
  u64 tst;
  asm volatile("s_memrealtime %0\n\ts_waitcnt lgkmcnt(0)" : "=s"(tst));
  const unsigned tstart = (unsigned)tst;
  const rsrc_t rws = mk_rsrc(ws, WS_END);

  if (isS) {
    const int sw = wave - 4;
    const rsrc_t r0 = mk_rsrc(kc, 2 * 32 * 8192 * 384), rv = mk_rsrc(vc, 2 * 32 * 8192 * 384);
    const int voff = ((b * 32 + h) * 8192 + 6144 + 32 * s + 8 * sw + g) * 384 + 16 * j;
    u32x4 buf[96];
    u32x4 qv[3];
    float sc[16];
    unsigned pd[16];
    f32x4 acc[3] = {{0.f, 0.f, 0.f, 0.f}, {0.f, 0.f, 0.f, 0.f}, {0.f, 0.f, 0.f, 0.f}};
    unsigned msk[4];
#pragma unroll
    for (int d = 0; d < 4; ++d) msk[d] = (j >> 1) == d ? ((j & 1) ? 0xffff0000u : 0x0000ffffu) : 0u;
    constexpr float C2 = 0.07216878364870322f * 1.4426950408889634f;
        const int stag = ((2 * (uq >> 2) - 7) * STAG) >> 1;
    unsigned tprev = tstart + S_DELAY + stag, tend = tstart + T_PLAN + stag, lat = 0;

    auto consume = [&](auto G) {
      constexpr int t = decltype(G)::value;
      if constexpr (t < 16) {
        float a0 = 0.f, a1 = 0.f, a2 = 0.f;
#pragma unroll
        for (int e = 0; e < 4; ++e) { a0 = dot2(buf[3 * t][e], qv[0][e], a0); a1 = dot2(buf[3 * t + 1][e], qv[1][e], a1); a2 = dot2(buf[3 * t + 2][e], qv[2][e], a2); }
        float a = (a0 + a1) + a2;
        a += dpp<XOR1>(a); a += dpp<XOR2>(a); a += dpp<HMIR>(a);
        sc[t] = a;
        if constexpr (t == 15) {
          float m = sc[0];
#pragma unroll
          for (int u = 1; u < 16; ++u) m = fmaxf(m, sc[u]);
          m = fmaxf(m, dpp<ROR8>(m)); m = x16_max(m); m = x32_max(m);
          const float mc = m * C2;
          float l = 0.f;
#pragma unroll
          for (int u = 0; u < 16; ++u) { const float p = __builtin_amdgcn_exp2f(fmaf(sc[u], C2, -mc)); pd[u] = cvtpk(p, p); l += bfhi(pd[u]); }
          l += dpp<ROR8>(l); l = x16_sum(l); l = x32_sum(l);
          if (lane == 0) { *(f32x2*)(l_ml + 2 * sw) = f32x2{mc, l}; MEMFENCE(); l_f[FA + sw] = 1; }
        }
      } else {
        constexpr int u = t - 16;
        const u32x4 a = {pd[u] & msk[0], pd[u] & msk[1], pd[u] & msk[2], pd[u] & msk[3]};
#pragma unroll
        for (int i = 0; i < 3; ++i) acc[i] = mfma32(a, buf[3 * t + i], acc[i]);
      }
    };
    static_for<0, 16>([&](auto K) {
      constexpr int k = decltype(K)::value;
      if constexpr (k > 6) {
        MEMFENCE();
        unsigned tv = *l_tend;
        asm volatile("" : "+v"(tv));
        tv = __builtin_amdgcn_readfirstlane(tv);
        tend = tv ? tv + stag : tend;
      }
      unsigned tdone = tprev + LAT0;
      const unsigned now = pace_step<(k ? 65536 / (16 - k) : 0)>(tprev, tend, tdone);
      SB();
#pragma unroll
      for (int i = 0; i < 6; ++i) { buf[6 * k + i] = ld_nt(k < 8 ? r0 : rv, voff + (i % 3) * 128, ((2 * k + i / 3) & 15) * 49152); SB(); }
      if constexpr (k == 15) { if (lane == 0) l_f[FB + sw] = 1; }
      if constexpr (k >= 1 && k < 15) asm volatile("s_waitcnt vmcnt(6)" ::: "memory");
      if constexpr (k >= 2 && k < 13) {
        if (tdone == now) tdone = (unsigned)__builtin_amdgcn_s_memrealtime();
        lat = (lat << 16) | (tdone - tprev);
        *(u32x2*)(l_est + 2 * sw) = u32x2{(now - stag + ((6 * (15 - k) * RATE16) >> 4)) | 1u, lat};
      }
      tprev = now;
      if constexpr (k == QK) {
        wait_lds<0>(fbase + FG, ALL4);
#pragma unroll
        for (int i = 0; i < 3; ++i) qv[i] = *(const u32x4*)(l_q + 32 * i + 4 * j);
      }
      SB();
      constexpr auto upto = [](int kk) constexpr { if (kk < QK) return 0; const int c = CU * (kk - QK + 1), l = kk < 15 ? 2 * kk : 28; return c < l ? c : l; };
      static_for<upto(k - 1), upto(k)>([&](auto G) { consume(G); SB(); });
      SB();
    });
    __builtin_amdgcn_s_setprio(3);
    wait_lds<0>(fbase + FE, ALL4);
    float cq = l_c[lane >> 4];
    unsigned dst = (unsigned)(size_t)(lfloat*)(l_acc + (sw * 2 + (g & 1)) * 192 + 8 * j + 4 * ((lane >> 4) & 1));
    unsigned src = (unsigned)(size_t)(lfloat*)(l_acc + (lane >> 4) * 384 + 64 * sw + 8 * j + 4 * (g & 1));
    unsigned fla = fbase + FC + sw, one = 1u, flr = fbase + FC;
    int pvo = (lane & 16) ? OOB : (g & 1) * 128 + j * 16 + (lane >> 5) * 8;
    int pso = WS_OP + h * OP_STRIDE + (b * 4 + s) * 768 + sw * 256;
    unsigned tagv = *l_tag;
    asm volatile("" : "+v"(cq), "+v"(dst), "+v"(src), "+v"(fla), "+v"(flr), "+v"(one), "+v"(pvo), "+s"(pso), "+v"(tagv));
    const bool wr = (lane >> 5) == (g & 1);
    SB();
    static_for<28, 32>([&](auto G) { consume(G); SB(); });
    if (wr) {
#pragma unroll
      for (int i = 0; i < 3; ++i) *(lf32x4*)(size_t)(dst + 256 * i) = acc[i];
      MEMFENCE();
      *(lbyte*)(size_t)fla = (unsigned char)one;
    }
    if (sw < 3) {
      f32x4 u0, u1;
      unsigned fv, fs;
      asm volatile("1:\n\tds_read_b32 %0, %4\n\tds_read_b128 %2, %5\n\tds_read_b128 %3, %5 offset:768\n\ts_waitcnt lgkmcnt(2)\n\tv_readfirstlane_b32 %1, %0\n\t"
                   "s_cmp_lg_u32 %1, 0x01010101\n\ts_waitcnt lgkmcnt(0)\n\ts_cbranch_scc1 1b"
                   : "=&v"(fv), "=&s"(fs), "=&v"(u0), "=&v"(u1) : "v"(flr), "v"(src) : "scc", "memory");
      float o[4];
#pragma unroll
      for (int e = 0; e < 4; ++e) o[e] = cq * (u0[e] + u1[e]);
      const auto s0 = __builtin_amdgcn_permlane32_swap(__float_as_uint(o[0]), __float_as_uint(o[2]), false, false);
      const auto s1 = __builtin_amdgcn_permlane32_swap(__float_as_uint(o[1]), __float_as_uint(o[3]), false, false);
      const float p0 = x16_sum(__uint_as_float(s0[0]) + __uint_as_float(s0[1])), p1 = x16_sum(__uint_as_float(s1[0]) + __uint_as_float(s1[1]));
      __builtin_amdgcn_raw_buffer_store_b64(u32x2{cvtpk(p0, p1), tagv}, rws, pvo, pso, AUX_SC1);
    }
    MEMFENCE();
    *(lbyte*)(size_t)(fla + (FD - FC)) = (unsigned char)one;
    return;
  }

  __builtin_amdgcn_s_setprio(2);
  unsigned tag, ep;
  asm volatile("s_load_dword %0, %1, 0x0" : "=&s"(ep) : "s"(ws));
#define TAG_READY() asm volatile("s_waitcnt lgkmcnt(0)\n\ts_add_u32 %0, %1, 1\n\ts_addc_u32 %0, %0, 0" : "=&s"(tag) : "s"(ep) : "scc", "memory")
  const int w = wave;
  const unsigned idx = w < 2 ? 2 * j8 + w : uq >> 1, ur = (idx * 11) >> 5, uc = idx - 3 * ur, up = uq & 1;
  const bool has_front = (w < 3) & (idx < 15);
  const unsigned m0 = e4 == 0 ? 0x0000ffffu : e4 == 1 ? 0xffff0000u : 0u;
  const unsigned m1 = e4 == 2 ? 0x0000ffffu : e4 == 3 ? 0xffff0000u : 0u;
  const int st_col = (g & 1) ? OOB : (lane >> 5) * 128 + j * 16;
  const rsrc_t rx = mk_rsrc(x, 2560);
  if (has_front) {
    const int xo = (lane >> 5) * 1280 + (lane & 31) * 16;
    const bool x3 = (lane & 16) == 0;
    const u32x4 xa = __builtin_amdgcn_raw_buffer_load_b128(rx, xo, 0, 0), xb = __builtin_amdgcn_raw_buffer_load_b128(rx, xo + 512, 0, 0);
    const u32x4 xc = __builtin_amdgcn_raw_buffer_load_b128(rx, x3 ? xo + 1024 : OOB, 0, 0);
    const u32x4 xr0 = __builtin_amdgcn_raw_buffer_load_b128(rx, bl * 1280 + g * 32, 256 * ur, 0);
    const u32x4 xr1 = __builtin_amdgcn_raw_buffer_load_b128(rx, bl * 1280 + g * 32 + 16, 256 * ur, 0);
    SB();
    TAG_READY();
    float v[24];
#pragma unroll
    for (int e = 0; e < 4; ++e) { v[2 * e] = bflo(xa[e]); v[2 * e + 1] = bfhi(xa[e]); v[8 + 2 * e] = bflo(xb[e]); v[9 + 2 * e] = bfhi(xb[e]); v[16 + 2 * e] = bflo(xc[e]); v[17 + 2 * e] = bfhi(xc[e]); }
    float sum = 0.f;
#pragma unroll
    for (int e = 0; e < 24; ++e) sum += v[e];
    sum = x16_sum(row_sum(sum));
    const float mean = sum * (1.f / 640.f);
    float sq = 0.f, sq3 = 0.f;
#pragma unroll
    for (int e = 0; e < 16; ++e) { const float d = v[e] - mean; sq = fmaf(d, d, sq); }
#pragma unroll
    for (int e = 16; e < 24; ++e) { const float d = v[e] - mean; sq3 = fmaf(d, d, sq3); }
    sq = x16_sum(row_sum(sq + (x3 ? sq3 : 0.f)));
    const float mb = rbf(mean), den = rbf(__builtin_amdgcn_sqrtf(rbf(rbf(sq * (1.f / 640.f)) + 1e-5f)));
    const float inv = __builtin_amdgcn_rcpf(den);
    const float mb0 = __builtin_bit_cast(float, __builtin_amdgcn_readlane(__builtin_bit_cast(int, mb), 0));
    const float mb1 = __builtin_bit_cast(float, __builtin_amdgcn_readlane(__builtin_bit_cast(int, mb), 32));
    const float iv0 = __builtin_bit_cast(float, __builtin_amdgcn_readlane(__builtin_bit_cast(int, inv), 0));
    const float iv1 = __builtin_bit_cast(float, __builtin_amdgcn_readlane(__builtin_bit_cast(int, inv), 32));
    const float mm = bl ? mb1 : mb0, ii = bl ? iv1 : iv0;
    asm volatile("s_waitcnt vmcnt(0)" : "+v"(fw[0]), "+v"(fw[1]), "+v"(fw[2]), "+v"(fw[3]), "+v"(fw[4]), "+v"(fw[5]), "+v"(fw[6]), "+v"(fw[7]),
                 "+v"(fw[8]), "+v"(fw[9]), "+v"(fw[10]), "+v"(fw[11]), "+v"(fw[12]), "+v"(fw[13]), "+v"(fw[14]), "+v"(fw[15]));
    f32x4 acc0 = {0.f, 0.f, 0.f, 0.f}, acc1 = {0.f, 0.f, 0.f, 0.f};
#pragma unroll
    for (int n = 0; n < 16; ++n) {
      const unsigned xw = n < 8 ? xr0[n >> 1] : xr1[(n - 8) >> 1];
      const float xv = (n & 1) ? bfhi(xw) : bflo(xw);
      const float xn = rbf(xv - mm) * ii;
      const unsigned d = cvtpk(xn, xn), a0 = d & m0, a1 = d & m1;
      acc0 = mfma16(a0, a1, fw[n][0], fw[n][1], acc0);
      acc1 = mfma16(a0, a1, fw[n][2], fw[n][3], acc1);
    }
    const int rec = (w < 2 ? WS_QP + (h * 5 + ur) * 1536 : WS_GU + ((xq * 5 + ur) * 2 + up) * 1536) + uc * 256;
    __builtin_amdgcn_raw_buffer_store_b128(proj_epilogue(acc0, acc1, tag), rws, ((lane >> 4) & 1) * 768 + st_col, rec, AUX_SC1);
    asm volatile("s_waitcnt vmcnt(0)" ::: "memory");
  } else {
    asm volatile("s_waitcnt vmcnt(0)" ::"v"(fw[0]), "v"(fw[1]), "v"(fw[2]), "v"(fw[3]), "v"(fw[4]), "v"(fw[5]), "v"(fw[6]), "v"(fw[7]),
                 "v"(fw[8]), "v"(fw[9]), "v"(fw[10]), "v"(fw[11]), "v"(fw[12]), "v"(fw[13]), "v"(fw[14]), "v"(fw[15]));
    TAG_READY();
  }
  const int slot = w * 8 + j8;
  const bool has_back = slot < 30;
  const int t3 = slot / 10, cc = slot - 10 * t3;
  const int rid = xq * 20 + uq;
  const int rb = rid / 80, rc = (rid - 80 * rb) >> 3, ro = rid & 7;
  const bool reducer = (w == 0) && (bid < 160);
  unsigned ha[2][2] = {{0u, 0u}, {0u, 0u}};
  unsigned tend = tstart + T_PLAN;
  auto pace_round = [&](unsigned t_sample, unsigned t_read, int so, int dead, int gain16, int tdead, int trend, int keep16) {
    wait_until(t_sample);
    u32x4 e, f;
    do { MEMFENCE(); e = *(const u32x4*)l_est; f = *(const u32x4*)(l_est + 4); } while (!__builtin_amdgcn_readfirstlane(e[0] & e[2] & f[0] & f[2] & 1u));
    const u32x4 pv = {tstart + (unsigned)((int)((e[0] - tstart) + (e[2] - tstart) + (f[0] - tstart) + (f[2] - tstart)) >> 2), tag, e[1] + e[3] + f[1] + f[3], tag};
    __builtin_amdgcn_raw_buffer_store_b128(pv, rws, (lane == 0 && j8 == (((uq >> 3) * 5) & 7)) ? ((uq >> 3) * 8 + xq) * 16 : OOB, so, AUX_SC1);
    wait_until(t_read);
    MEMFENCE();
    const u32x4 tv = ld_sc1(rws, (lane & 31) * 16, so);
    if (__all((tv[1] == tag) & (tv[3] == tag))) {
      const float d = x16_sum(row_sum((float)(int)(tv[0] - tstart))) * (1.f / 32.f);
      const float l1 = x16_sum(row_sum((float)(tv[2] & 0xffffu))) * (1.f / 128.f), l0 = x16_sum(row_sum((float)(tv[2] >> 16))) * (1.f / 128.f);
      const float dr = l1 - (float)LAT0, sign = l1 - l0;
      int di = (int)(d + fmaxf(dr, 0.f) + fmaxf(dr - (float)dead, 0.f) * (float)gain16 * (1.f / 16.f) + fmaxf(sign - (float)tdead, 0.f) * (float)trend);
      const int dold = (int)(tend - tstart);
      if (keep16 && di < dold) di += ((dold - di) * keep16) >> 4;
      di = di < 0 ? 0 : (di > 2500 ? 2500 : di);
      tend = (tstart + __builtin_amdgcn_readfirstlane(di)) | 1u;
      *l_tend = tend;
    }
  };
  if (w < 3) {
    if (has_front) __builtin_amdgcn_s_sleep(16); else wait_until(tstart + 360);
    u32x2 t[3];
    const int vo = (lane >> 5) * 1536 + (lane & 31) * 8, so = WS_QP + h * 7680 + b * 768 + w * 256;
    for (unsigned spin = 0;; ++spin) {
      MEMFENCE();
      t[0] = ld2_sc1(rws, vo, so); t[1] = ld2_sc1(rws, vo, so + 3072); t[2] = ld2_sc1(rws, lane < 32 ? vo : OOB, so + 6144);
      const bool ok = (t[0][1] == tag) & (t[1][1] == tag) & ((t[2][1] == tag) | (lane >= 32));
      if (__all(ok) || spin > SPIN_CAP) break;
    }
    const float a = x32_sum((bflo(t[0][0]) + bflo(t[1][0])) + bflo(t[2][0]));
    const float c = x32_sum((bfhi(t[0][0]) + bfhi(t[1][0])) + bfhi(t[2][0]));
    if (lane < 32) { l_q[32 * w + ((lane >> 1) & 7) * 4 + (lane >> 4) * 2 + (lane & 1)] = cvtpk(a, c); MEMFENCE(); l_f[FG + w] = 1; }
  } else {
    *l_tag = tag;
    MEMFENCE();
    l_f[FG + 3] = 1;
    pace_round(tstart + SMP0, tstart + SMP0 + WAIT0, WS_TS, DEAD0, GAIN0, TDEAD0, TREND0, 0);
  }
  if (has_back) {
    u32x2 t[3];
    const int f16 = (h & 3) * 3 + t3;
    const int so = WS_GU + xq * 15360 + (f16 >> 2) * 256 + (f16 & 3) * 32;
    const int vo = bl * 768 + ((g >> 1) & 1) * 128 + (g >> 2) * 16 + (g & 1) * 8;
    for (unsigned spin = 0;; ++spin) {
      MEMFENCE();
      t[0] = ld2_sc1(rws, vo + e4 * 3072, so);
      t[1] = ld2_sc1(rws, vo + e4 * 3072 + 1536, so);
      t[2] = ld2_sc1(rws, vo + 4 * 3072 + (e4 & 1) * 1536, so);
      if (__all((t[0][1] == tag) & (t[1][1] == tag) & (t[2][1] == tag)) || spin > SPIN_CAP) break;
    }
#pragma unroll
    for (int z = 0; z < 2; ++z) {
      const float ex = z ? bfhi(t[2][0]) : bflo(t[2][0]);
      const float gg = rbf(quad_sum((z ? bfhi(t[0][0]) : bflo(t[0][0])) + (e4 == 0 ? ex : 0.f)));
      const float uu = rbf(quad_sum((z ? bfhi(t[1][0]) : bflo(t[1][0])) + (e4 == 1 ? ex : 0.f)));
      const float zz = 0.7978845608028654f * (gg + 0.044715f * gg * gg * gg);
      const float ge = rbf(gg * __builtin_amdgcn_rcpf(1.f + __builtin_amdgcn_exp2f(-2.8853900817779268f * zz)));
      const float hv = ge * uu;
      const unsigned d = cvtpk(hv, hv);
      ha[z][0] = d & m0; ha[z][1] = d & m1;
    }
  }
  const int oc = 16 * (ro & 3) + 8 * (lane & 1) + 4 * (ro >> 2) + 2 * (lane >> 5) + ((lane >> 4) & 1);
  const rsrc_t rout = mk_rsrc(out, 2560);
  int out_vo = (lane & 14) == 0 ? oc * 2 + rb * 1280 + rc * 128 : OOB;
  float xres = 0.f;
  if (reducer) {
    xres = bflo(__builtin_amdgcn_raw_buffer_load_b16(rx, oc * 2, rb * 1280 + rc * 128, 0));
    __builtin_amdgcn_raw_buffer_store_b16((unsigned short)0, rout, out_vo, 0, AUX_SC1);
  }
  if (w == 1) {
    wait_lds<4>(fbase + FA, ALL4);
    const int so = WS_ML + (h * 2 + b) * 256;
    if (lane < 4) { const f32x2 ml = *(const f32x2*)(l_ml + 2 * lane); st_pair(rws, lane * 16, so + s * 64, ml[0], ml[1], tag); }
    asm volatile("s_waitcnt vmcnt(0)" ::: "memory");
    __builtin_amdgcn_s_sleep(32);
    u32x4 t;
    for (unsigned spin = 0;; ++spin) {
      MEMFENCE();
      t = ld_sc1(rws, (lane & 15) * 16, so);
      if (__all((t[1] == tag) & (t[3] == tag)) || spin > SPIN_CAP) break;
    }
    const float mv = __uint_as_float(t[0]);
    const float M = row_max(mv);
    const float f = __builtin_amdgcn_exp2f(mv - M);
    const float L = row_sum(f * __uint_as_float(t[2]));
    if ((lane >> 2) == s) { l_c[lane & 3] = f / L; MEMFENCE(); l_f[FE + (lane & 3)] = 1; }
  }
  if (w == 3) {
    pace_round(tend - SMP1, tend - SMP1 + WAIT1, WS_TS + 512, DEAD1, GAIN1, TDEAD1, TREND1, KEEP1);
  }
  if (has_back) {
    const unsigned sLo = (e4 & 1) ? 0x01000c0cu : 0x0c0c0100u, sHi = (e4 & 1) ? 0x03020c0cu : 0x0c0c0302u;
    unsigned sel0 = e4 < 2 ? sLo : 0x0c0c0c0cu, sel1 = e4 < 2 ? 0x0c0c0c0cu : sLo, sel2 = e4 < 2 ? sHi : 0x0c0c0c0cu, sel3 = e4 < 2 ? 0x0c0c0c0cu : sHi;
    int op_so = WS_OP + h * OP_STRIDE + t3 * 256, op_vo = (bl * 4 + e4) * 768 + 16 * g;
    int d_vo = ((48 * h + 16 * t3 + 2 * g) * 640 + 64 * cc) * 2 + 16 * j, o_vo = ((192 * h + 64 * t3 + 8 * g) * 640 + 64 * cc) * 2 + 16 * j;
    int yvo = ((lane >> 4) & 1) * 256 + st_col, yso = WS_YP + ((h * 3 + t3) * 10 + cc) * 512;
    int r_vo = (lane >> 1) * 5120 + (lane & 1) * 16 + WS_YP + rc * 512 + rb * 256 + ro * 32;
    unsigned tagv = tag, fd = fbase + FD;
    asm volatile("" : "+v"(sel0), "+v"(sel1), "+v"(sel2), "+v"(sel3), "+s"(op_so), "+v"(op_vo), "+v"(d_vo), "+v"(o_vo), "+v"(yvo), "+s"(yso), "+v"(r_vo), "+v"(out_vo), "+v"(tagv), "+v"(xres), "+v"(fd));
    const rsrc_t rd = mk_rsrc(wd, 1536 * 1280), ro_ = mk_rsrc(wo, 6144 * 1280);
    wait_lds<1>(fbase + FB, ALL4);
    {
      unsigned tf = *l_tend;
      asm volatile("" : "+v"(tf));
      tf = __builtin_amdgcn_readfirstlane(tf);
      const unsigned tn = (unsigned)__builtin_amdgcn_s_memrealtime();
      int wt = tf ? (int)(tf + BACK_AT - tn) : 0;
      asm("s_max_i32 %0, %0, %1\n\ts_min_i32 %0, %0, 0xc8" : "+s"(wt) : "s"(BACK_DELAY) : "scc");
      wait_until(tn + wt);
    }
    u32x4 wb[10], t0, t1;
    unsigned fdv = 0;
#pragma unroll
    for (int n = 0; n < 10; ++n) {
      if (n == POLLPOS) {
        const rsrc_t rp = mk_rsrc(ws, __builtin_amdgcn_readfirstlane(fdv) == ALL4 ? WS_END : 0);
        t0 = ld_sc1(rp, op_vo, op_so); t1 = ld_sc1(rp, op_vo + 128, op_so);
        SB();
      }
      if (n < 2) wb[8 + n] = ld_nt(rd, d_vo + n * 1280, 0); else wb[n - 2] = ld_nt(ro_, o_vo, (n - 2) * 1280);
      if (n == POLLPOS - 1) { MEMFENCE(); fdv = *(luint*)(size_t)fd; }
      SB();
      if (n < 9) __builtin_amdgcn_s_sleep(BPACE);
      SB();
    }
    u32x4 bq[10];
#define PAIR(p) do { bq[2 * (p)] = u32x4{wb[2 * (p)][0], wb[2 * (p)][1], wb[2 * (p) + 1][0], wb[2 * (p) + 1][1]}; \
                     bq[2 * (p) + 1] = u32x4{wb[2 * (p)][2], wb[2 * (p)][3], wb[2 * (p) + 1][2], wb[2 * (p) + 1][3]}; } while (0)
    PAIR(4); PAIR(0); PAIR(1); PAIR(2);
    const u32x4 hA = {ha[0][0], ha[0][1], ha[1][0], ha[1][1]};
    f32x4 acc0 = mfma32(hA, bq[8], f32x4{0.f, 0.f, 0.f, 0.f}), acc1 = mfma32(hA, bq[9], f32x4{0.f, 0.f, 0.f, 0.f});
    asm volatile("" : "+v"(bq[0]), "+v"(bq[1]), "+v"(bq[2]), "+v"(bq[3]), "+v"(bq[4]), "+v"(bq[5]), "+v"(acc0), "+v"(acc1));
    SB();
    unsigned np1 = 1;
    if (__builtin_expect(__builtin_amdgcn_ballot_w64((((t0[1] ^ tagv) | (t0[3] ^ tagv)) | ((t1[1] ^ tagv) | (t1[3] ^ tagv))) != 0u) != 0, 0)) {
      do {
        ++np1;
        MEMFENCE();
        if (__builtin_amdgcn_readfirstlane(*(luint*)(size_t)fd) != ALL4) { wait_lds<1>(fd, ALL4); delay_ticks(LATE_DELAY); }
        t0 = ld_sc1(rws, op_vo, op_so); t1 = ld_sc1(rws, op_vo + 128, op_so);
      } while (!__all((t0[1] == tag) & (t0[3] == tag) & (t1[1] == tag) & (t1[3] == tag)) && np1 < SPIN_CAP);
    }
    unsigned pk[4];
    {
      float u0, u1, u2, u3, u4, u5, u6, u7;
#define DPPADD(r, P) "v_add_f32_dpp " r ", " r ", " r " quad_perm:" P " row_mask:0xf bank_mask:0xf\n\t"
#define DPP8(P) DPPADD("%4", P) DPPADD("%5", P) DPPADD("%6", P) DPPADD("%7", P) DPPADD("%8", P) DPPADD("%9", P) DPPADD("%10", P) DPPADD("%11", P)
      asm volatile("v_lshlrev_b32 %4, 16, %12\n\tv_and_b32 %5, 0xffff0000, %12\n\tv_lshlrev_b32 %6, 16, %13\n\tv_and_b32 %7, 0xffff0000, %13\n\t"
                   "v_lshlrev_b32 %8, 16, %14\n\tv_and_b32 %9, 0xffff0000, %14\n\tv_lshlrev_b32 %10, 16, %15\n\tv_and_b32 %11, 0xffff0000, %15\n\t"
                   DPP8("[1,0,3,2]") DPP8("[2,3,0,1]")
                   "v_cvt_pk_bf16_f32 %0, %4, %5\n\tv_cvt_pk_bf16_f32 %1, %6, %7\n\tv_cvt_pk_bf16_f32 %2, %8, %9\n\tv_cvt_pk_bf16_f32 %3, %10, %11"
                   : "=&v"(pk[0]), "=&v"(pk[1]), "=&v"(pk[2]), "=&v"(pk[3]), "=&v"(u0), "=&v"(u1), "=&v"(u2), "=&v"(u3), "=&v"(u4), "=&v"(u5), "=&v"(u6), "=&v"(u7)
                   : "v"(t0[0]), "v"(t0[2]), "v"(t1[0]), "v"(t1[2]));
    }
    u32x4 aq[4];
#pragma unroll
    for (int p = 0; p < 4; ++p)
      aq[p] = u32x4{__builtin_amdgcn_perm(0u, pk[p], sel0), __builtin_amdgcn_perm(0u, pk[p], sel1), __builtin_amdgcn_perm(0u, pk[p], sel2), __builtin_amdgcn_perm(0u, pk[p], sel3)};
#pragma unroll
    for (int p = 0; p < 3; ++p) { acc0 = mfma32(aq[p], bq[2 * p], acc0); acc1 = mfma32(aq[p], bq[2 * p + 1], acc1); }
    asm volatile("" : "+v"(aq[3]), "+v"(acc0), "+v"(acc1));
    SB();
    acc0 = mfma16(aq[3][0], aq[3][1], wb[6][0], wb[6][1], acc0); acc1 = mfma16(aq[3][0], aq[3][1], wb[6][2], wb[6][3], acc1);
    acc0 = mfma16(aq[3][2], aq[3][3], wb[7][0], wb[7][1], acc0); acc1 = mfma16(aq[3][2], aq[3][3], wb[7][2], wb[7][3], acc1);
    __builtin_amdgcn_raw_buffer_store_b128(proj_epilogue(acc0, acc1, tagv), rws, yvo, yso, AUX_SC1);
    if (reducer) {
      delay_ticks(FIN_DELAY);
      float y0, y1, y2, y3;
      unsigned np2 = 0;
      for (;;) {
        MEMFENCE();
        const u32x4 a = ld_sc1(rws, r_vo, 0), c = ld_sc1(rws, r_vo, 32 * 5120), d = ld_sc1(rws, r_vo, 64 * 5120);
        unsigned bad = ((a[1] ^ tagv) | (a[3] ^ tagv)) | ((c[1] ^ tagv) | (c[3] ^ tagv));
        y0 = bflo(a[0]) + bflo(c[0]); y1 = bfhi(a[0]) + bfhi(c[0]); y2 = bflo(a[2]) + bflo(c[2]); y3 = bfhi(a[2]) + bfhi(c[2]);
        asm volatile("" : "+v"(bad), "+v"(y0), "+v"(y1), "+v"(y2), "+v"(y3));
        bad |= (d[1] ^ tagv) | (d[3] ^ tagv);
        y0 += bflo(d[0]); y1 += bfhi(d[0]); y2 += bflo(d[2]); y3 += bfhi(d[2]);
        if (__builtin_expect(__builtin_amdgcn_ballot_w64(bad != 0u) == 0, 1)) break;
        if (++np2 > SPIN_CAP) break;
      }
      const auto s0 = __builtin_amdgcn_permlane32_swap(__float_as_uint(y0), __float_as_uint(y2), false, false);
      const auto s1 = __builtin_amdgcn_permlane32_swap(__float_as_uint(y1), __float_as_uint(y3), false, false);
      const float z0 = __uint_as_float(s0[0]) + __uint_as_float(s0[1]), z1 = __uint_as_float(s1[0]) + __uint_as_float(s1[1]);
      const auto s2 = __builtin_amdgcn_permlane16_swap(__float_as_uint(z0), __float_as_uint(z1), false, false);
      float y = __uint_as_float(s2[0]) + __uint_as_float(s2[1]);
      y += dpp<ROR8>(y); y += dpp<ROR4>(y); y += dpp<XOR2>(y);
      __builtin_amdgcn_raw_buffer_store_b16((unsigned short)cvtpk(xres + y, 0.f), rout, out_vo, 0, AUX_SC1);
      if (bid == 0) __builtin_amdgcn_raw_buffer_store_b32(tagv, rws, lane == 0 ? WS_EPOCH : OOB, 0, AUX_SC1);
    }
  }
}

extern "C" void kernel_launch(void* const* d_in, const int* in_sizes, int n_in, void* d_out, int out_size, void* d_ws, size_t ws_size,
                              hipStream_t stream) {
  fused_decode<<<256, 512, 0, stream>>>((const u16*)d_in[0], (const u16*)d_in[1], (const u16*)d_in[2], (const u16*)d_in[3], (const u16*)d_in[4],
                                        (const u16*)d_in[6], (const u16*)d_in[7], (const u16*)d_in[8], (u16*)d_out, (unsigned char*)d_ws);
}
```

```cpp
#include <hip/hip_runtime.h>
#include <hip/hip_bf16.h>
#include <stdint.h>
#include <type_traits>

typedef unsigned short u16;
typedef unsigned u32x4 __attribute__((ext_vector_type(4)));
typedef unsigned u32x2 __attribute__((ext_vector_type(2)));
typedef float f32x4 __attribute__((ext_vector_type(4)));
typedef float f32x2 __attribute__((ext_vector_type(2)));
typedef short s16x4 __attribute__((ext_vector_type(4)));
typedef __bf16 bf16x8 __attribute__((ext_vector_type(8)));
typedef __bf16 bf16x2 __attribute__((ext_vector_type(2)));
typedef __amdgpu_buffer_rsrc_t rsrc_t;

constexpr int QK = 6, CU = 6;
constexpr int STAG = 10;
constexpr int BPACE = 5;
constexpr int POLLPOS = 8;
constexpr int KEEP1 = 10;
constexpr int S_DELAY = 148;
constexpr int T_PLAN = 1388;
constexpr int RATE16 = 214;
constexpr int LAT0 = 79;
constexpr int SMP0 = 493, WAIT0 = 195, DEAD0 = 15, GAIN0 = 64;
constexpr int SMP1 = 385, WAIT1 = 135, DEAD1 = 7, GAIN1 = 52;
constexpr int Q_AT = 430, GU_AT = 440;
constexpr int TDEAD0 = 0, TREND0 = 0, TDEAD1 = 1, TREND1 = 11;
constexpr int BACK_AT = 70;
constexpr int BACK_DELAY = 12;
constexpr int LATE_DELAY = 20;
constexpr int FIN_DELAY = 48;
constexpr int CODE_BYTES = 16640;

constexpr int AUX_NT = 2, AUX_SC1 = 16;
constexpr int RSRC_W3 = 0x00020000;
constexpr unsigned SPIN_CAP = 1u << 18;
constexpr int OOB = 0x40000000;
constexpr unsigned ALL4 = 0x01010101u;

constexpr int WS_EPOCH = 0;
constexpr int WS_TS = 1024;
constexpr int WS_FP = 4096;
constexpr int WS_ML = WS_FP + 8 * 18 * 5 * 512;
constexpr int WS_YP = WS_ML + 32 * 2 * 4 * 4 * 16;
constexpr int WS_OP = 2 << 20, OP_STRIDE = 1 << 20;
constexpr int WS_END = WS_OP + 32 * OP_STRIDE;
static_assert(WS_YP + 96 * 10 * 2 * 32 * 8 <= WS_OP, "workspace map");

template <int I, int N, class F> __device__ __forceinline__ void static_for(F&& f) {
  if constexpr (I < N) { f(std::integral_constant<int, I>{}); static_for<I + 1, N>(f); }
}
__device__ __forceinline__ rsrc_t mk_rsrc(const void* p, int bytes) {
  return __builtin_amdgcn_make_buffer_rsrc(const_cast<void*>(p), 0, bytes, RSRC_W3);
}
__device__ __forceinline__ u32x4 ld_nt(rsrc_t r, int voff, int soff) { return __builtin_amdgcn_raw_buffer_load_b128(r, voff, soff, AUX_NT); }
__device__ __forceinline__ u32x4 ld_sc1(rsrc_t r, int voff, int soff) { return __builtin_amdgcn_raw_buffer_load_b128(r, voff, soff, AUX_SC1); }
__device__ __forceinline__ u32x2 ld2_sc1(rsrc_t r, int voff, int soff) { return __builtin_amdgcn_raw_buffer_load_b64(r, voff, soff, AUX_SC1); }
__device__ __forceinline__ void st_pair(rsrc_t r, int voff, int soff, float a, float b, unsigned tag) {
  u32x4 v = {__float_as_uint(a), tag, __float_as_uint(b), tag};
  __builtin_amdgcn_raw_buffer_store_b128(v, r, voff, soff, AUX_SC1);
}
__device__ __forceinline__ float bflo(unsigned v) { return __uint_as_float(v << 16); }
__device__ __forceinline__ float bfhi(unsigned v) { return __uint_as_float(v & 0xffff0000u); }
__device__ __forceinline__ unsigned cvtpk(float lo, float hi) {
  bf16x2 r = {(__bf16)lo, (__bf16)hi};
  return __builtin_bit_cast(unsigned, r);
}
__device__ __forceinline__ float rbf(float x) { return (float)(__bf16)x; }
template <int CTRL> __device__ __forceinline__ float dpp(float x) {
  return __builtin_bit_cast(float, __builtin_amdgcn_mov_dpp(__builtin_bit_cast(int, x), CTRL, 0xf, 0xf, true));
}
constexpr int XOR1 = 0xB1, XOR2 = 0x4E, HMIR = 0x141, ROR8 = 0x128, ROR4 = 0x124;
__device__ __forceinline__ float x16_sum(float x) { auto s = __builtin_amdgcn_permlane16_swap(__float_as_uint(x), __float_as_uint(x), false, false); return __uint_as_float(s[0]) + __uint_as_float(s[1]); }
__device__ __forceinline__ float x32_sum(float x) { auto s = __builtin_amdgcn_permlane32_swap(__float_as_uint(x), __float_as_uint(x), false, false); return __uint_as_float(s[0]) + __uint_as_float(s[1]); }
__device__ __forceinline__ float x16_max(float x) { auto s = __builtin_amdgcn_permlane16_swap(__float_as_uint(x), __float_as_uint(x), false, false); return fmaxf(__uint_as_float(s[0]), __uint_as_float(s[1])); }
__device__ __forceinline__ float x32_max(float x) { auto s = __builtin_amdgcn_permlane32_swap(__float_as_uint(x), __float_as_uint(x), false, false); return fmaxf(__uint_as_float(s[0]), __uint_as_float(s[1])); }
__device__ __forceinline__ float row_sum(float x) { x += dpp<XOR1>(x); x += dpp<XOR2>(x); x += dpp<HMIR>(x); x += dpp<ROR8>(x); return x; }
__device__ __forceinline__ float row_max(float x) { x = fmaxf(x, dpp<XOR1>(x)); x = fmaxf(x, dpp<XOR2>(x)); x = fmaxf(x, dpp<HMIR>(x)); x = fmaxf(x, dpp<ROR8>(x)); return x; }
__device__ __forceinline__ float quad_sum(float x) { x += dpp<XOR1>(x); x += dpp<XOR2>(x); return x; }
__device__ __forceinline__ float dot2(unsigned a, unsigned b, float c) {
  return __builtin_amdgcn_fdot2_f32_bf16(__builtin_bit_cast(bf16x2, a), __builtin_bit_cast(bf16x2, b), c, false);
}
__device__ __forceinline__ f32x4 mfma16(unsigned a0, unsigned a1, unsigned b0, unsigned b1, f32x4 c) {
  u32x2 a = {a0, a1}, b = {b0, b1};
  return __builtin_amdgcn_mfma_f32_16x16x16bf16_1k(__builtin_bit_cast(s16x4, a), __builtin_bit_cast(s16x4, b), c, 0, 0, 0);
}
__device__ __forceinline__ f32x4 mfma32(u32x4 a, u32x4 b, f32x4 c) {
  return __builtin_amdgcn_mfma_f32_16x16x32_bf16(__builtin_bit_cast(bf16x8, a), __builtin_bit_cast(bf16x8, b), c, 0, 0, 0);
}
template <int RECIP> __device__ __forceinline__ unsigned pace_step(unsigned tprev, unsigned tend, unsigned& tdone) {
  unsigned d, now, tgt;
  asm volatile("s_sub_i32 %0, %5, %4\n\ts_max_i32 %0, %0, 0\n\ts_min_i32 %0, %0, 0x7d0\n\ts_mul_i32 %0, %0, %6\n\ts_lshr_b32 %0, %0, 16\n\ts_add_u32 %2, %4, %0\n"
               "1:\n\ts_memrealtime vcc\n\ts_getreg_b32 %0, hwreg(HW_REG_IB_STS, 0, 4)\n\ts_waitcnt lgkmcnt(0)\n\ts_cmp_lg_u32 %0, 0\n\ts_cselect_b32 %3, vcc_lo, %3\n\t"
               "s_sub_u32 %0, vcc_lo, %2\n\ts_cmp_lt_i32 %0, 0\n\ts_cbranch_scc1 1b\n\ts_mov_b32 %1, vcc_lo"
               : "=&s"(d), "=&s"(now), "=&s"(tgt), "+s"(tdone) : "s"(tprev), "s"(tend), "i"(RECIP) : "vcc", "scc");
  return now;
}
__device__ __forceinline__ void wait_until(unsigned tgt) {
  asm volatile("1:\n\ts_memrealtime vcc\n\ts_waitcnt lgkmcnt(0)\n\ts_sub_u32 vcc_lo, vcc_lo, %0\n\ts_cmp_lt_i32 vcc_lo, 0\n\ts_cbranch_scc1 1b" :: "s"(tgt) : "vcc", "scc", "memory");
}
#define SB() __builtin_amdgcn_sched_barrier(0)
#define MEMFENCE() asm volatile("" ::: "memory")

__device__ __forceinline__ void delay_ticks(int n) {
  if (n > 0) wait_until((unsigned)__builtin_amdgcn_s_memrealtime() + n);
}

typedef __attribute__((address_space(3))) float lfloat;
typedef __attribute__((address_space(3))) f32x4 lf32x4;
typedef __attribute__((address_space(3))) unsigned luint;
typedef __attribute__((address_space(3))) unsigned char lbyte;
template <int SLP> __device__ __forceinline__ void wait_lds(unsigned a, unsigned want) {
  unsigned v, sv;
  asm volatile("s_branch 2f\n1:\n\ts_sleep %4\n2:\n\tds_read_b32 %0, %2\n\ts_waitcnt lgkmcnt(0)\n\tv_readfirstlane_b32 %1, %0\n\ts_cmp_lg_u32 %1, %3\n\ts_cbranch_scc1 1b"
               : "=&v"(v), "=&s"(sv) : "v"(a), "s"(want), "i"(SLP) : "scc", "memory");
}

__device__ __forceinline__ f32x4 proj_epilogue(f32x4 acc0, f32x4 acc1) {
  f32x4 r;
#pragma unroll
  for (int i = 0; i < 4; ++i) {
    auto s = __builtin_amdgcn_permlane32_swap(__float_as_uint(acc0[i]), __float_as_uint(acc1[i]), false, false);
    r[i] = __uint_as_float(s[0]) + dpp<ROR8>(__uint_as_float(s[1]));
  }
  return r;
}
__device__ __forceinline__ u32x4 granules(f32x4 r, unsigned tag) { return u32x4{cvtpk(r[0], r[1]), tag, cvtpk(r[2], r[3]), tag}; }

__global__ void __launch_bounds__(512, 2)
fused_decode(const u16* x_, const u16* kc_, const u16* vc_, const u16* wq_, const u16* wo_, const u16* wg_, const u16* wu_, const u16* wd_, u16* out_, unsigned char* ws_) {
  __shared__ __attribute__((aligned(16))) float lds[96 + 8 + 4 + 32 + 8 * 192 + 4];
  unsigned* l_q = (unsigned*)lds;
  float* l_ml = lds + 96;
  float* l_c = lds + 104;
  unsigned* l_est = (unsigned*)(lds + 108);
  unsigned char* l_f = (unsigned char*)(lds + 116);
  constexpr int FA = 0, FB = 4, FC = 8, FD = 12, FE = 16, FG = 20, FH = 28, FX = 32;
  unsigned* l_tend = (unsigned*)(lds + 122);
  unsigned* l_tag = (unsigned*)(lds + 140 + 8 * 192);
  const unsigned fbase = (unsigned)(size_t)(lbyte*)l_f;
  float* l_acc = lds + 140;
  unsigned char* l_xn = (unsigned char*)(l_acc + 512);

  typedef unsigned long long u64;
  typedef u64 u64x8 __attribute__((ext_vector_type(8)));
  typedef u64 u64x2 __attribute__((ext_vector_type(2)));
  u64x8 ka; u64x2 kb; u64 pc;
  u32x4 fw[16];
  int bid = blockIdx.x, tid = threadIdx.x;
  {
    unsigned v1, v2;
    u64x8 ka0; u64x2 kb0;
    asm volatile("s_load_dwordx16 %0, %3, 0x0\n\ts_load_dwordx4 %1, %3, 0x40\n\ts_getpc_b64 %2" : "=&{s[36:51]}"(ka0), "=&{s[52:55]}"(kb0), "=&s"(pc) : "s"(__builtin_amdgcn_kernarg_segment_ptr()));
    asm volatile(
               "v_readfirstlane_b32 s14, %20\n\t"
               "s_lshr_b32 s15, %19, 3\n\t"
               "s_cmpk_ge_u32 s14, 0xc0\n\ts_cbranch_scc1 1f\n\t"
               "s_setprio 2\n\t"
               "s_and_b32 s16, %19, 7\n\ts_lshr_b32 s17, %19, 4\n\t"
               "s_and_b32 s18, s15, 1\n\ts_mul_i32 s18, s18, 3\n\ts_lshr_b32 s19, s14, 6\n\ts_add_u32 s18, s18, s19\n\t"
               "v_bfe_u32 %16, %20, 3, 3\n\tv_and_b32 %17, 7, %20\n\t"
               "s_cmp_ge_u32 s17, 5\n\ts_cselect_b32 s12, 17, 16\n\ts_cselect_b32 s13, 5, 0\n\ts_sub_u32 s13, s17, s13\n\t"
               "s_cmp_eq_u32 s18, 5\n\ts_cselect_b32 s17, s12, s17\n\ts_cselect_b32 s18, s13, s18\n\t"
               "s_cmp_ge_u32 s17, 15\n\ts_cselect_b32 s12, 15, 12\n\ts_movk_i32 s13, 0x3000\n\t"
               "s_cmp_lt_u32 s17, 12\n\ts_cselect_b32 s12, 0, s12\n\ts_cselect_b32 s13, s13, 0xc00\n\ts_cselect_b32 s19, 12, 3\n\t"
               "s_mul_i32 s16, s16, s19\n\ts_sub_u32 s12, s17, s12\n\ts_add_u32 s12, s12, s16\n\t"
               "v_mad_u32_u24 %16, %16, s13, %17\n\t"
               "s_mul_i32 s19, s18, s13\n\ts_add_u32 s12, s12, s19\n\ts_lshl_b32 s12, s12, 7\n\t"
               "v_lshlrev_b32 %16, 4, %16\n\t"
               "s_mul_i32 s10, s13, 0x280\n\ts_mov_b32 s11, 0x20000\n\ts_cmp_ge_u32 s18, 5\n\ts_cselect_b32 s10, 0, s10\n\t"
               "s_cmp_ge_u32 s17, 15\n\t"
               "s_waitcnt lgkmcnt(0)\n\t"
               "s_cselect_b64 s[8:9], s[48:49], s[46:47]\n\t"
               "s_cmp_lt_u32 s17, 12\n\ts_cselect_b64 s[8:9], s[42:43], s[8:9]\n\t"
               "s_and_b32 s9, s9, 0xffff\n\t"
               "buffer_load_dwordx4 %0, %16, s[8:11], s12 offen nt\n\t" "s_add_u32 s12, s12, s13\n\t"
               "buffer_load_dwordx4 %1, %16, s[8:11], s12 offen nt\n\t" "s_add_u32 s12, s12, s13\n\t"
               "buffer_load_dwordx4 %2, %16, s[8:11], s12 offen nt\n\t" "s_add_u32 s12, s12, s13\n\t"
               "buffer_load_dwordx4 %3, %16, s[8:11], s12 offen nt\n\t" "s_add_u32 s12, s12, s13\n\t"
               "buffer_load_dwordx4 %4, %16, s[8:11], s12 offen nt\n\t" "s_add_u32 s12, s12, s13\n\t"
               "buffer_load_dwordx4 %5, %16, s[8:11], s12 offen nt\n\t" "s_add_u32 s12, s12, s13\n\t"
               "buffer_load_dwordx4 %6, %16, s[8:11], s12 offen nt\n\t" "s_add_u32 s12, s12, s13\n\t"
               "buffer_load_dwordx4 %7, %16, s[8:11], s12 offen nt\n\t" "s_add_u32 s12, s12, s13\n\t"
               "buffer_load_dwordx4 %8, %16, s[8:11], s12 offen nt\n\t" "s_add_u32 s12, s12, s13\n\t"
               "buffer_load_dwordx4 %9, %16, s[8:11], s12 offen nt\n\t" "s_add_u32 s12, s12, s13\n\t"
               "buffer_load_dwordx4 %10, %16, s[8:11], s12 offen nt\n\t" "s_add_u32 s12, s12, s13\n\t"
               "buffer_load_dwordx4 %11, %16, s[8:11], s12 offen nt\n\t" "s_add_u32 s12, s12, s13\n\t" "s_barrier\n\t"
               "buffer_load_dwordx4 %12, %16, s[8:11], s12 offen nt\n\t" "s_add_u32 s12, s12, s13\n\t"
               "buffer_load_dwordx4 %13, %16, s[8:11], s12 offen nt\n\t" "s_add_u32 s12, s12, s13\n\t"
               "buffer_load_dwordx4 %14, %16, s[8:11], s12 offen nt\n\t" "s_add_u32 s12, s12, s13\n\t"
               "buffer_load_dwordx4 %15, %16, s[8:11], s12 offen nt\n\t"
               "s_branch 2f\n"
               "1:\n\t"
               "v_and_b32 %16, 31, %20\n\tv_lshl_add_u32 %16, %16, 2, %21\n\tv_mov_b32 %17, 0\n\tds_write_b32 %16, %17\n\t"
               "s_cmpk_lg_u32 s14, 0xc0\n\ts_cbranch_scc1 3f\n\t"
               "v_and_b32 %16, 7, %20\n\tv_lshl_add_u32 %16, %16, 5, s15\n\tv_lshlrev_b32 %16, 7, %16\n\tv_min_u32 %16, %24, %16\n\t"
               "global_load_dwordx4 %15, %16, %23\n\t"
               "v_bfe_u32 %17, %20, 5, 1\n\tv_mul_u32_u24 %17, 0x50, %17\n\tv_and_b32 %16, 31, %20\n\tv_add_lshl_u32 %16, %16, %17, 4\n\t"
               "v_and_or_b32 %17, %20, 15, %17\n\tv_lshlrev_b32 %17, 4, %17\n\t"
               "s_waitcnt lgkmcnt(0)\n\t"
               "global_load_dwordx4 %0, %16, s[36:37]\n\tglobal_load_dwordx4 %1, %16, s[36:37] offset:512\n\tglobal_load_dwordx4 %2, %17, s[36:37] offset:1024\n"
               "3:\n\t"
               "s_waitcnt lgkmcnt(0)\n\ts_barrier\n"
               "2:"
               : "=&v"(fw[0]), "=&v"(fw[1]), "=&v"(fw[2]), "=&v"(fw[3]), "=&v"(fw[4]), "=&v"(fw[5]), "=&v"(fw[6]), "=&v"(fw[7]),
                 "=&v"(fw[8]), "=&v"(fw[9]), "=&v"(fw[10]), "=&v"(fw[11]), "=&v"(fw[12]), "=&v"(fw[13]), "=&v"(fw[14]), "=&v"(fw[15]),
                 "=&v"(v1), "=&v"(v2)
               : "{s[36:51]}"(ka0), "s"(bid), "v"(tid), "s"(fbase - 32), "{s[52:55]}"(kb0), "s"(pc), "i"(CODE_BYTES - 128)
               : "s8", "s9", "s10", "s11", "s12", "s13", "s14", "s15", "s16", "s17", "s18", "s19", "scc", "memory");
    asm volatile("" : "={s[36:51]}"(ka), "={s[52:55]}"(kb), "+s"(bid) : "0"(ka0), "1"(kb0));
    asm volatile("" : "+v"(tid));
  }
  const int xq = bid & 7, uq = bid >> 3;
  const int h = xq * 4 + (uq >> 3), j8 = uq & 7, b = j8 >> 2, s = j8 & 3;
  const int lane = tid & 63, wave = __builtin_amdgcn_readfirstlane(tid >> 6);
  const int g = lane >> 3, j = lane & 7;
  const int bl = (lane >> 2) & 1, e4 = lane & 3;
  const bool isS = wave >= 4;
  const u16 *x = (const u16*)ka[0], *kc = (const u16*)ka[1], *vc = (const u16*)ka[2], *wo = (const u16*)ka[4], *wd = (const u16*)ka[7];
  u16* out = (u16*)kb[0];
  unsigned char* ws = (unsigned char*)kb[1];
  u64 tst;
  asm volatile("s_memrealtime %0\n\ts_waitcnt lgkmcnt(0)" : "=s"(tst));
  const unsigned tstart = (unsigned)tst;
  const rsrc_t rws = mk_rsrc(ws, WS_END);

  if (isS) {
    const int sw = wave - 4;
    const rsrc_t r0 = mk_rsrc(kc, 2 * 32 * 8192 * 384), rv = mk_rsrc(vc, 2 * 32 * 8192 * 384);
    const int voff = ((b * 32 + h) * 8192 + 6144 + 32 * s + 8 * sw + g) * 384 + 16 * j;
    u32x4 buf[96];
    u32x4 qv[3];
    float sc[16];
    unsigned pd[16];
    f32x4 acc[3] = {{0.f, 0.f, 0.f, 0.f}, {0.f, 0.f, 0.f, 0.f}, {0.f, 0.f, 0.f, 0.f}};
    unsigned msk[4];
#pragma unroll
    for (int d = 0; d < 4; ++d) msk[d] = (j >> 1) == d ? ((j & 1) ? 0xffff0000u : 0x0000ffffu) : 0u;
    constexpr float C2 = 0.07216878364870322f * 1.4426950408889634f;
        const int stag = ((2 * (uq >> 2) - 7) * STAG) >> 1;
    unsigned tprev = tstart + S_DELAY + stag, tend = tstart + T_PLAN + stag, lat = 0;

    auto consume = [&](auto G) {
      constexpr int t = decltype(G)::value;
      if constexpr (t < 16) {
        float a0 = 0.f, a1 = 0.f, a2 = 0.f;
#pragma unroll
        for (int e = 0; e < 4; ++e) { a0 = dot2(buf[3 * t][e], qv[0][e], a0); a1 = dot2(buf[3 * t + 1][e], qv[1][e], a1); a2 = dot2(buf[3 * t + 2][e], qv[2][e], a2); }
        float a = (a0 + a1) + a2;
        a += dpp<XOR1>(a); a += dpp<XOR2>(a); a += dpp<HMIR>(a);
        sc[t] = a;
        if constexpr (t == 15) {
          float m = sc[0];
#pragma unroll
          for (int u = 1; u < 16; ++u) m = fmaxf(m, sc[u]);
          m = fmaxf(m, dpp<ROR8>(m)); m = x16_max(m); m = x32_max(m);
          const float mc = m * C2;
          float l = 0.f;
#pragma unroll
          for (int u = 0; u < 16; ++u) { const float p = __builtin_amdgcn_exp2f(fmaf(sc[u], C2, -mc)); pd[u] = cvtpk(p, p); l += bfhi(pd[u]); }
          l += dpp<ROR8>(l); l = x16_sum(l); l = x32_sum(l);
          if (lane == 0) { *(f32x2*)(l_ml + 2 * sw) = f32x2{mc, l}; MEMFENCE(); l_f[FA + sw] = 1; }
        }
      } else {
        constexpr int u = t - 16;
        const u32x4 a = {pd[u] & msk[0], pd[u] & msk[1], pd[u] & msk[2], pd[u] & msk[3]};
#pragma unroll
        for (int i = 0; i < 3; ++i) acc[i] = mfma32(a, buf[3 * t + i], acc[i]);
      }
    };
    static_for<0, 16>([&](auto K) {
      constexpr int k = decltype(K)::value;
      if constexpr (k > 6) {
        MEMFENCE();
        unsigned tv = *l_tend;
        asm volatile("" : "+v"(tv));
        tv = __builtin_amdgcn_readfirstlane(tv);
        tend = tv ? tv + stag : tend;
      }
      unsigned tdone = tprev + LAT0;
      const unsigned now = pace_step<(k ? 65536 / (16 - k) : 0)>(tprev, tend, tdone);
      SB();
#pragma unroll
      for (int i = 0; i < 6; ++i) { buf[6 * k + i] = ld_nt(k < 8 ? r0 : rv, voff + (i % 3) * 128, ((2 * k + i / 3) & 15) * 49152); SB(); }
      if constexpr (k == 15) { if (lane == 0) l_f[FB + sw] = 1; }
      if constexpr (k >= 1 && k < 15) asm volatile("s_waitcnt vmcnt(6)" ::: "memory");
      if constexpr (k >= 2 && k < 13) {
        if (tdone == now) tdone = (unsigned)__builtin_amdgcn_s_memrealtime();
        lat = (lat << 16) | (tdone - tprev);
        *(u32x2*)(l_est + 2 * sw) = u32x2{(now - stag + ((6 * (15 - k) * RATE16) >> 4)) | 1u, lat};
      }
      tprev = now;
      if constexpr (k == QK) {
        wait_lds<0>(fbase + FG, ALL4);
#pragma unroll
        for (int i = 0; i < 3; ++i) qv[i] = *(const u32x4*)(l_q + 32 * i + 4 * j);
      }
      SB();
      constexpr auto upto = [](int kk) constexpr { if (kk < QK) return 0; const int c = CU * (kk - QK + 1), l = kk < 15 ? 2 * kk : 28; return c < l ? c : l; };
      static_for<upto(k - 1), upto(k)>([&](auto G) { consume(G); SB(); });
      SB();
    });
    __builtin_amdgcn_s_setprio(3);
    wait_lds<0>(fbase + FE, ALL4);
    float cq = l_c[lane >> 4];
    unsigned dst = (unsigned)(size_t)(lfloat*)(l_acc + (sw * 2 + (g & 1)) * 192 + 8 * j + 4 * ((lane >> 4) & 1));
    unsigned src = (unsigned)(size_t)(lfloat*)(l_acc + (lane >> 4) * 384 + 64 * sw + 8 * j + 4 * (g & 1));
    unsigned fla = fbase + FC + sw, one = 1u, flr = fbase + FC;
    int pvo = (lane & 16) ? OOB : (g & 1) * 128 + j * 16 + (lane >> 5) * 8;
    int pso = WS_OP + h * OP_STRIDE + (b * 4 + s) * 768 + sw * 256;
    unsigned tagv = *l_tag;
    asm volatile("" : "+v"(cq), "+v"(dst), "+v"(src), "+v"(fla), "+v"(flr), "+v"(one), "+v"(pvo), "+s"(pso), "+v"(tagv));
    const bool wr = (lane >> 5) == (g & 1);
    SB();
    static_for<28, 32>([&](auto G) { consume(G); SB(); });
    if (wr) {
#pragma unroll
      for (int i = 0; i < 3; ++i) *(lf32x4*)(size_t)(dst + 256 * i) = acc[i];
      MEMFENCE();
      *(lbyte*)(size_t)fla = (unsigned char)one;
    }
    if (sw < 3) {
      f32x4 u0, u1;
      unsigned fv, fs;
      asm volatile("1:\n\tds_read_b32 %0, %4\n\tds_read_b128 %2, %5\n\tds_read_b128 %3, %5 offset:768\n\ts_waitcnt lgkmcnt(2)\n\tv_readfirstlane_b32 %1, %0\n\t"
                   "s_cmp_lg_u32 %1, 0x01010101\n\ts_waitcnt lgkmcnt(0)\n\ts_cbranch_scc1 1b"
                   : "=&v"(fv), "=&s"(fs), "=&v"(u0), "=&v"(u1) : "v"(flr), "v"(src) : "scc", "memory");
      float o[4];
#pragma unroll
      for (int e = 0; e < 4; ++e) o[e] = cq * (u0[e] + u1[e]);
      const auto s0 = __builtin_amdgcn_permlane32_swap(__float_as_uint(o[0]), __float_as_uint(o[2]), false, false);
      const auto s1 = __builtin_amdgcn_permlane32_swap(__float_as_uint(o[1]), __float_as_uint(o[3]), false, false);
      const float p0 = x16_sum(__uint_as_float(s0[0]) + __uint_as_float(s0[1])), p1 = x16_sum(__uint_as_float(s1[0]) + __uint_as_float(s1[1]));
      __builtin_amdgcn_raw_buffer_store_b64(u32x2{cvtpk(p0, p1), tagv}, rws, pvo, pso, AUX_SC1);
    }
    MEMFENCE();
    *(lbyte*)(size_t)(fla + (FD - FC)) = (unsigned char)one;
    return;
  }

  __builtin_amdgcn_s_setprio(2);
  unsigned tag, ep;
  asm volatile("s_load_dword %0, %1, 0x0" : "=&s"(ep) : "s"(ws));
#define TAG_READY() asm volatile("s_waitcnt lgkmcnt(0)\n\ts_add_u32 %0, %1, 1\n\ts_addc_u32 %0, %0, 0" : "=&s"(tag) : "s"(ep) : "scc", "memory")
  const int w = wave;
  const bool second = uq & 1, single = second && w == 2;
  const unsigned c0 = uq >> 1, uc = single ? (c0 >= 5 ? 17 : 16) : c0, ur = single ? (c0 >= 5 ? c0 - 5 : c0) : 3 * second + w;
  const bool has_front = (w < 3) & (ur < 5);
  const int st_col = (g & 1) ? OOB : (lane >> 5) * 128 + j * 16;
  const rsrc_t rx = mk_rsrc(x, 2560);
  const unsigned sLo = (e4 & 1) ? 0x01000c0cu : 0x0c0c0100u, sHi = (e4 & 1) ? 0x03020c0cu : 0x0c0c0302u;
  unsigned sel0 = e4 < 2 ? sLo : 0x0c0c0c0cu, sel1 = e4 < 2 ? 0x0c0c0c0cu : sLo, sel2 = e4 < 2 ? sHi : 0x0c0c0c0cu, sel3 = e4 < 2 ? 0x0c0c0c0cu : sHi;
  if (w == 3) {
    const int xo = (lane >> 5) * 1280 + (lane & 31) * 16;
    const bool x3 = (lane & 16) == 0;
    asm volatile("s_waitcnt vmcnt(0)" : "+v"(fw[0]), "+v"(fw[1]), "+v"(fw[2]) : "v"(fw[15]));
    u32x4 xv[3] = {fw[0], fw[1], x3 ? fw[2] : u32x4{0u, 0u, 0u, 0u}};
    float v[24];
#pragma unroll
    for (int e = 0; e < 12; ++e) { v[2 * e] = bflo(xv[e >> 2][e & 3]); v[2 * e + 1] = bfhi(xv[e >> 2][e & 3]); }
    float sum = 0.f;
#pragma unroll
    for (int e = 0; e < 24; ++e) sum += v[e];
    sum = x16_sum(row_sum(sum));
    const float mean = sum * (1.f / 640.f);
    float sq = 0.f, sq3 = 0.f;
#pragma unroll
    for (int e = 0; e < 16; ++e) { const float d = v[e] - mean; sq = fmaf(d, d, sq); }
#pragma unroll
    for (int e = 16; e < 24; ++e) { const float d = v[e] - mean; sq3 = fmaf(d, d, sq3); }
    sq = x16_sum(row_sum(sq + (x3 ? sq3 : 0.f)));
    const float mb = rbf(mean), den = rbf(__builtin_amdgcn_sqrtf(rbf(rbf(sq * (1.f / 640.f)) + 1e-5f)));
    const float inv = __builtin_amdgcn_rcpf(den);
#pragma unroll
    for (int e = 0; e < 12; ++e) xv[e >> 2][e & 3] = cvtpk(rbf(v[2 * e] - mb) * inv, rbf(v[2 * e + 1] - mb) * inv);
    *(u32x4*)(l_xn + xo) = xv[0];
    *(u32x4*)(l_xn + xo + 512) = xv[1];
    if (x3) *(u32x4*)(l_xn + xo + 1024) = xv[2];
    MEMFENCE();
    l_f[FX] = 1;
  }
  if (has_front) {
    wait_lds<1>(fbase + FX, 1u);
    const u32x4 xr[2] = {*(const u32x4*)(l_xn + bl * 1280 + 256 * ur + g * 32), *(const u32x4*)(l_xn + bl * 1280 + 256 * ur + g * 32 + 16)};
    TAG_READY();
    f32x4 acc0 = {0.f, 0.f, 0.f, 0.f}, acc1 = {0.f, 0.f, 0.f, 0.f};
    asm volatile("s_waitcnt vmcnt(4)" : "+v"(fw[0]), "+v"(fw[1]), "+v"(fw[2]), "+v"(fw[3]), "+v"(fw[4]), "+v"(fw[5]), "+v"(fw[6]), "+v"(fw[7]),
                 "+v"(fw[8]), "+v"(fw[9]), "+v"(fw[10]), "+v"(fw[11]));
#pragma unroll
    for (int n = 0; n < 16; ++n) {
      if (n == 12) { SB(); asm volatile("s_waitcnt vmcnt(0)" : "+v"(fw[12]), "+v"(fw[13]), "+v"(fw[14]), "+v"(fw[15])); }
      const unsigned xw = xr[n >> 3][(n >> 1) & 3];
      const unsigned a0 = __builtin_amdgcn_perm(0u, xw, (n & 1) ? sel2 : sel0), a1 = __builtin_amdgcn_perm(0u, xw, (n & 1) ? sel3 : sel1);
      acc0 = mfma16(a0, a1, fw[n][0], fw[n][1], acc0);
      acc1 = mfma16(a0, a1, fw[n][2], fw[n][3], acc1);
    }
    f32x4 r = proj_epilogue(acc0, acc1);
    f32x4* l_u = (f32x4*)l_acc;
    if (w == 0) {
      wait_lds<1>(fbase + FH, second ? 0x0100u : 0x010100u);
      r += l_u[lane];
      if (!second) r += l_u[64 + lane];
    } else if (!single) {
      l_u[(w - 1) * 64 + lane] = r;
      MEMFENCE();
      l_f[FH + w] = 1;
    }
    if (w == 0 || single)
      __builtin_amdgcn_raw_buffer_store_b128(granules(r, tag), rws, ((lane >> 4) & 1) * 256 + st_col, WS_FP + ((xq * 18 + uc) * 5 + (single ? ur : second)) * 512, AUX_SC1);
    asm volatile("s_waitcnt vmcnt(0)" ::: "memory");
  } else {
    asm volatile("s_waitcnt vmcnt(0)" ::"v"(fw[0]), "v"(fw[1]), "v"(fw[2]), "v"(fw[3]), "v"(fw[4]), "v"(fw[5]), "v"(fw[6]), "v"(fw[7]),
                 "v"(fw[8]), "v"(fw[9]), "v"(fw[10]), "v"(fw[11]), "v"(fw[12]), "v"(fw[13]), "v"(fw[14]), "v"(fw[15]));
    TAG_READY();
  }
  const int slot = w * 8 + j8;
  const bool has_back = slot < 30;
  const int t3 = slot / 10, cc = slot - 10 * t3;
  const int rid = xq * 20 + uq;
  const int rb = rid / 80, rc = (rid - 80 * rb) >> 3, ro = rid & 7;
  const bool reducer = (w == 0) && (bid < 160);
  unsigned ha[2][2] = {{0u, 0u}, {0u, 0u}};
  unsigned tend = tstart + T_PLAN;
  auto pace_round = [&](unsigned t_sample, unsigned t_read, int so, int dead, int gain16, int tdead, int trend, int keep16) {
    wait_until(t_sample);
    u32x4 e, f;
    do { MEMFENCE(); e = *(const u32x4*)l_est; f = *(const u32x4*)(l_est + 4); } while (!__builtin_amdgcn_readfirstlane(e[0] & e[2] & f[0] & f[2] & 1u));
    const u32x4 pv = {tstart + (unsigned)((int)((e[0] - tstart) + (e[2] - tstart) + (f[0] - tstart) + (f[2] - tstart)) >> 2), tag, e[1] + e[3] + f[1] + f[3], tag};
    __builtin_amdgcn_raw_buffer_store_b128(pv, rws, (lane == 0 && j8 == (((uq >> 3) * 5) & 7)) ? ((uq >> 3) * 8 + xq) * 16 : OOB, so, AUX_SC1);
    wait_until(t_read);
    MEMFENCE();
    const u32x4 tv = ld_sc1(rws, (lane & 31) * 16, so);
    if (__all((tv[1] == tag) & (tv[3] == tag))) {
      const float d = x16_sum(row_sum((float)(int)(tv[0] - tstart))) * (1.f / 32.f);
      const float l1 = x16_sum(row_sum((float)(tv[2] & 0xffffu))) * (1.f / 128.f), l0 = x16_sum(row_sum((float)(tv[2] >> 16))) * (1.f / 128.f);
      const float dr = l1 - (float)LAT0, sign = l1 - l0;
      int di = (int)(d + fmaxf(dr, 0.f) + fmaxf(dr - (float)dead, 0.f) * (float)gain16 * (1.f / 16.f) + fmaxf(sign - (float)tdead, 0.f) * (float)trend);
      const int dold = (int)(tend - tstart);
      if (keep16 && di < dold) di += ((dold - di) * keep16) >> 4;
      di = di < 0 ? 0 : (di > 2500 ? 2500 : di);
      tend = (tstart + __builtin_amdgcn_readfirstlane(di)) | 1u;
      *l_tend = tend;
    }
  };
  if (w == 0) {
    wait_until(tstart + Q_AT);
    u32x2 t[3];
    const int vo = (lane >> 5) * 512 + (lane & 31) * 8, so = WS_FP + (xq * 18 + (h & 3) * 3) * 2560 + b * 256;
    for (unsigned spin = 0;; ++spin) {
      MEMFENCE();
      t[0] = ld2_sc1(rws, vo, so); t[1] = ld2_sc1(rws, vo, so + 2560); t[2] = ld2_sc1(rws, vo, so + 5120);
      if (__all((t[0][1] == tag) & (t[1][1] == tag) & (t[2][1] == tag)) || spin > SPIN_CAP) break;
    }
#pragma unroll
    for (int i = 0; i < 3; ++i) l_q[32 * i + ((lane >> 1) & 7) * 4 + ((lane >> 4) & 1) * 2 + (lane & 1)] = cvtpk(x32_sum(bflo(t[i][0])), x32_sum(bfhi(t[i][0])));
    *l_tag = tag;
    MEMFENCE();
  }
  l_f[FG + w] = 1;
  if (w == 3) {
    pace_round(tstart + SMP0, tstart + SMP0 + WAIT0, WS_TS, DEAD0, GAIN0, TDEAD0, TREND0, 0);
  }
  if (has_back) {
    wait_until(tstart + GU_AT);
    u32x2 t[2];
    const int f16 = (h & 3) * 3 + t3;
    const int so = WS_FP + (xq * 18 + 12 + (f16 >> 2)) * 2560 + (f16 & 3) * 32;
    const int vo = bl * 256 + ((g >> 1) & 1) * 128 + (g >> 2) * 16 + (g & 1) * 8;
    const bool more = (f16 >= 4) & (e4 < 3);
    for (unsigned spin = 0;; ++spin) {
      MEMFENCE();
      t[0] = ld2_sc1(rws, vo + (e4 >> 1) * 7680 + (e4 & 1) * 512, so);
      t[1] = ld2_sc1(rws, more ? vo + 7680 + (2 + e4) * 512 : OOB, so);
      if (__all((t[0][1] == tag) & ((t[1][1] == tag) | !more)) || spin > SPIN_CAP) break;
    }
#pragma unroll
    for (int z = 0; z < 2; ++z) {
      const float a = z ? bfhi(t[0][0]) : bflo(t[0][0]), c = z ? bfhi(t[1][0]) : bflo(t[1][0]);
      const float gg = rbf(quad_sum(e4 < 2 ? a : 0.f));
      const float uu = rbf(quad_sum((e4 < 2 ? 0.f : a) + c));
      const float zz = 0.7978845608028654f * (gg + 0.044715f * gg * gg * gg);
      const float ge = rbf(gg * __builtin_amdgcn_rcpf(1.f + __builtin_amdgcn_exp2f(-2.8853900817779268f * zz)));
      const float hv = ge * uu;
      const unsigned d = cvtpk(hv, hv);
      ha[z][0] = __builtin_amdgcn_perm(0u, d, sel0); ha[z][1] = __builtin_amdgcn_perm(0u, d, sel1);
    }
  }
  const int oc = 16 * (ro & 3) + 8 * (lane & 1) + 4 * (ro >> 2) + 2 * (lane >> 5) + ((lane >> 4) & 1);
  const rsrc_t rout = mk_rsrc(out, 2560);
  int out_vo = (lane & 14) == 0 ? oc * 2 + rb * 1280 + rc * 128 : OOB;
  float xres = 0.f;
  if (reducer) {
    xres = bflo(__builtin_amdgcn_raw_buffer_load_b16(rx, oc * 2, rb * 1280 + rc * 128, 0));
    __builtin_amdgcn_raw_buffer_store_b16((unsigned short)0, rout, out_vo, 0, AUX_SC1);
  }
  if (w == 1) {
    wait_lds<4>(fbase + FA, ALL4);
    const int so = WS_ML + (h * 2 + b) * 256;
    if (lane < 4) { const f32x2 ml = *(const f32x2*)(l_ml + 2 * lane); st_pair(rws, lane * 16, so + s * 64, ml[0], ml[1], tag); }
    asm volatile("s_waitcnt vmcnt(0)" ::: "memory");
    __builtin_amdgcn_s_sleep(32);
    u32x4 t;
    for (unsigned spin = 0;; ++spin) {
      MEMFENCE();
      t = ld_sc1(rws, (lane & 15) * 16, so);
      if (__all((t[1] == tag) & (t[3] == tag)) || spin > SPIN_CAP) break;
    }
    const float mv = __uint_as_float(t[0]);
    const float M = row_max(mv);
    const float f = __builtin_amdgcn_exp2f(mv - M);
    const float L = row_sum(f * __uint_as_float(t[2]));
    if ((lane >> 2) == s) { l_c[lane & 3] = f / L; MEMFENCE(); l_f[FE + (lane & 3)] = 1; }
  }
  if (w == 3) {
    pace_round(tend - SMP1, tend - SMP1 + WAIT1, WS_TS + 512, DEAD1, GAIN1, TDEAD1, TREND1, KEEP1);
  }
  if (has_back) {
    int op_so = WS_OP + h * OP_STRIDE + t3 * 256, op_vo = (bl * 4 + e4) * 768 + 16 * g;
    int d_vo = ((48 * h + 16 * t3 + 2 * g) * 640 + 64 * cc) * 2 + 16 * j, o_vo = ((192 * h + 64 * t3 + 8 * g) * 640 + 64 * cc) * 2 + 16 * j;
    int yvo = ((lane >> 4) & 1) * 256 + st_col, yso = WS_YP + ((h * 3 + t3) * 10 + cc) * 512;
    int r_vo = (lane >> 1) * 5120 + (lane & 1) * 16 + WS_YP + rc * 512 + rb * 256 + ro * 32;
    unsigned tagv = tag, fd = fbase + FD;
    asm volatile("" : "+v"(sel0), "+v"(sel1), "+v"(sel2), "+v"(sel3), "+s"(op_so), "+v"(op_vo), "+v"(d_vo), "+v"(o_vo), "+v"(yvo), "+s"(yso), "+v"(r_vo), "+v"(out_vo), "+v"(tagv), "+v"(xres), "+v"(fd));
    const rsrc_t rd = mk_rsrc(wd, 1536 * 1280), ro_ = mk_rsrc(wo, 6144 * 1280);
    wait_lds<1>(fbase + FB, ALL4);
    {
      unsigned tf = *l_tend;
      asm volatile("" : "+v"(tf));
      tf = __builtin_amdgcn_readfirstlane(tf);
      const unsigned tn = (unsigned)__builtin_amdgcn_s_memrealtime();
      int wt = tf ? (int)(tf + BACK_AT - tn) : 0;
      asm("s_max_i32 %0, %0, %1\n\ts_min_i32 %0, %0, 0xc8" : "+s"(wt) : "s"(BACK_DELAY) : "scc");
      wait_until(tn + wt);
    }
    u32x4 wb[10], t0, t1;
    unsigned fdv = 0;
#pragma unroll
    for (int n = 0; n < 10; ++n) {
      if (n == POLLPOS) {
        const rsrc_t rp = mk_rsrc(ws, __builtin_amdgcn_readfirstlane(fdv) == ALL4 ? WS_END : 0);
        t0 = ld_sc1(rp, op_vo, op_so); t1 = ld_sc1(rp, op_vo + 128, op_so);
        SB();
      }
      if (n < 2) wb[8 + n] = ld_nt(rd, d_vo + n * 1280, 0); else wb[n - 2] = ld_nt(ro_, o_vo, (n - 2) * 1280);
      if (n == POLLPOS - 1) { MEMFENCE(); fdv = *(luint*)(size_t)fd; }
      SB();
      if (n < 9) __builtin_amdgcn_s_sleep(BPACE);
      SB();
    }
    u32x4 bq[10];
#define PAIR(p) do { bq[2 * (p)] = u32x4{wb[2 * (p)][0], wb[2 * (p)][1], wb[2 * (p) + 1][0], wb[2 * (p) + 1][1]}; \
                     bq[2 * (p) + 1] = u32x4{wb[2 * (p)][2], wb[2 * (p)][3], wb[2 * (p) + 1][2], wb[2 * (p) + 1][3]}; } while (0)
    PAIR(4); PAIR(0); PAIR(1); PAIR(2);
    const u32x4 hA = {ha[0][0], ha[0][1], ha[1][0], ha[1][1]};
    f32x4 acc0 = mfma32(hA, bq[8], f32x4{0.f, 0.f, 0.f, 0.f}), acc1 = mfma32(hA, bq[9], f32x4{0.f, 0.f, 0.f, 0.f});
    asm volatile("" : "+v"(bq[0]), "+v"(bq[1]), "+v"(bq[2]), "+v"(bq[3]), "+v"(bq[4]), "+v"(bq[5]), "+v"(acc0), "+v"(acc1));
    SB();
    unsigned np1 = 1;
    if (__builtin_expect(__builtin_amdgcn_ballot_w64((((t0[1] ^ tagv) | (t0[3] ^ tagv)) | ((t1[1] ^ tagv) | (t1[3] ^ tagv))) != 0u) != 0, 0)) {
      do {
        ++np1;
        MEMFENCE();
        if (__builtin_amdgcn_readfirstlane(*(luint*)(size_t)fd) != ALL4) { wait_lds<1>(fd, ALL4); delay_ticks(LATE_DELAY); }
        t0 = ld_sc1(rws, op_vo, op_so); t1 = ld_sc1(rws, op_vo + 128, op_so);
      } while (!__all((t0[1] == tag) & (t0[3] == tag) & (t1[1] == tag) & (t1[3] == tag)) && np1 < SPIN_CAP);
    }
    unsigned pk[4];
    {
      float u0, u1, u2, u3, u4, u5, u6, u7;
#define DPPADD(r, P) "v_add_f32_dpp " r ", " r ", " r " quad_perm:" P " row_mask:0xf bank_mask:0xf\n\t"
#define DPP8(P) DPPADD("%4", P) DPPADD("%5", P) DPPADD("%6", P) DPPADD("%7", P) DPPADD("%8", P) DPPADD("%9", P) DPPADD("%10", P) DPPADD("%11", P)
      asm volatile("v_lshlrev_b32 %4, 16, %12\n\tv_and_b32 %5, 0xffff0000, %12\n\tv_lshlrev_b32 %6, 16, %13\n\tv_and_b32 %7, 0xffff0000, %13\n\t"
                   "v_lshlrev_b32 %8, 16, %14\n\tv_and_b32 %9, 0xffff0000, %14\n\tv_lshlrev_b32 %10, 16, %15\n\tv_and_b32 %11, 0xffff0000, %15\n\t"
                   DPP8("[1,0,3,2]") DPP8("[2,3,0,1]")
                   "v_cvt_pk_bf16_f32 %0, %4, %5\n\tv_cvt_pk_bf16_f32 %1, %6, %7\n\tv_cvt_pk_bf16_f32 %2, %8, %9\n\tv_cvt_pk_bf16_f32 %3, %10, %11"
                   : "=&v"(pk[0]), "=&v"(pk[1]), "=&v"(pk[2]), "=&v"(pk[3]), "=&v"(u0), "=&v"(u1), "=&v"(u2), "=&v"(u3), "=&v"(u4), "=&v"(u5), "=&v"(u6), "=&v"(u7)
                   : "v"(t0[0]), "v"(t0[2]), "v"(t1[0]), "v"(t1[2]));
    }
    u32x4 aq[4];
#pragma unroll
    for (int p = 0; p < 4; ++p)
      aq[p] = u32x4{__builtin_amdgcn_perm(0u, pk[p], sel0), __builtin_amdgcn_perm(0u, pk[p], sel1), __builtin_amdgcn_perm(0u, pk[p], sel2), __builtin_amdgcn_perm(0u, pk[p], sel3)};
#pragma unroll
    for (int p = 0; p < 3; ++p) { acc0 = mfma32(aq[p], bq[2 * p], acc0); acc1 = mfma32(aq[p], bq[2 * p + 1], acc1); }
    asm volatile("" : "+v"(aq[3]), "+v"(acc0), "+v"(acc1));
    SB();
    acc0 = mfma16(aq[3][0], aq[3][1], wb[6][0], wb[6][1], acc0); acc1 = mfma16(aq[3][0], aq[3][1], wb[6][2], wb[6][3], acc1);
    acc0 = mfma16(aq[3][2], aq[3][3], wb[7][0], wb[7][1], acc0); acc1 = mfma16(aq[3][2], aq[3][3], wb[7][2], wb[7][3], acc1);
    __builtin_amdgcn_raw_buffer_store_b128(granules(proj_epilogue(acc0, acc1), tagv), rws, yvo, yso, AUX_SC1);
    if (reducer) {
      delay_ticks(FIN_DELAY);
      float y0, y1, y2, y3;
      unsigned np2 = 0;
      for (;;) {
        MEMFENCE();
        const u32x4 a = ld_sc1(rws, r_vo, 0), c = ld_sc1(rws, r_vo, 32 * 5120), d = ld_sc1(rws, r_vo, 64 * 5120);
        unsigned bad = ((a[1] ^ tagv) | (a[3] ^ tagv)) | ((c[1] ^ tagv) | (c[3] ^ tagv));
        y0 = bflo(a[0]) + bflo(c[0]); y1 = bfhi(a[0]) + bfhi(c[0]); y2 = bflo(a[2]) + bflo(c[2]); y3 = bfhi(a[2]) + bfhi(c[2]);
        asm volatile("" : "+v"(bad), "+v"(y0), "+v"(y1), "+v"(y2), "+v"(y3));
        bad |= (d[1] ^ tagv) | (d[3] ^ tagv);
        y0 += bflo(d[0]); y1 += bfhi(d[0]); y2 += bflo(d[2]); y3 += bfhi(d[2]);
        if (__builtin_expect(__builtin_amdgcn_ballot_w64(bad != 0u) == 0, 1)) break;
        if (++np2 > SPIN_CAP) break;
      }
      const auto s0 = __builtin_amdgcn_permlane32_swap(__float_as_uint(y0), __float_as_uint(y2), false, false);
      const auto s1 = __builtin_amdgcn_permlane32_swap(__float_as_uint(y1), __float_as_uint(y3), false, false);
      const float z0 = __uint_as_float(s0[0]) + __uint_as_float(s0[1]), z1 = __uint_as_float(s1[0]) + __uint_as_float(s1[1]);
      const auto s2 = __builtin_amdgcn_permlane16_swap(__float_as_uint(z0), __float_as_uint(z1), false, false);
      float y = __uint_as_float(s2[0]) + __uint_as_float(s2[1]);
      y += dpp<ROR8>(y); y += dpp<ROR4>(y); y += dpp<XOR2>(y);
      __builtin_amdgcn_raw_buffer_store_b16((unsigned short)cvtpk(xres + y, 0.f), rout, out_vo, 0, AUX_SC1);
      if (bid == 0) __builtin_amdgcn_raw_buffer_store_b32(tagv, rws, lane == 0 ? WS_EPOCH : OOB, 0, AUX_SC1);
    }
  }
}

extern "C" void kernel_launch(void* const* d_in, const int* in_sizes, int n_in, void* d_out, int out_size, void* d_ws, size_t ws_size,
                              hipStream_t stream) {
  fused_decode<<<256, 512, 0, stream>>>((const u16*)d_in[0], (const u16*)d_in[1], (const u16*)d_in[2], (const u16*)d_in[3], (const u16*)d_in[4],
                                        (const u16*)d_in[6], (const u16*)d_in[7], (const u16*)d_in[8], (u16*)d_out, (unsigned char*)d_ws);
}
```

```cpp
#include <hip/hip_runtime.h>
#include <hip/hip_bf16.h>
#include <stdint.h>
#include <type_traits>

typedef unsigned short u16;
typedef unsigned u32x4 __attribute__((ext_vector_type(4)));
typedef unsigned u32x2 __attribute__((ext_vector_type(2)));
typedef float f32x4 __attribute__((ext_vector_type(4)));
typedef float f32x2 __attribute__((ext_vector_type(2)));
typedef short s16x4 __attribute__((ext_vector_type(4)));
typedef __bf16 bf16x8 __attribute__((ext_vector_type(8)));
typedef __bf16 bf16x2 __attribute__((ext_vector_type(2)));
typedef __amdgpu_buffer_rsrc_t rsrc_t;

constexpr int QK = 6, CU = 6;
constexpr int STAG = 10;
constexpr int BPACE = 5;
constexpr int POLLPOS = 8;
constexpr int KEEP1 = 10;
constexpr int S_DELAY = 156;
constexpr int T_PLAN = 1396;
constexpr int RATE16 = 215;
constexpr int LAT0 = 79;
constexpr int SMP0 = 500, WAIT0 = 195, DEAD0 = 13, GAIN0 = 58;
constexpr int SMP1 = 385, WAIT1 = 135, DEAD1 = 7, GAIN1 = 52;
constexpr int TDEAD0 = 0, TREND0 = 0, TDEAD1 = 1, TREND1 = 11;
constexpr int BACK_AT = 70;
constexpr int BACK_DELAY = 12;
constexpr int LATE_DELAY = 20;
constexpr int FIN_DELAY = 48;
constexpr int CODE_BYTES = 16256;

constexpr int AUX_NT = 2, AUX_SC1 = 16;
constexpr int RSRC_W3 = 0x00020000;
constexpr unsigned SPIN_CAP = 1u << 18;
constexpr int OOB = 0x40000000;
constexpr unsigned ALL4 = 0x01010101u;

constexpr int WS_EPOCH = 0;
constexpr int WS_TS = 1024;
constexpr int WS_QP = 4096;
constexpr int WS_GU = WS_QP + 32 * 5 * 2 * 96 * 8;
constexpr int WS_ML = WS_GU + 8 * 5 * 2 * 2 * 96 * 8;
constexpr int WS_YP = WS_ML + 32 * 2 * 4 * 4 * 16;
constexpr int WS_OP = 2 << 20, OP_STRIDE = 1 << 20;
constexpr int WS_END = WS_OP + 32 * OP_STRIDE;
static_assert(WS_YP + 96 * 10 * 2 * 32 * 8 <= WS_OP, "workspace map");

template <int I, int N, class F> __device__ __forceinline__ void static_for(F&& f) {
  if constexpr (I < N) { f(std::integral_constant<int, I>{}); static_for<I + 1, N>(f); }
}
__device__ __forceinline__ rsrc_t mk_rsrc(const void* p, int bytes) {
  return __builtin_amdgcn_make_buffer_rsrc(const_cast<void*>(p), 0, bytes, RSRC_W3);
}
__device__ __forceinline__ u32x4 ld_nt(rsrc_t r, int voff, int soff) { return __builtin_amdgcn_raw_buffer_load_b128(r, voff, soff, AUX_NT); }
__device__ __forceinline__ u32x4 ld_sc1(rsrc_t r, int voff, int soff) { return __builtin_amdgcn_raw_buffer_load_b128(r, voff, soff, AUX_SC1); }
__device__ __forceinline__ u32x2 ld2_sc1(rsrc_t r, int voff, int soff) { return __builtin_amdgcn_raw_buffer_load_b64(r, voff, soff, AUX_SC1); }
__device__ __forceinline__ void st_pair(rsrc_t r, int voff, int soff, float a, float b, unsigned tag) {
  u32x4 v = {__float_as_uint(a), tag, __float_as_uint(b), tag};
  __builtin_amdgcn_raw_buffer_store_b128(v, r, voff, soff, AUX_SC1);
}
__device__ __forceinline__ float bflo(unsigned v) { return __uint_as_float(v << 16); }
__device__ __forceinline__ float bfhi(unsigned v) { return __uint_as_float(v & 0xffff0000u); }
__device__ __forceinline__ unsigned cvtpk(float lo, float hi) {
  bf16x2 r = {(__bf16)lo, (__bf16)hi};
  return __builtin_bit_cast(unsigned, r);
}
__device__ __forceinline__ float rbf(float x) { return (float)(__bf16)x; }
template <int CTRL> __device__ __forceinline__ float dpp(float x) {
  return __builtin_bit_cast(float, __builtin_amdgcn_mov_dpp(__builtin_bit_cast(int, x), CTRL, 0xf, 0xf, true));
}
constexpr int XOR1 = 0xB1, XOR2 = 0x4E, HMIR = 0x141, ROR8 = 0x128, ROR4 = 0x124;
__device__ __forceinline__ float x16_sum(float x) { auto s = __builtin_amdgcn_permlane16_swap(__float_as_uint(x), __float_as_uint(x), false, false); return __uint_as_float(s[0]) + __uint_as_float(s[1]); }
__device__ __forceinline__ float x32_sum(float x) { auto s = __builtin_amdgcn_permlane32_swap(__float_as_uint(x), __float_as_uint(x), false, false); return __uint_as_float(s[0]) + __uint_as_float(s[1]); }
__device__ __forceinline__ float x16_max(float x) { auto s = __builtin_amdgcn_permlane16_swap(__float_as_uint(x), __float_as_uint(x), false, false); return fmaxf(__uint_as_float(s[0]), __uint_as_float(s[1])); }
__device__ __forceinline__ float x32_max(float x) { auto s = __builtin_amdgcn_permlane32_swap(__float_as_uint(x), __float_as_uint(x), false, false); return fmaxf(__uint_as_float(s[0]), __uint_as_float(s[1])); }
__device__ __forceinline__ float row_sum(float x) { x += dpp<XOR1>(x); x += dpp<XOR2>(x); x += dpp<HMIR>(x); x += dpp<ROR8>(x); return x; }
__device__ __forceinline__ float row_max(float x) { x = fmaxf(x, dpp<XOR1>(x)); x = fmaxf(x, dpp<XOR2>(x)); x = fmaxf(x, dpp<HMIR>(x)); x = fmaxf(x, dpp<ROR8>(x)); return x; }
__device__ __forceinline__ float quad_sum(float x) { x += dpp<XOR1>(x); x += dpp<XOR2>(x); return x; }
__device__ __forceinline__ float dot2(unsigned a, unsigned b, float c) {
  return __builtin_amdgcn_fdot2_f32_bf16(__builtin_bit_cast(bf16x2, a), __builtin_bit_cast(bf16x2, b), c, false);
}
__device__ __forceinline__ f32x4 mfma16(unsigned a0, unsigned a1, unsigned b0, unsigned b1, f32x4 c) {
  u32x2 a = {a0, a1}, b = {b0, b1};
  return __builtin_amdgcn_mfma_f32_16x16x16bf16_1k(__builtin_bit_cast(s16x4, a), __builtin_bit_cast(s16x4, b), c, 0, 0, 0);
}
__device__ __forceinline__ f32x4 mfma32(u32x4 a, u32x4 b, f32x4 c) {
  return __builtin_amdgcn_mfma_f32_16x16x32_bf16(__builtin_bit_cast(bf16x8, a), __builtin_bit_cast(bf16x8, b), c, 0, 0, 0);
}
template <int RECIP> __device__ __forceinline__ unsigned pace_step(unsigned tprev, unsigned tend, unsigned& tdone) {
  unsigned d, now, tgt;
  asm volatile("s_sub_i32 %0, %5, %4\n\ts_max_i32 %0, %0, 0\n\ts_min_i32 %0, %0, 0x7d0\n\ts_mul_i32 %0, %0, %6\n\ts_lshr_b32 %0, %0, 16\n\ts_add_u32 %2, %4, %0\n"
               "1:\n\ts_memrealtime vcc\n\ts_getreg_b32 %0, hwreg(HW_REG_IB_STS, 0, 4)\n\ts_waitcnt lgkmcnt(0)\n\ts_cmp_lg_u32 %0, 0\n\ts_cselect_b32 %3, vcc_lo, %3\n\t"
               "s_sub_u32 %0, vcc_lo, %2\n\ts_cmp_lt_i32 %0, 0\n\ts_cbranch_scc1 1b\n\ts_mov_b32 %1, vcc_lo"
               : "=&s"(d), "=&s"(now), "=&s"(tgt), "+s"(tdone) : "s"(tprev), "s"(tend), "i"(RECIP) : "vcc", "scc");
  return now;
}
__device__ __forceinline__ void wait_until(unsigned tgt) {
  asm volatile("1:\n\ts_memrealtime vcc\n\ts_waitcnt lgkmcnt(0)\n\ts_sub_u32 vcc_lo, vcc_lo, %0\n\ts_cmp_lt_i32 vcc_lo, 0\n\ts_cbranch_scc1 1b" :: "s"(tgt) : "vcc", "scc", "memory");
}
#define SB() __builtin_amdgcn_sched_barrier(0)
#define MEMFENCE() asm volatile("" ::: "memory")

__device__ __forceinline__ void delay_ticks(int n) {
  if (n > 0) wait_until((unsigned)__builtin_amdgcn_s_memrealtime() + n);
}

typedef __attribute__((address_space(3))) float lfloat;
typedef __attribute__((address_space(3))) f32x4 lf32x4;
typedef __attribute__((address_space(3))) unsigned luint;
typedef __attribute__((address_space(3))) unsigned char lbyte;
template <int SLP> __device__ __forceinline__ void wait_lds(unsigned a, unsigned want) {
  unsigned v, sv;
  asm volatile("s_branch 2f\n1:\n\ts_sleep %4\n2:\n\tds_read_b32 %0, %2\n\ts_waitcnt lgkmcnt(0)\n\tv_readfirstlane_b32 %1, %0\n\ts_cmp_lg_u32 %1, %3\n\ts_cbranch_scc1 1b"
               : "=&v"(v), "=&s"(sv) : "v"(a), "s"(want), "i"(SLP) : "scc", "memory");
}

__device__ __forceinline__ u32x4 proj_epilogue(f32x4 acc0, f32x4 acc1, unsigned tag) {
  float r[4];
#pragma unroll
  for (int i = 0; i < 4; ++i) {
    auto s = __builtin_amdgcn_permlane32_swap(__float_as_uint(acc0[i]), __float_as_uint(acc1[i]), false, false);
    r[i] = __uint_as_float(s[0]) + dpp<ROR8>(__uint_as_float(s[1]));
  }
  return u32x4{cvtpk(r[0], r[1]), tag, cvtpk(r[2], r[3]), tag};
}

__global__ void __launch_bounds__(512, 2)
fused_decode(const u16* x_, const u16* kc_, const u16* vc_, const u16* wq_, const u16* wo_, const u16* wg_, const u16* wu_, const u16* wd_, u16* out_, unsigned char* ws_) {
  __shared__ __attribute__((aligned(16))) float lds[96 + 8 + 4 + 16 + 8 * 192];
  unsigned* l_q = (unsigned*)lds;
  float* l_ml = lds + 96;
  float* l_c = lds + 104;
  unsigned* l_est = (unsigned*)(lds + 108);
  unsigned char* l_f = (unsigned char*)(lds + 116);
  constexpr int FA = 0, FB = 4, FC = 8, FD = 12, FE = 16, FG = 20;
  unsigned* l_tend = (unsigned*)(lds + 122);
  unsigned* l_tag = (unsigned*)(lds + 123);
  const unsigned fbase = (unsigned)(size_t)(lbyte*)l_f;
  float* l_acc = lds + 124;

  typedef unsigned long long u64;
  typedef u64 u64x8 __attribute__((ext_vector_type(8)));
  typedef u64 u64x2 __attribute__((ext_vector_type(2)));
  u64x8 ka; u64x2 kb; u64 pc;
  u32x4 fw[16];
  int bid = blockIdx.x, tid = threadIdx.x;
  {
    unsigned v1, v2;
    u64x8 ka0; u64x2 kb0;
    asm volatile("s_load_dwordx16 %0, %3, 0x0\n\ts_load_dwordx4 %1, %3, 0x40\n\ts_getpc_b64 %2" : "=&{s[36:51]}"(ka0), "=&{s[52:55]}"(kb0), "=&s"(pc) : "s"(__builtin_amdgcn_kernarg_segment_ptr()));
    asm volatile(
               "v_readfirstlane_b32 s14, %20\n\t"
               "s_lshr_b32 s15, %19, 3\n\t"
               "s_cmpk_ge_u32 s14, 0xc0\n\ts_cbranch_scc1 1f\n\t"
               "s_setprio 2\n\t"
               "s_and_b32 s16, %19, 7\n\t"
               "s_lshr_b32 s17, %19, 6\n\ts_lshl2_add_u32 s17, s16, s17\n\t"
               "s_and_b32 s18, s15, 7\n\ts_lshr_b32 s19, s14, 6\n\ts_lshl1_add_u32 s18, s18, s19\n\t"
               "s_lshr_b32 s19, %19, 4\n\t"
               "v_bfe_u32 %16, %20, 3, 3\n\tv_and_b32 %17, 7, %20\n\t"
               "s_movk_i32 s13, 0x3000\n\t"
               "s_cmpk_lt_u32 s14, 0x80\n\t"
               "s_cselect_b32 s18, s18, s19\n\ts_cselect_b32 s17, s17, s16\n\ts_cselect_b32 s13, s13, 0xc00\n\t"
               "s_mul_i32 s19, s18, 11\n\ts_lshr_b32 s19, s19, 5\n\t"
               "v_mad_u32_u24 %16, %16, s13, %17\n\t"
               "s_sub_u32 s17, s17, s19\n\ts_mul_i32 s17, s17, 3\n\ts_add_u32 s17, s17, s18\n\t"
               "s_mul_i32 s19, s19, s13\n\ts_add_u32 s12, s17, s19\n\ts_lshl_b32 s12, s12, 7\n\t"
               "v_lshlrev_b32 %16, 4, %16\n\t"
               "s_mul_i32 s10, s13, 0x280\n\ts_mov_b32 s11, 0x20000\n\ts_cmp_eq_u32 s18, 15\n\ts_cselect_b32 s10, 0, s10\n\t"
               "s_bitcmp1_b32 s15, 0\n\t"
               "s_waitcnt lgkmcnt(0)\n\t"
               "s_cselect_b64 s[8:9], s[48:49], s[46:47]\n\t"
               "s_cmpk_lt_u32 s14, 0x80\n\ts_cselect_b64 s[8:9], s[42:43], s[8:9]\n\t"
               "s_and_b32 s9, s9, 0xffff\n\t"
               "buffer_load_dwordx4 %0, %16, s[8:11], s12 offen nt\n\t" "s_add_u32 s12, s12, s13\n\t"
               "buffer_load_dwordx4 %1, %16, s[8:11], s12 offen nt\n\t" "s_add_u32 s12, s12, s13\n\t"
               "buffer_load_dwordx4 %2, %16, s[8:11], s12 offen nt\n\t" "s_add_u32 s12, s12, s13\n\t"
               "buffer_load_dwordx4 %3, %16, s[8:11], s12 offen nt\n\t" "s_add_u32 s12, s12, s13\n\t"
               "buffer_load_dwordx4 %4, %16, s[8:11], s12 offen nt\n\t" "s_add_u32 s12, s12, s13\n\t"
               "buffer_load_dwordx4 %5, %16, s[8:11], s12 offen nt\n\t" "s_add_u32 s12, s12, s13\n\t"
               "buffer_load_dwordx4 %6, %16, s[8:11], s12 offen nt\n\t" "s_add_u32 s12, s12, s13\n\t"
               "buffer_load_dwordx4 %7, %16, s[8:11], s12 offen nt\n\t" "s_add_u32 s12, s12, s13\n\t"
               "buffer_load_dwordx4 %8, %16, s[8:11], s12 offen nt\n\t" "s_add_u32 s12, s12, s13\n\t"
               "buffer_load_dwordx4 %9, %16, s[8:11], s12 offen nt\n\t" "s_add_u32 s12, s12, s13\n\t"
               "buffer_load_dwordx4 %10, %16, s[8:11], s12 offen nt\n\t" "s_add_u32 s12, s12, s13\n\t"
               "buffer_load_dwordx4 %11, %16, s[8:11], s12 offen nt\n\t" "s_add_u32 s12, s12, s13\n\t" "s_barrier\n\t"
               "buffer_load_dwordx4 %12, %16, s[8:11], s12 offen nt\n\t" "s_add_u32 s12, s12, s13\n\t"
               "buffer_load_dwordx4 %13, %16, s[8:11], s12 offen nt\n\t" "s_add_u32 s12, s12, s13\n\t"
               "buffer_load_dwordx4 %14, %16, s[8:11], s12 offen nt\n\t" "s_add_u32 s12, s12, s13\n\t"
               "buffer_load_dwordx4 %15, %16, s[8:11], s12 offen nt\n\t"
               "s_branch 2f\n"
               "1:\n\t"
               "s_cmpk_lg_u32 s14, 0xc0\n\ts_cbranch_scc1 3f\n\t"
               "v_and_b32 %16, 3, %20\n\tv_lshl_add_u32 %16, %16, 5, s15\n\tv_lshlrev_b32 %16, 7, %16\n\tv_min_u32 %16, %24, %16\n\t"
               "global_load_dwordx4 %15, %16, %23\n"
               "3:\n\t"
               "v_and_b32 %16, 15, %20\n\tv_lshl_add_u32 %16, %16, 2, %21\n\tv_mov_b32 %17, 0\n\tds_write_b32 %16, %17\n\t"
               "s_waitcnt lgkmcnt(0)\n\ts_barrier\n"
               "2:"
               : "=&v"(fw[0]), "=&v"(fw[1]), "=&v"(fw[2]), "=&v"(fw[3]), "=&v"(fw[4]), "=&v"(fw[5]), "=&v"(fw[6]), "=&v"(fw[7]),
                 "=&v"(fw[8]), "=&v"(fw[9]), "=&v"(fw[10]), "=&v"(fw[11]), "=&v"(fw[12]), "=&v"(fw[13]), "=&v"(fw[14]), "=&v"(fw[15]),
                 "=&v"(v1), "=&v"(v2)
               : "{s[36:51]}"(ka0), "s"(bid), "v"(tid), "s"(fbase - 32), "{s[52:55]}"(kb0), "s"(pc), "i"(CODE_BYTES - 128)
               : "s8", "s9", "s10", "s11", "s12", "s13", "s14", "s15", "s16", "s17", "s18", "s19", "scc", "memory");
    asm volatile("" : "={s[36:51]}"(ka), "={s[52:55]}"(kb), "+s"(bid) : "0"(ka0), "1"(kb0));
    asm volatile("" : "+v"(tid));
  }
  const int xq = bid & 7, uq = bid >> 3;
  const int h = xq * 4 + (uq >> 3), j8 = uq & 7, b = j8 >> 2, s = j8 & 3;
  const int lane = tid & 63, wave = __builtin_amdgcn_readfirstlane(tid >> 6);
  const int g = lane >> 3, j = lane & 7;
  const int bl = (lane >> 2) & 1, e4 = lane & 3;
  const bool isS = wave >= 4;
  const u16 *x = (const u16*)ka[0], *kc = (const u16*)ka[1], *vc = (const u16*)ka[2], *wo = (const u16*)ka[4], *wd = (const u16*)ka[7];
  u16* out = (u16*)kb[0];
  unsigned char* ws = (unsigned char*)kb[1];
  u64 tst;
  asm volatile("s_memrealtime %0\n\ts_waitcnt lgkmcnt(0)" : "=s"(tst));
  const unsigned tstart = (unsigned)tst;
  const rsrc_t rws = mk_rsrc(ws, WS_END);

  if (isS) {
    const int sw = wave - 4;
    const rsrc_t r0 = mk_rsrc(kc, 2 * 32 * 8192 * 384), rv = mk_rsrc(vc, 2 * 32 * 8192 * 384);
    const int voff = ((b * 32 + h) * 8192 + 6144 + 32 * s + 8 * sw + g) * 384 + 16 * j;
    u32x4 buf[96];
    u32x4 qv[3];
    float sc[16];
    unsigned pd[16];
    f32x4 acc[3] = {{0.f, 0.f, 0.f, 0.f}, {0.f, 0.f, 0.f, 0.f}, {0.f, 0.f, 0.f, 0.f}};
    unsigned msk[4];
#pragma unroll
    for (int d = 0; d < 4; ++d) msk[d] = (j >> 1) == d ? ((j & 1) ? 0xffff0000u : 0x0000ffffu) : 0u;
    constexpr float C2 = 0.07216878364870322f * 1.4426950408889634f;
        const int stag = ((2 * (uq >> 2) - 7) * STAG) >> 1;
    unsigned tprev = tstart + S_DELAY + stag, tend = tstart + T_PLAN + stag, lat = 0;

    auto consume = [&](auto G) {
      constexpr int t = decltype(G)::value;
      if constexpr (t < 16) {
        float a0 = 0.f, a1 = 0.f, a2 = 0.f;
#pragma unroll
        for (int e = 0; e < 4; ++e) { a0 = dot2(buf[3 * t][e], qv[0][e], a0); a1 = dot2(buf[3 * t + 1][e], qv[1][e], a1); a2 = dot2(buf[3 * t + 2][e], qv[2][e], a2); }
        float a = (a0 + a1) + a2;
        a += dpp<XOR1>(a); a += dpp<XOR2>(a); a += dpp<HMIR>(a);
        sc[t] = a;
        if constexpr (t == 15) {
          float m = sc[0];
#pragma unroll
          for (int u = 1; u < 16; ++u) m = fmaxf(m, sc[u]);
          m = fmaxf(m, dpp<ROR8>(m)); m = x16_max(m); m = x32_max(m);
          const float mc = m * C2;
          float l = 0.f;
#pragma unroll
          for (int u = 0; u < 16; ++u) { const float p = __builtin_amdgcn_exp2f(fmaf(sc[u], C2, -mc)); pd[u] = cvtpk(p, p); l += bfhi(pd[u]); }
          l += dpp<ROR8>(l); l = x16_sum(l); l = x32_sum(l);
          if (lane == 0) { *(f32x2*)(l_ml + 2 * sw) = f32x2{mc, l}; MEMFENCE(); l_f[FA + sw] = 1; }
        }
      } else {
        constexpr int u = t - 16;
        const u32x4 a = {pd[u] & msk[0], pd[u] & msk[1], pd[u] & msk[2], pd[u] & msk[3]};
#pragma unroll
        for (int i = 0; i < 3; ++i) acc[i] = mfma32(a, buf[3 * t + i], acc[i]);
      }
    };
    static_for<0, 16>([&](auto K) {
      constexpr int k = decltype(K)::value;
      if constexpr (k > 6) {
        MEMFENCE();
        unsigned tv = *l_tend;
        asm volatile("" : "+v"(tv));
        tv = __builtin_amdgcn_readfirstlane(tv);
        tend = tv ? tv + stag : tend;
      }
      unsigned tdone = tprev + LAT0;
      const unsigned now = pace_step<(k ? 65536 / (16 - k) : 0)>(tprev, tend, tdone);
      SB();
#pragma unroll
      for (int i = 0; i < 6; ++i) { buf[6 * k + i] = ld_nt(k < 8 ? r0 : rv, voff + (i % 3) * 128, ((2 * k + i / 3) & 15) * 49152); SB(); }
      if constexpr (k == 15) { if (lane == 0) l_f[FB + sw] = 1; }
      if constexpr (k >= 1 && k < 15) asm volatile("s_waitcnt vmcnt(6)" ::: "memory");
      if constexpr (k >= 2 && k < 13) {
        if (tdone == now) tdone = (unsigned)__builtin_amdgcn_s_memrealtime();
        lat = (lat << 16) | (tdone - tprev);
        *(u32x2*)(l_est + 2 * sw) = u32x2{(now - stag + ((6 * (15 - k) * RATE16) >> 4)) | 1u, lat};
      }
      tprev = now;
      if constexpr (k == QK) {
        wait_lds<0>(fbase + FG, ALL4);
#pragma unroll
        for (int i = 0; i < 3; ++i) qv[i] = *(const u32x4*)(l_q + 32 * i + 4 * j);
      }
      SB();
      constexpr auto upto = [](int kk) constexpr { if (kk < QK) return 0; const int c = CU * (kk - QK + 1), l = kk < 15 ? 2 * kk : 28; return c < l ? c : l; };
      static_for<upto(k - 1), upto(k)>([&](auto G) { consume(G); SB(); });
      SB();
    });
    __builtin_amdgcn_s_setprio(3);
    wait_lds<0>(fbase + FE, ALL4);
    float cq = l_c[lane >> 4];
    unsigned dst = (unsigned)(size_t)(lfloat*)(l_acc + (sw * 2 + (g & 1)) * 192 + 8 * j + 4 * ((lane >> 4) & 1));
    unsigned src = (unsigned)(size_t)(lfloat*)(l_acc + (lane >> 4) * 384 + 64 * sw + 8 * j + 4 * (g & 1));
    unsigned fla = fbase + FC + sw, one = 1u, flr = fbase + FC;
    int pvo = (lane & 16) ? OOB : (g & 1) * 128 + j * 16 + (lane >> 5) * 8;
    int pso = WS_OP + h * OP_STRIDE + (b * 4 + s) * 768 + sw * 256;
    unsigned tagv = *l_tag;
    asm volatile("" : "+v"(cq), "+v"(dst), "+v"(src), "+v"(fla), "+v"(flr), "+v"(one), "+v"(pvo), "+s"(pso), "+v"(tagv));
    const bool wr = (lane >> 5) == (g & 1);
    SB();
    static_for<28, 32>([&](auto G) { consume(G); SB(); });
    if (wr) {
#pragma unroll
      for (int i = 0; i < 3; ++i) *(lf32x4*)(size_t)(dst + 256 * i) = acc[i];
      MEMFENCE();
      *(lbyte*)(size_t)fla = (unsigned char)one;
    }
    if (sw < 3) {
      f32x4 u0, u1;
      unsigned fv, fs;
      asm volatile("1:\n\tds_read_b32 %0, %4\n\tds_read_b128 %2, %5\n\tds_read_b128 %3, %5 offset:768\n\ts_waitcnt lgkmcnt(2)\n\tv_readfirstlane_b32 %1, %0\n\t"
                   "s_cmp_lg_u32 %1, 0x01010101\n\ts_waitcnt lgkmcnt(0)\n\ts_cbranch_scc1 1b"
                   : "=&v"(fv), "=&s"(fs), "=&v"(u0), "=&v"(u1) : "v"(flr), "v"(src) : "scc", "memory");
      float o[4];
#pragma unroll
      for (int e = 0; e < 4; ++e) o[e] = cq * (u0[e] + u1[e]);
      const auto s0 = __builtin_amdgcn_permlane32_swap(__float_as_uint(o[0]), __float_as_uint(o[2]), false, false);
      const auto s1 = __builtin_amdgcn_permlane32_swap(__float_as_uint(o[1]), __float_as_uint(o[3]), false, false);
      const float p0 = x16_sum(__uint_as_float(s0[0]) + __uint_as_float(s0[1])), p1 = x16_sum(__uint_as_float(s1[0]) + __uint_as_float(s1[1]));
      __builtin_amdgcn_raw_buffer_store_b64(u32x2{cvtpk(p0, p1), tagv}, rws, pvo, pso, AUX_SC1);
    }
    MEMFENCE();
    *(lbyte*)(size_t)(fla + (FD - FC)) = (unsigned char)one;
    return;
  }

  __builtin_amdgcn_s_setprio(2);
  unsigned tag, ep;
  asm volatile("s_load_dword %0, %1, 0x0" : "=&s"(ep) : "s"(ws));
#define TAG_READY() asm volatile("s_waitcnt lgkmcnt(0)\n\ts_add_u32 %0, %1, 1\n\ts_addc_u32 %0, %0, 0" : "=&s"(tag) : "s"(ep) : "scc", "memory")
  const int w = wave;
  const unsigned idx = w < 2 ? 2 * j8 + w : uq >> 1, ur = (idx * 11) >> 5, uc = idx - 3 * ur, up = uq & 1;
  const bool has_front = (w < 3) & (idx < 15);
  const unsigned m0 = e4 == 0 ? 0x0000ffffu : e4 == 1 ? 0xffff0000u : 0u;
  const unsigned m1 = e4 == 2 ? 0x0000ffffu : e4 == 3 ? 0xffff0000u : 0u;
  const int st_col = (g & 1) ? OOB : (lane >> 5) * 128 + j * 16;
  const rsrc_t rx = mk_rsrc(x, 2560);
  if (has_front) {
    const int xo = (lane >> 5) * 1280 + (lane & 31) * 16;
    const bool x3 = (lane & 16) == 0;
    const u32x4 xa = __builtin_amdgcn_raw_buffer_load_b128(rx, xo, 0, 0), xb = __builtin_amdgcn_raw_buffer_load_b128(rx, xo + 512, 0, 0);
    const u32x4 xc = __builtin_amdgcn_raw_buffer_load_b128(rx, x3 ? xo + 1024 : OOB, 0, 0);
    const u32x4 xr0 = __builtin_amdgcn_raw_buffer_load_b128(rx, bl * 1280 + g * 32, 256 * ur, 0);
    const u32x4 xr1 = __builtin_amdgcn_raw_buffer_load_b128(rx, bl * 1280 + g * 32 + 16, 256 * ur, 0);
    SB();
    TAG_READY();
    float v[24];
#pragma unroll
    for (int e = 0; e < 4; ++e) { v[2 * e] = bflo(xa[e]); v[2 * e + 1] = bfhi(xa[e]); v[8 + 2 * e] = bflo(xb[e]); v[9 + 2 * e] = bfhi(xb[e]); v[16 + 2 * e] = bflo(xc[e]); v[17 + 2 * e] = bfhi(xc[e]); }
    float sum = 0.f;
#pragma unroll
    for (int e = 0; e < 24; ++e) sum += v[e];
    sum = x16_sum(row_sum(sum));
    const float mean = sum * (1.f / 640.f);
    float sq = 0.f, sq3 = 0.f;
#pragma unroll
    for (int e = 0; e < 16; ++e) { const float d = v[e] - mean; sq = fmaf(d, d, sq); }
#pragma unroll
    for (int e = 16; e < 24; ++e) { const float d = v[e] - mean; sq3 = fmaf(d, d, sq3); }
    sq = x16_sum(row_sum(sq + (x3 ? sq3 : 0.f)));
    const float mb = rbf(mean), den = rbf(__builtin_amdgcn_sqrtf(rbf(rbf(sq * (1.f / 640.f)) + 1e-5f)));
    const float inv = __builtin_amdgcn_rcpf(den);
    const float mb0 = __builtin_bit_cast(float, __builtin_amdgcn_readlane(__builtin_bit_cast(int, mb), 0));
    const float mb1 = __builtin_bit_cast(float, __builtin_amdgcn_readlane(__builtin_bit_cast(int, mb), 32));
    const float iv0 = __builtin_bit_cast(float, __builtin_amdgcn_readlane(__builtin_bit_cast(int, inv), 0));
    const float iv1 = __builtin_bit_cast(float, __builtin_amdgcn_readlane(__builtin_bit_cast(int, inv), 32));
    const float mm = bl ? mb1 : mb0, ii = bl ? iv1 : iv0;
    asm volatile("s_waitcnt vmcnt(0)" : "+v"(fw[0]), "+v"(fw[1]), "+v"(fw[2]), "+v"(fw[3]), "+v"(fw[4]), "+v"(fw[5]), "+v"(fw[6]), "+v"(fw[7]),
                 "+v"(fw[8]), "+v"(fw[9]), "+v"(fw[10]), "+v"(fw[11]), "+v"(fw[12]), "+v"(fw[13]), "+v"(fw[14]), "+v"(fw[15]));
    f32x4 acc0 = {0.f, 0.f, 0.f, 0.f}, acc1 = {0.f, 0.f, 0.f, 0.f};
#pragma unroll
    for (int n = 0; n < 16; ++n) {
      const unsigned xw = n < 8 ? xr0[n >> 1] : xr1[(n - 8) >> 1];
      const float xv = (n & 1) ? bfhi(xw) : bflo(xw);
      const float xn = rbf(xv - mm) * ii;
      const unsigned d = cvtpk(xn, xn), a0 = d & m0, a1 = d & m1;
      acc0 = mfma16(a0, a1, fw[n][0], fw[n][1], acc0);
      acc1 = mfma16(a0, a1, fw[n][2], fw[n][3], acc1);
    }
    const int rec = (w < 2 ? WS_QP + (h * 5 + ur) * 1536 : WS_GU + ((xq * 5 + ur) * 2 + up) * 1536) + uc * 256;
    __builtin_amdgcn_raw_buffer_store_b128(proj_epilogue(acc0, acc1, tag), rws, ((lane >> 4) & 1) * 768 + st_col, rec, AUX_SC1);
    asm volatile("s_waitcnt vmcnt(0)" ::: "memory");
  } else {
    asm volatile("s_waitcnt vmcnt(0)" ::"v"(fw[0]), "v"(fw[1]), "v"(fw[2]), "v"(fw[3]), "v"(fw[4]), "v"(fw[5]), "v"(fw[6]), "v"(fw[7]),
                 "v"(fw[8]), "v"(fw[9]), "v"(fw[10]), "v"(fw[11]), "v"(fw[12]), "v"(fw[13]), "v"(fw[14]), "v"(fw[15]));
    TAG_READY();
  }
  const int slot = w * 8 + j8;
  const bool has_back = slot < 30;
  const int t3 = slot / 10, cc = slot - 10 * t3;
  const int rid = xq * 20 + uq;
  const int rb = rid / 80, rc = (rid - 80 * rb) >> 3, ro = rid & 7;
  const bool reducer = (w == 0) && (bid < 160);
  unsigned ha[2][2] = {{0u, 0u}, {0u, 0u}};
  unsigned tend = tstart + T_PLAN;
  auto pace_round = [&](unsigned t_sample, unsigned t_read, int so, int dead, int gain16, int tdead, int trend, int keep16) {
    wait_until(t_sample);
    u32x4 e, f;
    do { MEMFENCE(); e = *(const u32x4*)l_est; f = *(const u32x4*)(l_est + 4); } while (!__builtin_amdgcn_readfirstlane(e[0] & e[2] & f[0] & f[2] & 1u));
    const u32x4 pv = {tstart + (unsigned)((int)((e[0] - tstart) + (e[2] - tstart) + (f[0] - tstart) + (f[2] - tstart)) >> 2), tag, e[1] + e[3] + f[1] + f[3], tag};
    __builtin_amdgcn_raw_buffer_store_b128(pv, rws, (lane == 0 && j8 == (((uq >> 3) * 5) & 7)) ? ((uq >> 3) * 8 + xq) * 16 : OOB, so, AUX_SC1);
    wait_until(t_read);
    MEMFENCE();
    const u32x4 tv = ld_sc1(rws, (lane & 31) * 16, so);
    if (__all((tv[1] == tag) & (tv[3] == tag))) {
      const float d = x16_sum(row_sum((float)(int)(tv[0] - tstart))) * (1.f / 32.f);
      const float l1 = x16_sum(row_sum((float)(tv[2] & 0xffffu))) * (1.f / 128.f), l0 = x16_sum(row_sum((float)(tv[2] >> 16))) * (1.f / 128.f);
      const float dr = l1 - (float)LAT0, sign = l1 - l0;
      int di = (int)(d + fmaxf(dr, 0.f) + fmaxf(dr - (float)dead, 0.f) * (float)gain16 * (1.f / 16.f) + fmaxf(sign - (float)tdead, 0.f) * (float)trend);
      const int dold = (int)(tend - tstart);
      if (keep16 && di < dold) di += ((dold - di) * keep16) >> 4;
      di = di < 0 ? 0 : (di > 2500 ? 2500 : di);
      tend = (tstart + __builtin_amdgcn_readfirstlane(di)) | 1u;
      *l_tend = tend;
    }
  };
  if (w < 3) {
    if (has_front) __builtin_amdgcn_s_sleep(16); else wait_until(tstart + 360);
    u32x2 t[3];
    const int vo = (lane >> 5) * 1536 + (lane & 31) * 8, so = WS_QP + h * 7680 + b * 768 + w * 256;
    for (unsigned spin = 0;; ++spin) {
      MEMFENCE();
      t[0] = ld2_sc1(rws, vo, so); t[1] = ld2_sc1(rws, vo, so + 3072); t[2] = ld2_sc1(rws, lane < 32 ? vo : OOB, so + 6144);
      const bool ok = (t[0][1] == tag) & (t[1][1] == tag) & ((t[2][1] == tag) | (lane >= 32));
      if (__all(ok) || spin > SPIN_CAP) break;
    }
    const float a = x32_sum((bflo(t[0][0]) + bflo(t[1][0])) + bflo(t[2][0]));
    const float c = x32_sum((bfhi(t[0][0]) + bfhi(t[1][0])) + bfhi(t[2][0]));
    if (lane < 32) { l_q[32 * w + ((lane >> 1) & 7) * 4 + (lane >> 4) * 2 + (lane & 1)] = cvtpk(a, c); MEMFENCE(); l_f[FG + w] = 1; }
  } else {
    *l_tag = tag;
    MEMFENCE();
    l_f[FG + 3] = 1;
    pace_round(tstart + SMP0, tstart + SMP0 + WAIT0, WS_TS, DEAD0, GAIN0, TDEAD0, TREND0, 0);
  }
  if (has_back) {
    u32x2 t[3];
    const int f16 = (h & 3) * 3 + t3;
    const int so = WS_GU + xq * 15360 + (f16 >> 2) * 256 + (f16 & 3) * 32;
    const int vo = bl * 768 + ((g >> 1) & 1) * 128 + (g >> 2) * 16 + (g & 1) * 8;
    for (unsigned spin = 0;; ++spin) {
      MEMFENCE();
      t[0] = ld2_sc1(rws, vo + e4 * 3072, so);
      t[1] = ld2_sc1(rws, vo + e4 * 3072 + 1536, so);
      t[2] = ld2_sc1(rws, vo + 4 * 3072 + (e4 & 1) * 1536, so);
      if (__all((t[0][1] == tag) & (t[1][1] == tag) & (t[2][1] == tag)) || spin > SPIN_CAP) break;
    }
#pragma unroll
    for (int z = 0; z < 2; ++z) {
      const float ex = z ? bfhi(t[2][0]) : bflo(t[2][0]);
      const float gg = rbf(quad_sum((z ? bfhi(t[0][0]) : bflo(t[0][0])) + (e4 == 0 ? ex : 0.f)));
      const float uu = rbf(quad_sum((z ? bfhi(t[1][0]) : bflo(t[1][0])) + (e4 == 1 ? ex : 0.f)));
      const float zz = 0.7978845608028654f * (gg + 0.044715f * gg * gg * gg);
      const float ge = rbf(gg * __builtin_amdgcn_rcpf(1.f + __builtin_amdgcn_exp2f(-2.8853900817779268f * zz)));
      const float hv = ge * uu;
      const unsigned d = cvtpk(hv, hv);
      ha[z][0] = d & m0; ha[z][1] = d & m1;
    }
  }
  const int oc = 16 * (ro & 3) + 8 * (lane & 1) + 4 * (ro >> 2) + 2 * (lane >> 5) + ((lane >> 4) & 1);
  const rsrc_t rout = mk_rsrc(out, 2560);
  int out_vo = (lane & 14) == 0 ? oc * 2 + rb * 1280 + rc * 128 : OOB;
  float xres = 0.f;
  if (reducer) {
    xres = bflo(__builtin_amdgcn_raw_buffer_load_b16(rx, oc * 2, rb * 1280 + rc * 128, 0));
    __builtin_amdgcn_raw_buffer_store_b16((unsigned short)0, rout, out_vo, 0, AUX_SC1);
  }
  if (w == 1) {
    wait_lds<4>(fbase + FA, ALL4);
    const int so = WS_ML + (h * 2 + b) * 256;
    if (lane < 4) { const f32x2 ml = *(const f32x2*)(l_ml + 2 * lane); st_pair(rws, lane * 16, so + s * 64, ml[0], ml[1], tag); }
    asm volatile("s_waitcnt vmcnt(0)" ::: "memory");
    __builtin_amdgcn_s_sleep(32);
    u32x4 t;
    for (unsigned spin = 0;; ++spin) {
      MEMFENCE();
      t = ld_sc1(rws, (lane & 15) * 16, so);
      if (__all((t[1] == tag) & (t[3] == tag)) || spin > SPIN_CAP) break;
    }
    const float mv = __uint_as_float(t[0]);
    const float M = row_max(mv);
    const float f = __builtin_amdgcn_exp2f(mv - M);
    const float L = row_sum(f * __uint_as_float(t[2]));
    if ((lane >> 2) == s) { l_c[lane & 3] = f / L; MEMFENCE(); l_f[FE + (lane & 3)] = 1; }
  }
  if (w == 3) {
    pace_round(tend - SMP1, tend - SMP1 + WAIT1, WS_TS + 512, DEAD1, GAIN1, TDEAD1, TREND1, KEEP1);
  }
  if (has_back) {
    const unsigned sLo = (e4 & 1) ? 0x01000c0cu : 0x0c0c0100u, sHi = (e4 & 1) ? 0x03020c0cu : 0x0c0c0302u;
    unsigned sel0 = e4 < 2 ? sLo : 0x0c0c0c0cu, sel1 = e4 < 2 ? 0x0c0c0c0cu : sLo, sel2 = e4 < 2 ? sHi : 0x0c0c0c0cu, sel3 = e4 < 2 ? 0x0c0c0c0cu : sHi;
    int op_so = WS_OP + h * OP_STRIDE + t3 * 256, op_vo = (bl * 4 + e4) * 768 + 16 * g;
    int d_vo = ((48 * h + 16 * t3 + 2 * g) * 640 + 64 * cc) * 2 + 16 * j, o_vo = ((192 * h + 64 * t3 + 8 * g) * 640 + 64 * cc) * 2 + 16 * j;
    int yvo = ((lane >> 4) & 1) * 256 + st_col, yso = WS_YP + ((h * 3 + t3) * 10 + cc) * 512;
    int r_vo = (lane >> 1) * 5120 + (lane & 1) * 16 + WS_YP + rc * 512 + rb * 256 + ro * 32;
    unsigned tagv = tag, fd = fbase + FD;
    asm volatile("" : "+v"(sel0), "+v"(sel1), "+v"(sel2), "+v"(sel3), "+s"(op_so), "+v"(op_vo), "+v"(d_vo), "+v"(o_vo), "+v"(yvo), "+s"(yso), "+v"(r_vo), "+v"(out_vo), "+v"(tagv), "+v"(xres), "+v"(fd));
    const rsrc_t rd = mk_rsrc(wd, 1536 * 1280), ro_ = mk_rsrc(wo, 6144 * 1280);
    wait_lds<1>(fbase + FB, ALL4);
    {
      unsigned tf = *l_tend;
      asm volatile("" : "+v"(tf));
      tf = __builtin_amdgcn_readfirstlane(tf);
      const unsigned tn = (unsigned)__builtin_amdgcn_s_memrealtime();
      int wt = tf ? (int)(tf + BACK_AT - tn) : 0;
      asm("s_max_i32 %0, %0, %1\n\ts_min_i32 %0, %0, 0xc8" : "+s"(wt) : "s"(BACK_DELAY) : "scc");
      wait_until(tn + wt);
    }
    u32x4 wb[10], t0, t1;
    unsigned fdv = 0;
#pragma unroll
    for (int n = 0; n < 10; ++n) {
      if (n == POLLPOS) {
        const rsrc_t rp = mk_rsrc(ws, __builtin_amdgcn_readfirstlane(fdv) == ALL4 ? WS_END : 0);
        t0 = ld_sc1(rp, op_vo, op_so); t1 = ld_sc1(rp, op_vo + 128, op_so);
        SB();
      }
      if (n < 2) wb[8 + n] = ld_nt(rd, d_vo + n * 1280, 0); else wb[n - 2] = ld_nt(ro_, o_vo, (n - 2) * 1280);
      if (n == POLLPOS - 1) { MEMFENCE(); fdv = *(luint*)(size_t)fd; }
      SB();
      if (n < 9) __builtin_amdgcn_s_sleep(BPACE);
      SB();
    }
    u32x4 bq[10];
#define PAIR(p) do { bq[2 * (p)] = u32x4{wb[2 * (p)][0], wb[2 * (p)][1], wb[2 * (p) + 1][0], wb[2 * (p) + 1][1]}; \
                     bq[2 * (p) + 1] = u32x4{wb[2 * (p)][2], wb[2 * (p)][3], wb[2 * (p) + 1][2], wb[2 * (p) + 1][3]}; } while (0)
    PAIR(4); PAIR(0); PAIR(1); PAIR(2);
    const u32x4 hA = {ha[0][0], ha[0][1], ha[1][0], ha[1][1]};
    f32x4 acc0 = mfma32(hA, bq[8], f32x4{0.f, 0.f, 0.f, 0.f}), acc1 = mfma32(hA, bq[9], f32x4{0.f, 0.f, 0.f, 0.f});
    asm volatile("" : "+v"(bq[0]), "+v"(bq[1]), "+v"(bq[2]), "+v"(bq[3]), "+v"(bq[4]), "+v"(bq[5]), "+v"(acc0), "+v"(acc1));
    SB();
    unsigned np1 = 1;
    if (__builtin_expect(__builtin_amdgcn_ballot_w64((((t0[1] ^ tagv) | (t0[3] ^ tagv)) | ((t1[1] ^ tagv) | (t1[3] ^ tagv))) != 0u) != 0, 0)) {
      do {
        ++np1;
        MEMFENCE();
        if (__builtin_amdgcn_readfirstlane(*(luint*)(size_t)fd) != ALL4) { wait_lds<1>(fd, ALL4); delay_ticks(LATE_DELAY); }
        t0 = ld_sc1(rws, op_vo, op_so); t1 = ld_sc1(rws, op_vo + 128, op_so);
      } while (!__all((t0[1] == tag) & (t0[3] == tag) & (t1[1] == tag) & (t1[3] == tag)) && np1 < SPIN_CAP);
    }
    unsigned pk[4];
    {
      float u0, u1, u2, u3, u4, u5, u6, u7;
#define DPPADD(r, P) "v_add_f32_dpp " r ", " r ", " r " quad_perm:" P " row_mask:0xf bank_mask:0xf\n\t"
#define DPP8(P) DPPADD("%4", P) DPPADD("%5", P) DPPADD("%6", P) DPPADD("%7", P) DPPADD("%8", P) DPPADD("%9", P) DPPADD("%10", P) DPPADD("%11", P)
      asm volatile("v_lshlrev_b32 %4, 16, %12\n\tv_and_b32 %5, 0xffff0000, %12\n\tv_lshlrev_b32 %6, 16, %13\n\tv_and_b32 %7, 0xffff0000, %13\n\t"
                   "v_lshlrev_b32 %8, 16, %14\n\tv_and_b32 %9, 0xffff0000, %14\n\tv_lshlrev_b32 %10, 16, %15\n\tv_and_b32 %11, 0xffff0000, %15\n\t"
                   DPP8("[1,0,3,2]") DPP8("[2,3,0,1]")
                   "v_cvt_pk_bf16_f32 %0, %4, %5\n\tv_cvt_pk_bf16_f32 %1, %6, %7\n\tv_cvt_pk_bf16_f32 %2, %8, %9\n\tv_cvt_pk_bf16_f32 %3, %10, %11"
                   : "=&v"(pk[0]), "=&v"(pk[1]), "=&v"(pk[2]), "=&v"(pk[3]), "=&v"(u0), "=&v"(u1), "=&v"(u2), "=&v"(u3), "=&v"(u4), "=&v"(u5), "=&v"(u6), "=&v"(u7)
                   : "v"(t0[0]), "v"(t0[2]), "v"(t1[0]), "v"(t1[2]));
    }
    u32x4 aq[4];
#pragma unroll
    for (int p = 0; p < 4; ++p)
      aq[p] = u32x4{__builtin_amdgcn_perm(0u, pk[p], sel0), __builtin_amdgcn_perm(0u, pk[p], sel1), __builtin_amdgcn_perm(0u, pk[p], sel2), __builtin_amdgcn_perm(0u, pk[p], sel3)};
#pragma unroll
    for (int p = 0; p < 3; ++p) { acc0 = mfma32(aq[p], bq[2 * p], acc0); acc1 = mfma32(aq[p], bq[2 * p + 1], acc1); }
    asm volatile("" : "+v"(aq[3]), "+v"(acc0), "+v"(acc1));
    SB();
    acc0 = mfma16(aq[3][0], aq[3][1], wb[6][0], wb[6][1], acc0); acc1 = mfma16(aq[3][0], aq[3][1], wb[6][2], wb[6][3], acc1);
    acc0 = mfma16(aq[3][2], aq[3][3], wb[7][0], wb[7][1], acc0); acc1 = mfma16(aq[3][2], aq[3][3], wb[7][2], wb[7][3], acc1);
    __builtin_amdgcn_raw_buffer_store_b128(proj_epilogue(acc0, acc1, tagv), rws, yvo, yso, AUX_SC1);
    if (reducer) {
      delay_ticks(FIN_DELAY);
      float y0, y1, y2, y3;
      unsigned np2 = 0;
      for (;;) {
        MEMFENCE();
        const u32x4 a = ld_sc1(rws, r_vo, 0), c = ld_sc1(rws, r_vo, 32 * 5120), d = ld_sc1(rws, r_vo, 64 * 5120);
        unsigned bad = ((a[1] ^ tagv) | (a[3] ^ tagv)) | ((c[1] ^ tagv) | (c[3] ^ tagv));
        y0 = bflo(a[0]) + bflo(c[0]); y1 = bfhi(a[0]) + bfhi(c[0]); y2 = bflo(a[2]) + bflo(c[2]); y3 = bfhi(a[2]) + bfhi(c[2]);
        asm volatile("" : "+v"(bad), "+v"(y0), "+v"(y1), "+v"(y2), "+v"(y3));
        bad |= (d[1] ^ tagv) | (d[3] ^ tagv);
        y0 += bflo(d[0]); y1 += bfhi(d[0]); y2 += bflo(d[2]); y3 += bfhi(d[2]);
        if (__builtin_expect(__builtin_amdgcn_ballot_w64(bad != 0u) == 0, 1)) break;
        if (++np2 > SPIN_CAP) break;
      }
      const auto s0 = __builtin_amdgcn_permlane32_swap(__float_as_uint(y0), __float_as_uint(y2), false, false);
      const auto s1 = __builtin_amdgcn_permlane32_swap(__float_as_uint(y1), __float_as_uint(y3), false, false);
      const float z0 = __uint_as_float(s0[0]) + __uint_as_float(s0[1]), z1 = __uint_as_float(s1[0]) + __uint_as_float(s1[1]);
      const auto s2 = __builtin_amdgcn_permlane16_swap(__float_as_uint(z0), __float_as_uint(z1), false, false);
      float y = __uint_as_float(s2[0]) + __uint_as_float(s2[1]);
      y += dpp<ROR8>(y); y += dpp<ROR4>(y); y += dpp<XOR2>(y);
      __builtin_amdgcn_raw_buffer_store_b16((unsigned short)cvtpk(xres + y, 0.f), rout, out_vo, 0, AUX_SC1);
      if (bid == 0) __builtin_amdgcn_raw_buffer_store_b32(tagv, rws, lane == 0 ? WS_EPOCH : OOB, 0, AUX_SC1);
    }
  }
}

extern "C" void kernel_launch(void* const* d_in, const int* in_sizes, int n_in, void* d_out, int out_size, void* d_ws, size_t ws_size,
                              hipStream_t stream) {
  fused_decode<<<256, 512, 0, stream>>>((const u16*)d_in[0], (const u16*)d_in[1], (const u16*)d_in[2], (const u16*)d_in[3], (const u16*)d_in[4],
                                        (const u16*)d_in[6], (const u16*)d_in[7], (const u16*)d_in[8], (u16*)d_out, (unsigned char*)d_ws);
}
```

```cpp
#define AMP 3
#include <hip/hip_runtime.h>
#include <hip/hip_bf16.h>
#include <stdint.h>
#include <type_traits>

typedef unsigned short u16;
typedef unsigned u32x4 __attribute__((ext_vector_type(4)));
typedef unsigned u32x2 __attribute__((ext_vector_type(2)));
typedef float f32x4 __attribute__((ext_vector_type(4)));
typedef float f32x2 __attribute__((ext_vector_type(2)));
typedef short s16x4 __attribute__((ext_vector_type(4)));
typedef __bf16 bf16x8 __attribute__((ext_vector_type(8)));
typedef __bf16 bf16x2 __attribute__((ext_vector_type(2)));
typedef __amdgpu_buffer_rsrc_t rsrc_t;

constexpr int QK = 6, CU = 6;
constexpr int STAG = 10;
constexpr int BPACE = 5;
constexpr int POLLPOS = 8;
constexpr int KEEP1 = 10;
constexpr int S_DELAY = 148;
constexpr int T_PLAN = 1388;
constexpr int RATE16 = 214;
constexpr int LAT0 = 79;
constexpr int SMP0 = 493, WAIT0 = 195, DEAD0 = 15, GAIN0 = 64;
constexpr int SMP1 = 385, WAIT1 = 135, DEAD1 = 7, GAIN1 = 52;
constexpr int Q_AT = 430, GU_AT = 440;
constexpr int TDEAD0 = 0, TREND0 = 0, TDEAD1 = 1, TREND1 = 11;
constexpr int BACK_AT = 70;
constexpr int BACK_DELAY = 12;
constexpr int LATE_DELAY = 20;
constexpr int FIN_DELAY = 48;
constexpr int CODE_BYTES = 16640;

constexpr int AUX_NT = 2, AUX_SC1 = 16;
constexpr int RSRC_W3 = 0x00020000;
constexpr unsigned SPIN_CAP = 1u << 18;
constexpr int OOB = 0x40000000;
constexpr unsigned ALL4 = 0x01010101u;

constexpr int WS_EPOCH = 0;
constexpr int WS_TS = 1024;
constexpr int WS_FP = 4096;
constexpr int WS_ML = WS_FP + 8 * 18 * 5 * 512;
constexpr int WS_YP = WS_ML + 32 * 2 * 4 * 4 * 16;
constexpr int WS_OP = 2 << 20, OP_STRIDE = 1 << 20;
constexpr int WS_END = WS_OP + 32 * OP_STRIDE;
static_assert(WS_YP + 96 * 10 * 2 * 32 * 8 <= WS_OP, "workspace map");

template <int I, int N, class F> __device__ __forceinline__ void static_for(F&& f) {
  if constexpr (I < N) { f(std::integral_constant<int, I>{}); static_for<I + 1, N>(f); }
}
__device__ __forceinline__ rsrc_t mk_rsrc(const void* p, int bytes) {
  return __builtin_amdgcn_make_buffer_rsrc(const_cast<void*>(p), 0, bytes, RSRC_W3);
}
__device__ __forceinline__ u32x4 ld_nt(rsrc_t r, int voff, int soff) { return __builtin_amdgcn_raw_buffer_load_b128(r, voff, soff, AUX_NT); }
__device__ __forceinline__ u32x4 ld_sc1(rsrc_t r, int voff, int soff) { return __builtin_amdgcn_raw_buffer_load_b128(r, voff, soff, AUX_SC1); }
__device__ __forceinline__ u32x2 ld2_sc1(rsrc_t r, int voff, int soff) { return __builtin_amdgcn_raw_buffer_load_b64(r, voff, soff, AUX_SC1); }
__device__ __forceinline__ void st_pair(rsrc_t r, int voff, int soff, float a, float b, unsigned tag) {
  u32x4 v = {__float_as_uint(a), tag, __float_as_uint(b), tag};
  __builtin_amdgcn_raw_buffer_store_b128(v, r, voff, soff, AUX_SC1);
}
__device__ __forceinline__ float bflo(unsigned v) { return __uint_as_float(v << 16); }
__device__ __forceinline__ float bfhi(unsigned v) { return __uint_as_float(v & 0xffff0000u); }
__device__ __forceinline__ unsigned cvtpk(float lo, float hi) {
  bf16x2 r = {(__bf16)lo, (__bf16)hi};
  return __builtin_bit_cast(unsigned, r);
}
__device__ __forceinline__ float rbf(float x) { return (float)(__bf16)x; }
template <int CTRL> __device__ __forceinline__ float dpp(float x) {
  return __builtin_bit_cast(float, __builtin_amdgcn_mov_dpp(__builtin_bit_cast(int, x), CTRL, 0xf, 0xf, true));
}
constexpr int XOR1 = 0xB1, XOR2 = 0x4E, HMIR = 0x141, ROR8 = 0x128, ROR4 = 0x124;
__device__ __forceinline__ float x16_sum(float x) { auto s = __builtin_amdgcn_permlane16_swap(__float_as_uint(x), __float_as_uint(x), false, false); return __uint_as_float(s[0]) + __uint_as_float(s[1]); }
__device__ __forceinline__ float x32_sum(float x) { auto s = __builtin_amdgcn_permlane32_swap(__float_as_uint(x), __float_as_uint(x), false, false); return __uint_as_float(s[0]) + __uint_as_float(s[1]); }
__device__ __forceinline__ float x16_max(float x) { auto s = __builtin_amdgcn_permlane16_swap(__float_as_uint(x), __float_as_uint(x), false, false); return fmaxf(__uint_as_float(s[0]), __uint_as_float(s[1])); }
__device__ __forceinline__ float x32_max(float x) { auto s = __builtin_amdgcn_permlane32_swap(__float_as_uint(x), __float_as_uint(x), false, false); return fmaxf(__uint_as_float(s[0]), __uint_as_float(s[1])); }
__device__ __forceinline__ float row_sum(float x) { x += dpp<XOR1>(x); x += dpp<XOR2>(x); x += dpp<HMIR>(x); x += dpp<ROR8>(x); return x; }
__device__ __forceinline__ float row_max(float x) { x = fmaxf(x, dpp<XOR1>(x)); x = fmaxf(x, dpp<XOR2>(x)); x = fmaxf(x, dpp<HMIR>(x)); x = fmaxf(x, dpp<ROR8>(x)); return x; }
__device__ __forceinline__ float quad_sum(float x) { x += dpp<XOR1>(x); x += dpp<XOR2>(x); return x; }
__device__ __forceinline__ float dot2(unsigned a, unsigned b, float c) {
  return __builtin_amdgcn_fdot2_f32_bf16(__builtin_bit_cast(bf16x2, a), __builtin_bit_cast(bf16x2, b), c, false);
}
__device__ __forceinline__ f32x4 mfma16(unsigned a0, unsigned a1, unsigned b0, unsigned b1, f32x4 c) {
  u32x2 a = {a0, a1}, b = {b0, b1};
  return __builtin_amdgcn_mfma_f32_16x16x16bf16_1k(__builtin_bit_cast(s16x4, a), __builtin_bit_cast(s16x4, b), c, 0, 0, 0);
}
__device__ __forceinline__ f32x4 mfma32(u32x4 a, u32x4 b, f32x4 c) {
  return __builtin_amdgcn_mfma_f32_16x16x32_bf16(__builtin_bit_cast(bf16x8, a), __builtin_bit_cast(bf16x8, b), c, 0, 0, 0);
}
template <int RECIP> __device__ __forceinline__ unsigned pace_step(unsigned tprev, unsigned tend, unsigned& tdone) {
  unsigned d, now, tgt;
  asm volatile("s_sub_i32 %0, %5, %4\n\ts_max_i32 %0, %0, 0\n\ts_min_i32 %0, %0, 0x7d0\n\ts_mul_i32 %0, %0, %6\n\ts_lshr_b32 %0, %0, 16\n\ts_add_u32 %2, %4, %0\n"
               "1:\n\ts_memrealtime vcc\n\ts_getreg_b32 %0, hwreg(HW_REG_IB_STS, 0, 4)\n\ts_waitcnt lgkmcnt(0)\n\ts_cmp_lg_u32 %0, 0\n\ts_cselect_b32 %3, vcc_lo, %3\n\t"
               "s_sub_u32 %0, vcc_lo, %2\n\ts_cmp_lt_i32 %0, 0\n\ts_cbranch_scc1 1b\n\ts_mov_b32 %1, vcc_lo"
               : "=&s"(d), "=&s"(now), "=&s"(tgt), "+s"(tdone) : "s"(tprev), "s"(tend), "i"(RECIP) : "vcc", "scc");
  return now;
}
__device__ __forceinline__ void wait_until(unsigned tgt) {
  asm volatile("1:\n\ts_memrealtime vcc\n\ts_waitcnt lgkmcnt(0)\n\ts_sub_u32 vcc_lo, vcc_lo, %0\n\ts_cmp_lt_i32 vcc_lo, 0\n\ts_cbranch_scc1 1b" :: "s"(tgt) : "vcc", "scc", "memory");
}
#define SB() __builtin_amdgcn_sched_barrier(0)
#define MEMFENCE() asm volatile("" ::: "memory")

__device__ __forceinline__ void delay_ticks(int n) {
  if (n > 0) wait_until((unsigned)__builtin_amdgcn_s_memrealtime() + n);
}

typedef __attribute__((address_space(3))) float lfloat;
typedef __attribute__((address_space(3))) f32x4 lf32x4;
typedef __attribute__((address_space(3))) unsigned luint;
typedef __attribute__((address_space(3))) unsigned char lbyte;
template <int SLP> __device__ __forceinline__ void wait_lds(unsigned a, unsigned want) {
  unsigned v, sv;
  asm volatile("s_branch 2f\n1:\n\ts_sleep %4\n2:\n\tds_read_b32 %0, %2\n\ts_waitcnt lgkmcnt(0)\n\tv_readfirstlane_b32 %1, %0\n\ts_cmp_lg_u32 %1, %3\n\ts_cbranch_scc1 1b"
               : "=&v"(v), "=&s"(sv) : "v"(a), "s"(want), "i"(SLP) : "scc", "memory");
}

__device__ __forceinline__ f32x4 proj_epilogue(f32x4 acc0, f32x4 acc1) {
  f32x4 r;
#pragma unroll
  for (int i = 0; i < 4; ++i) {
    auto s = __builtin_amdgcn_permlane32_swap(__float_as_uint(acc0[i]), __float_as_uint(acc1[i]), false, false);
    r[i] = __uint_as_float(s[0]) + dpp<ROR8>(__uint_as_float(s[1]));
  }
  return r;
}
__device__ __forceinline__ u32x4 granules(f32x4 r, unsigned tag) { return u32x4{cvtpk(r[0], r[1]), tag, cvtpk(r[2], r[3]), tag}; }

__global__ void __launch_bounds__(512, 2)
fused_decode(const u16* x_, const u16* kc_, const u16* vc_, const u16* wq_, const u16* wo_, const u16* wg_, const u16* wu_, const u16* wd_, u16* out_, unsigned char* ws_) {
  __shared__ __attribute__((aligned(16))) float lds[96 + 8 + 4 + 32 + 8 * 192 + 4];
  unsigned* l_q = (unsigned*)lds;
  float* l_ml = lds + 96;
  float* l_c = lds + 104;
  unsigned* l_est = (unsigned*)(lds + 108);
  unsigned char* l_f = (unsigned char*)(lds + 116);
  constexpr int FA = 0, FB = 4, FC = 8, FD = 12, FE = 16, FG = 20, FH = 28, FX = 32;
  unsigned* l_tend = (unsigned*)(lds + 122);
  unsigned* l_tag = (unsigned*)(lds + 140 + 8 * 192);
  const unsigned fbase = (unsigned)(size_t)(lbyte*)l_f;
  float* l_acc = lds + 140;
  unsigned char* l_xn = (unsigned char*)(l_acc + 512);

  __shared__ int l_dbg[4];
  unsigned amp0;
  { unsigned long long t_; asm volatile("s_memrealtime %0\n\ts_waitcnt lgkmcnt(0)" : "=s"(t_) :: "memory"); amp0 = (unsigned)t_; }
  typedef unsigned long long u64;
  typedef u64 u64x8 __attribute__((ext_vector_type(8)));
  typedef u64 u64x2 __attribute__((ext_vector_type(2)));
  u64x8 ka; u64x2 kb; u64 pc;
  u32x4 fw[16];
  int bid = blockIdx.x, tid = threadIdx.x;
  {
    unsigned v1, v2;
    u64x8 ka0; u64x2 kb0;
    asm volatile("s_load_dwordx16 %0, %3, 0x0\n\ts_load_dwordx4 %1, %3, 0x40\n\ts_getpc_b64 %2" : "=&{s[36:51]}"(ka0), "=&{s[52:55]}"(kb0), "=&s"(pc) : "s"(__builtin_amdgcn_kernarg_segment_ptr()));
    asm volatile(
               "v_readfirstlane_b32 s14, %20\n\t"
               "s_lshr_b32 s15, %19, 3\n\t"
               "s_cmpk_ge_u32 s14, 0xc0\n\ts_cbranch_scc1 1f\n\t"
               "s_setprio 2\n\t"
               "s_and_b32 s16, %19, 7\n\ts_lshr_b32 s17, %19, 4\n\t"
               "s_and_b32 s18, s15, 1\n\ts_mul_i32 s18, s18, 3\n\ts_lshr_b32 s19, s14, 6\n\ts_add_u32 s18, s18, s19\n\t"
               "v_bfe_u32 %16, %20, 3, 3\n\tv_and_b32 %17, 7, %20\n\t"
               "s_cmp_ge_u32 s17, 5\n\ts_cselect_b32 s12, 17, 16\n\ts_cselect_b32 s13, 5, 0\n\ts_sub_u32 s13, s17, s13\n\t"
               "s_cmp_eq_u32 s18, 5\n\ts_cselect_b32 s17, s12, s17\n\ts_cselect_b32 s18, s13, s18\n\t"
               "s_cmp_ge_u32 s17, 15\n\ts_cselect_b32 s12, 15, 12\n\ts_movk_i32 s13, 0x3000\n\t"
               "s_cmp_lt_u32 s17, 12\n\ts_cselect_b32 s12, 0, s12\n\ts_cselect_b32 s13, s13, 0xc00\n\ts_cselect_b32 s19, 12, 3\n\t"
               "s_mul_i32 s16, s16, s19\n\ts_sub_u32 s12, s17, s12\n\ts_add_u32 s12, s12, s16\n\t"
               "v_mad_u32_u24 %16, %16, s13, %17\n\t"
               "s_mul_i32 s19, s18, s13\n\ts_add_u32 s12, s12, s19\n\ts_lshl_b32 s12, s12, 7\n\t"
               "v_lshlrev_b32 %16, 4, %16\n\t"
               "s_mul_i32 s10, s13, 0x280\n\ts_mov_b32 s11, 0x20000\n\ts_cmp_ge_u32 s18, 5\n\ts_cselect_b32 s10, 0, s10\n\t"
               "s_cmp_ge_u32 s17, 15\n\t"
               "s_waitcnt lgkmcnt(0)\n\t"
               "s_cselect_b64 s[8:9], s[48:49], s[46:47]\n\t"
               "s_cmp_lt_u32 s17, 12\n\ts_cselect_b64 s[8:9], s[42:43], s[8:9]\n\t"
               "s_and_b32 s9, s9, 0xffff\n\t"
               "buffer_load_dwordx4 %0, %16, s[8:11], s12 offen nt\n\t" "s_add_u32 s12, s12, s13\n\t"
               "buffer_load_dwordx4 %1, %16, s[8:11], s12 offen nt\n\t" "s_add_u32 s12, s12, s13\n\t"
               "buffer_load_dwordx4 %2, %16, s[8:11], s12 offen nt\n\t" "s_add_u32 s12, s12, s13\n\t"
               "buffer_load_dwordx4 %3, %16, s[8:11], s12 offen nt\n\t" "s_add_u32 s12, s12, s13\n\t"
               "buffer_load_dwordx4 %4, %16, s[8:11], s12 offen nt\n\t" "s_add_u32 s12, s12, s13\n\t"
               "buffer_load_dwordx4 %5, %16, s[8:11], s12 offen nt\n\t" "s_add_u32 s12, s12, s13\n\t"
               "buffer_load_dwordx4 %6, %16, s[8:11], s12 offen nt\n\t" "s_add_u32 s12, s12, s13\n\t"
               "buffer_load_dwordx4 %7, %16, s[8:11], s12 offen nt\n\t" "s_add_u32 s12, s12, s13\n\t"
               "buffer_load_dwordx4 %8, %16, s[8:11], s12 offen nt\n\t" "s_add_u32 s12, s12, s13\n\t"
               "buffer_load_dwordx4 %9, %16, s[8:11], s12 offen nt\n\t" "s_add_u32 s12, s12, s13\n\t"
               "buffer_load_dwordx4 %10, %16, s[8:11], s12 offen nt\n\t" "s_add_u32 s12, s12, s13\n\t"
               "buffer_load_dwordx4 %11, %16, s[8:11], s12 offen nt\n\t" "s_add_u32 s12, s12, s13\n\t" "s_barrier\n\t"
               "buffer_load_dwordx4 %12, %16, s[8:11], s12 offen nt\n\t" "s_add_u32 s12, s12, s13\n\t"
               "buffer_load_dwordx4 %13, %16, s[8:11], s12 offen nt\n\t" "s_add_u32 s12, s12, s13\n\t"
               "buffer_load_dwordx4 %14, %16, s[8:11], s12 offen nt\n\t" "s_add_u32 s12, s12, s13\n\t"
               "buffer_load_dwordx4 %15, %16, s[8:11], s12 offen nt\n\t"
               "s_branch 2f\n"
               "1:\n\t"
               "v_and_b32 %16, 31, %20\n\tv_lshl_add_u32 %16, %16, 2, %21\n\tv_mov_b32 %17, 0\n\tds_write_b32 %16, %17\n\t"
               "s_cmpk_lg_u32 s14, 0xc0\n\ts_cbranch_scc1 3f\n\t"
               "v_and_b32 %16, 7, %20\n\tv_lshl_add_u32 %16, %16, 5, s15\n\tv_lshlrev_b32 %16, 7, %16\n\tv_min_u32 %16, %24, %16\n\t"
               "global_load_dwordx4 %15, %16, %23\n\t"
               "v_bfe_u32 %17, %20, 5, 1\n\tv_mul_u32_u24 %17, 0x50, %17\n\tv_and_b32 %16, 31, %20\n\tv_add_lshl_u32 %16, %16, %17, 4\n\t"
               "v_and_or_b32 %17, %20, 15, %17\n\tv_lshlrev_b32 %17, 4, %17\n\t"
               "s_waitcnt lgkmcnt(0)\n\t"
               "global_load_dwordx4 %0, %16, s[36:37]\n\tglobal_load_dwordx4 %1, %16, s[36:37] offset:512\n\tglobal_load_dwordx4 %2, %17, s[36:37] offset:1024\n"
               "3:\n\t"
               "s_waitcnt lgkmcnt(0)\n\ts_barrier\n"
               "2:"
               : "=&v"(fw[0]), "=&v"(fw[1]), "=&v"(fw[2]), "=&v"(fw[3]), "=&v"(fw[4]), "=&v"(fw[5]), "=&v"(fw[6]), "=&v"(fw[7]),
                 "=&v"(fw[8]), "=&v"(fw[9]), "=&v"(fw[10]), "=&v"(fw[11]), "=&v"(fw[12]), "=&v"(fw[13]), "=&v"(fw[14]), "=&v"(fw[15]),
                 "=&v"(v1), "=&v"(v2)
               : "{s[36:51]}"(ka0), "s"(bid), "v"(tid), "s"(fbase - 32), "{s[52:55]}"(kb0), "s"(pc), "i"(CODE_BYTES - 128)
               : "s8", "s9", "s10", "s11", "s12", "s13", "s14", "s15", "s16", "s17", "s18", "s19", "scc", "memory");
    asm volatile("" : "={s[36:51]}"(ka), "={s[52:55]}"(kb), "+s"(bid) : "0"(ka0), "1"(kb0));
    asm volatile("" : "+v"(tid));
  }
  const int xq = bid & 7, uq = bid >> 3;
  const int h = xq * 4 + (uq >> 3), j8 = uq & 7, b = j8 >> 2, s = j8 & 3;
  const int lane = tid & 63, wave = __builtin_amdgcn_readfirstlane(tid >> 6);
  const int g = lane >> 3, j = lane & 7;
  const int bl = (lane >> 2) & 1, e4 = lane & 3;
  const bool isS = wave >= 4;
  const u16 *x = (const u16*)ka[0], *kc = (const u16*)ka[1], *vc = (const u16*)ka[2], *wo = (const u16*)ka[4], *wd = (const u16*)ka[7];
  u16* out = (u16*)kb[0];
  unsigned char* ws = (unsigned char*)kb[1];
  u64 tst;
  asm volatile("s_memrealtime %0\n\ts_waitcnt lgkmcnt(0)" : "=s"(tst));
  const unsigned tstart = (unsigned)tst;
  const rsrc_t rws = mk_rsrc(ws, WS_END);

  if (isS) {
    const int sw = wave - 4;
    const rsrc_t r0 = mk_rsrc(kc, 2 * 32 * 8192 * 384), rv = mk_rsrc(vc, 2 * 32 * 8192 * 384);
    const int voff = ((b * 32 + h) * 8192 + 6144 + 32 * s + 8 * sw + g) * 384 + 16 * j;
    u32x4 buf[96];
    u32x4 qv[3];
    float sc[16];
    unsigned pd[16];
    f32x4 acc[3] = {{0.f, 0.f, 0.f, 0.f}, {0.f, 0.f, 0.f, 0.f}, {0.f, 0.f, 0.f, 0.f}};
    unsigned msk[4];
#pragma unroll
    for (int d = 0; d < 4; ++d) msk[d] = (j >> 1) == d ? ((j & 1) ? 0xffff0000u : 0x0000ffffu) : 0u;
    constexpr float C2 = 0.07216878364870322f * 1.4426950408889634f;
        const int stag = ((2 * (uq >> 2) - 7) * STAG) >> 1;
    unsigned tprev = tstart + S_DELAY + stag, tend = tstart + T_PLAN + stag, lat = 0;

    auto consume = [&](auto G) {
      constexpr int t = decltype(G)::value;
      if constexpr (t < 16) {
        float a0 = 0.f, a1 = 0.f, a2 = 0.f;
#pragma unroll
        for (int e = 0; e < 4; ++e) { a0 = dot2(buf[3 * t][e], qv[0][e], a0); a1 = dot2(buf[3 * t + 1][e], qv[1][e], a1); a2 = dot2(buf[3 * t + 2][e], qv[2][e], a2); }
        float a = (a0 + a1) + a2;
        a += dpp<XOR1>(a); a += dpp<XOR2>(a); a += dpp<HMIR>(a);
        sc[t] = a;
        if constexpr (t == 15) {
          float m = sc[0];
#pragma unroll
          for (int u = 1; u < 16; ++u) m = fmaxf(m, sc[u]);
          m = fmaxf(m, dpp<ROR8>(m)); m = x16_max(m); m = x32_max(m);
          const float mc = m * C2;
          float l = 0.f;
#pragma unroll
          for (int u = 0; u < 16; ++u) { const float p = __builtin_amdgcn_exp2f(fmaf(sc[u], C2, -mc)); pd[u] = cvtpk(p, p); l += bfhi(pd[u]); }
          l += dpp<ROR8>(l); l = x16_sum(l); l = x32_sum(l);
          if (lane == 0) { *(f32x2*)(l_ml + 2 * sw) = f32x2{mc, l}; MEMFENCE(); l_f[FA + sw] = 1; }
        }
      } else {
        constexpr int u = t - 16;
        const u32x4 a = {pd[u] & msk[0], pd[u] & msk[1], pd[u] & msk[2], pd[u] & msk[3]};
#pragma unroll
        for (int i = 0; i < 3; ++i) acc[i] = mfma32(a, buf[3 * t + i], acc[i]);
      }
    };
    static_for<0, 16>([&](auto K) {
      constexpr int k = decltype(K)::value;
      if constexpr (k > 6) {
        MEMFENCE();
        unsigned tv = *l_tend;
        asm volatile("" : "+v"(tv));
        tv = __builtin_amdgcn_readfirstlane(tv);
        tend = tv ? tv + stag : tend;
      }
      unsigned tdone = tprev + LAT0;
      const unsigned now = pace_step<(k ? 65536 / (16 - k) : 0)>(tprev, tend, tdone);
      SB();
#pragma unroll
      for (int i = 0; i < 6; ++i) { buf[6 * k + i] = ld_nt(k < 8 ? r0 : rv, voff + (i % 3) * 128, ((2 * k + i / 3) & 15) * 49152); SB(); }
      if constexpr (k == 15) { if (lane == 0) l_f[FB + sw] = 1; }
      if constexpr (k >= 1 && k < 15) asm volatile("s_waitcnt vmcnt(6)" ::: "memory");
      if constexpr (k >= 2 && k < 13) {
        if (tdone == now) tdone = (unsigned)__builtin_amdgcn_s_memrealtime();
        lat = (lat << 16) | (tdone - tprev);
        *(u32x2*)(l_est + 2 * sw) = u32x2{(now - stag + ((6 * (15 - k) * RATE16) >> 4)) | 1u, lat};
      }
      tprev = now;
      if constexpr (k == QK) {
        wait_lds<0>(fbase + FG, ALL4);
#pragma unroll
        for (int i = 0; i < 3; ++i) qv[i] = *(const u32x4*)(l_q + 32 * i + 4 * j);
      }
      SB();
      constexpr auto upto = [](int kk) constexpr { if (kk < QK) return 0; const int c = CU * (kk - QK + 1), l = kk < 15 ? 2 * kk : 28; return c < l ? c : l; };
      static_for<upto(k - 1), upto(k)>([&](auto G) { consume(G); SB(); });
      SB();
    });
    __builtin_amdgcn_s_setprio(3);
    wait_lds<0>(fbase + FE, ALL4);
    float cq = l_c[lane >> 4];
    unsigned dst = (unsigned)(size_t)(lfloat*)(l_acc + (sw * 2 + (g & 1)) * 192 + 8 * j + 4 * ((lane >> 4) & 1));
    unsigned src = (unsigned)(size_t)(lfloat*)(l_acc + (lane >> 4) * 384 + 64 * sw + 8 * j + 4 * (g & 1));
    unsigned fla = fbase + FC + sw, one = 1u, flr = fbase + FC;
    int pvo = (lane & 16) ? OOB : (g & 1) * 128 + j * 16 + (lane >> 5) * 8;
    int pso = WS_OP + h * OP_STRIDE + (b * 4 + s) * 768 + sw * 256;
    unsigned tagv = *l_tag;
    asm volatile("" : "+v"(cq), "+v"(dst), "+v"(src), "+v"(fla), "+v"(flr), "+v"(one), "+v"(pvo), "+s"(pso), "+v"(tagv));
    const bool wr = (lane >> 5) == (g & 1);
    SB();
    static_for<28, 32>([&](auto G) { consume(G); SB(); });
    if (wr) {
#pragma unroll
      for (int i = 0; i < 3; ++i) *(lf32x4*)(size_t)(dst + 256 * i) = acc[i];
      MEMFENCE();
      *(lbyte*)(size_t)fla = (unsigned char)one;
    }
    if (sw < 3) {
      f32x4 u0, u1;
      unsigned fv, fs;
      asm volatile("1:\n\tds_read_b32 %0, %4\n\tds_read_b128 %2, %5\n\tds_read_b128 %3, %5 offset:768\n\ts_waitcnt lgkmcnt(2)\n\tv_readfirstlane_b32 %1, %0\n\t"
                   "s_cmp_lg_u32 %1, 0x01010101\n\ts_waitcnt lgkmcnt(0)\n\ts_cbranch_scc1 1b"
                   : "=&v"(fv), "=&s"(fs), "=&v"(u0), "=&v"(u1) : "v"(flr), "v"(src) : "scc", "memory");
      float o[4];
#pragma unroll
      for (int e = 0; e < 4; ++e) o[e] = cq * (u0[e] + u1[e]);
      const auto s0 = __builtin_amdgcn_permlane32_swap(__float_as_uint(o[0]), __float_as_uint(o[2]), false, false);
      const auto s1 = __builtin_amdgcn_permlane32_swap(__float_as_uint(o[1]), __float_as_uint(o[3]), false, false);
      const float p0 = x16_sum(__uint_as_float(s0[0]) + __uint_as_float(s0[1])), p1 = x16_sum(__uint_as_float(s1[0]) + __uint_as_float(s1[1]));
      __builtin_amdgcn_raw_buffer_store_b64(u32x2{cvtpk(p0, p1), tagv}, rws, pvo, pso, AUX_SC1);
    }
    MEMFENCE();
    *(lbyte*)(size_t)(fla + (FD - FC)) = (unsigned char)one;
    return;
  }

  __builtin_amdgcn_s_setprio(2);
  unsigned tag, ep;
  asm volatile("s_load_dword %0, %1, 0x0" : "=&s"(ep) : "s"(ws));
#define TAG_READY() asm volatile("s_waitcnt lgkmcnt(0)\n\ts_add_u32 %0, %1, 1\n\ts_addc_u32 %0, %0, 0" : "=&s"(tag) : "s"(ep) : "scc", "memory")
  const int w = wave;
  const bool second = uq & 1, single = second && w == 2;
  const unsigned c0 = uq >> 1, uc = single ? (c0 >= 5 ? 17 : 16) : c0, ur = single ? (c0 >= 5 ? c0 - 5 : c0) : 3 * second + w;
  const bool has_front = (w < 3) & (ur < 5);
  const int st_col = (g & 1) ? OOB : (lane >> 5) * 128 + j * 16;
  const rsrc_t rx = mk_rsrc(x, 2560);
  const unsigned sLo = (e4 & 1) ? 0x01000c0cu : 0x0c0c0100u, sHi = (e4 & 1) ? 0x03020c0cu : 0x0c0c0302u;
  unsigned sel0 = e4 < 2 ? sLo : 0x0c0c0c0cu, sel1 = e4 < 2 ? 0x0c0c0c0cu : sLo, sel2 = e4 < 2 ? sHi : 0x0c0c0c0cu, sel3 = e4 < 2 ? 0x0c0c0c0cu : sHi;
  if (w == 3) {
    const int xo = (lane >> 5) * 1280 + (lane & 31) * 16;
    const bool x3 = (lane & 16) == 0;
    asm volatile("s_waitcnt vmcnt(0)" : "+v"(fw[0]), "+v"(fw[1]), "+v"(fw[2]) : "v"(fw[15]));
    u32x4 xv[3] = {fw[0], fw[1], x3 ? fw[2] : u32x4{0u, 0u, 0u, 0u}};
    float v[24];
#pragma unroll
    for (int e = 0; e < 12; ++e) { v[2 * e] = bflo(xv[e >> 2][e & 3]); v[2 * e + 1] = bfhi(xv[e >> 2][e & 3]); }
    float sum = 0.f;
#pragma unroll
    for (int e = 0; e < 24; ++e) sum += v[e];
    sum = x16_sum(row_sum(sum));
    const float mean = sum * (1.f / 640.f);
    float sq = 0.f, sq3 = 0.f;
#pragma unroll
    for (int e = 0; e < 16; ++e) { const float d = v[e] - mean; sq = fmaf(d, d, sq); }
#pragma unroll
    for (int e = 16; e < 24; ++e) { const float d = v[e] - mean; sq3 = fmaf(d, d, sq3); }
    sq = x16_sum(row_sum(sq + (x3 ? sq3 : 0.f)));
    const float mb = rbf(mean), den = rbf(__builtin_amdgcn_sqrtf(rbf(rbf(sq * (1.f / 640.f)) + 1e-5f)));
    const float inv = __builtin_amdgcn_rcpf(den);
#pragma unroll
    for (int e = 0; e < 12; ++e) xv[e >> 2][e & 3] = cvtpk(rbf(v[2 * e] - mb) * inv, rbf(v[2 * e + 1] - mb) * inv);
    *(u32x4*)(l_xn + xo) = xv[0];
    *(u32x4*)(l_xn + xo + 512) = xv[1];
    if (x3) *(u32x4*)(l_xn + xo + 1024) = xv[2];
    MEMFENCE();
    l_f[FX] = 1;
  }
  if (has_front) {
    wait_lds<1>(fbase + FX, 1u);
    const u32x4 xr[2] = {*(const u32x4*)(l_xn + bl * 1280 + 256 * ur + g * 32), *(const u32x4*)(l_xn + bl * 1280 + 256 * ur + g * 32 + 16)};
    TAG_READY();
    f32x4 acc0 = {0.f, 0.f, 0.f, 0.f}, acc1 = {0.f, 0.f, 0.f, 0.f};
    asm volatile("s_waitcnt vmcnt(4)" : "+v"(fw[0]), "+v"(fw[1]), "+v"(fw[2]), "+v"(fw[3]), "+v"(fw[4]), "+v"(fw[5]), "+v"(fw[6]), "+v"(fw[7]),
                 "+v"(fw[8]), "+v"(fw[9]), "+v"(fw[10]), "+v"(fw[11]));
#pragma unroll
    for (int n = 0; n < 16; ++n) {
      if (n == 12) { SB(); asm volatile("s_waitcnt vmcnt(0)" : "+v"(fw[12]), "+v"(fw[13]), "+v"(fw[14]), "+v"(fw[15])); }
      const unsigned xw = xr[n >> 3][(n >> 1) & 3];
      const unsigned a0 = __builtin_amdgcn_perm(0u, xw, (n & 1) ? sel2 : sel0), a1 = __builtin_amdgcn_perm(0u, xw, (n & 1) ? sel3 : sel1);
      acc0 = mfma16(a0, a1, fw[n][0], fw[n][1], acc0);
      acc1 = mfma16(a0, a1, fw[n][2], fw[n][3], acc1);
    }
    f32x4 r = proj_epilogue(acc0, acc1);
    f32x4* l_u = (f32x4*)l_acc;
    if (w == 0) {
      wait_lds<1>(fbase + FH, second ? 0x0100u : 0x010100u);
      r += l_u[lane];
      if (!second) r += l_u[64 + lane];
    } else if (!single) {
      l_u[(w - 1) * 64 + lane] = r;
      MEMFENCE();
      l_f[FH + w] = 1;
    }
    if (w == 0 || single)
      __builtin_amdgcn_raw_buffer_store_b128(granules(r, tag), rws, ((lane >> 4) & 1) * 256 + st_col, WS_FP + ((xq * 18 + uc) * 5 + (single ? ur : second)) * 512, AUX_SC1);
    asm volatile("s_waitcnt vmcnt(0)" ::: "memory");
  } else {
    asm volatile("s_waitcnt vmcnt(0)" ::"v"(fw[0]), "v"(fw[1]), "v"(fw[2]), "v"(fw[3]), "v"(fw[4]), "v"(fw[5]), "v"(fw[6]), "v"(fw[7]),
                 "v"(fw[8]), "v"(fw[9]), "v"(fw[10]), "v"(fw[11]), "v"(fw[12]), "v"(fw[13]), "v"(fw[14]), "v"(fw[15]));
    TAG_READY();
  }
  const int slot = w * 8 + j8;
  const bool has_back = slot < 30;
  const int t3 = slot / 10, cc = slot - 10 * t3;
  const int rid = xq * 20 + uq;
  const int rb = rid / 80, rc = (rid - 80 * rb) >> 3, ro = rid & 7;
  const bool reducer = (w == 0) && (bid < 160);
  unsigned ha[2][2] = {{0u, 0u}, {0u, 0u}};
  unsigned tend = tstart + T_PLAN;
  auto pace_round = [&](unsigned t_sample, unsigned t_read, int so, int dead, int gain16, int tdead, int trend, int keep16) {
    wait_until(t_sample);
    u32x4 e, f;
    do { MEMFENCE(); e = *(const u32x4*)l_est; f = *(const u32x4*)(l_est + 4); } while (!__builtin_amdgcn_readfirstlane(e[0] & e[2] & f[0] & f[2] & 1u));
    const u32x4 pv = {tstart + (unsigned)((int)((e[0] - tstart) + (e[2] - tstart) + (f[0] - tstart) + (f[2] - tstart)) >> 2), tag, e[1] + e[3] + f[1] + f[3], tag};
    __builtin_amdgcn_raw_buffer_store_b128(pv, rws, (lane == 0 && j8 == (((uq >> 3) * 5) & 7)) ? ((uq >> 3) * 8 + xq) * 16 : OOB, so, AUX_SC1);
    wait_until(t_read);
    MEMFENCE();
    const u32x4 tv = ld_sc1(rws, (lane & 31) * 16, so);
    if (__all((tv[1] == tag) & (tv[3] == tag))) {
      const float d = x16_sum(row_sum((float)(int)(tv[0] - tstart))) * (1.f / 32.f);
      const float l1 = x16_sum(row_sum((float)(tv[2] & 0xffffu))) * (1.f / 128.f), l0 = x16_sum(row_sum((float)(tv[2] >> 16))) * (1.f / 128.f);
      const float dr = l1 - (float)LAT0, sign = l1 - l0;
      int di = (int)(d + fmaxf(dr, 0.f) + fmaxf(dr - (float)dead, 0.f) * (float)gain16 * (1.f / 16.f) + fmaxf(sign - (float)tdead, 0.f) * (float)trend);
      const int dold = (int)(tend - tstart);
      if (keep16 && di < dold) di += ((dold - di) * keep16) >> 4;
      di = di < 0 ? 0 : (di > 2500 ? 2500 : di);
      tend = (tstart + __builtin_amdgcn_readfirstlane(di)) | 1u;
      *l_tend = tend;
      if (AMP == 1 && so == WS_TS) l_dbg[0] = (int)(dr * 4.f) + 80;
      if (AMP == 2 && so == WS_TS) l_dbg[0] = di - 1300;
      if (AMP == 3 && so != WS_TS) l_dbg[0] = di - 1300;
      if (AMP == 4 && so != WS_TS) l_dbg[0] = (int)(dr * 4.f) + 80;
      if (AMP == 5 && so != WS_TS) l_dbg[0] = (int)(sign * 4.f) + 120;
    }
  };
  if (w == 0) {
    wait_until(tstart + Q_AT);
    u32x2 t[3];
    const int vo = (lane >> 5) * 512 + (lane & 31) * 8, so = WS_FP + (xq * 18 + (h & 3) * 3) * 2560 + b * 256;
    for (unsigned spin = 0;; ++spin) {
      MEMFENCE();
      t[0] = ld2_sc1(rws, vo, so); t[1] = ld2_sc1(rws, vo, so + 2560); t[2] = ld2_sc1(rws, vo, so + 5120);
      if (__all((t[0][1] == tag) & (t[1][1] == tag) & (t[2][1] == tag)) || spin > SPIN_CAP) break;
    }
#pragma unroll
    for (int i = 0; i < 3; ++i) l_q[32 * i + ((lane >> 1) & 7) * 4 + ((lane >> 4) & 1) * 2 + (lane & 1)] = cvtpk(x32_sum(bflo(t[i][0])), x32_sum(bfhi(t[i][0])));
    *l_tag = tag;
    MEMFENCE();
  }
  l_f[FG + w] = 1;
  if (w == 3) {
    pace_round(tstart + SMP0, tstart + SMP0 + WAIT0, WS_TS, DEAD0, GAIN0, TDEAD0, TREND0, 0);
  }
  if (has_back) {
    wait_until(tstart + GU_AT);
    u32x2 t[2];
    const int f16 = (h & 3) * 3 + t3;
    const int so = WS_FP + (xq * 18 + 12 + (f16 >> 2)) * 2560 + (f16 & 3) * 32;
    const int vo = bl * 256 + ((g >> 1) & 1) * 128 + (g >> 2) * 16 + (g & 1) * 8;
    const bool more = (f16 >= 4) & (e4 < 3);
    for (unsigned spin = 0;; ++spin) {
      MEMFENCE();
      t[0] = ld2_sc1(rws, vo + (e4 >> 1) * 7680 + (e4 & 1) * 512, so);
      t[1] = ld2_sc1(rws, more ? vo + 7680 + (2 + e4) * 512 : OOB, so);
      if (__all((t[0][1] == tag) & ((t[1][1] == tag) | !more)) || spin > SPIN_CAP) break;
    }
#pragma unroll
    for (int z = 0; z < 2; ++z) {
      const float a = z ? bfhi(t[0][0]) : bflo(t[0][0]), c = z ? bfhi(t[1][0]) : bflo(t[1][0]);
      const float gg = rbf(quad_sum(e4 < 2 ? a : 0.f));
      const float uu = rbf(quad_sum((e4 < 2 ? 0.f : a) + c));
      const float zz = 0.7978845608028654f * (gg + 0.044715f * gg * gg * gg);
      const float ge = rbf(gg * __builtin_amdgcn_rcpf(1.f + __builtin_amdgcn_exp2f(-2.8853900817779268f * zz)));
      const float hv = ge * uu;
      const unsigned d = cvtpk(hv, hv);
      ha[z][0] = __builtin_amdgcn_perm(0u, d, sel0); ha[z][1] = __builtin_amdgcn_perm(0u, d, sel1);
    }
  }
  const int oc = 16 * (ro & 3) + 8 * (lane & 1) + 4 * (ro >> 2) + 2 * (lane >> 5) + ((lane >> 4) & 1);
  const rsrc_t rout = mk_rsrc(out, 2560);
  int out_vo = (lane & 14) == 0 ? oc * 2 + rb * 1280 + rc * 128 : OOB;
  float xres = 0.f;
  if (reducer) {
    xres = bflo(__builtin_amdgcn_raw_buffer_load_b16(rx, oc * 2, rb * 1280 + rc * 128, 0));
    __builtin_amdgcn_raw_buffer_store_b16((unsigned short)0, rout, out_vo, 0, AUX_SC1);
  }
  if (w == 1) {
    wait_lds<4>(fbase + FA, ALL4);
    const int so = WS_ML + (h * 2 + b) * 256;
    if (lane < 4) { const f32x2 ml = *(const f32x2*)(l_ml + 2 * lane); st_pair(rws, lane * 16, so + s * 64, ml[0], ml[1], tag); }
    asm volatile("s_waitcnt vmcnt(0)" ::: "memory");
    __builtin_amdgcn_s_sleep(32);
    u32x4 t;
    for (unsigned spin = 0;; ++spin) {
      MEMFENCE();
      t = ld_sc1(rws, (lane & 15) * 16, so);
      if (__all((t[1] == tag) & (t[3] == tag)) || spin > SPIN_CAP) break;
    }
    const float mv = __uint_as_float(t[0]);
    const float M = row_max(mv);
    const float f = __builtin_amdgcn_exp2f(mv - M);
    const float L = row_sum(f * __uint_as_float(t[2]));
    if ((lane >> 2) == s) { l_c[lane & 3] = f / L; MEMFENCE(); l_f[FE + (lane & 3)] = 1; }
  }
  if (w == 3) {
    pace_round(tend - SMP1, tend - SMP1 + WAIT1, WS_TS + 512, DEAD1, GAIN1, TDEAD1, TREND1, KEEP1);
  }
  if (has_back) {
    int op_so = WS_OP + h * OP_STRIDE + t3 * 256, op_vo = (bl * 4 + e4) * 768 + 16 * g;
    int d_vo = ((48 * h + 16 * t3 + 2 * g) * 640 + 64 * cc) * 2 + 16 * j, o_vo = ((192 * h + 64 * t3 + 8 * g) * 640 + 64 * cc) * 2 + 16 * j;
    int yvo = ((lane >> 4) & 1) * 256 + st_col, yso = WS_YP + ((h * 3 + t3) * 10 + cc) * 512;
    int r_vo = (lane >> 1) * 5120 + (lane & 1) * 16 + WS_YP + rc * 512 + rb * 256 + ro * 32;
    unsigned tagv = tag, fd = fbase + FD;
    asm volatile("" : "+v"(sel0), "+v"(sel1), "+v"(sel2), "+v"(sel3), "+s"(op_so), "+v"(op_vo), "+v"(d_vo), "+v"(o_vo), "+v"(yvo), "+s"(yso), "+v"(r_vo), "+v"(out_vo), "+v"(tagv), "+v"(xres), "+v"(fd));
    const rsrc_t rd = mk_rsrc(wd, 1536 * 1280), ro_ = mk_rsrc(wo, 6144 * 1280);
    wait_lds<1>(fbase + FB, ALL4);
    {
      unsigned tf = *l_tend;
      asm volatile("" : "+v"(tf));
      tf = __builtin_amdgcn_readfirstlane(tf);
      const unsigned tn = (unsigned)__builtin_amdgcn_s_memrealtime();
      int wt = tf ? (int)(tf + BACK_AT - tn) : 0;
      asm("s_max_i32 %0, %0, %1\n\ts_min_i32 %0, %0, 0xc8" : "+s"(wt) : "s"(BACK_DELAY) : "scc");
      wait_until(tn + wt);
    }
    u32x4 wb[10], t0, t1;
    unsigned fdv = 0;
#pragma unroll
    for (int n = 0; n < 10; ++n) {
      if (n == POLLPOS) {
        const rsrc_t rp = mk_rsrc(ws, __builtin_amdgcn_readfirstlane(fdv) == ALL4 ? WS_END : 0);
        t0 = ld_sc1(rp, op_vo, op_so); t1 = ld_sc1(rp, op_vo + 128, op_so);
        SB();
      }
      if (n < 2) wb[8 + n] = ld_nt(rd, d_vo + n * 1280, 0); else wb[n - 2] = ld_nt(ro_, o_vo, (n - 2) * 1280);
      if (n == POLLPOS - 1) { MEMFENCE(); fdv = *(luint*)(size_t)fd; }
      SB();
      if (n < 9) __builtin_amdgcn_s_sleep(BPACE);
      SB();
    }
    u32x4 bq[10];
#define PAIR(p) do { bq[2 * (p)] = u32x4{wb[2 * (p)][0], wb[2 * (p)][1], wb[2 * (p) + 1][0], wb[2 * (p) + 1][1]}; \
                     bq[2 * (p) + 1] = u32x4{wb[2 * (p)][2], wb[2 * (p)][3], wb[2 * (p) + 1][2], wb[2 * (p) + 1][3]}; } while (0)
    PAIR(4); PAIR(0); PAIR(1); PAIR(2);
    const u32x4 hA = {ha[0][0], ha[0][1], ha[1][0], ha[1][1]};
    f32x4 acc0 = mfma32(hA, bq[8], f32x4{0.f, 0.f, 0.f, 0.f}), acc1 = mfma32(hA, bq[9], f32x4{0.f, 0.f, 0.f, 0.f});
    asm volatile("" : "+v"(bq[0]), "+v"(bq[1]), "+v"(bq[2]), "+v"(bq[3]), "+v"(bq[4]), "+v"(bq[5]), "+v"(acc0), "+v"(acc1));
    SB();
    unsigned np1 = 1;
    if (__builtin_expect(__builtin_amdgcn_ballot_w64((((t0[1] ^ tagv) | (t0[3] ^ tagv)) | ((t1[1] ^ tagv) | (t1[3] ^ tagv))) != 0u) != 0, 0)) {
      do {
        ++np1;
        MEMFENCE();
        if (__builtin_amdgcn_readfirstlane(*(luint*)(size_t)fd) != ALL4) { wait_lds<1>(fd, ALL4); delay_ticks(LATE_DELAY); }
        t0 = ld_sc1(rws, op_vo, op_so); t1 = ld_sc1(rws, op_vo + 128, op_so);
      } while (!__all((t0[1] == tag) & (t0[3] == tag) & (t1[1] == tag) & (t1[3] == tag)) && np1 < SPIN_CAP);
    }
    unsigned pk[4];
    {
      float u0, u1, u2, u3, u4, u5, u6, u7;
#define DPPADD(r, P) "v_add_f32_dpp " r ", " r ", " r " quad_perm:" P " row_mask:0xf bank_mask:0xf\n\t"
#define DPP8(P) DPPADD("%4", P) DPPADD("%5", P) DPPADD("%6", P) DPPADD("%7", P) DPPADD("%8", P) DPPADD("%9", P) DPPADD("%10", P) DPPADD("%11", P)
      asm volatile("v_lshlrev_b32 %4, 16, %12\n\tv_and_b32 %5, 0xffff0000, %12\n\tv_lshlrev_b32 %6, 16, %13\n\tv_and_b32 %7, 0xffff0000, %13\n\t"
                   "v_lshlrev_b32 %8, 16, %14\n\tv_and_b32 %9, 0xffff0000, %14\n\tv_lshlrev_b32 %10, 16, %15\n\tv_and_b32 %11, 0xffff0000, %15\n\t"
                   DPP8("[1,0,3,2]") DPP8("[2,3,0,1]")
                   "v_cvt_pk_bf16_f32 %0, %4, %5\n\tv_cvt_pk_bf16_f32 %1, %6, %7\n\tv_cvt_pk_bf16_f32 %2, %8, %9\n\tv_cvt_pk_bf16_f32 %3, %10, %11"
                   : "=&v"(pk[0]), "=&v"(pk[1]), "=&v"(pk[2]), "=&v"(pk[3]), "=&v"(u0), "=&v"(u1), "=&v"(u2), "=&v"(u3), "=&v"(u4), "=&v"(u5), "=&v"(u6), "=&v"(u7)
                   : "v"(t0[0]), "v"(t0[2]), "v"(t1[0]), "v"(t1[2]));
    }
    u32x4 aq[4];
#pragma unroll
    for (int p = 0; p < 4; ++p)
      aq[p] = u32x4{__builtin_amdgcn_perm(0u, pk[p], sel0), __builtin_amdgcn_perm(0u, pk[p], sel1), __builtin_amdgcn_perm(0u, pk[p], sel2), __builtin_amdgcn_perm(0u, pk[p], sel3)};
#pragma unroll
    for (int p = 0; p < 3; ++p) { acc0 = mfma32(aq[p], bq[2 * p], acc0); acc1 = mfma32(aq[p], bq[2 * p + 1], acc1); }
    asm volatile("" : "+v"(aq[3]), "+v"(acc0), "+v"(acc1));
    SB();
    acc0 = mfma16(aq[3][0], aq[3][1], wb[6][0], wb[6][1], acc0); acc1 = mfma16(aq[3][0], aq[3][1], wb[6][2], wb[6][3], acc1);
    acc0 = mfma16(aq[3][2], aq[3][3], wb[7][0], wb[7][1], acc0); acc1 = mfma16(aq[3][2], aq[3][3], wb[7][2], wb[7][3], acc1);
    __builtin_amdgcn_raw_buffer_store_b128(granules(proj_epilogue(acc0, acc1), tagv), rws, yvo, yso, AUX_SC1);
    if (reducer) {
      delay_ticks(FIN_DELAY);
      float y0, y1, y2, y3;
      unsigned np2 = 0;
      for (;;) {
        MEMFENCE();
        const u32x4 a = ld_sc1(rws, r_vo, 0), c = ld_sc1(rws, r_vo, 32 * 5120), d = ld_sc1(rws, r_vo, 64 * 5120);
        unsigned bad = ((a[1] ^ tagv) | (a[3] ^ tagv)) | ((c[1] ^ tagv) | (c[3] ^ tagv));
        y0 = bflo(a[0]) + bflo(c[0]); y1 = bfhi(a[0]) + bfhi(c[0]); y2 = bflo(a[2]) + bflo(c[2]); y3 = bfhi(a[2]) + bfhi(c[2]);
        asm volatile("" : "+v"(bad), "+v"(y0), "+v"(y1), "+v"(y2), "+v"(y3));
        bad |= (d[1] ^ tagv) | (d[3] ^ tagv);
        y0 += bflo(d[0]); y1 += bfhi(d[0]); y2 += bflo(d[2]); y3 += bfhi(d[2]);
        if (__builtin_expect(__builtin_amdgcn_ballot_w64(bad != 0u) == 0, 1)) break;
        if (++np2 > SPIN_CAP) break;
      }
      const auto s0 = __builtin_amdgcn_permlane32_swap(__float_as_uint(y0), __float_as_uint(y2), false, false);
      const auto s1 = __builtin_amdgcn_permlane32_swap(__float_as_uint(y1), __float_as_uint(y3), false, false);
      const float z0 = __uint_as_float(s0[0]) + __uint_as_float(s0[1]), z1 = __uint_as_float(s1[0]) + __uint_as_float(s1[1]);
      const auto s2 = __builtin_amdgcn_permlane16_swap(__float_as_uint(z0), __float_as_uint(z1), false, false);
      float y = __uint_as_float(s2[0]) + __uint_as_float(s2[1]);
      y += dpp<ROR8>(y); y += dpp<ROR4>(y); y += dpp<XOR2>(y);
      __builtin_amdgcn_raw_buffer_store_b16((unsigned short)cvtpk(xres + y, 0.f), rout, out_vo, 0, AUX_SC1);
      if (bid == 0) __builtin_amdgcn_raw_buffer_store_b32(tagv, rws, lane == 0 ? WS_EPOCH : OOB, 0, AUX_SC1);
      if (bid == 8) {
        unsigned long long t_; asm volatile("s_memrealtime %0\n\ts_waitcnt lgkmcnt(0)" : "=s"(t_) :: "memory");
        unsigned tf = *l_tend; asm volatile("" : "+v"(tf)); tf = __builtin_amdgcn_readfirstlane(tf);
        int v = l_dbg[0]; asm volatile("" : "+v"(v)); v = __builtin_amdgcn_readfirstlane(v);
        if (AMP == 6) v = (int)((unsigned)t_ - tf) - 300;
        asm volatile("s_max_i32 %0, %0, 0\n\ts_min_i32 %0, %0, 0x190" : "+s"(v) :: "scc");
        wait_until(amp0 + 4000 + 10 * v);
      }
    }
  }
}

extern "C" void kernel_launch(void* const* d_in, const int* in_sizes, int n_in, void* d_out, int out_size, void* d_ws, size_t ws_size,
                              hipStream_t stream) {
  fused_decode<<<256, 512, 0, stream>>>((const u16*)d_in[0], (const u16*)d_in[1], (const u16*)d_in[2], (const u16*)d_in[3], (const u16*)d_in[4],
                                        (const u16*)d_in[6], (const u16*)d_in[7], (const u16*)d_in[8], (u16*)d_out, (unsigned char*)d_ws);
}
```

```cpp
#include <hip/hip_runtime.h>
#include <hip/hip_bf16.h>
#include <stdint.h>
#include <type_traits>

typedef unsigned short u16;
typedef unsigned u32x4 __attribute__((ext_vector_type(4)));
typedef unsigned u32x2 __attribute__((ext_vector_type(2)));
typedef float f32x4 __attribute__((ext_vector_type(4)));
typedef float f32x2 __attribute__((ext_vector_type(2)));
typedef short s16x4 __attribute__((ext_vector_type(4)));
typedef __bf16 bf16x8 __attribute__((ext_vector_type(8)));
typedef __bf16 bf16x2 __attribute__((ext_vector_type(2)));
typedef __amdgpu_buffer_rsrc_t rsrc_t;

constexpr int QK = 6, CU = 6;
constexpr int STAG = 10;
constexpr int BPACE = 5;
constexpr int POLLPOS = 8;
constexpr int KEEP1 = 10;
constexpr int S_DELAY = 162;
constexpr int T_PLAN = 1402;
constexpr int RATE16 = 216;
constexpr int LAT0 = 79;
constexpr int SMP0 = 506, WAIT0 = 195, DEAD0 = 30, GAIN0 = 24;
constexpr int SMP1 = 385, WAIT1 = 135, DEAD1 = 7, GAIN1 = 52;
constexpr int Q_AT = 430, GU_AT = 440;
constexpr int TDEAD0 = 5, TREND0 = 5, TDEAD1 = 1, TREND1 = 11;
constexpr int BACK_AT = 70;
constexpr int BACK_DELAY = 12;
constexpr int LATE_DELAY = 20;
constexpr int FIN_DELAY = 48;
constexpr int CODE_BYTES = 16640;

constexpr int AUX_NT = 2, AUX_SC1 = 16;
constexpr int RSRC_W3 = 0x00020000;
constexpr unsigned SPIN_CAP = 1u << 18;
constexpr int OOB = 0x40000000;
constexpr unsigned ALL4 = 0x01010101u;

constexpr int WS_EPOCH = 0;
constexpr int WS_TS = 1024;
constexpr int WS_FP = 4096;
constexpr int WS_ML = WS_FP + 8 * 18 * 5 * 512;
constexpr int WS_YP = WS_ML + 32 * 2 * 4 * 4 * 16;
constexpr int WS_OP = 2 << 20, OP_STRIDE = 1 << 20;
constexpr int WS_END = WS_OP + 32 * OP_STRIDE;
static_assert(WS_YP + 96 * 10 * 2 * 32 * 8 <= WS_OP, "workspace map");

template <int I, int N, class F> __device__ __forceinline__ void static_for(F&& f) {
  if constexpr (I < N) { f(std::integral_constant<int, I>{}); static_for<I + 1, N>(f); }
}
__device__ __forceinline__ rsrc_t mk_rsrc(const void* p, int bytes) {
  return __builtin_amdgcn_make_buffer_rsrc(const_cast<void*>(p), 0, bytes, RSRC_W3);
}
__device__ __forceinline__ u32x4 ld_nt(rsrc_t r, int voff, int soff) { return __builtin_amdgcn_raw_buffer_load_b128(r, voff, soff, AUX_NT); }
__device__ __forceinline__ u32x4 ld_sc1(rsrc_t r, int voff, int soff) { return __builtin_amdgcn_raw_buffer_load_b128(r, voff, soff, AUX_SC1); }
__device__ __forceinline__ u32x2 ld2_sc1(rsrc_t r, int voff, int soff) { return __builtin_amdgcn_raw_buffer_load_b64(r, voff, soff, AUX_SC1); }
__device__ __forceinline__ void st_pair(rsrc_t r, int voff, int soff, float a, float b, unsigned tag) {
  u32x4 v = {__float_as_uint(a), tag, __float_as_uint(b), tag};
  __builtin_amdgcn_raw_buffer_store_b128(v, r, voff, soff, AUX_SC1);
}
__device__ __forceinline__ float bflo(unsigned v) { return __uint_as_float(v << 16); }
__device__ __forceinline__ float bfhi(unsigned v) { return __uint_as_float(v & 0xffff0000u); }
__device__ __forceinline__ unsigned cvtpk(float lo, float hi) {
  bf16x2 r = {(__bf16)lo, (__bf16)hi};
  return __builtin_bit_cast(unsigned, r);
}
__device__ __forceinline__ float rbf(float x) { return (float)(__bf16)x; }
template <int CTRL> __device__ __forceinline__ float dpp(float x) {
  return __builtin_bit_cast(float, __builtin_amdgcn_mov_dpp(__builtin_bit_cast(int, x), CTRL, 0xf, 0xf, true));
}
constexpr int XOR1 = 0xB1, XOR2 = 0x4E, HMIR = 0x141, ROR8 = 0x128, ROR4 = 0x124;
__device__ __forceinline__ float x16_sum(float x) { auto s = __builtin_amdgcn_permlane16_swap(__float_as_uint(x), __float_as_uint(x), false, false); return __uint_as_float(s[0]) + __uint_as_float(s[1]); }
__device__ __forceinline__ float x32_sum(float x) { auto s = __builtin_amdgcn_permlane32_swap(__float_as_uint(x), __float_as_uint(x), false, false); return __uint_as_float(s[0]) + __uint_as_float(s[1]); }
__device__ __forceinline__ float x16_max(float x) { auto s = __builtin_amdgcn_permlane16_swap(__float_as_uint(x), __float_as_uint(x), false, false); return fmaxf(__uint_as_float(s[0]), __uint_as_float(s[1])); }
__device__ __forceinline__ float x32_max(float x) { auto s = __builtin_amdgcn_permlane32_swap(__float_as_uint(x), __float_as_uint(x), false, false); return fmaxf(__uint_as_float(s[0]), __uint_as_float(s[1])); }
__device__ __forceinline__ float row_sum(float x) { x += dpp<XOR1>(x); x += dpp<XOR2>(x); x += dpp<HMIR>(x); x += dpp<ROR8>(x); return x; }
__device__ __forceinline__ float row_max(float x) { x = fmaxf(x, dpp<XOR1>(x)); x = fmaxf(x, dpp<XOR2>(x)); x = fmaxf(x, dpp<HMIR>(x)); x = fmaxf(x, dpp<ROR8>(x)); return x; }
__device__ __forceinline__ float quad_sum(float x) { x += dpp<XOR1>(x); x += dpp<XOR2>(x); return x; }
__device__ __forceinline__ float dot2(unsigned a, unsigned b, float c) {
  return __builtin_amdgcn_fdot2_f32_bf16(__builtin_bit_cast(bf16x2, a), __builtin_bit_cast(bf16x2, b), c, false);
}
__device__ __forceinline__ f32x4 mfma16(unsigned a0, unsigned a1, unsigned b0, unsigned b1, f32x4 c) {
  u32x2 a = {a0, a1}, b = {b0, b1};
  return __builtin_amdgcn_mfma_f32_16x16x16bf16_1k(__builtin_bit_cast(s16x4, a), __builtin_bit_cast(s16x4, b), c, 0, 0, 0);
}
__device__ __forceinline__ f32x4 mfma32(u32x4 a, u32x4 b, f32x4 c) {
  return __builtin_amdgcn_mfma_f32_16x16x32_bf16(__builtin_bit_cast(bf16x8, a), __builtin_bit_cast(bf16x8, b), c, 0, 0, 0);
}
template <int RECIP> __device__ __forceinline__ unsigned pace_step(unsigned tprev, unsigned tend, unsigned& tdone) {
  unsigned d, now, tgt;
  asm volatile("s_sub_i32 %0, %5, %4\n\ts_max_i32 %0, %0, 0\n\ts_min_i32 %0, %0, 0x7d0\n\ts_mul_i32 %0, %0, %6\n\ts_lshr_b32 %0, %0, 16\n\ts_add_u32 %2, %4, %0\n"
               "1:\n\ts_memrealtime vcc\n\ts_getreg_b32 %0, hwreg(HW_REG_IB_STS, 0, 4)\n\ts_waitcnt lgkmcnt(0)\n\ts_cmp_lg_u32 %0, 0\n\ts_cselect_b32 %3, vcc_lo, %3\n\t"
               "s_sub_u32 %0, vcc_lo, %2\n\ts_cmp_lt_i32 %0, 0\n\ts_cbranch_scc1 1b\n\ts_mov_b32 %1, vcc_lo"
               : "=&s"(d), "=&s"(now), "=&s"(tgt), "+s"(tdone) : "s"(tprev), "s"(tend), "i"(RECIP) : "vcc", "scc");
  return now;
}
__device__ __forceinline__ void wait_until(unsigned tgt) {
  asm volatile("1:\n\ts_memrealtime vcc\n\ts_waitcnt lgkmcnt(0)\n\ts_sub_u32 vcc_lo, vcc_lo, %0\n\ts_cmp_lt_i32 vcc_lo, 0\n\ts_cbranch_scc1 1b" :: "s"(tgt) : "vcc", "scc", "memory");
}
#define SB() __builtin_amdgcn_sched_barrier(0)
#define MEMFENCE() asm volatile("" ::: "memory")

__device__ __forceinline__ void delay_ticks(int n) {
  if (n > 0) wait_until((unsigned)__builtin_amdgcn_s_memrealtime() + n);
}

typedef __attribute__((address_space(3))) float lfloat;
typedef __attribute__((address_space(3))) f32x4 lf32x4;
typedef __attribute__((address_space(3))) unsigned luint;
typedef __attribute__((address_space(3))) unsigned char lbyte;
template <int SLP> __device__ __forceinline__ void wait_lds(unsigned a, unsigned want) {
  unsigned v, sv;
  asm volatile("s_branch 2f\n1:\n\ts_sleep %4\n2:\n\tds_read_b32 %0, %2\n\ts_waitcnt lgkmcnt(0)\n\tv_readfirstlane_b32 %1, %0\n\ts_cmp_lg_u32 %1, %3\n\ts_cbranch_scc1 1b"
               : "=&v"(v), "=&s"(sv) : "v"(a), "s"(want), "i"(SLP) : "scc", "memory");
}

__device__ __forceinline__ f32x4 proj_epilogue(f32x4 acc0, f32x4 acc1) {
  f32x4 r;
#pragma unroll
  for (int i = 0; i < 4; ++i) {
    auto s = __builtin_amdgcn_permlane32_swap(__float_as_uint(acc0[i]), __float_as_uint(acc1[i]), false, false);
    r[i] = __uint_as_float(s[0]) + dpp<ROR8>(__uint_as_float(s[1]));
  }
  return r;
}
__device__ __forceinline__ u32x4 granules(f32x4 r, unsigned tag) { return u32x4{cvtpk(r[0], r[1]), tag, cvtpk(r[2], r[3]), tag}; }

__global__ void __launch_bounds__(512, 2)
fused_decode(const u16* x_, const u16* kc_, const u16* vc_, const u16* wq_, const u16* wo_, const u16* wg_, const u16* wu_, const u16* wd_, u16* out_, unsigned char* ws_) {
  __shared__ __attribute__((aligned(16))) float lds[96 + 8 + 4 + 32 + 8 * 192 + 4];
  unsigned* l_q = (unsigned*)lds;
  float* l_ml = lds + 96;
  float* l_c = lds + 104;
  unsigned* l_est = (unsigned*)(lds + 108);
  unsigned char* l_f = (unsigned char*)(lds + 116);
  constexpr int FA = 0, FB = 4, FC = 8, FD = 12, FE = 16, FG = 20, FH = 28, FX = 32;
  unsigned* l_tend = (unsigned*)(lds + 122);
  unsigned* l_tag = (unsigned*)(lds + 140 + 8 * 192);
  const unsigned fbase = (unsigned)(size_t)(lbyte*)l_f;
  float* l_acc = lds + 140;
  unsigned char* l_xn = (unsigned char*)(l_acc + 512);

  typedef unsigned long long u64;
  typedef u64 u64x8 __attribute__((ext_vector_type(8)));
  typedef u64 u64x2 __attribute__((ext_vector_type(2)));
  u64x8 ka; u64x2 kb; u64 pc;
  u32x4 fw[16];
  int bid = blockIdx.x, tid = threadIdx.x;
  {
    unsigned v1, v2;
    u64x8 ka0; u64x2 kb0;
    asm volatile("s_load_dwordx16 %0, %3, 0x0\n\ts_load_dwordx4 %1, %3, 0x40\n\ts_getpc_b64 %2" : "=&{s[36:51]}"(ka0), "=&{s[52:55]}"(kb0), "=&s"(pc) : "s"(__builtin_amdgcn_kernarg_segment_ptr()));
    asm volatile(
               "v_readfirstlane_b32 s14, %20\n\t"
               "s_lshr_b32 s15, %19, 3\n\t"
               "s_cmpk_ge_u32 s14, 0xc0\n\ts_cbranch_scc1 1f\n\t"
               "s_setprio 2\n\t"
               "s_and_b32 s16, %19, 7\n\ts_lshr_b32 s17, %19, 4\n\t"
               "s_and_b32 s18, s15, 1\n\ts_mul_i32 s18, s18, 3\n\ts_lshr_b32 s19, s14, 6\n\ts_add_u32 s18, s18, s19\n\t"
               "v_bfe_u32 %16, %20, 3, 3\n\tv_and_b32 %17, 7, %20\n\t"
               "s_cmp_ge_u32 s17, 5\n\ts_cselect_b32 s12, 17, 16\n\ts_cselect_b32 s13, 5, 0\n\ts_sub_u32 s13, s17, s13\n\t"
               "s_cmp_eq_u32 s18, 5\n\ts_cselect_b32 s17, s12, s17\n\ts_cselect_b32 s18, s13, s18\n\t"
               "s_cmp_ge_u32 s17, 15\n\ts_cselect_b32 s12, 15, 12\n\ts_movk_i32 s13, 0x3000\n\t"
               "s_cmp_lt_u32 s17, 12\n\ts_cselect_b32 s12, 0, s12\n\ts_cselect_b32 s13, s13, 0xc00\n\ts_cselect_b32 s19, 12, 3\n\t"
               "s_mul_i32 s16, s16, s19\n\ts_sub_u32 s12, s17, s12\n\ts_add_u32 s12, s12, s16\n\t"
               "v_mad_u32_u24 %16, %16, s13, %17\n\t"
               "s_mul_i32 s19, s18, s13\n\ts_add_u32 s12, s12, s19\n\ts_lshl_b32 s12, s12, 7\n\t"
               "v_lshlrev_b32 %16, 4, %16\n\t"
               "s_mul_i32 s10, s13, 0x280\n\ts_mov_b32 s11, 0x20000\n\ts_cmp_ge_u32 s18, 5\n\ts_cselect_b32 s10, 0, s10\n\t"
               "s_cmp_ge_u32 s17, 15\n\t"
               "s_waitcnt lgkmcnt(0)\n\t"
               "s_cselect_b64 s[8:9], s[48:49], s[46:47]\n\t"
               "s_cmp_lt_u32 s17, 12\n\ts_cselect_b64 s[8:9], s[42:43], s[8:9]\n\t"
               "s_and_b32 s9, s9, 0xffff\n\t"
               "buffer_load_dwordx4 %0, %16, s[8:11], s12 offen nt\n\t" "s_add_u32 s12, s12, s13\n\t"
               "buffer_load_dwordx4 %1, %16, s[8:11], s12 offen nt\n\t" "s_add_u32 s12, s12, s13\n\t"
               "buffer_load_dwordx4 %2, %16, s[8:11], s12 offen nt\n\t" "s_add_u32 s12, s12, s13\n\t"
               "buffer_load_dwordx4 %3, %16, s[8:11], s12 offen nt\n\t" "s_add_u32 s12, s12, s13\n\t"
               "buffer_load_dwordx4 %4, %16, s[8:11], s12 offen nt\n\t" "s_add_u32 s12, s12, s13\n\t"
               "buffer_load_dwordx4 %5, %16, s[8:11], s12 offen nt\n\t" "s_add_u32 s12, s12, s13\n\t"
               "buffer_load_dwordx4 %6, %16, s[8:11], s12 offen nt\n\t" "s_add_u32 s12, s12, s13\n\t"
               "buffer_load_dwordx4 %7, %16, s[8:11], s12 offen nt\n\t" "s_add_u32 s12, s12, s13\n\t"
               "buffer_load_dwordx4 %8, %16, s[8:11], s12 offen nt\n\t" "s_add_u32 s12, s12, s13\n\t"
               "buffer_load_dwordx4 %9, %16, s[8:11], s12 offen nt\n\t" "s_add_u32 s12, s12, s13\n\t"
               "buffer_load_dwordx4 %10, %16, s[8:11], s12 offen nt\n\t" "s_add_u32 s12, s12, s13\n\t"
               "buffer_load_dwordx4 %11, %16, s[8:11], s12 offen nt\n\t" "s_add_u32 s12, s12, s13\n\t" "s_barrier\n\t"
               "buffer_load_dwordx4 %12, %16, s[8:11], s12 offen nt\n\t" "s_add_u32 s12, s12, s13\n\t"
               "buffer_load_dwordx4 %13, %16, s[8:11], s12 offen nt\n\t" "s_add_u32 s12, s12, s13\n\t"
               "buffer_load_dwordx4 %14, %16, s[8:11], s12 offen nt\n\t" "s_add_u32 s12, s12, s13\n\t"
               "buffer_load_dwordx4 %15, %16, s[8:11], s12 offen nt\n\t"
               "s_branch 2f\n"
               "1:\n\t"
               "v_and_b32 %16, 31, %20\n\tv_lshl_add_u32 %16, %16, 2, %21\n\tv_mov_b32 %17, 0\n\tds_write_b32 %16, %17\n\t"
               "s_cmpk_lg_u32 s14, 0xc0\n\ts_cbranch_scc1 3f\n\t"
               "v_and_b32 %16, 7, %20\n\tv_lshl_add_u32 %16, %16, 5, s15\n\tv_lshlrev_b32 %16, 7, %16\n\tv_min_u32 %16, %24, %16\n\t"
               "global_load_dwordx4 %15, %16, %23\n\t"
               "v_bfe_u32 %17, %20, 5, 1\n\tv_mul_u32_u24 %17, 0x50, %17\n\tv_and_b32 %16, 31, %20\n\tv_add_lshl_u32 %16, %16, %17, 4\n\t"
               "v_and_or_b32 %17, %20, 15, %17\n\tv_lshlrev_b32 %17, 4, %17\n\t"
               "s_waitcnt lgkmcnt(0)\n\t"
               "global_load_dwordx4 %0, %16, s[36:37]\n\tglobal_load_dwordx4 %1, %16, s[36:37] offset:512\n\tglobal_load_dwordx4 %2, %17, s[36:37] offset:1024\n"
               "3:\n\t"
               "s_waitcnt lgkmcnt(0)\n\ts_barrier\n"
               "2:"
               : "=&v"(fw[0]), "=&v"(fw[1]), "=&v"(fw[2]), "=&v"(fw[3]), "=&v"(fw[4]), "=&v"(fw[5]), "=&v"(fw[6]), "=&v"(fw[7]),
                 "=&v"(fw[8]), "=&v"(fw[9]), "=&v"(fw[10]), "=&v"(fw[11]), "=&v"(fw[12]), "=&v"(fw[13]), "=&v"(fw[14]), "=&v"(fw[15]),
                 "=&v"(v1), "=&v"(v2)
               : "{s[36:51]}"(ka0), "s"(bid), "v"(tid), "s"(fbase - 32), "{s[52:55]}"(kb0), "s"(pc), "i"(CODE_BYTES - 128)
               : "s8", "s9", "s10", "s11", "s12", "s13", "s14", "s15", "s16", "s17", "s18", "s19", "scc", "memory");
    asm volatile("" : "={s[36:51]}"(ka), "={s[52:55]}"(kb), "+s"(bid) : "0"(ka0), "1"(kb0));
    asm volatile("" : "+v"(tid));
  }
  const int xq = bid & 7, uq = bid >> 3;
  const int h = xq * 4 + (uq >> 3), j8 = uq & 7, b = j8 >> 2, s = j8 & 3;
  const int lane = tid & 63, wave = __builtin_amdgcn_readfirstlane(tid >> 6);
  const int g = lane >> 3, j = lane & 7;
  const int bl = (lane >> 2) & 1, e4 = lane & 3;
  const bool isS = wave >= 4;
  const u16 *x = (const u16*)ka[0], *kc = (const u16*)ka[1], *vc = (const u16*)ka[2], *wo = (const u16*)ka[4], *wd = (const u16*)ka[7];
  u16* out = (u16*)kb[0];
  unsigned char* ws = (unsigned char*)kb[1];
  u64 tst;
  asm volatile("s_memrealtime %0\n\ts_waitcnt lgkmcnt(0)" : "=s"(tst));
  const unsigned tstart = (unsigned)tst;
  const rsrc_t rws = mk_rsrc(ws, WS_END);

  if (isS) {
    const int sw = wave - 4;
    const rsrc_t r0 = mk_rsrc(kc, 2 * 32 * 8192 * 384), rv = mk_rsrc(vc, 2 * 32 * 8192 * 384);
    const int voff = ((b * 32 + h) * 8192 + 6144 + 32 * s + 8 * sw + g) * 384 + 16 * j;
    u32x4 buf[96];
    u32x4 qv[3];
    float sc[16];
    unsigned pd[16];
    f32x4 acc[3] = {{0.f, 0.f, 0.f, 0.f}, {0.f, 0.f, 0.f, 0.f}, {0.f, 0.f, 0.f, 0.f}};
    unsigned msk[4];
#pragma unroll
    for (int d = 0; d < 4; ++d) msk[d] = (j >> 1) == d ? ((j & 1) ? 0xffff0000u : 0x0000ffffu) : 0u;
    constexpr float C2 = 0.07216878364870322f * 1.4426950408889634f;
        const int stag = ((2 * (uq >> 2) - 7) * STAG) >> 1;
    unsigned tprev = tstart + S_DELAY + stag, tend = tstart + T_PLAN + stag, lat = 0;

    auto consume = [&](auto G) {
      constexpr int t = decltype(G)::value;
      if constexpr (t < 16) {
        float a0 = 0.f, a1 = 0.f, a2 = 0.f;
#pragma unroll
        for (int e = 0; e < 4; ++e) { a0 = dot2(buf[3 * t][e], qv[0][e], a0); a1 = dot2(buf[3 * t + 1][e], qv[1][e], a1); a2 = dot2(buf[3 * t + 2][e], qv[2][e], a2); }
        float a = (a0 + a1) + a2;
        a += dpp<XOR1>(a); a += dpp<XOR2>(a); a += dpp<HMIR>(a);
        sc[t] = a;
        if constexpr (t == 15) {
          float m = sc[0];
#pragma unroll
          for (int u = 1; u < 16; ++u) m = fmaxf(m, sc[u]);
          m = fmaxf(m, dpp<ROR8>(m)); m = x16_max(m); m = x32_max(m);
          const float mc = m * C2;
          float l = 0.f;
#pragma unroll
          for (int u = 0; u < 16; ++u) { const float p = __builtin_amdgcn_exp2f(fmaf(sc[u], C2, -mc)); pd[u] = cvtpk(p, p); l += bfhi(pd[u]); }
          l += dpp<ROR8>(l); l = x16_sum(l); l = x32_sum(l);
          if (lane == 0) { *(f32x2*)(l_ml + 2 * sw) = f32x2{mc, l}; MEMFENCE(); l_f[FA + sw] = 1; }
        }
      } else {
        constexpr int u = t - 16;
        const u32x4 a = {pd[u] & msk[0], pd[u] & msk[1], pd[u] & msk[2], pd[u] & msk[3]};
#pragma unroll
        for (int i = 0; i < 3; ++i) acc[i] = mfma32(a, buf[3 * t + i], acc[i]);
      }
    };
    static_for<0, 16>([&](auto K) {
      constexpr int k = decltype(K)::value;
      if constexpr (k > 6) {
        MEMFENCE();
        unsigned tv = *l_tend;
        asm volatile("" : "+v"(tv));
        tv = __builtin_amdgcn_readfirstlane(tv);
        tend = tv ? tv + stag : tend;
      }
      unsigned tdone = tprev + LAT0;
      const unsigned now = pace_step<(k ? 65536 / (16 - k) : 0)>(tprev, tend, tdone);
      SB();
#pragma unroll
      for (int i = 0; i < 6; ++i) { buf[6 * k + i] = ld_nt(k < 8 ? r0 : rv, voff + (i % 3) * 128, ((2 * k + i / 3) & 15) * 49152); SB(); }
      if constexpr (k == 15) { if (lane == 0) l_f[FB + sw] = 1; }
      if constexpr (k >= 1 && k < 15) asm volatile("s_waitcnt vmcnt(6)" ::: "memory");
      if constexpr (k >= 2 && k < 13) {
        if (tdone == now) tdone = (unsigned)__builtin_amdgcn_s_memrealtime();
        lat = (lat << 16) | (tdone - tprev);
        *(u32x2*)(l_est + 2 * sw) = u32x2{(now - stag + ((6 * (15 - k) * RATE16) >> 4)) | 1u, lat};
      }
      tprev = now;
      if constexpr (k == QK) {
        wait_lds<0>(fbase + FG, ALL4);
#pragma unroll
        for (int i = 0; i < 3; ++i) qv[i] = *(const u32x4*)(l_q + 32 * i + 4 * j);
      }
      SB();
      constexpr auto upto = [](int kk) constexpr { if (kk < QK) return 0; const int c = CU * (kk - QK + 1), l = kk < 15 ? 2 * kk : 28; return c < l ? c : l; };
      static_for<upto(k - 1), upto(k)>([&](auto G) { consume(G); SB(); });
      SB();
    });
    __builtin_amdgcn_s_setprio(3);
    wait_lds<0>(fbase + FE, ALL4);
    float cq = l_c[lane >> 4];
    unsigned dst = (unsigned)(size_t)(lfloat*)(l_acc + (sw * 2 + (g & 1)) * 192 + 8 * j + 4 * ((lane >> 4) & 1));
    unsigned src = (unsigned)(size_t)(lfloat*)(l_acc + (lane >> 4) * 384 + 64 * sw + 8 * j + 4 * (g & 1));
    unsigned fla = fbase + FC + sw, one = 1u, flr = fbase + FC;
    int pvo = (lane & 16) ? OOB : (g & 1) * 128 + j * 16 + (lane >> 5) * 8;
    int pso = WS_OP + h * OP_STRIDE + (b * 4 + s) * 768 + sw * 256;
    unsigned tagv = *l_tag;
    asm volatile("" : "+v"(cq), "+v"(dst), "+v"(src), "+v"(fla), "+v"(flr), "+v"(one), "+v"(pvo), "+s"(pso), "+v"(tagv));
    const bool wr = (lane >> 5) == (g & 1);
    SB();
    static_for<28, 32>([&](auto G) { consume(G); SB(); });
    if (wr) {
#pragma unroll
      for (int i = 0; i < 3; ++i) *(lf32x4*)(size_t)(dst + 256 * i) = acc[i];
      MEMFENCE();
      *(lbyte*)(size_t)fla = (unsigned char)one;
    }
    if (sw < 3) {
      f32x4 u0, u1;
      unsigned fv, fs;
      asm volatile("1:\n\tds_read_b32 %0, %4\n\tds_read_b128 %2, %5\n\tds_read_b128 %3, %5 offset:768\n\ts_waitcnt lgkmcnt(2)\n\tv_readfirstlane_b32 %1, %0\n\t"
                   "s_cmp_lg_u32 %1, 0x01010101\n\ts_waitcnt lgkmcnt(0)\n\ts_cbranch_scc1 1b"
                   : "=&v"(fv), "=&s"(fs), "=&v"(u0), "=&v"(u1) : "v"(flr), "v"(src) : "scc", "memory");
      float o[4];
#pragma unroll
      for (int e = 0; e < 4; ++e) o[e] = cq * (u0[e] + u1[e]);
      const auto s0 = __builtin_amdgcn_permlane32_swap(__float_as_uint(o[0]), __float_as_uint(o[2]), false, false);
      const auto s1 = __builtin_amdgcn_permlane32_swap(__float_as_uint(o[1]), __float_as_uint(o[3]), false, false);
      const float p0 = x16_sum(__uint_as_float(s0[0]) + __uint_as_float(s0[1])), p1 = x16_sum(__uint_as_float(s1[0]) + __uint_as_float(s1[1]));
      __builtin_amdgcn_raw_buffer_store_b64(u32x2{cvtpk(p0, p1), tagv}, rws, pvo, pso, AUX_SC1);
    }
    MEMFENCE();
    *(lbyte*)(size_t)(fla + (FD - FC)) = (unsigned char)one;
    return;
  }

  __builtin_amdgcn_s_setprio(2);
  unsigned tag, ep;
  asm volatile("s_load_dword %0, %1, 0x0" : "=&s"(ep) : "s"(ws));
#define TAG_READY() asm volatile("s_waitcnt lgkmcnt(0)\n\ts_add_u32 %0, %1, 1\n\ts_addc_u32 %0, %0, 0" : "=&s"(tag) : "s"(ep) : "scc", "memory")
  const int w = wave;
  const bool second = uq & 1, single = second && w == 2;
  const unsigned c0 = uq >> 1, uc = single ? (c0 >= 5 ? 17 : 16) : c0, ur = single ? (c0 >= 5 ? c0 - 5 : c0) : 3 * second + w;
  const bool has_front = (w < 3) & (ur < 5);
  const int st_col = (g & 1) ? OOB : (lane >> 5) * 128 + j * 16;
  const rsrc_t rx = mk_rsrc(x, 2560);
  const unsigned sLo = (e4 & 1) ? 0x01000c0cu : 0x0c0c0100u, sHi = (e4 & 1) ? 0x03020c0cu : 0x0c0c0302u;
  unsigned sel0 = e4 < 2 ? sLo : 0x0c0c0c0cu, sel1 = e4 < 2 ? 0x0c0c0c0cu : sLo, sel2 = e4 < 2 ? sHi : 0x0c0c0c0cu, sel3 = e4 < 2 ? 0x0c0c0c0cu : sHi;
  if (w == 3) {
    const int xo = (lane >> 5) * 1280 + (lane & 31) * 16;
    const bool x3 = (lane & 16) == 0;
    asm volatile("s_waitcnt vmcnt(0)" : "+v"(fw[0]), "+v"(fw[1]), "+v"(fw[2]) : "v"(fw[15]));
    u32x4 xv[3] = {fw[0], fw[1], x3 ? fw[2] : u32x4{0u, 0u, 0u, 0u}};
    float v[24];
#pragma unroll
    for (int e = 0; e < 12; ++e) { v[2 * e] = bflo(xv[e >> 2][e & 3]); v[2 * e + 1] = bfhi(xv[e >> 2][e & 3]); }
    float sum = 0.f;
#pragma unroll
    for (int e = 0; e < 24; ++e) sum += v[e];
    sum = x16_sum(row_sum(sum));
    const float mean = sum * (1.f / 640.f);
    float sq = 0.f, sq3 = 0.f;
#pragma unroll
    for (int e = 0; e < 16; ++e) { const float d = v[e] - mean; sq = fmaf(d, d, sq); }
#pragma unroll
    for (int e = 16; e < 24; ++e) { const float d = v[e] - mean; sq3 = fmaf(d, d, sq3); }
    sq = x16_sum(row_sum(sq + (x3 ? sq3 : 0.f)));
    const float mb = rbf(mean), den = rbf(__builtin_amdgcn_sqrtf(rbf(rbf(sq * (1.f / 640.f)) + 1e-5f)));
    const float inv = __builtin_amdgcn_rcpf(den);
#pragma unroll
    for (int e = 0; e < 12; ++e) xv[e >> 2][e & 3] = cvtpk(rbf(v[2 * e] - mb) * inv, rbf(v[2 * e + 1] - mb) * inv);
    *(u32x4*)(l_xn + xo) = xv[0];
    *(u32x4*)(l_xn + xo + 512) = xv[1];
    if (x3) *(u32x4*)(l_xn + xo + 1024) = xv[2];
    MEMFENCE();
    l_f[FX] = 1;
  }
  if (has_front) {
    wait_lds<1>(fbase + FX, 1u);
    const u32x4 xr[2] = {*(const u32x4*)(l_xn + bl * 1280 + 256 * ur + g * 32), *(const u32x4*)(l_xn + bl * 1280 + 256 * ur + g * 32 + 16)};
    TAG_READY();
    f32x4 acc0 = {0.f, 0.f, 0.f, 0.f}, acc1 = {0.f, 0.f, 0.f, 0.f};
    asm volatile("s_waitcnt vmcnt(4)" : "+v"(fw[0]), "+v"(fw[1]), "+v"(fw[2]), "+v"(fw[3]), "+v"(fw[4]), "+v"(fw[5]), "+v"(fw[6]), "+v"(fw[7]),
                 "+v"(fw[8]), "+v"(fw[9]), "+v"(fw[10]), "+v"(fw[11]));
#pragma unroll
    for (int n = 0; n < 16; ++n) {
      if (n == 12) { SB(); asm volatile("s_waitcnt vmcnt(0)" : "+v"(fw[12]), "+v"(fw[13]), "+v"(fw[14]), "+v"(fw[15])); }
      const unsigned xw = xr[n >> 3][(n >> 1) & 3];
      const unsigned a0 = __builtin_amdgcn_perm(0u, xw, (n & 1) ? sel2 : sel0), a1 = __builtin_amdgcn_perm(0u, xw, (n & 1) ? sel3 : sel1);
      acc0 = mfma16(a0, a1, fw[n][0], fw[n][1], acc0);
      acc1 = mfma16(a0, a1, fw[n][2], fw[n][3], acc1);
    }
    f32x4 r = proj_epilogue(acc0, acc1);
    f32x4* l_u = (f32x4*)l_acc;
    if (w == 0) {
      wait_lds<1>(fbase + FH, second ? 0x0100u : 0x010100u);
      r += l_u[lane];
      if (!second) r += l_u[64 + lane];
    } else if (!single) {
      l_u[(w - 1) * 64 + lane] = r;
      MEMFENCE();
      l_f[FH + w] = 1;
    }
    if (w == 0 || single)
      __builtin_amdgcn_raw_buffer_store_b128(granules(r, tag), rws, ((lane >> 4) & 1) * 256 + st_col, WS_FP + ((xq * 18 + uc) * 5 + (single ? ur : second)) * 512, AUX_SC1);
    asm volatile("s_waitcnt vmcnt(0)" ::: "memory");
  } else {
    asm volatile("s_waitcnt vmcnt(0)" ::"v"(fw[0]), "v"(fw[1]), "v"(fw[2]), "v"(fw[3]), "v"(fw[4]), "v"(fw[5]), "v"(fw[6]), "v"(fw[7]),
                 "v"(fw[8]), "v"(fw[9]), "v"(fw[10]), "v"(fw[11]), "v"(fw[12]), "v"(fw[13]), "v"(fw[14]), "v"(fw[15]));
    TAG_READY();
  }
  const int slot = w * 8 + j8;
  const bool has_back = slot < 30;
  const int t3 = slot / 10, cc = slot - 10 * t3;
  const int rid = xq * 20 + uq;
  const int rb = rid / 80, rc = (rid - 80 * rb) >> 3, ro = rid & 7;
  const bool reducer = (w == 0) && (bid < 160);
  unsigned ha[2][2] = {{0u, 0u}, {0u, 0u}};
  unsigned tend = tstart + T_PLAN;
  auto pace_round = [&](unsigned t_sample, unsigned t_read, int so, int dead, int gain16, int tdead, int trend, int keep16) {
    wait_until(t_sample);
    u32x4 e, f;
    do { MEMFENCE(); e = *(const u32x4*)l_est; f = *(const u32x4*)(l_est + 4); } while (!__builtin_amdgcn_readfirstlane(e[0] & e[2] & f[0] & f[2] & 1u));
    const u32x4 pv = {tstart + (unsigned)((int)((e[0] - tstart) + (e[2] - tstart) + (f[0] - tstart) + (f[2] - tstart)) >> 2), tag, e[1] + e[3] + f[1] + f[3], tag};
    __builtin_amdgcn_raw_buffer_store_b128(pv, rws, (lane == 0 && j8 == (((uq >> 3) * 5) & 7)) ? ((uq >> 3) * 8 + xq) * 16 : OOB, so, AUX_SC1);
    wait_until(t_read);
    MEMFENCE();
    const u32x4 tv = ld_sc1(rws, (lane & 31) * 16, so);
    if (__all((tv[1] == tag) & (tv[3] == tag))) {
      const float d = x16_sum(row_sum((float)(int)(tv[0] - tstart))) * (1.f / 32.f);
      const float l1 = x16_sum(row_sum((float)(tv[2] & 0xffffu))) * (1.f / 128.f), l0 = x16_sum(row_sum((float)(tv[2] >> 16))) * (1.f / 128.f);
      const float dr = l1 - (float)LAT0, sign = l1 - l0;
      int di = (int)(d + fmaxf(dr, 0.f) + fmaxf(dr - (float)dead, 0.f) * (float)gain16 * (1.f / 16.f) + fmaxf(sign - (float)tdead, 0.f) * (float)trend);
      const int dold = (int)(tend - tstart);
      if (keep16 && di < dold) di += ((dold - di) * keep16) >> 4;
      di = di < 0 ? 0 : (di > 2500 ? 2500 : di);
      tend = (tstart + __builtin_amdgcn_readfirstlane(di)) | 1u;
      *l_tend = tend;
    }
  };
  if (w == 0) {
    wait_until(tstart + Q_AT);
    u32x2 t[3];
    const int vo = (lane >> 5) * 512 + (lane & 31) * 8, so = WS_FP + (xq * 18 + (h & 3) * 3) * 2560 + b * 256;
    for (unsigned spin = 0;; ++spin) {
      MEMFENCE();
      t[0] = ld2_sc1(rws, vo, so); t[1] = ld2_sc1(rws, vo, so + 2560); t[2] = ld2_sc1(rws, vo, so + 5120);
      if (__all((t[0][1] == tag) & (t[1][1] == tag) & (t[2][1] == tag)) || spin > SPIN_CAP) break;
    }
#pragma unroll
    for (int i = 0; i < 3; ++i) l_q[32 * i + ((lane >> 1) & 7) * 4 + ((lane >> 4) & 1) * 2 + (lane & 1)] = cvtpk(x32_sum(bflo(t[i][0])), x32_sum(bfhi(t[i][0])));
    *l_tag = tag;
    MEMFENCE();
  }
  l_f[FG + w] = 1;
  if (w == 3) {
    pace_round(tstart + SMP0, tstart + SMP0 + WAIT0, WS_TS, DEAD0, GAIN0, TDEAD0, TREND0, 0);
  }
  if (has_back) {
    wait_until(tstart + GU_AT);
    u32x2 t[2];
    const int f16 = (h & 3) * 3 + t3;
    const int so = WS_FP + (xq * 18 + 12 + (f16 >> 2)) * 2560 + (f16 & 3) * 32;
    const int vo = bl * 256 + ((g >> 1) & 1) * 128 + (g >> 2) * 16 + (g & 1) * 8;
    const bool more = (f16 >= 4) & (e4 < 3);
    for (unsigned spin = 0;; ++spin) {
      MEMFENCE();
      t[0] = ld2_sc1(rws, vo + (e4 >> 1) * 7680 + (e4 & 1) * 512, so);
      t[1] = ld2_sc1(rws, more ? vo + 7680 + (2 + e4) * 512 : OOB, so);
      if (__all((t[0][1] == tag) & ((t[1][1] == tag) | !more)) || spin > SPIN_CAP) break;
    }
#pragma unroll
    for (int z = 0; z < 2; ++z) {
      const float a = z ? bfhi(t[0][0]) : bflo(t[0][0]), c = z ? bfhi(t[1][0]) : bflo(t[1][0]);
      const float gg = rbf(quad_sum(e4 < 2 ? a : 0.f));
      const float uu = rbf(quad_sum((e4 < 2 ? 0.f : a) + c));
      const float zz = 0.7978845608028654f * (gg + 0.044715f * gg * gg * gg);
      const float ge = rbf(gg * __builtin_amdgcn_rcpf(1.f + __builtin_amdgcn_exp2f(-2.8853900817779268f * zz)));
      const float hv = ge * uu;
      const unsigned d = cvtpk(hv, hv);
      ha[z][0] = __builtin_amdgcn_perm(0u, d, sel0); ha[z][1] = __builtin_amdgcn_perm(0u, d, sel1);
    }
  }
  const int oc = 16 * (ro & 3) + 8 * (lane & 1) + 4 * (ro >> 2) + 2 * (lane >> 5) + ((lane >> 4) & 1);
  const rsrc_t rout = mk_rsrc(out, 2560);
  int out_vo = (lane & 14) == 0 ? oc * 2 + rb * 1280 + rc * 128 : OOB;
  float xres = 0.f;
  if (reducer) {
    xres = bflo(__builtin_amdgcn_raw_buffer_load_b16(rx, oc * 2, rb * 1280 + rc * 128, 0));
    __builtin_amdgcn_raw_buffer_store_b16((unsigned short)0, rout, out_vo, 0, AUX_SC1);
  }
  if (w == 1) {
    wait_lds<4>(fbase + FA, ALL4);
    const int so = WS_ML + (h * 2 + b) * 256;
    if (lane < 4) { const f32x2 ml = *(const f32x2*)(l_ml + 2 * lane); st_pair(rws, lane * 16, so + s * 64, ml[0], ml[1], tag); }
    asm volatile("s_waitcnt vmcnt(0)" ::: "memory");
    __builtin_amdgcn_s_sleep(32);
    u32x4 t;
    for (unsigned spin = 0;; ++spin) {
      MEMFENCE();
      t = ld_sc1(rws, (lane & 15) * 16, so);
      if (__all((t[1] == tag) & (t[3] == tag)) || spin > SPIN_CAP) break;
    }
    const float mv = __uint_as_float(t[0]);
    const float M = row_max(mv);
    const float f = __builtin_amdgcn_exp2f(mv - M);
    const float L = row_sum(f * __uint_as_float(t[2]));
    if ((lane >> 2) == s) { l_c[lane & 3] = f / L; MEMFENCE(); l_f[FE + (lane & 3)] = 1; }
  }
  if (w == 3) {
    pace_round(tend - SMP1, tend - SMP1 + WAIT1, WS_TS + 512, DEAD1, GAIN1, TDEAD1, TREND1, KEEP1);
  }
  if (has_back) {
    int op_so = WS_OP + h * OP_STRIDE + t3 * 256, op_vo = (bl * 4 + e4) * 768 + 16 * g;
    int d_vo = ((48 * h + 16 * t3 + 2 * g) * 640 + 64 * cc) * 2 + 16 * j, o_vo = ((192 * h + 64 * t3 + 8 * g) * 640 + 64 * cc) * 2 + 16 * j;
    int yvo = ((lane >> 4) & 1) * 256 + st_col, yso = WS_YP + ((h * 3 + t3) * 10 + cc) * 512;
    int r_vo = (lane >> 1) * 5120 + (lane & 1) * 16 + WS_YP + rc * 512 + rb * 256 + ro * 32;
    unsigned tagv = tag, fd = fbase + FD;
    asm volatile("" : "+v"(sel0), "+v"(sel1), "+v"(sel2), "+v"(sel3), "+s"(op_so), "+v"(op_vo), "+v"(d_vo), "+v"(o_vo), "+v"(yvo), "+s"(yso), "+v"(r_vo), "+v"(out_vo), "+v"(tagv), "+v"(xres), "+v"(fd));
    const rsrc_t rd = mk_rsrc(wd, 1536 * 1280), ro_ = mk_rsrc(wo, 6144 * 1280);
    wait_lds<1>(fbase + FB, ALL4);
    {
      unsigned tf = *l_tend;
      asm volatile("" : "+v"(tf));
      tf = __builtin_amdgcn_readfirstlane(tf);
      const unsigned tn = (unsigned)__builtin_amdgcn_s_memrealtime();
      int wt = tf ? (int)(tf + BACK_AT - tn) : 0;
      asm("s_max_i32 %0, %0, %1\n\ts_min_i32 %0, %0, 0xc8" : "+s"(wt) : "s"(BACK_DELAY) : "scc");
      wait_until(tn + wt);
    }
    u32x4 wb[10], t0, t1;
    unsigned fdv = 0;
#pragma unroll
    for (int n = 0; n < 10; ++n) {
      if (n == POLLPOS) {
        const rsrc_t rp = mk_rsrc(ws, __builtin_amdgcn_readfirstlane(fdv) == ALL4 ? WS_END : 0);
        t0 = ld_sc1(rp, op_vo, op_so); t1 = ld_sc1(rp, op_vo + 128, op_so);
        SB();
      }
      if (n < 2) wb[8 + n] = ld_nt(rd, d_vo + n * 1280, 0); else wb[n - 2] = ld_nt(ro_, o_vo, (n - 2) * 1280);
      if (n == POLLPOS - 1) { MEMFENCE(); fdv = *(luint*)(size_t)fd; }
      SB();
      if (n < 9) __builtin_amdgcn_s_sleep(BPACE);
      SB();
    }
    u32x4 bq[10];
#define PAIR(p) do { bq[2 * (p)] = u32x4{wb[2 * (p)][0], wb[2 * (p)][1], wb[2 * (p) + 1][0], wb[2 * (p) + 1][1]}; \
                     bq[2 * (p) + 1] = u32x4{wb[2 * (p)][2], wb[2 * (p)][3], wb[2 * (p) + 1][2], wb[2 * (p) + 1][3]}; } while (0)
    PAIR(4); PAIR(0); PAIR(1); PAIR(2);
    const u32x4 hA = {ha[0][0], ha[0][1], ha[1][0], ha[1][1]};
    f32x4 acc0 = mfma32(hA, bq[8], f32x4{0.f, 0.f, 0.f, 0.f}), acc1 = mfma32(hA, bq[9], f32x4{0.f, 0.f, 0.f, 0.f});
    asm volatile("" : "+v"(bq[0]), "+v"(bq[1]), "+v"(bq[2]), "+v"(bq[3]), "+v"(bq[4]), "+v"(bq[5]), "+v"(acc0), "+v"(acc1));
    SB();
    unsigned np1 = 1;
    if (__builtin_expect(__builtin_amdgcn_ballot_w64((((t0[1] ^ tagv) | (t0[3] ^ tagv)) | ((t1[1] ^ tagv) | (t1[3] ^ tagv))) != 0u) != 0, 0)) {
      do {
        ++np1;
        MEMFENCE();
        if (__builtin_amdgcn_readfirstlane(*(luint*)(size_t)fd) != ALL4) { wait_lds<1>(fd, ALL4); delay_ticks(LATE_DELAY); }
        t0 = ld_sc1(rws, op_vo, op_so); t1 = ld_sc1(rws, op_vo + 128, op_so);
      } while (!__all((t0[1] == tag) & (t0[3] == tag) & (t1[1] == tag) & (t1[3] == tag)) && np1 < SPIN_CAP);
    }
    unsigned pk[4];
    {
      float u0, u1, u2, u3, u4, u5, u6, u7;
#define DPPADD(r, P) "v_add_f32_dpp " r ", " r ", " r " quad_perm:" P " row_mask:0xf bank_mask:0xf\n\t"
#define DPP8(P) DPPADD("%4", P) DPPADD("%5", P) DPPADD("%6", P) DPPADD("%7", P) DPPADD("%8", P) DPPADD("%9", P) DPPADD("%10", P) DPPADD("%11", P)
      asm volatile("v_lshlrev_b32 %4, 16, %12\n\tv_and_b32 %5, 0xffff0000, %12\n\tv_lshlrev_b32 %6, 16, %13\n\tv_and_b32 %7, 0xffff0000, %13\n\t"
                   "v_lshlrev_b32 %8, 16, %14\n\tv_and_b32 %9, 0xffff0000, %14\n\tv_lshlrev_b32 %10, 16, %15\n\tv_and_b32 %11, 0xffff0000, %15\n\t"
                   DPP8("[1,0,3,2]") DPP8("[2,3,0,1]")
                   "v_cvt_pk_bf16_f32 %0, %4, %5\n\tv_cvt_pk_bf16_f32 %1, %6, %7\n\tv_cvt_pk_bf16_f32 %2, %8, %9\n\tv_cvt_pk_bf16_f32 %3, %10, %11"
                   : "=&v"(pk[0]), "=&v"(pk[1]), "=&v"(pk[2]), "=&v"(pk[3]), "=&v"(u0), "=&v"(u1), "=&v"(u2), "=&v"(u3), "=&v"(u4), "=&v"(u5), "=&v"(u6), "=&v"(u7)
                   : "v"(t0[0]), "v"(t0[2]), "v"(t1[0]), "v"(t1[2]));
    }
    u32x4 aq[4];
#pragma unroll
    for (int p = 0; p < 4; ++p)
      aq[p] = u32x4{__builtin_amdgcn_perm(0u, pk[p], sel0), __builtin_amdgcn_perm(0u, pk[p], sel1), __builtin_amdgcn_perm(0u, pk[p], sel2), __builtin_amdgcn_perm(0u, pk[p], sel3)};
#pragma unroll
    for (int p = 0; p < 3; ++p) { acc0 = mfma32(aq[p], bq[2 * p], acc0); acc1 = mfma32(aq[p], bq[2 * p + 1], acc1); }
    asm volatile("" : "+v"(aq[3]), "+v"(acc0), "+v"(acc1));
    SB();
    acc0 = mfma16(aq[3][0], aq[3][1], wb[6][0], wb[6][1], acc0); acc1 = mfma16(aq[3][0], aq[3][1], wb[6][2], wb[6][3], acc1);
    acc0 = mfma16(aq[3][2], aq[3][3], wb[7][0], wb[7][1], acc0); acc1 = mfma16(aq[3][2], aq[3][3], wb[7][2], wb[7][3], acc1);
    __builtin_amdgcn_raw_buffer_store_b128(granules(proj_epilogue(acc0, acc1), tagv), rws, yvo, yso, AUX_SC1);
    if (reducer) {
      delay_ticks(FIN_DELAY);
      float y0, y1, y2, y3;
      unsigned np2 = 0;
      for (;;) {
        MEMFENCE();
        const u32x4 a = ld_sc1(rws, r_vo, 0), c = ld_sc1(rws, r_vo, 32 * 5120), d = ld_sc1(rws, r_vo, 64 * 5120);
        unsigned bad = ((a[1] ^ tagv) | (a[3] ^ tagv)) | ((c[1] ^ tagv) | (c[3] ^ tagv));
        y0 = bflo(a[0]) + bflo(c[0]); y1 = bfhi(a[0]) + bfhi(c[0]); y2 = bflo(a[2]) + bflo(c[2]); y3 = bfhi(a[2]) + bfhi(c[2]);
        asm volatile("" : "+v"(bad), "+v"(y0), "+v"(y1), "+v"(y2), "+v"(y3));
        bad |= (d[1] ^ tagv) | (d[3] ^ tagv);
        y0 += bflo(d[0]); y1 += bfhi(d[0]); y2 += bflo(d[2]); y3 += bfhi(d[2]);
        if (__builtin_expect(__builtin_amdgcn_ballot_w64(bad != 0u) == 0, 1)) break;
        if (++np2 > SPIN_CAP) break;
      }
      const auto s0 = __builtin_amdgcn_permlane32_swap(__float_as_uint(y0), __float_as_uint(y2), false, false);
      const auto s1 = __builtin_amdgcn_permlane32_swap(__float_as_uint(y1), __float_as_uint(y3), false, false);
      const float z0 = __uint_as_float(s0[0]) + __uint_as_float(s0[1]), z1 = __uint_as_float(s1[0]) + __uint_as_float(s1[1]);
      const auto s2 = __builtin_amdgcn_permlane16_swap(__float_as_uint(z0), __float_as_uint(z1), false, false);
      float y = __uint_as_float(s2[0]) + __uint_as_float(s2[1]);
      y += dpp<ROR8>(y); y += dpp<ROR4>(y); y += dpp<XOR2>(y);
      __builtin_amdgcn_raw_buffer_store_b16((unsigned short)cvtpk(xres + y, 0.f), rout, out_vo, 0, AUX_SC1);
      if (bid == 0) __builtin_amdgcn_raw_buffer_store_b32(tagv, rws, lane == 0 ? WS_EPOCH : OOB, 0, AUX_SC1);
    }
  }
}

extern "C" void kernel_launch(void* const* d_in, const int* in_sizes, int n_in, void* d_out, int out_size, void* d_ws, size_t ws_size,
                              hipStream_t stream) {
  fused_decode<<<256, 512, 0, stream>>>((const u16*)d_in[0], (const u16*)d_in[1], (const u16*)d_in[2], (const u16*)d_in[3], (const u16*)d_in[4],
                                        (const u16*)d_in[6], (const u16*)d_in[7], (const u16*)d_in[8], (u16*)d_out, (unsigned char*)d_ws);
}
```

```cpp
#include <hip/hip_runtime.h>
#include <hip/hip_bf16.h>
#include <stdint.h>
#include <type_traits>

typedef unsigned short u16;
typedef unsigned u32x4 __attribute__((ext_vector_type(4)));
typedef unsigned u32x2 __attribute__((ext_vector_type(2)));
typedef float f32x4 __attribute__((ext_vector_type(4)));
typedef float f32x2 __attribute__((ext_vector_type(2)));
typedef short s16x4 __attribute__((ext_vector_type(4)));
typedef __bf16 bf16x8 __attribute__((ext_vector_type(8)));
typedef __bf16 bf16x2 __attribute__((ext_vector_type(2)));
typedef __amdgpu_buffer_rsrc_t rsrc_t;

constexpr int QK = 6, CU = 6;
constexpr int STAG = 10;
constexpr int BPACE = 5;
constexpr int POLLPOS = 8;
constexpr int KEEP1 = 10;
constexpr int S_DELAY = 156;
constexpr int T_PLAN = 1396;
constexpr int RATE16 = 216;
constexpr int LAT0 = 79;
constexpr int SMP0 = 500, WAIT0 = 195, DEAD0 = 30, GAIN0 = 24;
constexpr int SMP1 = 385, WAIT1 = 135, DEAD1 = 7, GAIN1 = 52;
constexpr int Q_AT = 430, GU_AT = 440;
constexpr int TDEAD0 = 5, TREND0 = 5, TDEAD1 = 1, TREND1 = 11;
constexpr int BACK_AT = 70;
constexpr int BACK_DELAY = 12;
constexpr int LATE_DELAY = 20;
constexpr int FIN_DELAY = 48;
constexpr int CODE_BYTES = 16512;

constexpr int AUX_NT = 2, AUX_SC1 = 16;
constexpr int RSRC_W3 = 0x00020000;
constexpr unsigned SPIN_CAP = 1u << 18;
constexpr int OOB = 0x40000000;
constexpr unsigned ALL4 = 0x01010101u;

constexpr int WS_EPOCH = 0;
constexpr int WS_TS = 1024;
constexpr int WS_FP = 4096;
constexpr int WS_ML = WS_FP + 8 * 18 * 5 * 512;
constexpr int WS_YP = WS_ML + 32 * 2 * 4 * 4 * 16;
constexpr int WS_OP = 2 << 20, OP_STRIDE = 1 << 20;
constexpr int WS_END = WS_OP + 32 * OP_STRIDE;
static_assert(WS_YP + 96 * 10 * 2 * 32 * 8 <= WS_OP, "workspace map");

template <int I, int N, class F> __device__ __forceinline__ void static_for(F&& f) {
  if constexpr (I < N) { f(std::integral_constant<int, I>{}); static_for<I + 1, N>(f); }
}
__device__ __forceinline__ rsrc_t mk_rsrc(const void* p, int bytes) {
  return __builtin_amdgcn_make_buffer_rsrc(const_cast<void*>(p), 0, bytes, RSRC_W3);
}
__device__ __forceinline__ u32x4 ld_nt(rsrc_t r, int voff, int soff) { return __builtin_amdgcn_raw_buffer_load_b128(r, voff, soff, AUX_NT); }
__device__ __forceinline__ u32x4 ld_sc1(rsrc_t r, int voff, int soff) { return __builtin_amdgcn_raw_buffer_load_b128(r, voff, soff, AUX_SC1); }
__device__ __forceinline__ u32x2 ld2_sc1(rsrc_t r, int voff, int soff) { return __builtin_amdgcn_raw_buffer_load_b64(r, voff, soff, AUX_SC1); }
__device__ __forceinline__ void st_pair(rsrc_t r, int voff, int soff, float a, float b, unsigned tag) {
  u32x4 v = {__float_as_uint(a), tag, __float_as_uint(b), tag};
  __builtin_amdgcn_raw_buffer_store_b128(v, r, voff, soff, AUX_SC1);
}
__device__ __forceinline__ float bflo(unsigned v) { return __uint_as_float(v << 16); }
__device__ __forceinline__ float bfhi(unsigned v) { return __uint_as_float(v & 0xffff0000u); }
__device__ __forceinline__ unsigned cvtpk(float lo, float hi) {
  bf16x2 r = {(__bf16)lo, (__bf16)hi};
  return __builtin_bit_cast(unsigned, r);
}
__device__ __forceinline__ float rbf(float x) { return (float)(__bf16)x; }
template <int CTRL> __device__ __forceinline__ float dpp(float x) {
  return __builtin_bit_cast(float, __builtin_amdgcn_mov_dpp(__builtin_bit_cast(int, x), CTRL, 0xf, 0xf, true));
}
constexpr int XOR1 = 0xB1, XOR2 = 0x4E, HMIR = 0x141, ROR8 = 0x128, ROR4 = 0x124;
__device__ __forceinline__ float x16_sum(float x) { auto s = __builtin_amdgcn_permlane16_swap(__float_as_uint(x), __float_as_uint(x), false, false); return __uint_as_float(s[0]) + __uint_as_float(s[1]); }
__device__ __forceinline__ float x32_sum(float x) { auto s = __builtin_amdgcn_permlane32_swap(__float_as_uint(x), __float_as_uint(x), false, false); return __uint_as_float(s[0]) + __uint_as_float(s[1]); }
__device__ __forceinline__ float x16_max(float x) { auto s = __builtin_amdgcn_permlane16_swap(__float_as_uint(x), __float_as_uint(x), false, false); return fmaxf(__uint_as_float(s[0]), __uint_as_float(s[1])); }
__device__ __forceinline__ float x32_max(float x) { auto s = __builtin_amdgcn_permlane32_swap(__float_as_uint(x), __float_as_uint(x), false, false); return fmaxf(__uint_as_float(s[0]), __uint_as_float(s[1])); }
__device__ __forceinline__ float row_sum(float x) { x += dpp<XOR1>(x); x += dpp<XOR2>(x); x += dpp<HMIR>(x); x += dpp<ROR8>(x); return x; }
__device__ __forceinline__ float row_max(float x) { x = fmaxf(x, dpp<XOR1>(x)); x = fmaxf(x, dpp<XOR2>(x)); x = fmaxf(x, dpp<HMIR>(x)); x = fmaxf(x, dpp<ROR8>(x)); return x; }
__device__ __forceinline__ float quad_sum(float x) { x += dpp<XOR1>(x); x += dpp<XOR2>(x); return x; }
__device__ __forceinline__ float dot2(unsigned a, unsigned b, float c) {
  return __builtin_amdgcn_fdot2_f32_bf16(__builtin_bit_cast(bf16x2, a), __builtin_bit_cast(bf16x2, b), c, false);
}
__device__ __forceinline__ f32x4 mfma16(unsigned a0, unsigned a1, unsigned b0, unsigned b1, f32x4 c) {
  u32x2 a = {a0, a1}, b = {b0, b1};
  return __builtin_amdgcn_mfma_f32_16x16x16bf16_1k(__builtin_bit_cast(s16x4, a), __builtin_bit_cast(s16x4, b), c, 0, 0, 0);
}
__device__ __forceinline__ f32x4 mfma32(u32x4 a, u32x4 b, f32x4 c) {
  return __builtin_amdgcn_mfma_f32_16x16x32_bf16(__builtin_bit_cast(bf16x8, a), __builtin_bit_cast(bf16x8, b), c, 0, 0, 0);
}
template <int RECIP> __device__ __forceinline__ unsigned pace_step(unsigned tprev, unsigned tend, unsigned& tdone) {
  unsigned d, now, tgt;
  asm volatile("s_sub_i32 %0, %5, %4\n\ts_max_i32 %0, %0, 0\n\ts_min_i32 %0, %0, 0x7d0\n\ts_mul_i32 %0, %0, %6\n\ts_lshr_b32 %0, %0, 16\n\ts_add_u32 %2, %4, %0\n"
               "1:\n\ts_memrealtime vcc\n\ts_getreg_b32 %0, hwreg(HW_REG_IB_STS, 0, 4)\n\ts_waitcnt lgkmcnt(0)\n\ts_cmp_lg_u32 %0, 0\n\ts_cselect_b32 %3, vcc_lo, %3\n\t"
               "s_sub_u32 %0, vcc_lo, %2\n\ts_cmp_lt_i32 %0, 0\n\ts_cbranch_scc1 1b\n\ts_mov_b32 %1, vcc_lo"
               : "=&s"(d), "=&s"(now), "=&s"(tgt), "+s"(tdone) : "s"(tprev), "s"(tend), "i"(RECIP) : "vcc", "scc");
  return now;
}
__device__ __forceinline__ void wait_until(unsigned tgt) {
  asm volatile("1:\n\ts_memrealtime vcc\n\ts_waitcnt lgkmcnt(0)\n\ts_sub_u32 vcc_lo, vcc_lo, %0\n\ts_cmp_lt_i32 vcc_lo, 0\n\ts_cbranch_scc1 1b" :: "s"(tgt) : "vcc", "scc", "memory");
}
#define SB() __builtin_amdgcn_sched_barrier(0)
#define MEMFENCE() asm volatile("" ::: "memory")

__device__ __forceinline__ void delay_ticks(int n) {
  if (n > 0) wait_until((unsigned)__builtin_amdgcn_s_memrealtime() + n);
}

typedef __attribute__((address_space(3))) float lfloat;
typedef __attribute__((address_space(3))) f32x4 lf32x4;
typedef __attribute__((address_space(3))) unsigned luint;
typedef __attribute__((address_space(3))) unsigned char lbyte;
template <int SLP> __device__ __forceinline__ void wait_lds(unsigned a, unsigned want) {
  unsigned v, sv;
  asm volatile("s_branch 2f\n1:\n\ts_sleep %4\n2:\n\tds_read_b32 %0, %2\n\ts_waitcnt lgkmcnt(0)\n\tv_readfirstlane_b32 %1, %0\n\ts_cmp_lg_u32 %1, %3\n\ts_cbranch_scc1 1b"
               : "=&v"(v), "=&s"(sv) : "v"(a), "s"(want), "i"(SLP) : "scc", "memory");
}

__device__ __forceinline__ f32x4 proj_epilogue(f32x4 acc0, f32x4 acc1) {
  f32x4 r;
#pragma unroll
  for (int i = 0; i < 4; ++i) {
    auto s = __builtin_amdgcn_permlane32_swap(__float_as_uint(acc0[i]), __float_as_uint(acc1[i]), false, false);
    r[i] = __uint_as_float(s[0]) + dpp<ROR8>(__uint_as_float(s[1]));
  }
  return r;
}
__device__ __forceinline__ u32x4 granules(f32x4 r, unsigned tag) { return u32x4{cvtpk(r[0], r[1]), tag, cvtpk(r[2], r[3]), tag}; }

__global__ void __launch_bounds__(512, 2)
fused_decode(const u16* x_, const u16* kc_, const u16* vc_, const u16* wq_, const u16* wo_, const u16* wg_, const u16* wu_, const u16* wd_, u16* out_, unsigned char* ws_) {
  __shared__ __attribute__((aligned(16))) float lds[96 + 8 + 4 + 32 + 8 * 192 + 4];
  unsigned* l_q = (unsigned*)lds;
  float* l_ml = lds + 96;
  float* l_c = lds + 104;
  unsigned* l_est = (unsigned*)(lds + 108);
  unsigned char* l_f = (unsigned char*)(lds + 116);
  constexpr int FA = 0, FB = 4, FC = 8, FD = 12, FE = 16, FG = 20, FH = 28, FX = 32;
  unsigned* l_tend = (unsigned*)(lds + 122);
  unsigned* l_tag = (unsigned*)(lds + 140 + 8 * 192);
  const unsigned fbase = (unsigned)(size_t)(lbyte*)l_f;
  float* l_acc = lds + 140;
  unsigned char* l_xn = (unsigned char*)(l_acc + 512);

  typedef unsigned long long u64;
  typedef u64 u64x8 __attribute__((ext_vector_type(8)));
  typedef u64 u64x2 __attribute__((ext_vector_type(2)));
  u64x8 ka; u64x2 kb; u64 pc;
  u32x4 fw[16];
  int bid = blockIdx.x, tid = threadIdx.x;
  {
    unsigned v1, v2;
    u64x8 ka0; u64x2 kb0;
    asm volatile("s_load_dwordx16 %0, %3, 0x0\n\ts_load_dwordx4 %1, %3, 0x40\n\ts_getpc_b64 %2" : "=&{s[36:51]}"(ka0), "=&{s[52:55]}"(kb0), "=&s"(pc) : "s"(__builtin_amdgcn_kernarg_segment_ptr()));
    asm volatile(
               "v_readfirstlane_b32 s14, %20\n\t"
               "s_lshr_b32 s15, %19, 3\n\t"
               "s_cmpk_ge_u32 s14, 0xc0\n\ts_cbranch_scc1 1f\n\t"
               "s_setprio 2\n\t"
               "s_and_b32 s16, %19, 7\n\ts_lshr_b32 s17, %19, 4\n\t"
               "s_and_b32 s18, s15, 1\n\ts_mul_i32 s18, s18, 3\n\ts_lshr_b32 s19, s14, 6\n\ts_add_u32 s18, s18, s19\n\t"
               "v_bfe_u32 %16, %20, 3, 3\n\tv_and_b32 %17, 7, %20\n\t"
               "s_cmp_ge_u32 s17, 5\n\ts_cselect_b32 s12, 17, 16\n\ts_cselect_b32 s13, 5, 0\n\ts_sub_u32 s13, s17, s13\n\t"
               "s_cmp_eq_u32 s18, 5\n\ts_cselect_b32 s17, s12, s17\n\ts_cselect_b32 s18, s13, s18\n\t"
               "s_cmp_ge_u32 s17, 15\n\ts_cselect_b32 s12, 15, 12\n\ts_movk_i32 s13, 0x3000\n\t"
               "s_cmp_lt_u32 s17, 12\n\ts_cselect_b32 s12, 0, s12\n\ts_cselect_b32 s13, s13, 0xc00\n\ts_cselect_b32 s19, 12, 3\n\t"
               "s_mul_i32 s16, s16, s19\n\ts_sub_u32 s12, s17, s12\n\ts_add_u32 s12, s12, s16\n\t"
               "v_mad_u32_u24 %16, %16, s13, %17\n\t"
               "s_mul_i32 s19, s18, s13\n\ts_add_u32 s12, s12, s19\n\ts_lshl_b32 s12, s12, 7\n\t"
               "v_lshlrev_b32 %16, 4, %16\n\t"
               "s_mul_i32 s10, s13, 0x280\n\ts_mov_b32 s11, 0x20000\n\ts_cmp_ge_u32 s18, 5\n\ts_cselect_b32 s10, 0, s10\n\t"
               "s_cmp_ge_u32 s17, 15\n\t"
               "s_waitcnt lgkmcnt(0)\n\t"
               "s_cselect_b64 s[8:9], s[48:49], s[46:47]\n\t"
               "s_cmp_lt_u32 s17, 12\n\ts_cselect_b64 s[8:9], s[42:43], s[8:9]\n\t"
               "s_and_b32 s9, s9, 0xffff\n\t"
               "buffer_load_dwordx4 %0, %16, s[8:11], s12 offen nt\n\t" "s_add_u32 s12, s12, s13\n\t"
               "buffer_load_dwordx4 %1, %16, s[8:11], s12 offen nt\n\t" "s_add_u32 s12, s12, s13\n\t"
               "buffer_load_dwordx4 %2, %16, s[8:11], s12 offen nt\n\t" "s_add_u32 s12, s12, s13\n\t"
               "buffer_load_dwordx4 %3, %16, s[8:11], s12 offen nt\n\t" "s_add_u32 s12, s12, s13\n\t"
               "buffer_load_dwordx4 %4, %16, s[8:11], s12 offen nt\n\t" "s_add_u32 s12, s12, s13\n\t"
               "buffer_load_dwordx4 %5, %16, s[8:11], s12 offen nt\n\t" "s_add_u32 s12, s12, s13\n\t"
               "buffer_load_dwordx4 %6, %16, s[8:11], s12 offen nt\n\t" "s_add_u32 s12, s12, s13\n\t"
               "buffer_load_dwordx4 %7, %16, s[8:11], s12 offen nt\n\t" "s_add_u32 s12, s12, s13\n\t"
               "buffer_load_dwordx4 %8, %16, s[8:11], s12 offen nt\n\t" "s_add_u32 s12, s12, s13\n\t"
               "buffer_load_dwordx4 %9, %16, s[8:11], s12 offen nt\n\t" "s_add_u32 s12, s12, s13\n\t"
               "buffer_load_dwordx4 %10, %16, s[8:11], s12 offen nt\n\t" "s_add_u32 s12, s12, s13\n\t"
               "buffer_load_dwordx4 %11, %16, s[8:11], s12 offen nt\n\t" "s_add_u32 s12, s12, s13\n\t" "s_barrier\n\t"
               "buffer_load_dwordx4 %12, %16, s[8:11], s12 offen nt\n\t" "s_add_u32 s12, s12, s13\n\t"
               "buffer_load_dwordx4 %13, %16, s[8:11], s12 offen nt\n\t" "s_add_u32 s12, s12, s13\n\t"
               "buffer_load_dwordx4 %14, %16, s[8:11], s12 offen nt\n\t" "s_add_u32 s12, s12, s13\n\t"
               "buffer_load_dwordx4 %15, %16, s[8:11], s12 offen nt\n\t"
               "s_branch 2f\n"
               "1:\n\t"
               "v_and_b32 %16, 31, %20\n\tv_lshl_add_u32 %16, %16, 2, %21\n\tv_mov_b32 %17, 0\n\tds_write_b32 %16, %17\n\t"
               "s_cmpk_lg_u32 s14, 0xc0\n\ts_cbranch_scc1 3f\n\t"
               "v_and_b32 %16, 7, %20\n\tv_lshl_add_u32 %16, %16, 5, s15\n\tv_lshlrev_b32 %16, 7, %16\n\tv_min_u32 %16, %24, %16\n\t"
               "global_load_dwordx4 %15, %16, %23\n\t"
               "v_bfe_u32 %17, %20, 5, 1\n\tv_mul_u32_u24 %17, 0x50, %17\n\tv_and_b32 %16, 31, %20\n\tv_add_lshl_u32 %16, %16, %17, 4\n\t"
               "v_and_or_b32 %17, %20, 15, %17\n\tv_lshlrev_b32 %17, 4, %17\n\t"
               "s_waitcnt lgkmcnt(0)\n\t"
               "global_load_dwordx4 %0, %16, s[36:37]\n\tglobal_load_dwordx4 %1, %16, s[36:37] offset:512\n\tglobal_load_dwordx4 %2, %17, s[36:37] offset:1024\n"
               "3:\n\t"
               "s_waitcnt lgkmcnt(0)\n\ts_barrier\n"
               "2:"
               : "=&v"(fw[0]), "=&v"(fw[1]), "=&v"(fw[2]), "=&v"(fw[3]), "=&v"(fw[4]), "=&v"(fw[5]), "=&v"(fw[6]), "=&v"(fw[7]),
                 "=&v"(fw[8]), "=&v"(fw[9]), "=&v"(fw[10]), "=&v"(fw[11]), "=&v"(fw[12]), "=&v"(fw[13]), "=&v"(fw[14]), "=&v"(fw[15]),
                 "=&v"(v1), "=&v"(v2)
               : "{s[36:51]}"(ka0), "s"(bid), "v"(tid), "s"(fbase - 32), "{s[52:55]}"(kb0), "s"(pc), "i"(CODE_BYTES - 128)
               : "s8", "s9", "s10", "s11", "s12", "s13", "s14", "s15", "s16", "s17", "s18", "s19", "scc", "memory");
    asm volatile("" : "={s[36:51]}"(ka), "={s[52:55]}"(kb), "+s"(bid) : "0"(ka0), "1"(kb0));
    asm volatile("" : "+v"(tid));
  }
  const int xq = bid & 7, uq = bid >> 3;
  const int h = xq * 4 + (uq >> 3), j8 = uq & 7, b = j8 >> 2, s = j8 & 3;
  const int lane = tid & 63, wave = __builtin_amdgcn_readfirstlane(tid >> 6);
  const int g = lane >> 3, j = lane & 7;
  const int bl = (lane >> 2) & 1, e4 = lane & 3;
  const bool isS = wave >= 4;
  const u16 *x = (const u16*)ka[0], *kc = (const u16*)ka[1], *vc = (const u16*)ka[2], *wo = (const u16*)ka[4], *wd = (const u16*)ka[7];
  u16* out = (u16*)kb[0];
  unsigned char* ws = (unsigned char*)kb[1];
  u64 tst;
  asm volatile("s_memrealtime %0\n\ts_waitcnt lgkmcnt(0)" : "=s"(tst));
  const unsigned tstart = (unsigned)tst;
  const rsrc_t rws = mk_rsrc(ws, WS_END);

  if (isS) {
    const int sw = wave - 4;
    const rsrc_t r0 = mk_rsrc(kc, 2 * 32 * 8192 * 384), rv = mk_rsrc(vc, 2 * 32 * 8192 * 384);
    const int voff = ((b * 32 + h) * 8192 + 6144 + 32 * s + 8 * sw + g) * 384 + 16 * j;
    u32x4 buf[96];
    u32x4 qv[3];
    float sc[16];
    unsigned pd[16];
    f32x4 acc[3] = {{0.f, 0.f, 0.f, 0.f}, {0.f, 0.f, 0.f, 0.f}, {0.f, 0.f, 0.f, 0.f}};
    unsigned msk[4];
#pragma unroll
    for (int d = 0; d < 4; ++d) msk[d] = (j >> 1) == d ? ((j & 1) ? 0xffff0000u : 0x0000ffffu) : 0u;
    constexpr float C2 = 0.07216878364870322f * 1.4426950408889634f;
        const int stag = ((2 * (uq >> 2) - 7) * STAG) >> 1;
    unsigned tprev = tstart + S_DELAY + stag, tend = tstart + T_PLAN + stag, lat = 0;

    auto consume = [&](auto G) {
      constexpr int t = decltype(G)::value;
      if constexpr (t < 16) {
        float a0 = 0.f, a1 = 0.f, a2 = 0.f;
#pragma unroll
        for (int e = 0; e < 4; ++e) { a0 = dot2(buf[3 * t][e], qv[0][e], a0); a1 = dot2(buf[3 * t + 1][e], qv[1][e], a1); a2 = dot2(buf[3 * t + 2][e], qv[2][e], a2); }
        float a = (a0 + a1) + a2;
        a += dpp<XOR1>(a); a += dpp<XOR2>(a); a += dpp<HMIR>(a);
        sc[t] = a;
        if constexpr (t == 15) {
          float m = sc[0];
#pragma unroll
          for (int u = 1; u < 16; ++u) m = fmaxf(m, sc[u]);
          m = fmaxf(m, dpp<ROR8>(m)); m = x16_max(m); m = x32_max(m);
          const float mc = m * C2;
          float l = 0.f;
#pragma unroll
          for (int u = 0; u < 16; ++u) { const float p = __builtin_amdgcn_exp2f(fmaf(sc[u], C2, -mc)); pd[u] = cvtpk(p, p); l += bfhi(pd[u]); }
          l += dpp<ROR8>(l); l = x16_sum(l); l = x32_sum(l);
          if (lane == 0) { *(f32x2*)(l_ml + 2 * sw) = f32x2{mc, l}; MEMFENCE(); l_f[FA + sw] = 1; }
        }
      } else {
        constexpr int u = t - 16;
        const u32x4 a = {pd[u] & msk[0], pd[u] & msk[1], pd[u] & msk[2], pd[u] & msk[3]};
#pragma unroll
        for (int i = 0; i < 3; ++i) acc[i] = mfma32(a, buf[3 * t + i], acc[i]);
      }
    };
    static_for<0, 16>([&](auto K) {
      constexpr int k = decltype(K)::value;
      if constexpr (k > 6) {
        MEMFENCE();
        unsigned tv = *l_tend;
        asm volatile("" : "+v"(tv));
        tv = __builtin_amdgcn_readfirstlane(tv);
        tend = tv ? tv + stag : tend;
      }
      unsigned tdone = tprev + LAT0;
      const unsigned now = pace_step<(k ? 65536 / (16 - k) : 0)>(tprev, tend, tdone);
      SB();
#pragma unroll
      for (int i = 0; i < 6; ++i) { buf[6 * k + i] = ld_nt(k < 8 ? r0 : rv, voff + (i % 3) * 128, ((2 * k + i / 3) & 15) * 49152); SB(); }
      if constexpr (k == 15) { if (lane == 0) l_f[FB + sw] = 1; }
      if constexpr (k >= 1 && k < 15) asm volatile("s_waitcnt vmcnt(6)" ::: "memory");
      if constexpr (k >= 2 && k < 13) {
        if (tdone == now) tdone = (unsigned)__builtin_amdgcn_s_memrealtime();
        lat = (lat << 16) | (tdone - tprev);
        *(u32x2*)(l_est + 2 * sw) = u32x2{(now - stag + ((6 * (15 - k) * RATE16) >> 4)) | 1u, lat};
      }
      tprev = now;
      if constexpr (k == QK) {
        wait_lds<0>(fbase + FG, ALL4);
#pragma unroll
        for (int i = 0; i < 3; ++i) qv[i] = *(const u32x4*)(l_q + 32 * i + 4 * j);
      }
      SB();
      constexpr auto upto = [](int kk) constexpr { if (kk < QK) return 0; const int c = CU * (kk - QK + 1), l = kk < 15 ? 2 * kk : 28; return c < l ? c : l; };
      static_for<upto(k - 1), upto(k)>([&](auto G) { consume(G); SB(); });
      SB();
    });
    __builtin_amdgcn_s_setprio(3);
    wait_lds<0>(fbase + FE, ALL4);
    float cq = l_c[lane >> 4];
    unsigned dst = (unsigned)(size_t)(lfloat*)(l_acc + (sw * 2 + (g & 1)) * 192 + 8 * j + 4 * ((lane >> 4) & 1));
    unsigned src = (unsigned)(size_t)(lfloat*)(l_acc + (lane >> 4) * 384 + 64 * sw + 8 * j + 4 * (g & 1));
    unsigned fla = fbase + FC + sw, one = 1u, flr = fbase + FC;
    int pvo = (lane & 16) ? OOB : (g & 1) * 128 + j * 16 + (lane >> 5) * 8;
    int pso = WS_OP + h * OP_STRIDE + (b * 4 + s) * 768 + sw * 256;
    unsigned tagv = *l_tag;
    asm volatile("" : "+v"(cq), "+v"(dst), "+v"(src), "+v"(fla), "+v"(flr), "+v"(one), "+v"(pvo), "+s"(pso), "+v"(tagv));
    const bool wr = (lane >> 5) == (g & 1);
    SB();
    static_for<28, 32>([&](auto G) { consume(G); SB(); });
    if (wr) {
#pragma unroll
      for (int i = 0; i < 3; ++i) *(lf32x4*)(size_t)(dst + 256 * i) = acc[i];
      MEMFENCE();
      *(lbyte*)(size_t)fla = (unsigned char)one;
    }
    if (sw < 3) {
      f32x4 u0, u1;
      unsigned fv, fs;
      asm volatile("1:\n\tds_read_b32 %0, %4\n\tds_read_b128 %2, %5\n\tds_read_b128 %3, %5 offset:768\n\ts_waitcnt lgkmcnt(2)\n\tv_readfirstlane_b32 %1, %0\n\t"
                   "s_cmp_lg_u32 %1, 0x01010101\n\ts_waitcnt lgkmcnt(0)\n\ts_cbranch_scc1 1b"
                   : "=&v"(fv), "=&s"(fs), "=&v"(u0), "=&v"(u1) : "v"(flr), "v"(src) : "scc", "memory");
      float o[4];
#pragma unroll
      for (int e = 0; e < 4; ++e) o[e] = cq * (u0[e] + u1[e]);
      const auto s0 = __builtin_amdgcn_permlane32_swap(__float_as_uint(o[0]), __float_as_uint(o[2]), false, false);
      const auto s1 = __builtin_amdgcn_permlane32_swap(__float_as_uint(o[1]), __float_as_uint(o[3]), false, false);
      const float p0 = x16_sum(__uint_as_float(s0[0]) + __uint_as_float(s0[1])), p1 = x16_sum(__uint_as_float(s1[0]) + __uint_as_float(s1[1]));
      __builtin_amdgcn_raw_buffer_store_b64(u32x2{cvtpk(p0, p1), tagv}, rws, pvo, pso, AUX_SC1);
    }
    MEMFENCE();
    *(lbyte*)(size_t)(fla + (FD - FC)) = (unsigned char)one;
    return;
  }

  __builtin_amdgcn_s_setprio(2);
  unsigned tag, ep;
  asm volatile("s_load_dword %0, %1, 0x0" : "=&s"(ep) : "s"(ws));
#define TAG_READY() asm volatile("s_waitcnt lgkmcnt(0)\n\ts_add_u32 %0, %1, 1\n\ts_addc_u32 %0, %0, 0" : "=&s"(tag) : "s"(ep) : "scc", "memory")
  const int w = wave;
  const bool second = uq & 1, single = second && w == 2;
  const unsigned c0 = uq >> 1, uc = single ? (c0 >= 5 ? 17 : 16) : c0, ur = single ? (c0 >= 5 ? c0 - 5 : c0) : 3 * second + w;
  const bool has_front = (w < 3) & (ur < 5);
  const int st_col = (g & 1) ? OOB : (lane >> 5) * 128 + j * 16;
  const rsrc_t rx = mk_rsrc(x, 2560);
  const unsigned sLo = (e4 & 1) ? 0x01000c0cu : 0x0c0c0100u, sHi = (e4 & 1) ? 0x03020c0cu : 0x0c0c0302u;
  unsigned sel0 = e4 < 2 ? sLo : 0x0c0c0c0cu, sel1 = e4 < 2 ? 0x0c0c0c0cu : sLo, sel2 = e4 < 2 ? sHi : 0x0c0c0c0cu, sel3 = e4 < 2 ? 0x0c0c0c0cu : sHi;
  if (w == 3) {
    const int xo = (lane >> 5) * 1280 + (lane & 31) * 16;
    const bool x3 = (lane & 16) == 0;
    asm volatile("s_waitcnt vmcnt(0)" : "+v"(fw[0]), "+v"(fw[1]), "+v"(fw[2]) : "v"(fw[15]));
    u32x4 xv[3] = {fw[0], fw[1], x3 ? fw[2] : u32x4{0u, 0u, 0u, 0u}};
    float v[24];
#pragma unroll
    for (int e = 0; e < 12; ++e) { v[2 * e] = bflo(xv[e >> 2][e & 3]); v[2 * e + 1] = bfhi(xv[e >> 2][e & 3]); }
    float sum = 0.f;
#pragma unroll
    for (int e = 0; e < 24; ++e) sum += v[e];
    sum = x16_sum(row_sum(sum));
    const float mean = sum * (1.f / 640.f);
    float sq = 0.f, sq3 = 0.f;
#pragma unroll
    for (int e = 0; e < 16; ++e) { const float d = v[e] - mean; sq = fmaf(d, d, sq); }
#pragma unroll
    for (int e = 16; e < 24; ++e) { const float d = v[e] - mean; sq3 = fmaf(d, d, sq3); }
    sq = x16_sum(row_sum(sq + (x3 ? sq3 : 0.f)));
    const float mb = rbf(mean), den = rbf(__builtin_amdgcn_sqrtf(rbf(rbf(sq * (1.f / 640.f)) + 1e-5f)));
    const float inv = __builtin_amdgcn_rcpf(den);
#pragma unroll
    for (int e = 0; e < 12; ++e) xv[e >> 2][e & 3] = cvtpk(rbf(v[2 * e] - mb) * inv, rbf(v[2 * e + 1] - mb) * inv);
    *(u32x4*)(l_xn + xo) = xv[0];
    *(u32x4*)(l_xn + xo + 512) = xv[1];
    if (x3) *(u32x4*)(l_xn + xo + 1024) = xv[2];
    MEMFENCE();
    l_f[FX] = 1;
  }
  if (has_front) {
    wait_lds<1>(fbase + FX, 1u);
    const u32x4 xr[2] = {*(const u32x4*)(l_xn + bl * 1280 + 256 * ur + g * 32), *(const u32x4*)(l_xn + bl * 1280 + 256 * ur + g * 32 + 16)};
    TAG_READY();
    f32x4 acc0 = {0.f, 0.f, 0.f, 0.f}, acc1 = {0.f, 0.f, 0.f, 0.f};
    asm volatile("s_waitcnt vmcnt(4)" : "+v"(fw[0]), "+v"(fw[1]), "+v"(fw[2]), "+v"(fw[3]), "+v"(fw[4]), "+v"(fw[5]), "+v"(fw[6]), "+v"(fw[7]),
                 "+v"(fw[8]), "+v"(fw[9]), "+v"(fw[10]), "+v"(fw[11]));
#pragma unroll
    for (int n = 0; n < 16; ++n) {
      if (n == 12) { SB(); asm volatile("s_waitcnt vmcnt(0)" : "+v"(fw[12]), "+v"(fw[13]), "+v"(fw[14]), "+v"(fw[15])); }
      const unsigned xw = xr[n >> 3][(n >> 1) & 3];
      const unsigned a0 = __builtin_amdgcn_perm(0u, xw, (n & 1) ? sel2 : sel0), a1 = __builtin_amdgcn_perm(0u, xw, (n & 1) ? sel3 : sel1);
      acc0 = mfma16(a0, a1, fw[n][0], fw[n][1], acc0);
      acc1 = mfma16(a0, a1, fw[n][2], fw[n][3], acc1);
    }
    f32x4 r = proj_epilogue(acc0, acc1);
    f32x4* l_u = (f32x4*)l_acc;
    if (w == 0) {
      wait_lds<1>(fbase + FH, second ? 0x0100u : 0x010100u);
      r += l_u[lane];
      if (!second) r += l_u[64 + lane];
    } else if (!single) {
      l_u[(w - 1) * 64 + lane] = r;
      MEMFENCE();
      l_f[FH + w] = 1;
    }
    if (w == 0 || single)
      __builtin_amdgcn_raw_buffer_store_b128(granules(r, tag), rws, ((lane >> 4) & 1) * 256 + st_col, WS_FP + ((xq * 18 + uc) * 5 + (single ? ur : second)) * 512, AUX_SC1);
    asm volatile("s_waitcnt vmcnt(0)" ::: "memory");
  } else {
    asm volatile("s_waitcnt vmcnt(0)" ::"v"(fw[0]), "v"(fw[1]), "v"(fw[2]), "v"(fw[3]), "v"(fw[4]), "v"(fw[5]), "v"(fw[6]), "v"(fw[7]),
                 "v"(fw[8]), "v"(fw[9]), "v"(fw[10]), "v"(fw[11]), "v"(fw[12]), "v"(fw[13]), "v"(fw[14]), "v"(fw[15]));
    TAG_READY();
  }
  const int slot = w * 8 + j8;
  const bool has_back = slot < 30;
  const int t3 = slot / 10, cc = slot - 10 * t3;
  const int rid = xq * 20 + uq;
  const int rb = rid / 80, rc = (rid - 80 * rb) >> 3, ro = rid & 7;
  const bool reducer = (w == 0) && (bid < 160);
  unsigned ha[2][2] = {{0u, 0u}, {0u, 0u}};
  unsigned tend = tstart + T_PLAN;
  auto pace_round = [&](unsigned t_sample, unsigned t_read, int so, int dead, int gain16, int tdead, int trend, int keep16) {
    wait_until(t_sample);
    u32x4 e, f;
    do { MEMFENCE(); e = *(const u32x4*)l_est; f = *(const u32x4*)(l_est + 4); } while (!__builtin_amdgcn_readfirstlane(e[0] & e[2] & f[0] & f[2] & 1u));
    const u32x4 pv = {tstart + (unsigned)((int)((e[0] - tstart) + (e[2] - tstart) + (f[0] - tstart) + (f[2] - tstart)) >> 2), tag, e[1] + e[3] + f[1] + f[3], tag};
    __builtin_amdgcn_raw_buffer_store_b128(pv, rws, (lane == 0 && j8 == (((uq >> 3) * 5) & 7)) ? ((uq >> 3) * 8 + xq) * 16 : OOB, so, AUX_SC1);
    wait_until(t_read);
    MEMFENCE();
    const u32x4 tv = ld_sc1(rws, (lane & 31) * 16, so);
    if (__all((tv[1] == tag) & (tv[3] == tag))) {
      const float d = x16_sum(row_sum((float)(int)(tv[0] - tstart))) * (1.f / 32.f);
      const float l1 = x16_sum(row_sum((float)(tv[2] & 0xffffu))) * (1.f / 128.f), l0 = x16_sum(row_sum((float)(tv[2] >> 16))) * (1.f / 128.f);
      const float dr = l1 - (float)LAT0, sign = l1 - l0;
      int di = (int)(d + fmaxf(dr, 0.f) + fmaxf(dr - (float)dead, 0.f) * (float)gain16 * (1.f / 16.f) + fmaxf(sign - (float)tdead, 0.f) * (float)trend);
      const int dold = (int)(tend - tstart);
      if (keep16 && di < dold) di += ((dold - di) * keep16) >> 4;
      di = di < 0 ? 0 : (di > 2500 ? 2500 : di);
      tend = (tstart + __builtin_amdgcn_readfirstlane(di)) | 1u;
      *l_tend = tend;
    }
  };
  if (w == 0) {
    wait_until(tstart + Q_AT);
    u32x2 t[3];
    const int vo = (lane >> 5) * 512 + (lane & 31) * 8, so = WS_FP + (xq * 18 + (h & 3) * 3) * 2560 + b * 256;
    for (unsigned spin = 0;; ++spin) {
      MEMFENCE();
      t[0] = ld2_sc1(rws, vo, so); t[1] = ld2_sc1(rws, vo, so + 2560); t[2] = ld2_sc1(rws, vo, so + 5120);
      if (__all((t[0][1] == tag) & (t[1][1] == tag) & (t[2][1] == tag)) || spin > SPIN_CAP) break;
    }
#pragma unroll
    for (int i = 0; i < 3; ++i) l_q[32 * i + ((lane >> 1) & 7) * 4 + ((lane >> 4) & 1) * 2 + (lane & 1)] = cvtpk(x32_sum(bflo(t[i][0])), x32_sum(bfhi(t[i][0])));
    *l_tag = tag;
    MEMFENCE();
  }
  l_f[FG + w] = 1;
  if (w == 3) {
    pace_round(tstart + SMP0, tstart + SMP0 + WAIT0, WS_TS, DEAD0, GAIN0, TDEAD0, TREND0, 0);
  }
  if (has_back) {
    wait_until(tstart + GU_AT);
    u32x2 t[2];
    const int f16 = (h & 3) * 3 + t3;
    const int so = WS_FP + (xq * 18 + 12 + (f16 >> 2)) * 2560 + (f16 & 3) * 32;
    const int vo = bl * 256 + ((g >> 1) & 1) * 128 + (g >> 2) * 16 + (g & 1) * 8;
    const bool more = (f16 >= 4) & (e4 < 3);
    for (unsigned spin = 0;; ++spin) {
      MEMFENCE();
      t[0] = ld2_sc1(rws, vo + (e4 >> 1) * 7680 + (e4 & 1) * 512, so);
      t[1] = ld2_sc1(rws, more ? vo + 7680 + (2 + e4) * 512 : OOB, so);
      if (__all((t[0][1] == tag) & ((t[1][1] == tag) | !more)) || spin > SPIN_CAP) break;
    }
#pragma unroll
    for (int z = 0; z < 2; ++z) {
      const float a = z ? bfhi(t[0][0]) : bflo(t[0][0]), c = z ? bfhi(t[1][0]) : bflo(t[1][0]);
      const float gg = rbf(quad_sum(e4 < 2 ? a : 0.f));
      const float uu = rbf(quad_sum((e4 < 2 ? 0.f : a) + c));
      const float zz = 0.7978845608028654f * (gg + 0.044715f * gg * gg * gg);
      const float ge = rbf(gg * __builtin_amdgcn_rcpf(1.f + __builtin_amdgcn_exp2f(-2.8853900817779268f * zz)));
      const float hv = ge * uu;
      const unsigned d = cvtpk(hv, hv);
      ha[z][0] = __builtin_amdgcn_perm(0u, d, sel0); ha[z][1] = __builtin_amdgcn_perm(0u, d, sel1);
    }
  }
  const int oc = 16 * (ro & 3) + 8 * (lane & 1) + 4 * (ro >> 2) + 2 * (lane >> 5) + ((lane >> 4) & 1);
  const rsrc_t rout = mk_rsrc(out, 2560);
  int out_vo = (lane & 14) == 0 ? oc * 2 + rb * 1280 + rc * 128 : OOB;
  float xres = 0.f;
  if (reducer) {
    xres = bflo(__builtin_amdgcn_raw_buffer_load_b16(rx, oc * 2, rb * 1280 + rc * 128, 0));
    __builtin_amdgcn_raw_buffer_store_b16((unsigned short)0, rout, out_vo, 0, AUX_SC1);
  }
  if (w == 1) {
    wait_lds<4>(fbase + FA, ALL4);
    const int so = WS_ML + (h * 2 + b) * 256;
    if (lane < 4) { const f32x2 ml = *(const f32x2*)(l_ml + 2 * lane); st_pair(rws, lane * 16, so + s * 64, ml[0], ml[1], tag); }
    asm volatile("s_waitcnt vmcnt(0)" ::: "memory");
    __builtin_amdgcn_s_sleep(32);
    u32x4 t;
    for (unsigned spin = 0;; ++spin) {
      MEMFENCE();
      t = ld_sc1(rws, (lane & 15) * 16, so);
      if (__all((t[1] == tag) & (t[3] == tag)) || spin > SPIN_CAP) break;
    }
    const float mv = __uint_as_float(t[0]);
    const float M = row_max(mv);
    const float f = __builtin_amdgcn_exp2f(mv - M);
    const float L = row_sum(f * __uint_as_float(t[2]));
    if ((lane >> 2) == s) { l_c[lane & 3] = f / L; MEMFENCE(); l_f[FE + (lane & 3)] = 1; }
  }
  if (w == 3) {
    pace_round(tend - SMP1, tend - SMP1 + WAIT1, WS_TS + 512, DEAD1, GAIN1, TDEAD1, TREND1, KEEP1);
  }
  if (has_back) {
    int op_so = WS_OP + h * OP_STRIDE + t3 * 256, op_vo = (bl * 4 + e4) * 768 + 16 * g;
    int d_vo = ((48 * h + 16 * t3 + 2 * g) * 640 + 64 * cc) * 2 + 16 * j, o_vo = ((192 * h + 64 * t3 + 8 * g) * 640 + 64 * cc) * 2 + 16 * j;
    int yvo = ((lane >> 4) & 1) * 256 + st_col, yso = WS_YP + ((h * 3 + t3) * 10 + cc) * 512;
    int r_vo = (lane >> 1) * 5120 + (lane & 1) * 16 + WS_YP + rc * 512 + rb * 256 + ro * 32;
    unsigned tagv = tag, fd = fbase + FD;
    asm volatile("" : "+v"(sel0), "+v"(sel1), "+v"(sel2), "+v"(sel3), "+s"(op_so), "+v"(op_vo), "+v"(d_vo), "+v"(o_vo), "+v"(yvo), "+s"(yso), "+v"(r_vo), "+v"(out_vo), "+v"(tagv), "+v"(xres), "+v"(fd));
    const rsrc_t rd = mk_rsrc(wd, 1536 * 1280), ro_ = mk_rsrc(wo, 6144 * 1280);
    wait_lds<1>(fbase + FB, ALL4);
    {
      unsigned tf = *l_tend;
      asm volatile("" : "+v"(tf));
      tf = __builtin_amdgcn_readfirstlane(tf);
      const unsigned tn = (unsigned)__builtin_amdgcn_s_memrealtime();
      int wt = tf ? (int)(tf + BACK_AT - tn) : 0;
      asm("s_max_i32 %0, %0, %1\n\ts_min_i32 %0, %0, 0xc8" : "+s"(wt) : "s"(BACK_DELAY) : "scc");
      wait_until(tn + wt);
    }
    u32x4 wb[10], t0, t1;
    unsigned fdv = 0;
#pragma unroll
    for (int n = 0; n < 10; ++n) {
      if (n == POLLPOS) {
        const rsrc_t rp = mk_rsrc(ws, __builtin_amdgcn_readfirstlane(fdv) == ALL4 ? WS_END : 0);
        t0 = ld_sc1(rp, op_vo, op_so); t1 = ld_sc1(rp, op_vo + 128, op_so);
        SB();
      }
      if (n < 2) wb[8 + n] = ld_nt(rd, d_vo + n * 1280, 0); else wb[n - 2] = ld_nt(ro_, o_vo, (n - 2) * 1280);
      if (n == POLLPOS - 1) { MEMFENCE(); fdv = *(luint*)(size_t)fd; }
      SB();
      if (n < 9) __builtin_amdgcn_s_sleep(BPACE);
      SB();
    }
    u32x4 bq[10];
#define PAIR(p) do { bq[2 * (p)] = u32x4{wb[2 * (p)][0], wb[2 * (p)][1], wb[2 * (p) + 1][0], wb[2 * (p) + 1][1]}; \
                     bq[2 * (p) + 1] = u32x4{wb[2 * (p)][2], wb[2 * (p)][3], wb[2 * (p) + 1][2], wb[2 * (p) + 1][3]}; } while (0)
    PAIR(4); PAIR(0); PAIR(1); PAIR(2);
    const u32x4 hA = {ha[0][0], ha[0][1], ha[1][0], ha[1][1]};
    f32x4 acc0 = mfma32(hA, bq[8], f32x4{0.f, 0.f, 0.f, 0.f}), acc1 = mfma32(hA, bq[9], f32x4{0.f, 0.f, 0.f, 0.f});
    asm volatile("" : "+v"(bq[0]), "+v"(bq[1]), "+v"(bq[2]), "+v"(bq[3]), "+v"(bq[4]), "+v"(bq[5]), "+v"(acc0), "+v"(acc1));
    SB();
    unsigned np1 = 1;
    if (__builtin_expect(__builtin_amdgcn_ballot_w64((((t0[1] ^ tagv) | (t0[3] ^ tagv)) | ((t1[1] ^ tagv) | (t1[3] ^ tagv))) != 0u) != 0, 0)) {
      do {
        ++np1;
        MEMFENCE();
        if (__builtin_amdgcn_readfirstlane(*(luint*)(size_t)fd) != ALL4) { wait_lds<1>(fd, ALL4); delay_ticks(LATE_DELAY); }
        t0 = ld_sc1(rws, op_vo, op_so); t1 = ld_sc1(rws, op_vo + 128, op_so);
      } while (!__all((t0[1] == tag) & (t0[3] == tag) & (t1[1] == tag) & (t1[3] == tag)) && np1 < SPIN_CAP);
    }
    unsigned pk[4];
    {
      float u0, u1, u2, u3, u4, u5, u6, u7;
#define DPPADD(r, P) "v_add_f32_dpp " r ", " r ", " r " quad_perm:" P " row_mask:0xf bank_mask:0xf\n\t"
#define DPP8(P) DPPADD("%4", P) DPPADD("%5", P) DPPADD("%6", P) DPPADD("%7", P) DPPADD("%8", P) DPPADD("%9", P) DPPADD("%10", P) DPPADD("%11", P)
      asm volatile("v_lshlrev_b32 %4, 16, %12\n\tv_and_b32 %5, 0xffff0000, %12\n\tv_lshlrev_b32 %6, 16, %13\n\tv_and_b32 %7, 0xffff0000, %13\n\t"
                   "v_lshlrev_b32 %8, 16, %14\n\tv_and_b32 %9, 0xffff0000, %14\n\tv_lshlrev_b32 %10, 16, %15\n\tv_and_b32 %11, 0xffff0000, %15\n\t"
                   DPP8("[1,0,3,2]") DPP8("[2,3,0,1]")
                   "v_cvt_pk_bf16_f32 %0, %4, %5\n\tv_cvt_pk_bf16_f32 %1, %6, %7\n\tv_cvt_pk_bf16_f32 %2, %8, %9\n\tv_cvt_pk_bf16_f32 %3, %10, %11"
                   : "=&v"(pk[0]), "=&v"(pk[1]), "=&v"(pk[2]), "=&v"(pk[3]), "=&v"(u0), "=&v"(u1), "=&v"(u2), "=&v"(u3), "=&v"(u4), "=&v"(u5), "=&v"(u6), "=&v"(u7)
                   : "v"(t0[0]), "v"(t0[2]), "v"(t1[0]), "v"(t1[2]));
    }
    u32x4 aq[4];
#pragma unroll
    for (int p = 0; p < 4; ++p)
      aq[p] = u32x4{__builtin_amdgcn_perm(0u, pk[p], sel0), __builtin_amdgcn_perm(0u, pk[p], sel1), __builtin_amdgcn_perm(0u, pk[p], sel2), __builtin_amdgcn_perm(0u, pk[p], sel3)};
#pragma unroll
    for (int p = 0; p < 3; ++p) { acc0 = mfma32(aq[p], bq[2 * p], acc0); acc1 = mfma32(aq[p], bq[2 * p + 1], acc1); }
    asm volatile("" : "+v"(aq[3]), "+v"(acc0), "+v"(acc1));
    SB();
    acc0 = mfma16(aq[3][0], aq[3][1], wb[6][0], wb[6][1], acc0); acc1 = mfma16(aq[3][0], aq[3][1], wb[6][2], wb[6][3], acc1);
    acc0 = mfma16(aq[3][2], aq[3][3], wb[7][0], wb[7][1], acc0); acc1 = mfma16(aq[3][2], aq[3][3], wb[7][2], wb[7][3], acc1);
    __builtin_amdgcn_raw_buffer_store_b128(granules(proj_epilogue(acc0, acc1), tagv), rws, yvo, yso, AUX_SC1);
    if (reducer) {
      delay_ticks(FIN_DELAY);
      float y0, y1, y2, y3;
      unsigned np2 = 0;
      for (;;) {
        MEMFENCE();
        const u32x4 a = ld_sc1(rws, r_vo, 0), c = ld_sc1(rws, r_vo, 32 * 5120), d = ld_sc1(rws, r_vo, 64 * 5120);
        unsigned bad = ((a[1] ^ tagv) | (a[3] ^ tagv)) | ((c[1] ^ tagv) | (c[3] ^ tagv));
        y0 = bflo(a[0]) + bflo(c[0]); y1 = bfhi(a[0]) + bfhi(c[0]); y2 = bflo(a[2]) + bflo(c[2]); y3 = bfhi(a[2]) + bfhi(c[2]);
        asm volatile("" : "+v"(bad), "+v"(y0), "+v"(y1), "+v"(y2), "+v"(y3));
        bad |= (d[1] ^ tagv) | (d[3] ^ tagv);
        y0 += bflo(d[0]); y1 += bfhi(d[0]); y2 += bflo(d[2]); y3 += bfhi(d[2]);
        if (__builtin_expect(__builtin_amdgcn_ballot_w64(bad != 0u) == 0, 1)) break;
        if (++np2 > SPIN_CAP) break;
      }
      const auto s0 = __builtin_amdgcn_permlane32_swap(__float_as_uint(y0), __float_as_uint(y2), false, false);
      const auto s1 = __builtin_amdgcn_permlane32_swap(__float_as_uint(y1), __float_as_uint(y3), false, false);
      const float z0 = __uint_as_float(s0[0]) + __uint_as_float(s0[1]), z1 = __uint_as_float(s1[0]) + __uint_as_float(s1[1]);
      const auto s2 = __builtin_amdgcn_permlane16_swap(__float_as_uint(z0), __float_as_uint(z1), false, false);
      float y = __uint_as_float(s2[0]) + __uint_as_float(s2[1]);
      y += dpp<ROR8>(y); y += dpp<ROR4>(y); y += dpp<XOR2>(y);
      __builtin_amdgcn_raw_buffer_store_b16((unsigned short)cvtpk(xres + y, 0.f), rout, out_vo, 0, AUX_SC1);
      if (bid == 0) __builtin_amdgcn_raw_buffer_store_b32(tagv, rws, lane == 0 ? WS_EPOCH : OOB, 0, AUX_SC1);
    }
  }
}

extern "C" void kernel_launch(void* const* d_in, const int* in_sizes, int n_in, void* d_out, int out_size, void* d_ws, size_t ws_size,
                              hipStream_t stream) {
  fused_decode<<<256, 512, 0, stream>>>((const u16*)d_in[0], (const u16*)d_in[1], (const u16*)d_in[2], (const u16*)d_in[3], (const u16*)d_in[4],
                                        (const u16*)d_in[6], (const u16*)d_in[7], (const u16*)d_in[8], (u16*)d_out, (unsigned char*)d_ws);
}
```

```cpp
#include <hip/hip_runtime.h>
#include <hip/hip_bf16.h>
#include <stdint.h>
#include <type_traits>

typedef unsigned short u16;
typedef unsigned u32x4 __attribute__((ext_vector_type(4)));
typedef unsigned u32x2 __attribute__((ext_vector_type(2)));
typedef float f32x4 __attribute__((ext_vector_type(4)));
typedef float f32x2 __attribute__((ext_vector_type(2)));
typedef short s16x4 __attribute__((ext_vector_type(4)));
typedef __bf16 bf16x8 __attribute__((ext_vector_type(8)));
typedef __bf16 bf16x2 __attribute__((ext_vector_type(2)));
typedef __amdgpu_buffer_rsrc_t rsrc_t;

constexpr int QK = 6, CU = 6;
constexpr int STAG = 10;
constexpr int BPACE = 5;
constexpr int POLLPOS = 8;
constexpr int KEEP1 = 10;
constexpr int S_DELAY = 156;
constexpr int T_PLAN = 1396;
constexpr int RATE16 = 216;
constexpr int LAT0 = 79;
constexpr int SMP0 = 500, WAIT0 = 195, DEAD0 = 30, GAIN0 = 24;
constexpr int SMP1 = 385, WAIT1 = 135, DEAD1 = 7, GAIN1 = 52;
constexpr int Q_AT = 430, GU_AT = 440;
constexpr int TDEAD0 = 5, TREND0 = 5, TDEAD1 = 1, TREND1 = 11;
constexpr int BACK_AT = 70;
constexpr int BACK_DELAY = 12;
constexpr int LATE_DELAY = 20;
constexpr int FIN_DELAY = 48;
constexpr int CODE_BYTES = 16512;

constexpr int AUX_NT = 2, AUX_SC1 = 16;
constexpr int RSRC_W3 = 0x00020000;
constexpr unsigned SPIN_CAP = 1u << 18;
constexpr int OOB = 0x40000000;
constexpr unsigned ALL4 = 0x01010101u;

constexpr int WS_EPOCH = 0;
constexpr int WS_TS = 1024;
constexpr int WS_FP = 4096;
constexpr int WS_ML = WS_FP + 8 * 18 * 5 * 512;
constexpr int WS_YP = WS_ML + 32 * 2 * 4 * 4 * 16;
constexpr int WS_OP = 2 << 20, OP_STRIDE = 1 << 20;
constexpr int WS_END = WS_OP + 32 * OP_STRIDE;
static_assert(WS_YP + 96 * 10 * 2 * 32 * 8 <= WS_OP, "workspace map");

template <int I, int N, class F> __device__ __forceinline__ void static_for(F&& f) {
  if constexpr (I < N) { f(std::integral_constant<int, I>{}); static_for<I + 1, N>(f); }
}
__device__ __forceinline__ rsrc_t mk_rsrc(const void* p, int bytes) {
  return __builtin_amdgcn_make_buffer_rsrc(const_cast<void*>(p), 0, bytes, RSRC_W3);
}
__device__ __forceinline__ u32x4 ld_nt(rsrc_t r, int voff, int soff) { return __builtin_amdgcn_raw_buffer_load_b128(r, voff, soff, AUX_NT); }
__device__ __forceinline__ u32x4 ld_sc1(rsrc_t r, int voff, int soff) { return __builtin_amdgcn_raw_buffer_load_b128(r, voff, soff, AUX_SC1); }
__device__ __forceinline__ u32x2 ld2_sc1(rsrc_t r, int voff, int soff) { return __builtin_amdgcn_raw_buffer_load_b64(r, voff, soff, AUX_SC1); }
__device__ __forceinline__ void st_pair(rsrc_t r, int voff, int soff, float a, float b, unsigned tag) {
  u32x4 v = {__float_as_uint(a), tag, __float_as_uint(b), tag};
  __builtin_amdgcn_raw_buffer_store_b128(v, r, voff, soff, AUX_SC1);
}
__device__ __forceinline__ float bflo(unsigned v) { return __uint_as_float(v << 16); }
__device__ __forceinline__ float bfhi(unsigned v) { return __uint_as_float(v & 0xffff0000u); }
__device__ __forceinline__ unsigned cvtpk(float lo, float hi) {
  bf16x2 r = {(__bf16)lo, (__bf16)hi};
  return __builtin_bit_cast(unsigned, r);
}
__device__ __forceinline__ float rbf(float x) { return (float)(__bf16)x; }
template <int CTRL> __device__ __forceinline__ float dpp(float x) {
  return __builtin_bit_cast(float, __builtin_amdgcn_mov_dpp(__builtin_bit_cast(int, x), CTRL, 0xf, 0xf, true));
}
constexpr int XOR1 = 0xB1, XOR2 = 0x4E, HMIR = 0x141, ROR8 = 0x128, ROR4 = 0x124;
__device__ __forceinline__ float x16_sum(float x) { auto s = __builtin_amdgcn_permlane16_swap(__float_as_uint(x), __float_as_uint(x), false, false); return __uint_as_float(s[0]) + __uint_as_float(s[1]); }
__device__ __forceinline__ float x32_sum(float x) { auto s = __builtin_amdgcn_permlane32_swap(__float_as_uint(x), __float_as_uint(x), false, false); return __uint_as_float(s[0]) + __uint_as_float(s[1]); }
__device__ __forceinline__ float x16_max(float x) { auto s = __builtin_amdgcn_permlane16_swap(__float_as_uint(x), __float_as_uint(x), false, false); return fmaxf(__uint_as_float(s[0]), __uint_as_float(s[1])); }
__device__ __forceinline__ float x32_max(float x) { auto s = __builtin_amdgcn_permlane32_swap(__float_as_uint(x), __float_as_uint(x), false, false); return fmaxf(__uint_as_float(s[0]), __uint_as_float(s[1])); }
__device__ __forceinline__ float row_sum(float x) { x += dpp<XOR1>(x); x += dpp<XOR2>(x); x += dpp<HMIR>(x); x += dpp<ROR8>(x); return x; }
__device__ __forceinline__ float row_max(float x) { x = fmaxf(x, dpp<XOR1>(x)); x = fmaxf(x, dpp<XOR2>(x)); x = fmaxf(x, dpp<HMIR>(x)); x = fmaxf(x, dpp<ROR8>(x)); return x; }
__device__ __forceinline__ float quad_sum(float x) { x += dpp<XOR1>(x); x += dpp<XOR2>(x); return x; }
__device__ __forceinline__ float dot2(unsigned a, unsigned b, float c) {
  return __builtin_amdgcn_fdot2_f32_bf16(__builtin_bit_cast(bf16x2, a), __builtin_bit_cast(bf16x2, b), c, false);
}
__device__ __forceinline__ f32x4 mfma16(unsigned a0, unsigned a1, unsigned b0, unsigned b1, f32x4 c) {
  u32x2 a = {a0, a1}, b = {b0, b1};
  return __builtin_amdgcn_mfma_f32_16x16x16bf16_1k(__builtin_bit_cast(s16x4, a), __builtin_bit_cast(s16x4, b), c, 0, 0, 0);
}
__device__ __forceinline__ f32x4 mfma32(u32x4 a, u32x4 b, f32x4 c) {
  return __builtin_amdgcn_mfma_f32_16x16x32_bf16(__builtin_bit_cast(bf16x8, a), __builtin_bit_cast(bf16x8, b), c, 0, 0, 0);
}
template <int RECIP> __device__ __forceinline__ unsigned pace_step(unsigned tprev, unsigned tend, unsigned& tdone) {
  unsigned d, now, tgt;
  asm volatile("s_sub_i32 %0, %5, %4\n\ts_max_i32 %0, %0, 0\n\ts_min_i32 %0, %0, 0x7d0\n\ts_mul_i32 %0, %0, %6\n\ts_lshr_b32 %0, %0, 16\n\ts_add_u32 %2, %4, %0\n"
               "1:\n\ts_memrealtime vcc\n\ts_getreg_b32 %0, hwreg(HW_REG_IB_STS, 0, 4)\n\ts_waitcnt lgkmcnt(0)\n\ts_cmp_lg_u32 %0, 0\n\ts_cselect_b32 %3, vcc_lo, %3\n\t"
               "s_sub_u32 %0, vcc_lo, %2\n\ts_cmp_lt_i32 %0, 0\n\ts_cbranch_scc1 1b\n\ts_mov_b32 %1, vcc_lo"
               : "=&s"(d), "=&s"(now), "=&s"(tgt), "+s"(tdone) : "s"(tprev), "s"(tend), "i"(RECIP) : "vcc", "scc");
  return now;
}
__device__ __forceinline__ void wait_until(unsigned tgt) {
  asm volatile("1:\n\ts_memrealtime vcc\n\ts_waitcnt lgkmcnt(0)\n\ts_sub_u32 vcc_lo, vcc_lo, %0\n\ts_cmp_lt_i32 vcc_lo, 0\n\ts_cbranch_scc1 1b" :: "s"(tgt) : "vcc", "scc", "memory");
}
#define SB() __builtin_amdgcn_sched_barrier(0)
#define MEMFENCE() asm volatile("" ::: "memory")

__device__ __forceinline__ void delay_ticks(int n) {
  if (n > 0) wait_until((unsigned)__builtin_amdgcn_s_memrealtime() + n);
}

typedef __attribute__((address_space(3))) float lfloat;
typedef __attribute__((address_space(3))) f32x4 lf32x4;
typedef __attribute__((address_space(3))) unsigned luint;
typedef __attribute__((address_space(3))) unsigned char lbyte;
template <int SLP> __device__ __forceinline__ void wait_lds(unsigned a, unsigned want) {
  unsigned v, sv;
  asm volatile("s_branch 2f\n1:\n\ts_sleep %4\n2:\n\tds_read_b32 %0, %2\n\ts_waitcnt lgkmcnt(0)\n\tv_readfirstlane_b32 %1, %0\n\ts_cmp_lg_u32 %1, %3\n\ts_cbranch_scc1 1b"
               : "=&v"(v), "=&s"(sv) : "v"(a), "s"(want), "i"(SLP) : "scc", "memory");
}

__device__ __forceinline__ f32x4 proj_epilogue(f32x4 acc0, f32x4 acc1) {
  f32x4 r;
#pragma unroll
  for (int i = 0; i < 4; ++i) {
    auto s = __builtin_amdgcn_permlane32_swap(__float_as_uint(acc0[i]), __float_as_uint(acc1[i]), false, false);
    r[i] = __uint_as_float(s[0]) + dpp<ROR8>(__uint_as_float(s[1]));
  }
  return r;
}
__device__ __forceinline__ u32x4 granules(f32x4 r, unsigned tag) { return u32x4{cvtpk(r[0], r[1]), tag, cvtpk(r[2], r[3]), tag}; }

__global__ void __launch_bounds__(512, 2)
fused_decode(const u16* x_, const u16* kc_, const u16* vc_, const u16* wq_, const u16* wo_, const u16* wg_, const u16* wu_, const u16* wd_, u16* out_, unsigned char* ws_) {
  __shared__ __attribute__((aligned(16))) float lds[96 + 8 + 4 + 32 + 8 * 192 + 4];
  unsigned* l_q = (unsigned*)lds;
  float* l_ml = lds + 96;
  float* l_c = lds + 104;
  unsigned* l_est = (unsigned*)(lds + 108);
  unsigned char* l_f = (unsigned char*)(lds + 116);
  constexpr int FA = 0, FB = 4, FC = 8, FD = 12, FE = 16, FG = 20, FH = 28, FX = 32;
  unsigned* l_tend = (unsigned*)(lds + 122);
  unsigned* l_tag = (unsigned*)(lds + 140 + 8 * 192);
  const unsigned fbase = (unsigned)(size_t)(lbyte*)l_f;
  float* l_acc = lds + 140;
  unsigned char* l_xn = (unsigned char*)(l_acc + 512);

  typedef unsigned long long u64;
  typedef u64 u64x8 __attribute__((ext_vector_type(8)));
  typedef u64 u64x2 __attribute__((ext_vector_type(2)));
  u64x8 ka; u64x2 kb; u64 pc;
  u32x4 fw[16];
  int bid = blockIdx.x, tid = threadIdx.x;
  {
    unsigned v1, v2;
    u64x8 ka0; u64x2 kb0;
    asm volatile("s_load_dwordx16 %0, %3, 0x0\n\ts_load_dwordx4 %1, %3, 0x40\n\ts_getpc_b64 %2" : "=&{s[36:51]}"(ka0), "=&{s[52:55]}"(kb0), "=&s"(pc) : "s"(__builtin_amdgcn_kernarg_segment_ptr()));
    asm volatile(
               "v_readfirstlane_b32 s14, %20\n\t"
               "s_lshr_b32 s15, %19, 3\n\t"
               "s_cmpk_ge_u32 s14, 0xc0\n\ts_cbranch_scc1 1f\n\t"
               "s_setprio 2\n\t"
               "s_and_b32 s16, %19, 7\n\ts_lshr_b32 s17, %19, 4\n\t"
               "s_and_b32 s18, s15, 1\n\ts_mul_i32 s18, s18, 3\n\ts_lshr_b32 s19, s14, 6\n\ts_add_u32 s18, s18, s19\n\t"
               "v_bfe_u32 %16, %20, 3, 3\n\tv_and_b32 %17, 7, %20\n\t"
               "s_cmp_ge_u32 s17, 5\n\ts_cselect_b32 s12, 17, 16\n\ts_cselect_b32 s13, 5, 0\n\ts_sub_u32 s13, s17, s13\n\t"
               "s_cmp_eq_u32 s18, 5\n\ts_cselect_b32 s17, s12, s17\n\ts_cselect_b32 s18, s13, s18\n\t"
               "s_cmp_ge_u32 s17, 15\n\ts_cselect_b32 s12, 15, 12\n\ts_movk_i32 s13, 0x3000\n\t"
               "s_cmp_lt_u32 s17, 12\n\ts_cselect_b32 s12, 0, s12\n\ts_cselect_b32 s13, s13, 0xc00\n\ts_cselect_b32 s19, 12, 3\n\t"
               "s_mul_i32 s16, s16, s19\n\ts_sub_u32 s12, s17, s12\n\ts_add_u32 s12, s12, s16\n\t"
               "v_mad_u32_u24 %16, %16, s13, %17\n\t"
               "s_mul_i32 s19, s18, s13\n\ts_add_u32 s12, s12, s19\n\ts_lshl_b32 s12, s12, 7\n\t"
               "v_lshlrev_b32 %16, 4, %16\n\t"
               "s_mul_i32 s10, s13, 0x280\n\ts_mov_b32 s11, 0x20000\n\ts_cmp_ge_u32 s18, 5\n\ts_cselect_b32 s10, 0, s10\n\t"
               "s_cmp_ge_u32 s17, 15\n\t"
               "s_waitcnt lgkmcnt(0)\n\t"
               "s_cselect_b64 s[8:9], s[48:49], s[46:47]\n\t"
               "s_cmp_lt_u32 s17, 12\n\ts_cselect_b64 s[8:9], s[42:43], s[8:9]\n\t"
               "s_and_b32 s9, s9, 0xffff\n\t"
               "buffer_load_dwordx4 %0, %16, s[8:11], s12 offen nt\n\t" "s_add_u32 s12, s12, s13\n\t"
               "buffer_load_dwordx4 %1, %16, s[8:11], s12 offen nt\n\t" "s_add_u32 s12, s12, s13\n\t"
               "buffer_load_dwordx4 %2, %16, s[8:11], s12 offen nt\n\t" "s_add_u32 s12, s12, s13\n\t"
               "buffer_load_dwordx4 %3, %16, s[8:11], s12 offen nt\n\t" "s_add_u32 s12, s12, s13\n\t"
               "buffer_load_dwordx4 %4, %16, s[8:11], s12 offen nt\n\t" "s_add_u32 s12, s12, s13\n\t"
               "buffer_load_dwordx4 %5, %16, s[8:11], s12 offen nt\n\t" "s_add_u32 s12, s12, s13\n\t"
               "buffer_load_dwordx4 %6, %16, s[8:11], s12 offen nt\n\t" "s_add_u32 s12, s12, s13\n\t"
               "buffer_load_dwordx4 %7, %16, s[8:11], s12 offen nt\n\t" "s_add_u32 s12, s12, s13\n\t"
               "buffer_load_dwordx4 %8, %16, s[8:11], s12 offen nt\n\t" "s_add_u32 s12, s12, s13\n\t"
               "buffer_load_dwordx4 %9, %16, s[8:11], s12 offen nt\n\t" "s_add_u32 s12, s12, s13\n\t"
               "buffer_load_dwordx4 %10, %16, s[8:11], s12 offen nt\n\t" "s_add_u32 s12, s12, s13\n\t"
               "buffer_load_dwordx4 %11, %16, s[8:11], s12 offen nt\n\t" "s_add_u32 s12, s12, s13\n\t" "s_barrier\n\t"
               "buffer_load_dwordx4 %12, %16, s[8:11], s12 offen nt\n\t" "s_add_u32 s12, s12, s13\n\t"
               "buffer_load_dwordx4 %13, %16, s[8:11], s12 offen nt\n\t" "s_add_u32 s12, s12, s13\n\t"
               "buffer_load_dwordx4 %14, %16, s[8:11], s12 offen nt\n\t" "s_add_u32 s12, s12, s13\n\t"
               "buffer_load_dwordx4 %15, %16, s[8:11], s12 offen nt\n\t"
               "s_branch 2f\n"
               "1:\n\t"
               "v_and_b32 %16, 31, %20\n\tv_lshl_add_u32 %16, %16, 2, %21\n\tv_mov_b32 %17, 0\n\tds_write_b32 %16, %17\n\t"
               "s_cmpk_lg_u32 s14, 0xc0\n\ts_cbranch_scc1 3f\n\t"
               "v_and_b32 %16, 7, %20\n\tv_lshl_add_u32 %16, %16, 5, s15\n\tv_lshlrev_b32 %16, 7, %16\n\tv_min_u32 %16, %24, %16\n\t"
               "global_load_dwordx4 %15, %16, %23\n\t"
               "v_bfe_u32 %17, %20, 5, 1\n\tv_mul_u32_u24 %17, 0x50, %17\n\tv_and_b32 %16, 31, %20\n\tv_add_lshl_u32 %16, %16, %17, 4\n\t"
               "v_and_or_b32 %17, %20, 15, %17\n\tv_lshlrev_b32 %17, 4, %17\n\t"
               "s_waitcnt lgkmcnt(0)\n\t"
               "global_load_dwordx4 %0, %16, s[36:37]\n\tglobal_load_dwordx4 %1, %16, s[36:37] offset:512\n\tglobal_load_dwordx4 %2, %17, s[36:37] offset:1024\n"
               "3:\n\t"
               "s_waitcnt lgkmcnt(0)\n\ts_barrier\n"
               "2:"
               : "=&v"(fw[0]), "=&v"(fw[1]), "=&v"(fw[2]), "=&v"(fw[3]), "=&v"(fw[4]), "=&v"(fw[5]), "=&v"(fw[6]), "=&v"(fw[7]),
                 "=&v"(fw[8]), "=&v"(fw[9]), "=&v"(fw[10]), "=&v"(fw[11]), "=&v"(fw[12]), "=&v"(fw[13]), "=&v"(fw[14]), "=&v"(fw[15]),
                 "=&v"(v1), "=&v"(v2)
               : "{s[36:51]}"(ka0), "s"(bid), "v"(tid), "s"(fbase - 32), "{s[52:55]}"(kb0), "s"(pc), "i"(CODE_BYTES - 128)
               : "s8", "s9", "s10", "s11", "s12", "s13", "s14", "s15", "s16", "s17", "s18", "s19", "scc", "memory");
    asm volatile("" : "={s[36:51]}"(ka), "={s[52:55]}"(kb), "+s"(bid) : "0"(ka0), "1"(kb0));
    asm volatile("" : "+v"(tid));
  }
  const int xq = bid & 7, uq = bid >> 3;
  const int h = xq * 4 + (uq >> 3), j8 = uq & 7, b = j8 >> 2, s = j8 & 3;
  const int lane = tid & 63, wave = __builtin_amdgcn_readfirstlane(tid >> 6);
  const int g = lane >> 3, j = lane & 7;
  const int bl = (lane >> 2) & 1, e4 = lane & 3;
  const bool isS = wave >= 4;
  const u16 *x = (const u16*)ka[0], *kc = (const u16*)ka[1], *vc = (const u16*)ka[2], *wo = (const u16*)ka[4], *wd = (const u16*)ka[7];
  u16* out = (u16*)kb[0];
  unsigned char* ws = (unsigned char*)kb[1];
  u64 tst;
  asm volatile("s_memrealtime %0\n\ts_waitcnt lgkmcnt(0)" : "=s"(tst));
  const unsigned tstart = (unsigned)tst;
  const rsrc_t rws = mk_rsrc(ws, WS_END);

  if (isS) {
    const int sw = wave - 4;
    const rsrc_t r0 = mk_rsrc(kc, 2 * 32 * 8192 * 384), rv = mk_rsrc(vc, 2 * 32 * 8192 * 384);
    const int voff = ((b * 32 + h) * 8192 + 6144 + 32 * s + 8 * sw + g) * 384 + 16 * j;
    u32x4 buf[96];
    u32x4 qv[3];
    float sc[16];
    unsigned pd[16];
    f32x4 acc[3] = {{0.f, 0.f, 0.f, 0.f}, {0.f, 0.f, 0.f, 0.f}, {0.f, 0.f, 0.f, 0.f}};
    unsigned msk[4];
#pragma unroll
    for (int d = 0; d < 4; ++d) msk[d] = (j >> 1) == d ? ((j & 1) ? 0xffff0000u : 0x0000ffffu) : 0u;
    constexpr float C2 = 0.07216878364870322f * 1.4426950408889634f;
        const int stag = ((2 * (uq >> 2) - 7) * STAG) >> 1;
    unsigned tprev = tstart + S_DELAY + stag, tend = tstart + T_PLAN + stag, lat = 0;

    auto consume = [&](auto G) {
      constexpr int t = decltype(G)::value;
      if constexpr (t < 16) {
        float a0 = 0.f, a1 = 0.f, a2 = 0.f;
#pragma unroll
        for (int e = 0; e < 4; ++e) { a0 = dot2(buf[3 * t][e], qv[0][e], a0); a1 = dot2(buf[3 * t + 1][e], qv[1][e], a1); a2 = dot2(buf[3 * t + 2][e], qv[2][e], a2); }
        float a = (a0 + a1) + a2;
        a += dpp<XOR1>(a); a += dpp<XOR2>(a); a += dpp<HMIR>(a);
        sc[t] = a;
        if constexpr (t == 15) {
          float m = sc[0];
#pragma unroll
          for (int u = 1; u < 16; ++u) m = fmaxf(m, sc[u]);
          m = fmaxf(m, dpp<ROR8>(m)); m = x16_max(m); m = x32_max(m);
          const float mc = m * C2;
          float l = 0.f;
#pragma unroll
          for (int u = 0; u < 16; ++u) { const float p = __builtin_amdgcn_exp2f(fmaf(sc[u], C2, -mc)); pd[u] = cvtpk(p, p); l += bfhi(pd[u]); }
          l += dpp<ROR8>(l); l = x16_sum(l); l = x32_sum(l);
          if (lane == 0) { *(f32x2*)(l_ml + 2 * sw) = f32x2{mc, l}; MEMFENCE(); l_f[FA + sw] = 1; }
        }
      } else {
        constexpr int u = t - 16;
        const u32x4 a = {pd[u] & msk[0], pd[u] & msk[1], pd[u] & msk[2], pd[u] & msk[3]};
#pragma unroll
        for (int i = 0; i < 3; ++i) acc[i] = mfma32(a, buf[3 * t + i], acc[i]);
      }
    };
    static_for<0, 16>([&](auto K) {
      constexpr int k = decltype(K)::value;
      if constexpr (k > 6) {
        MEMFENCE();
        unsigned tv = *l_tend;
        asm volatile("" : "+v"(tv));
        tv = __builtin_amdgcn_readfirstlane(tv);
        tend = tv ? tv + stag : tend;
      }
      unsigned tdone = tprev + LAT0;
      const unsigned now = pace_step<(k ? 65536 / (16 - k) : 0)>(tprev, tend, tdone);
      SB();
#pragma unroll
      for (int i = 0; i < 6; ++i) { buf[6 * k + i] = ld_nt(k < 8 ? r0 : rv, voff + (i % 3) * 128, ((2 * k + i / 3) & 15) * 49152); SB(); }
      if constexpr (k == 15) { if (lane == 0) l_f[FB + sw] = 1; }
      if constexpr (k >= 1 && k < 15) asm volatile("s_waitcnt vmcnt(6)" ::: "memory");
      if constexpr (k >= 2 && k < 13) {
        if (tdone == now) tdone = (unsigned)__builtin_amdgcn_s_memrealtime();
        lat = (lat << 16) | (tdone - tprev);
        *(u32x2*)(l_est + 2 * sw) = u32x2{(now - stag + ((6 * (15 - k) * RATE16) >> 4)) | 1u, lat};
      }
      tprev = now;
      if constexpr (k == QK) {
        wait_lds<0>(fbase + FG, ALL4);
#pragma unroll
        for (int i = 0; i < 3; ++i) qv[i] = *(const u32x4*)(l_q + 32 * i + 4 * j);
      }
      SB();
      constexpr auto upto = [](int kk) constexpr { if (kk < QK) return 0; const int c = CU * (kk - QK + 1), l = kk < 15 ? 2 * kk : 28; return c < l ? c : l; };
      static_for<upto(k - 1), upto(k)>([&](auto G) { consume(G); SB(); });
      SB();
    });
    __builtin_amdgcn_s_setprio(3);
    wait_lds<0>(fbase + FE, ALL4);
    float cq = l_c[lane >> 4];
    unsigned dst = (unsigned)(size_t)(lfloat*)(l_acc + (sw * 2 + (g & 1)) * 192 + 8 * j + 4 * ((lane >> 4) & 1));
    unsigned src = (unsigned)(size_t)(lfloat*)(l_acc + (lane >> 4) * 384 + 64 * sw + 8 * j + 4 * (g & 1));
    unsigned fla = fbase + FC + sw, one = 1u, flr = fbase + FC;
    int pvo = (lane & 16) ? OOB : (g & 1) * 128 + j * 16 + (lane >> 5) * 8;
    int pso = WS_OP + h * OP_STRIDE + (b * 4 + s) * 768 + sw * 256;
    unsigned tagv = *l_tag;
    asm volatile("" : "+v"(cq), "+v"(dst), "+v"(src), "+v"(fla), "+v"(flr), "+v"(one), "+v"(pvo), "+s"(pso), "+v"(tagv));
    const bool wr = (lane >> 5) == (g & 1);
    SB();
    static_for<28, 32>([&](auto G) { consume(G); SB(); });
    if (wr) {
#pragma unroll
      for (int i = 0; i < 3; ++i) *(lf32x4*)(size_t)(dst + 256 * i) = acc[i];
      MEMFENCE();
      *(lbyte*)(size_t)fla = (unsigned char)one;
    }
    if (sw < 3) {
      f32x4 u0, u1;
      unsigned fv, fs;
      asm volatile("1:\n\tds_read_b32 %0, %4\n\tds_read_b128 %2, %5\n\tds_read_b128 %3, %5 offset:768\n\ts_waitcnt lgkmcnt(2)\n\tv_readfirstlane_b32 %1, %0\n\t"
                   "s_cmp_lg_u32 %1, 0x01010101\n\ts_waitcnt lgkmcnt(0)\n\ts_cbranch_scc1 1b"
                   : "=&v"(fv), "=&s"(fs), "=&v"(u0), "=&v"(u1) : "v"(flr), "v"(src) : "scc", "memory");
      float o[4];
#pragma unroll
      for (int e = 0; e < 4; ++e) o[e] = cq * (u0[e] + u1[e]);
      const auto s0 = __builtin_amdgcn_permlane32_swap(__float_as_uint(o[0]), __float_as_uint(o[2]), false, false);
      const auto s1 = __builtin_amdgcn_permlane32_swap(__float_as_uint(o[1]), __float_as_uint(o[3]), false, false);
      const float p0 = x16_sum(__uint_as_float(s0[0]) + __uint_as_float(s0[1])), p1 = x16_sum(__uint_as_float(s1[0]) + __uint_as_float(s1[1]));
      __builtin_amdgcn_raw_buffer_store_b64(u32x2{cvtpk(p0, p1), tagv}, rws, pvo, pso, AUX_SC1);
    }
    MEMFENCE();
    *(lbyte*)(size_t)(fla + (FD - FC)) = (unsigned char)one;
    return;
  }

  __builtin_amdgcn_s_setprio(2);
  unsigned tag, ep;
  asm volatile("s_load_dword %0, %1, 0x0" : "=&s"(ep) : "s"(ws));
#define TAG_READY() asm volatile("s_waitcnt lgkmcnt(0)\n\ts_add_u32 %0, %1, 1\n\ts_addc_u32 %0, %0, 0" : "=&s"(tag) : "s"(ep) : "scc", "memory")
  const int w = wave;
  const bool second = uq & 1, single = second && w == 2;
  const unsigned c0 = uq >> 1, uc = single ? (c0 >= 5 ? 17 : 16) : c0, ur = single ? (c0 >= 5 ? c0 - 5 : c0) : 3 * second + w;
  const bool has_front = (w < 3) & (ur < 5);
  const int st_col = (g & 1) ? OOB : (lane >> 5) * 128 + j * 16;
  const rsrc_t rx = mk_rsrc(x, 2560);
  const unsigned sLo = (e4 & 1) ? 0x01000c0cu : 0x0c0c0100u, sHi = (e4 & 1) ? 0x03020c0cu : 0x0c0c0302u;
  unsigned sel0 = e4 < 2 ? sLo : 0x0c0c0c0cu, sel1 = e4 < 2 ? 0x0c0c0c0cu : sLo, sel2 = e4 < 2 ? sHi : 0x0c0c0c0cu, sel3 = e4 < 2 ? 0x0c0c0c0cu : sHi;
  if (w == 3) {
    const int xo = (lane >> 5) * 1280 + (lane & 31) * 16;
    const bool x3 = (lane & 16) == 0;
    asm volatile("s_waitcnt vmcnt(0)" : "+v"(fw[0]), "+v"(fw[1]), "+v"(fw[2]) : "v"(fw[15]));
    u32x4 xv[3] = {fw[0], fw[1], x3 ? fw[2] : u32x4{0u, 0u, 0u, 0u}};
    float v[24];
#pragma unroll
    for (int e = 0; e < 12; ++e) { v[2 * e] = bflo(xv[e >> 2][e & 3]); v[2 * e + 1] = bfhi(xv[e >> 2][e & 3]); }
    float sum = 0.f;
#pragma unroll
    for (int e = 0; e < 24; ++e) sum += v[e];
    sum = x16_sum(row_sum(sum));
    const float mean = sum * (1.f / 640.f);
    float sq = 0.f, sq3 = 0.f;
#pragma unroll
    for (int e = 0; e < 16; ++e) { const float d = v[e] - mean; sq = fmaf(d, d, sq); }
#pragma unroll
    for (int e = 16; e < 24; ++e) { const float d = v[e] - mean; sq3 = fmaf(d, d, sq3); }
    sq = x16_sum(row_sum(sq + (x3 ? sq3 : 0.f)));
    const float mb = rbf(mean), den = rbf(__builtin_amdgcn_sqrtf(rbf(rbf(sq * (1.f / 640.f)) + 1e-5f)));
    const float inv = __builtin_amdgcn_rcpf(den);
#pragma unroll
    for (int e = 0; e < 12; ++e) xv[e >> 2][e & 3] = cvtpk(rbf(v[2 * e] - mb) * inv, rbf(v[2 * e + 1] - mb) * inv);
    *(u32x4*)(l_xn + xo) = xv[0];
    *(u32x4*)(l_xn + xo + 512) = xv[1];
    if (x3) *(u32x4*)(l_xn + xo + 1024) = xv[2];
    MEMFENCE();
    l_f[FX] = 1;
  }
  if (has_front) {
    wait_lds<1>(fbase + FX, 1u);
    const u32x4 xr[2] = {*(const u32x4*)(l_xn + bl * 1280 + 256 * ur + g * 32), *(const u32x4*)(l_xn + bl * 1280 + 256 * ur + g * 32 + 16)};
    TAG_READY();
    f32x4 acc0 = {0.f, 0.f, 0.f, 0.f}, acc1 = {0.f, 0.f, 0.f, 0.f};
    asm volatile("s_waitcnt vmcnt(4)" : "+v"(fw[0]), "+v"(fw[1]), "+v"(fw[2]), "+v"(fw[3]), "+v"(fw[4]), "+v"(fw[5]), "+v"(fw[6]), "+v"(fw[7]),
                 "+v"(fw[8]), "+v"(fw[9]), "+v"(fw[10]), "+v"(fw[11]));
#pragma unroll
    for (int n = 0; n < 16; ++n) {
      if (n == 12) { SB(); asm volatile("s_waitcnt vmcnt(0)" : "+v"(fw[12]), "+v"(fw[13]), "+v"(fw[14]), "+v"(fw[15])); }
      const unsigned xw = xr[n >> 3][(n >> 1) & 3];
      const unsigned a0 = __builtin_amdgcn_perm(0u, xw, (n & 1) ? sel2 : sel0), a1 = __builtin_amdgcn_perm(0u, xw, (n & 1) ? sel3 : sel1);
      acc0 = mfma16(a0, a1, fw[n][0], fw[n][1], acc0);
      acc1 = mfma16(a0, a1, fw[n][2], fw[n][3], acc1);
    }
    f32x4 r = proj_epilogue(acc0, acc1);
    f32x4* l_u = (f32x4*)l_acc;
    if (w == 0) {
      wait_lds<1>(fbase + FH, second ? 0x0100u : 0x010100u);
      r += l_u[lane];
      if (!second) r += l_u[64 + lane];
    } else if (!single) {
      l_u[(w - 1) * 64 + lane] = r;
      MEMFENCE();
      l_f[FH + w] = 1;
    }
    if (w == 0 || single)
      __builtin_amdgcn_raw_buffer_store_b128(granules(r, tag), rws, ((lane >> 4) & 1) * 256 + st_col, WS_FP + ((xq * 18 + uc) * 5 + (single ? ur : second)) * 512, AUX_SC1);
    asm volatile("s_waitcnt vmcnt(0)" ::: "memory");
  } else {
    asm volatile("s_waitcnt vmcnt(0)" ::"v"(fw[0]), "v"(fw[1]), "v"(fw[2]), "v"(fw[3]), "v"(fw[4]), "v"(fw[5]), "v"(fw[6]), "v"(fw[7]),
                 "v"(fw[8]), "v"(fw[9]), "v"(fw[10]), "v"(fw[11]), "v"(fw[12]), "v"(fw[13]), "v"(fw[14]), "v"(fw[15]));
    TAG_READY();
  }
  const int slot = w * 8 + j8;
  const bool has_back = slot < 30;
  const int t3 = slot / 10, cc = slot - 10 * t3;
  const int rid = xq * 20 + uq;
  const int rb = rid / 80, rc = (rid - 80 * rb) >> 3, ro = rid & 7;
  const bool reducer = (w == 0) && (bid < 160);
  unsigned ha[2][2] = {{0u, 0u}, {0u, 0u}};
  unsigned tend = tstart + T_PLAN;
  auto pace_round = [&](unsigned t_sample, unsigned t_read, int so, int dead, int gain16, int tdead, int trend, int keep16) {
    wait_until(t_sample);
    u32x4 e, f;
    do { MEMFENCE(); e = *(const u32x4*)l_est; f = *(const u32x4*)(l_est + 4); } while (!__builtin_amdgcn_readfirstlane(e[0] & e[2] & f[0] & f[2] & 1u));
    const u32x4 pv = {tstart + (unsigned)((int)((e[0] - tstart) + (e[2] - tstart) + (f[0] - tstart) + (f[2] - tstart)) >> 2), tag, e[1] + e[3] + f[1] + f[3], tag};
    __builtin_amdgcn_raw_buffer_store_b128(pv, rws, (lane == 0 && j8 == (((uq >> 3) * 5) & 7)) ? ((uq >> 3) * 8 + xq) * 16 : OOB, so, AUX_SC1);
    wait_until(t_read);
    MEMFENCE();
    const u32x4 tv = ld_sc1(rws, (lane & 31) * 16, so);
    if (__all((tv[1] == tag) & (tv[3] == tag))) {
      const float d = x16_sum(row_sum((float)(int)(tv[0] - tstart))) * (1.f / 32.f);
      const float l1 = x16_sum(row_sum((float)(tv[2] & 0xffffu))) * (1.f / 128.f), l0 = x16_sum(row_sum((float)(tv[2] >> 16))) * (1.f / 128.f);
      const float dr = l1 - (float)LAT0, sign = l1 - l0;
      int di = (int)(d + fmaxf(dr, 0.f) + fmaxf(dr - (float)dead, 0.f) * (float)gain16 * (1.f / 16.f) + fmaxf(sign - (float)tdead, 0.f) * (float)trend);
      const int dold = (int)(tend - tstart);
      if (keep16 && di < dold) di += ((dold - di) * keep16) >> 4;
      di = di < 0 ? 0 : (di > 2500 ? 2500 : di);
      tend = (tstart + __builtin_amdgcn_readfirstlane(di)) | 1u;
      *l_tend = tend;
    }
  };
  if (w == 0) {
    wait_until(tstart + Q_AT);
    u32x2 t[3];
    const int vo = (lane >> 5) * 512 + (lane & 31) * 8, so = WS_FP + (xq * 18 + (h & 3) * 3) * 2560 + b * 256;
    for (unsigned spin = 0;; ++spin) {
      MEMFENCE();
      t[0] = ld2_sc1(rws, vo, so); t[1] = ld2_sc1(rws, vo, so + 2560); t[2] = ld2_sc1(rws, vo, so + 5120);
      if (__all((t[0][1] == tag) & (t[1][1] == tag) & (t[2][1] == tag)) || spin > SPIN_CAP) break;
    }
#pragma unroll
    for (int i = 0; i < 3; ++i) l_q[32 * i + ((lane >> 1) & 7) * 4 + ((lane >> 4) & 1) * 2 + (lane & 1)] = cvtpk(x32_sum(bflo(t[i][0])), x32_sum(bfhi(t[i][0])));
    *l_tag = tag;
    MEMFENCE();
  }
  l_f[FG + w] = 1;
  if (w == 3) {
    pace_round(tstart + SMP0, tstart + SMP0 + WAIT0, WS_TS, DEAD0, GAIN0, TDEAD0, TREND0, 0);
  }
  if (has_back) {
    wait_until(tstart + GU_AT);
    u32x2 t[2];
    const int f16 = (h & 3) * 3 + t3;
    const int so = WS_FP + (xq * 18 + 12 + (f16 >> 2)) * 2560 + (f16 & 3) * 32;
    const int vo = bl * 256 + ((g >> 1) & 1) * 128 + (g >> 2) * 16 + (g & 1) * 8;
    const bool more = (f16 >= 4) & (e4 < 3);
    for (unsigned spin = 0;; ++spin) {
      MEMFENCE();
      t[0] = ld2_sc1(rws, vo + (e4 >> 1) * 7680 + (e4 & 1) * 512, so);
      t[1] = ld2_sc1(rws, more ? vo + 7680 + (2 + e4) * 512 : OOB, so);
      if (__all((t[0][1] == tag) & ((t[1][1] == tag) | !more)) || spin > SPIN_CAP) break;
    }
#pragma unroll
    for (int z = 0; z < 2; ++z) {
      const float a = z ? bfhi(t[0][0]) : bflo(t[0][0]), c = z ? bfhi(t[1][0]) : bflo(t[1][0]);
      const float gg = rbf(quad_sum(e4 < 2 ? a : 0.f));
      const float uu = rbf(quad_sum((e4 < 2 ? 0.f : a) + c));
      const float zz = 0.7978845608028654f * (gg + 0.044715f * gg * gg * gg);
      const float ge = rbf(gg * __builtin_amdgcn_rcpf(1.f + __builtin_amdgcn_exp2f(-2.8853900817779268f * zz)));
      const float hv = ge * uu;
      const unsigned d = cvtpk(hv, hv);
      ha[z][0] = __builtin_amdgcn_perm(0u, d, sel0); ha[z][1] = __builtin_amdgcn_perm(0u, d, sel1);
    }
  }
  const int oc = 16 * (ro & 3) + 8 * (lane & 1) + 4 * (ro >> 2) + 2 * (lane >> 5) + ((lane >> 4) & 1);
  const rsrc_t rout = mk_rsrc(out, 2560);
  int out_vo = (lane & 14) == 0 ? oc * 2 + rb * 1280 + rc * 128 : OOB;
  float xres = 0.f;
  if (reducer) {
    xres = bflo(__builtin_amdgcn_raw_buffer_load_b16(rx, oc * 2, rb * 1280 + rc * 128, 0));
    __builtin_amdgcn_raw_buffer_store_b16((unsigned short)0, rout, out_vo, 0, AUX_SC1);
  }
  if (w == 1) {
    wait_lds<4>(fbase + FA, ALL4);
    const int so = WS_ML + (h * 2 + b) * 256;
    if (lane < 4) { const f32x2 ml = *(const f32x2*)(l_ml + 2 * lane); st_pair(rws, lane * 16, so + s * 64, ml[0], ml[1], tag); }
    asm volatile("s_waitcnt vmcnt(0)" ::: "memory");
    __builtin_amdgcn_s_sleep(32);
    u32x4 t;
    for (unsigned spin = 0;; ++spin) {
      MEMFENCE();
      t = ld_sc1(rws, (lane & 15) * 16, so);
      if (__all((t[1] == tag) & (t[3] == tag)) || spin > SPIN_CAP) break;
    }
    const float mv = __uint_as_float(t[0]);
    const float M = row_max(mv);
    const float f = __builtin_amdgcn_exp2f(mv - M);
    const float L = row_sum(f * __uint_as_float(t[2]));
    if ((lane >> 2) == s) { l_c[lane & 3] = f / L; MEMFENCE(); l_f[FE + (lane & 3)] = 1; }
  }
  if (w == 3) {
    pace_round(tend - SMP1, tend - SMP1 + WAIT1, WS_TS + 512, DEAD1, GAIN1, TDEAD1, TREND1, KEEP1);
  }
  if (has_back) {
    int op_so = WS_OP + h * OP_STRIDE + t3 * 256, op_vo = (bl * 4 + e4) * 768 + 16 * g;
    int d_vo = ((48 * h + 16 * t3 + 2 * g) * 640 + 64 * cc) * 2 + 16 * j, o_vo = ((192 * h + 64 * t3 + 8 * g) * 640 + 64 * cc) * 2 + 16 * j;
    int yvo = ((lane >> 4) & 1) * 256 + st_col, yso = WS_YP + ((h * 3 + t3) * 10 + cc) * 512;
    int r_vo = (lane >> 1) * 5120 + (lane & 1) * 16 + WS_YP + rc * 512 + rb * 256 + ro * 32;
    unsigned tagv = tag, fd = fbase + FD;
    asm volatile("" : "+v"(sel0), "+v"(sel1), "+v"(sel2), "+v"(sel3), "+s"(op_so), "+v"(op_vo), "+v"(d_vo), "+v"(o_vo), "+v"(yvo), "+s"(yso), "+v"(r_vo), "+v"(out_vo), "+v"(tagv), "+v"(xres), "+v"(fd));
    const rsrc_t rd = mk_rsrc(wd, 1536 * 1280), ro_ = mk_rsrc(wo, 6144 * 1280);
    wait_lds<1>(fbase + FB, ALL4);
    {
      unsigned tf = *l_tend;
      asm volatile("" : "+v"(tf));
      tf = __builtin_amdgcn_readfirstlane(tf);
      const unsigned tn = (unsigned)__builtin_amdgcn_s_memrealtime();
      int wt = tf ? (int)(tf + BACK_AT - tn) : 0;
      asm("s_max_i32 %0, %0, %1\n\ts_min_i32 %0, %0, 0xc8" : "+s"(wt) : "s"(BACK_DELAY) : "scc");
      wait_until(tn + wt);
    }
    u32x4 wb[10], t0, t1;
    unsigned fdv = 0;
#pragma unroll
    for (int n = 0; n < 10; ++n) {
      if (n == POLLPOS) {
        const rsrc_t rp = mk_rsrc(ws, __builtin_amdgcn_readfirstlane(fdv) == ALL4 ? WS_END : 0);
        t0 = ld_sc1(rp, op_vo, op_so); t1 = ld_sc1(rp, op_vo + 128, op_so);
        SB();
      }
      if (n < 2) wb[8 + n] = ld_nt(rd, d_vo + n * 1280, 0); else wb[n - 2] = ld_nt(ro_, o_vo, (n - 2) * 1280);
      if (n == POLLPOS - 1) { MEMFENCE(); fdv = *(luint*)(size_t)fd; }
      SB();
      if (n < 9) __builtin_amdgcn_s_sleep(BPACE);
      SB();
    }
    u32x4 bq[10];
#define PAIR(p) do { bq[2 * (p)] = u32x4{wb[2 * (p)][0], wb[2 * (p)][1], wb[2 * (p) + 1][0], wb[2 * (p) + 1][1]}; \
                     bq[2 * (p) + 1] = u32x4{wb[2 * (p)][2], wb[2 * (p)][3], wb[2 * (p) + 1][2], wb[2 * (p) + 1][3]}; } while (0)
    PAIR(4); PAIR(0); PAIR(1); PAIR(2);
    const u32x4 hA = {ha[0][0], ha[0][1], ha[1][0], ha[1][1]};
    f32x4 acc0 = mfma32(hA, bq[8], f32x4{0.f, 0.f, 0.f, 0.f}), acc1 = mfma32(hA, bq[9], f32x4{0.f, 0.f, 0.f, 0.f});
    asm volatile("" : "+v"(bq[0]), "+v"(bq[1]), "+v"(bq[2]), "+v"(bq[3]), "+v"(bq[4]), "+v"(bq[5]), "+v"(acc0), "+v"(acc1));
    SB();
    unsigned np1 = 1;
    if (__builtin_expect(__builtin_amdgcn_ballot_w64((((t0[1] ^ tagv) | (t0[3] ^ tagv)) | ((t1[1] ^ tagv) | (t1[3] ^ tagv))) != 0u) != 0, 0)) {
      do {
        ++np1;
        MEMFENCE();
        if (__builtin_amdgcn_readfirstlane(*(luint*)(size_t)fd) != ALL4) { wait_lds<1>(fd, ALL4); delay_ticks(LATE_DELAY); }
        t0 = ld_sc1(rws, op_vo, op_so); t1 = ld_sc1(rws, op_vo + 128, op_so);
      } while (!__all((t0[1] == tag) & (t0[3] == tag) & (t1[1] == tag) & (t1[3] == tag)) && np1 < SPIN_CAP);
    }
    unsigned pk[4];
    {
      float u0, u1, u2, u3, u4, u5, u6, u7;
#define DPPADD(r, P) "v_add_f32_dpp " r ", " r ", " r " quad_perm:" P " row_mask:0xf bank_mask:0xf\n\t"
#define DPP8(P) DPPADD("%4", P) DPPADD("%5", P) DPPADD("%6", P) DPPADD("%7", P) DPPADD("%8", P) DPPADD("%9", P) DPPADD("%10", P) DPPADD("%11", P)
      asm volatile("v_lshlrev_b32 %4, 16, %12\n\tv_and_b32 %5, 0xffff0000, %12\n\tv_lshlrev_b32 %6, 16, %13\n\tv_and_b32 %7, 0xffff0000, %13\n\t"
                   "v_lshlrev_b32 %8, 16, %14\n\tv_and_b32 %9, 0xffff0000, %14\n\tv_lshlrev_b32 %10, 16, %15\n\tv_and_b32 %11, 0xffff0000, %15\n\t"
                   DPP8("[1,0,3,2]") DPP8("[2,3,0,1]")
                   "v_cvt_pk_bf16_f32 %0, %4, %5\n\tv_cvt_pk_bf16_f32 %1, %6, %7\n\tv_cvt_pk_bf16_f32 %2, %8, %9\n\tv_cvt_pk_bf16_f32 %3, %10, %11"
                   : "=&v"(pk[0]), "=&v"(pk[1]), "=&v"(pk[2]), "=&v"(pk[3]), "=&v"(u0), "=&v"(u1), "=&v"(u2), "=&v"(u3), "=&v"(u4), "=&v"(u5), "=&v"(u6), "=&v"(u7)
                   : "v"(t0[0]), "v"(t0[2]), "v"(t1[0]), "v"(t1[2]));
    }
    u32x4 aq[4];
#pragma unroll
    for (int p = 0; p < 4; ++p)
      aq[p] = u32x4{__builtin_amdgcn_perm(0u, pk[p], sel0), __builtin_amdgcn_perm(0u, pk[p], sel1), __builtin_amdgcn_perm(0u, pk[p], sel2), __builtin_amdgcn_perm(0u, pk[p], sel3)};
#pragma unroll
    for (int p = 0; p < 3; ++p) { acc0 = mfma32(aq[p], bq[2 * p], acc0); acc1 = mfma32(aq[p], bq[2 * p + 1], acc1); }
    asm volatile("" : "+v"(aq[3]), "+v"(acc0), "+v"(acc1));
    SB();
    acc0 = mfma16(aq[3][0], aq[3][1], wb[6][0], wb[6][1], acc0); acc1 = mfma16(aq[3][0], aq[3][1], wb[6][2], wb[6][3], acc1);
    acc0 = mfma16(aq[3][2], aq[3][3], wb[7][0], wb[7][1], acc0); acc1 = mfma16(aq[3][2], aq[3][3], wb[7][2], wb[7][3], acc1);
    __builtin_amdgcn_raw_buffer_store_b128(granules(proj_epilogue(acc0, acc1), tagv), rws, yvo, yso, AUX_SC1);
    if (reducer) {
      delay_ticks(FIN_DELAY);
      float y0, y1, y2, y3;
      unsigned np2 = 0;
      for (;;) {
        MEMFENCE();
        const u32x4 a = ld_sc1(rws, r_vo, 0), c = ld_sc1(rws, r_vo, 32 * 5120), d = ld_sc1(rws, r_vo, 64 * 5120);
        unsigned bad = ((a[1] ^ tagv) | (a[3] ^ tagv)) | ((c[1] ^ tagv) | (c[3] ^ tagv));
        y0 = bflo(a[0]) + bflo(c[0]); y1 = bfhi(a[0]) + bfhi(c[0]); y2 = bflo(a[2]) + bflo(c[2]); y3 = bfhi(a[2]) + bfhi(c[2]);
        asm volatile("" : "+v"(bad), "+v"(y0), "+v"(y1), "+v"(y2), "+v"(y3));
        bad |= (d[1] ^ tagv) | (d[3] ^ tagv);
        y0 += bflo(d[0]); y1 += bfhi(d[0]); y2 += bflo(d[2]); y3 += bfhi(d[2]);
        if (__builtin_expect(__builtin_amdgcn_ballot_w64(bad != 0u) == 0, 1)) break;
        if (++np2 > SPIN_CAP) break;
      }
      const auto s0 = __builtin_amdgcn_permlane32_swap(__float_as_uint(y0), __float_as_uint(y2), false, false);
      const auto s1 = __builtin_amdgcn_permlane32_swap(__float_as_uint(y1), __float_as_uint(y3), false, false);
      const float z0 = __uint_as_float(s0[0]) + __uint_as_float(s0[1]), z1 = __uint_as_float(s1[0]) + __uint_as_float(s1[1]);
      const auto s2 = __builtin_amdgcn_permlane16_swap(__float_as_uint(z0), __float_as_uint(z1), false, false);
      float y = __uint_as_float(s2[0]) + __uint_as_float(s2[1]);
      y += dpp<ROR8>(y); y += dpp<ROR4>(y); y += dpp<XOR2>(y);
      __builtin_amdgcn_raw_buffer_store_b16((unsigned short)cvtpk(xres + y, 0.f), rout, out_vo, 0, AUX_SC1);
      if (bid == 0) __builtin_amdgcn_raw_buffer_store_b32(tagv, rws, lane == 0 ? WS_EPOCH : OOB, 0, AUX_SC1);
      {
        unsigned long long t_; asm volatile("s_waitcnt vmcnt(0)\n\ts_memrealtime %0\n\ts_waitcnt lgkmcnt(0)" : "=s"(t_) :: "memory");
        unsigned tf = *l_tend; asm volatile("" : "+v"(tf)); tf = __builtin_amdgcn_readfirstlane(tf);
        int v = (int)((unsigned)t_ - tf) - 300;
        asm volatile("s_max_i32 %0, %0, 0\n\ts_min_i32 %0, %0, 0x190" : "+s"(v) :: "scc");
        wait_until(tstart + 3000 + 10 * v);
      }
    }
  }
}

extern "C" void kernel_launch(void* const* d_in, const int* in_sizes, int n_in, void* d_out, int out_size, void* d_ws, size_t ws_size,
                              hipStream_t stream) {
  fused_decode<<<256, 512, 0, stream>>>((const u16*)d_in[0], (const u16*)d_in[1], (const u16*)d_in[2], (const u16*)d_in[3], (const u16*)d_in[4],
                                        (const u16*)d_in[6], (const u16*)d_in[7], (const u16*)d_in[8], (u16*)d_out, (unsigned char*)d_ws);
}
```

```cpp
#include <hip/hip_runtime.h>
#include <hip/hip_bf16.h>
#include <stdint.h>
#include <type_traits>

typedef unsigned short u16;
typedef unsigned u32x4 __attribute__((ext_vector_type(4)));
typedef unsigned u32x2 __attribute__((ext_vector_type(2)));
typedef float f32x4 __attribute__((ext_vector_type(4)));
typedef float f32x2 __attribute__((ext_vector_type(2)));
typedef short s16x4 __attribute__((ext_vector_type(4)));
typedef __bf16 bf16x8 __attribute__((ext_vector_type(8)));
typedef __bf16 bf16x2 __attribute__((ext_vector_type(2)));
typedef __amdgpu_buffer_rsrc_t rsrc_t;

constexpr int QK = 6, CU = 6;
constexpr int STAG = 10;
constexpr int BPACE = 5;
constexpr int POLLPOS = 8;
constexpr int KEEP1 = 10;
constexpr int S_DELAY = 156;
constexpr int T_PLAN = 1396;
constexpr int RATE16 = 216;
constexpr int LAT0 = 79;
constexpr int SMP0 = 500, WAIT0 = 195, DEAD0 = 30, GAIN0 = 24;
constexpr int SMP1 = 385, WAIT1 = 135, DEAD1 = 7, GAIN1 = 52;
constexpr int Q_AT = 430, GU_AT = 440;
constexpr int TDEAD0 = 5, TREND0 = 5, TDEAD1 = 1, TREND1 = 11;
constexpr int BACK_AT = 72;
constexpr int BACK_DELAY = 12;
constexpr int LATE_DELAY = 20;
constexpr int FIN_DELAY = 48;
constexpr int CODE_BYTES = 16512;

constexpr int AUX_NT = 2, AUX_SC1 = 16;
constexpr int RSRC_W3 = 0x00020000;
constexpr unsigned SPIN_CAP = 1u << 18;
constexpr int OOB = 0x40000000;
constexpr unsigned ALL4 = 0x01010101u;

constexpr int WS_EPOCH = 0;
constexpr int WS_TS = 1024;
constexpr int WS_FP = 4096;
constexpr int WS_ML = WS_FP + 8 * 18 * 5 * 512;
constexpr int WS_YP = WS_ML + 32 * 2 * 4 * 4 * 16;
constexpr int WS_OP = 2 << 20, OP_STRIDE = 1 << 20;
constexpr int WS_END = WS_OP + 32 * OP_STRIDE;
static_assert(WS_YP + 96 * 10 * 2 * 32 * 8 <= WS_OP, "workspace map");

template <int I, int N, class F> __device__ __forceinline__ void static_for(F&& f) {
  if constexpr (I < N) { f(std::integral_constant<int, I>{}); static_for<I + 1, N>(f); }
}
__device__ __forceinline__ rsrc_t mk_rsrc(const void* p, int bytes) {
  return __builtin_amdgcn_make_buffer_rsrc(const_cast<void*>(p), 0, bytes, RSRC_W3);
}
__device__ __forceinline__ u32x4 ld_nt(rsrc_t r, int voff, int soff) { return __builtin_amdgcn_raw_buffer_load_b128(r, voff, soff, AUX_NT); }
__device__ __forceinline__ u32x4 ld_sc1(rsrc_t r, int voff, int soff) { return __builtin_amdgcn_raw_buffer_load_b128(r, voff, soff, AUX_SC1); }
__device__ __forceinline__ u32x2 ld2_sc1(rsrc_t r, int voff, int soff) { return __builtin_amdgcn_raw_buffer_load_b64(r, voff, soff, AUX_SC1); }
__device__ __forceinline__ void st_pair(rsrc_t r, int voff, int soff, float a, float b, unsigned tag) {
  u32x4 v = {__float_as_uint(a), tag, __float_as_uint(b), tag};
  __builtin_amdgcn_raw_buffer_store_b128(v, r, voff, soff, AUX_SC1);
}
__device__ __forceinline__ float bflo(unsigned v) { return __uint_as_float(v << 16); }
__device__ __forceinline__ float bfhi(unsigned v) { return __uint_as_float(v & 0xffff0000u); }
__device__ __forceinline__ unsigned cvtpk(float lo, float hi) {
  bf16x2 r = {(__bf16)lo, (__bf16)hi};
  return __builtin_bit_cast(unsigned, r);
}
__device__ __forceinline__ float rbf(float x) { return (float)(__bf16)x; }
template <int CTRL> __device__ __forceinline__ float dpp(float x) {
  return __builtin_bit_cast(float, __builtin_amdgcn_mov_dpp(__builtin_bit_cast(int, x), CTRL, 0xf, 0xf, true));
}
constexpr int XOR1 = 0xB1, XOR2 = 0x4E, HMIR = 0x141, ROR8 = 0x128, ROR4 = 0x124;
__device__ __forceinline__ float x16_sum(float x) { auto s = __builtin_amdgcn_permlane16_swap(__float_as_uint(x), __float_as_uint(x), false, false); return __uint_as_float(s[0]) + __uint_as_float(s[1]); }
__device__ __forceinline__ float x32_sum(float x) { auto s = __builtin_amdgcn_permlane32_swap(__float_as_uint(x), __float_as_uint(x), false, false); return __uint_as_float(s[0]) + __uint_as_float(s[1]); }
__device__ __forceinline__ float x16_max(float x) { auto s = __builtin_amdgcn_permlane16_swap(__float_as_uint(x), __float_as_uint(x), false, false); return fmaxf(__uint_as_float(s[0]), __uint_as_float(s[1])); }
__device__ __forceinline__ float x32_max(float x) { auto s = __builtin_amdgcn_permlane32_swap(__float_as_uint(x), __float_as_uint(x), false, false); return fmaxf(__uint_as_float(s[0]), __uint_as_float(s[1])); }
__device__ __forceinline__ float row_sum(float x) { x += dpp<XOR1>(x); x += dpp<XOR2>(x); x += dpp<HMIR>(x); x += dpp<ROR8>(x); return x; }
__device__ __forceinline__ float row_max(float x) { x = fmaxf(x, dpp<XOR1>(x)); x = fmaxf(x, dpp<XOR2>(x)); x = fmaxf(x, dpp<HMIR>(x)); x = fmaxf(x, dpp<ROR8>(x)); return x; }
__device__ __forceinline__ float quad_sum(float x) { x += dpp<XOR1>(x); x += dpp<XOR2>(x); return x; }
__device__ __forceinline__ float dot2(unsigned a, unsigned b, float c) {
  return __builtin_amdgcn_fdot2_f32_bf16(__builtin_bit_cast(bf16x2, a), __builtin_bit_cast(bf16x2, b), c, false);
}
__device__ __forceinline__ f32x4 mfma16(unsigned a0, unsigned a1, unsigned b0, unsigned b1, f32x4 c) {
  u32x2 a = {a0, a1}, b = {b0, b1};
  return __builtin_amdgcn_mfma_f32_16x16x16bf16_1k(__builtin_bit_cast(s16x4, a), __builtin_bit_cast(s16x4, b), c, 0, 0, 0);
}
__device__ __forceinline__ f32x4 mfma32(u32x4 a, u32x4 b, f32x4 c) {
  return __builtin_amdgcn_mfma_f32_16x16x32_bf16(__builtin_bit_cast(bf16x8, a), __builtin_bit_cast(bf16x8, b), c, 0, 0, 0);
}
template <int RECIP> __device__ __forceinline__ unsigned pace_step(unsigned tprev, unsigned tend, unsigned& tdone) {
  unsigned d, now, tgt;
  asm volatile("s_sub_i32 %0, %5, %4\n\ts_max_i32 %0, %0, 0\n\ts_min_i32 %0, %0, 0x7d0\n\ts_mul_i32 %0, %0, %6\n\ts_lshr_b32 %0, %0, 16\n\ts_add_u32 %2, %4, %0\n"
               "1:\n\ts_memrealtime vcc\n\ts_getreg_b32 %0, hwreg(HW_REG_IB_STS, 0, 4)\n\ts_waitcnt lgkmcnt(0)\n\ts_cmp_lg_u32 %0, 0\n\ts_cselect_b32 %3, vcc_lo, %3\n\t"
               "s_sub_u32 %0, vcc_lo, %2\n\ts_cmp_lt_i32 %0, 0\n\ts_cbranch_scc1 1b\n\ts_mov_b32 %1, vcc_lo"
               : "=&s"(d), "=&s"(now), "=&s"(tgt), "+s"(tdone) : "s"(tprev), "s"(tend), "i"(RECIP) : "vcc", "scc");
  return now;
}
__device__ __forceinline__ void wait_until(unsigned tgt) {
  asm volatile("1:\n\ts_memrealtime vcc\n\ts_waitcnt lgkmcnt(0)\n\ts_sub_u32 vcc_lo, vcc_lo, %0\n\ts_cmp_lt_i32 vcc_lo, 0\n\ts_cbranch_scc1 1b" :: "s"(tgt) : "vcc", "scc", "memory");
}
#define SB() __builtin_amdgcn_sched_barrier(0)
#define MEMFENCE() asm volatile("" ::: "memory")

__device__ __forceinline__ void delay_ticks(int n) {
  if (n > 0) wait_until((unsigned)__builtin_amdgcn_s_memrealtime() + n);
}

typedef __attribute__((address_space(3))) float lfloat;
typedef __attribute__((address_space(3))) f32x4 lf32x4;
typedef __attribute__((address_space(3))) unsigned luint;
typedef __attribute__((address_space(3))) unsigned char lbyte;
template <int SLP> __device__ __forceinline__ void wait_lds(unsigned a, unsigned want) {
  unsigned v, sv;
  asm volatile("s_branch 2f\n1:\n\ts_sleep %4\n2:\n\tds_read_b32 %0, %2\n\ts_waitcnt lgkmcnt(0)\n\tv_readfirstlane_b32 %1, %0\n\ts_cmp_lg_u32 %1, %3\n\ts_cbranch_scc1 1b"
               : "=&v"(v), "=&s"(sv) : "v"(a), "s"(want), "i"(SLP) : "scc", "memory");
}

__device__ __forceinline__ f32x4 proj_epilogue(f32x4 acc0, f32x4 acc1) {
  f32x4 r;
#pragma unroll
  for (int i = 0; i < 4; ++i) {
    auto s = __builtin_amdgcn_permlane32_swap(__float_as_uint(acc0[i]), __float_as_uint(acc1[i]), false, false);
    r[i] = __uint_as_float(s[0]) + dpp<ROR8>(__uint_as_float(s[1]));
  }
  return r;
}
__device__ __forceinline__ u32x4 granules(f32x4 r, unsigned tag) { return u32x4{cvtpk(r[0], r[1]), tag, cvtpk(r[2], r[3]), tag}; }

__global__ void __launch_bounds__(512, 2)
fused_decode(const u16* x_, const u16* kc_, const u16* vc_, const u16* wq_, const u16* wo_, const u16* wg_, const u16* wu_, const u16* wd_, u16* out_, unsigned char* ws_) {
  __shared__ __attribute__((aligned(16))) float lds[96 + 8 + 4 + 32 + 8 * 192 + 4];
  unsigned* l_q = (unsigned*)lds;
  float* l_ml = lds + 96;
  float* l_c = lds + 104;
  unsigned* l_est = (unsigned*)(lds + 108);
  unsigned char* l_f = (unsigned char*)(lds + 116);
  constexpr int FA = 0, FB = 4, FC = 8, FD = 12, FE = 16, FG = 20, FH = 28, FX = 32;
  unsigned* l_tend = (unsigned*)(lds + 122);
  unsigned* l_tag = (unsigned*)(lds + 140 + 8 * 192);
  const unsigned fbase = (unsigned)(size_t)(lbyte*)l_f;
  float* l_acc = lds + 140;
  unsigned char* l_xn = (unsigned char*)(l_acc + 512);

  typedef unsigned long long u64;
  typedef u64 u64x8 __attribute__((ext_vector_type(8)));
  typedef u64 u64x2 __attribute__((ext_vector_type(2)));
  u64x8 ka; u64x2 kb; u64 pc;
  u32x4 fw[16];
  int bid = blockIdx.x, tid = threadIdx.x;
  {
    unsigned v1, v2;
    u64x8 ka0; u64x2 kb0;
    asm volatile("s_load_dwordx16 %0, %3, 0x0\n\ts_load_dwordx4 %1, %3, 0x40\n\ts_getpc_b64 %2" : "=&{s[36:51]}"(ka0), "=&{s[52:55]}"(kb0), "=&s"(pc) : "s"(__builtin_amdgcn_kernarg_segment_ptr()));
    asm volatile(
               "v_readfirstlane_b32 s14, %20\n\t"
               "s_lshr_b32 s15, %19, 3\n\t"
               "s_cmpk_ge_u32 s14, 0xc0\n\ts_cbranch_scc1 1f\n\t"
               "s_setprio 2\n\t"
               "s_and_b32 s16, %19, 7\n\ts_lshr_b32 s17, %19, 4\n\t"
               "s_and_b32 s18, s15, 1\n\ts_mul_i32 s18, s18, 3\n\ts_lshr_b32 s19, s14, 6\n\ts_add_u32 s18, s18, s19\n\t"
               "v_bfe_u32 %16, %20, 3, 3\n\tv_and_b32 %17, 7, %20\n\t"
               "s_cmp_ge_u32 s17, 5\n\ts_cselect_b32 s12, 17, 16\n\ts_cselect_b32 s13, 5, 0\n\ts_sub_u32 s13, s17, s13\n\t"
               "s_cmp_eq_u32 s18, 5\n\ts_cselect_b32 s17, s12, s17\n\ts_cselect_b32 s18, s13, s18\n\t"
               "s_cmp_ge_u32 s17, 15\n\ts_cselect_b32 s12, 15, 12\n\ts_movk_i32 s13, 0x3000\n\t"
               "s_cmp_lt_u32 s17, 12\n\ts_cselect_b32 s12, 0, s12\n\ts_cselect_b32 s13, s13, 0xc00\n\ts_cselect_b32 s19, 12, 3\n\t"
               "s_mul_i32 s16, s16, s19\n\ts_sub_u32 s12, s17, s12\n\ts_add_u32 s12, s12, s16\n\t"
               "v_mad_u32_u24 %16, %16, s13, %17\n\t"
               "s_mul_i32 s19, s18, s13\n\ts_add_u32 s12, s12, s19\n\ts_lshl_b32 s12, s12, 7\n\t"
               "v_lshlrev_b32 %16, 4, %16\n\t"
               "s_mul_i32 s10, s13, 0x280\n\ts_mov_b32 s11, 0x20000\n\ts_cmp_ge_u32 s18, 5\n\ts_cselect_b32 s10, 0, s10\n\t"
               "s_cmp_ge_u32 s17, 15\n\t"
               "s_waitcnt lgkmcnt(0)\n\t"
               "s_cselect_b64 s[8:9], s[48:49], s[46:47]\n\t"
               "s_cmp_lt_u32 s17, 12\n\ts_cselect_b64 s[8:9], s[42:43], s[8:9]\n\t"
               "s_and_b32 s9, s9, 0xffff\n\t"
               "buffer_load_dwordx4 %0, %16, s[8:11], s12 offen nt\n\t" "s_add_u32 s12, s12, s13\n\t"
               "buffer_load_dwordx4 %1, %16, s[8:11], s12 offen nt\n\t" "s_add_u32 s12, s12, s13\n\t"
               "buffer_load_dwordx4 %2, %16, s[8:11], s12 offen nt\n\t" "s_add_u32 s12, s12, s13\n\t"
               "buffer_load_dwordx4 %3, %16, s[8:11], s12 offen nt\n\t" "s_add_u32 s12, s12, s13\n\t"
               "buffer_load_dwordx4 %4, %16, s[8:11], s12 offen nt\n\t" "s_add_u32 s12, s12, s13\n\t"
               "buffer_load_dwordx4 %5, %16, s[8:11], s12 offen nt\n\t" "s_add_u32 s12, s12, s13\n\t"
               "buffer_load_dwordx4 %6, %16, s[8:11], s12 offen nt\n\t" "s_add_u32 s12, s12, s13\n\t"
               "buffer_load_dwordx4 %7, %16, s[8:11], s12 offen nt\n\t" "s_add_u32 s12, s12, s13\n\t"
               "buffer_load_dwordx4 %8, %16, s[8:11], s12 offen nt\n\t" "s_add_u32 s12, s12, s13\n\t"
               "buffer_load_dwordx4 %9, %16, s[8:11], s12 offen nt\n\t" "s_add_u32 s12, s12, s13\n\t"
               "buffer_load_dwordx4 %10, %16, s[8:11], s12 offen nt\n\t" "s_add_u32 s12, s12, s13\n\t"
               "buffer_load_dwordx4 %11, %16, s[8:11], s12 offen nt\n\t" "s_add_u32 s12, s12, s13\n\t" "s_barrier\n\t"
               "buffer_load_dwordx4 %12, %16, s[8:11], s12 offen nt\n\t" "s_add_u32 s12, s12, s13\n\t"
               "buffer_load_dwordx4 %13, %16, s[8:11], s12 offen nt\n\t" "s_add_u32 s12, s12, s13\n\t"
               "buffer_load_dwordx4 %14, %16, s[8:11], s12 offen nt\n\t" "s_add_u32 s12, s12, s13\n\t"
               "buffer_load_dwordx4 %15, %16, s[8:11], s12 offen nt\n\t"
               "s_branch 2f\n"
               "1:\n\t"
               "v_and_b32 %16, 31, %20\n\tv_lshl_add_u32 %16, %16, 2, %21\n\tv_mov_b32 %17, 0\n\tds_write_b32 %16, %17\n\t"
               "s_cmpk_lg_u32 s14, 0xc0\n\ts_cbranch_scc1 3f\n\t"
               "v_and_b32 %16, 7, %20\n\tv_lshl_add_u32 %16, %16, 5, s15\n\tv_lshlrev_b32 %16, 7, %16\n\tv_min_u32 %16, %24, %16\n\t"
               "global_load_dwordx4 %15, %16, %23\n\t"
               "v_bfe_u32 %17, %20, 5, 1\n\tv_mul_u32_u24 %17, 0x50, %17\n\tv_and_b32 %16, 31, %20\n\tv_add_lshl_u32 %16, %16, %17, 4\n\t"
               "v_and_or_b32 %17, %20, 15, %17\n\tv_lshlrev_b32 %17, 4, %17\n\t"
               "s_waitcnt lgkmcnt(0)\n\t"
               "global_load_dwordx4 %0, %16, s[36:37]\n\tglobal_load_dwordx4 %1, %16, s[36:37] offset:512\n\tglobal_load_dwordx4 %2, %17, s[36:37] offset:1024\n"
               "3:\n\t"
               "s_waitcnt lgkmcnt(0)\n\ts_barrier\n"
               "2:"
               : "=&v"(fw[0]), "=&v"(fw[1]), "=&v"(fw[2]), "=&v"(fw[3]), "=&v"(fw[4]), "=&v"(fw[5]), "=&v"(fw[6]), "=&v"(fw[7]),
                 "=&v"(fw[8]), "=&v"(fw[9]), "=&v"(fw[10]), "=&v"(fw[11]), "=&v"(fw[12]), "=&v"(fw[13]), "=&v"(fw[14]), "=&v"(fw[15]),
                 "=&v"(v1), "=&v"(v2)
               : "{s[36:51]}"(ka0), "s"(bid), "v"(tid), "s"(fbase - 32), "{s[52:55]}"(kb0), "s"(pc), "i"(CODE_BYTES - 128)
               : "s8", "s9", "s10", "s11", "s12", "s13", "s14", "s15", "s16", "s17", "s18", "s19", "scc", "memory");
    asm volatile("" : "={s[36:51]}"(ka), "={s[52:55]}"(kb), "+s"(bid) : "0"(ka0), "1"(kb0));
    asm volatile("" : "+v"(tid));
  }
  const int xq = bid & 7, uq = bid >> 3;
  const int h = xq * 4 + (uq >> 3), j8 = uq & 7, b = j8 >> 2, s = j8 & 3;
  const int lane = tid & 63, wave = __builtin_amdgcn_readfirstlane(tid >> 6);
  const int g = lane >> 3, j = lane & 7;
  const int bl = (lane >> 2) & 1, e4 = lane & 3;
  const bool isS = wave >= 4;
  const u16 *x = (const u16*)ka[0], *kc = (const u16*)ka[1], *vc = (const u16*)ka[2], *wo = (const u16*)ka[4], *wd = (const u16*)ka[7];
  u16* out = (u16*)kb[0];
  unsigned char* ws = (unsigned char*)kb[1];
  u64 tst;
  asm volatile("s_memrealtime %0\n\ts_waitcnt lgkmcnt(0)" : "=s"(tst));
  const unsigned tstart = (unsigned)tst;
  const rsrc_t rws = mk_rsrc(ws, WS_END);

  if (isS) {
    const int sw = wave - 4;
    const rsrc_t r0 = mk_rsrc(kc, 2 * 32 * 8192 * 384), rv = mk_rsrc(vc, 2 * 32 * 8192 * 384);
    const int voff = ((b * 32 + h) * 8192 + 6144 + 32 * s + 8 * sw + g) * 384 + 16 * j;
    u32x4 buf[96];
    u32x4 qv[3];
    float sc[16];
    unsigned pd[16];
    f32x4 acc[3] = {{0.f, 0.f, 0.f, 0.f}, {0.f, 0.f, 0.f, 0.f}, {0.f, 0.f, 0.f, 0.f}};
    unsigned msk[4];
#pragma unroll
    for (int d = 0; d < 4; ++d) msk[d] = (j >> 1) == d ? ((j & 1) ? 0xffff0000u : 0x0000ffffu) : 0u;
    constexpr float C2 = 0.07216878364870322f * 1.4426950408889634f;
    const int stag = ((2 * (uq >> 2) - 7) * STAG) >> 1;
    unsigned tprev = tstart + S_DELAY + stag, tend = tstart + T_PLAN + stag, lat = 0;

    auto consume = [&](auto G) {
      constexpr int t = decltype(G)::value;
      if constexpr (t < 16) {
        float a0 = 0.f, a1 = 0.f, a2 = 0.f;
#pragma unroll
        for (int e = 0; e < 4; ++e) { a0 = dot2(buf[3 * t][e], qv[0][e], a0); a1 = dot2(buf[3 * t + 1][e], qv[1][e], a1); a2 = dot2(buf[3 * t + 2][e], qv[2][e], a2); }
        float a = (a0 + a1) + a2;
        a += dpp<XOR1>(a); a += dpp<XOR2>(a); a += dpp<HMIR>(a);
        sc[t] = a;
        if constexpr (t == 15) {
          float m = sc[0];
#pragma unroll
          for (int u = 1; u < 16; ++u) m = fmaxf(m, sc[u]);
          m = fmaxf(m, dpp<ROR8>(m)); m = x16_max(m); m = x32_max(m);
          const float mc = m * C2;
          float l = 0.f;
#pragma unroll
          for (int u = 0; u < 16; ++u) { const float p = __builtin_amdgcn_exp2f(fmaf(sc[u], C2, -mc)); pd[u] = cvtpk(p, p); l += bfhi(pd[u]); }
          l += dpp<ROR8>(l); l = x16_sum(l); l = x32_sum(l);
          if (lane == 0) { *(f32x2*)(l_ml + 2 * sw) = f32x2{mc, l}; MEMFENCE(); l_f[FA + sw] = 1; }
        }
      } else {
        constexpr int u = t - 16;
        const u32x4 a = {pd[u] & msk[0], pd[u] & msk[1], pd[u] & msk[2], pd[u] & msk[3]};
#pragma unroll
        for (int i = 0; i < 3; ++i) acc[i] = mfma32(a, buf[3 * t + i], acc[i]);
      }
    };
    static_for<0, 16>([&](auto K) {
      constexpr int k = decltype(K)::value;
      if constexpr (k > 6) {
        MEMFENCE();
        unsigned tv = *l_tend;
        asm volatile("" : "+v"(tv));
        tv = __builtin_amdgcn_readfirstlane(tv);
        tend = tv ? tv + stag : tend;
      }
      unsigned tdone = tprev + LAT0;
      const unsigned now = pace_step<(k ? 65536 / (16 - k) : 0)>(tprev, tend, tdone);
      SB();
#pragma unroll
      for (int i = 0; i < 6; ++i) { buf[6 * k + i] = ld_nt(k < 8 ? r0 : rv, voff + (i % 3) * 128, ((2 * k + i / 3) & 15) * 49152); SB(); }
      if constexpr (k == 15) { if (lane == 0) l_f[FB + sw] = 1; }
      if constexpr (k >= 1 && k < 15) asm volatile("s_waitcnt vmcnt(6)" ::: "memory");
      if constexpr (k >= 2 && k < 13) {
        if (tdone == now) tdone = (unsigned)__builtin_amdgcn_s_memrealtime();
        lat = (lat << 16) | (tdone - tprev);
        *(u32x2*)(l_est + 2 * sw) = u32x2{(now - stag + ((6 * (15 - k) * RATE16) >> 4)) | 1u, lat};
      }
      tprev = now;
      if constexpr (k == QK) {
        wait_lds<0>(fbase + FG, ALL4);
#pragma unroll
        for (int i = 0; i < 3; ++i) qv[i] = *(const u32x4*)(l_q + 32 * i + 4 * j);
      }
      SB();
      constexpr auto upto = [](int kk) constexpr { if (kk < QK) return 0; const int c = CU * (kk - QK + 1), l = kk < 15 ? 2 * kk : 28; return c < l ? c : l; };
      static_for<upto(k - 1), upto(k)>([&](auto G) { consume(G); SB(); });
      SB();
    });
    __builtin_amdgcn_s_setprio(3);
    wait_lds<0>(fbase + FE, ALL4);
    float cq = l_c[lane >> 4];
    unsigned dst = (unsigned)(size_t)(lfloat*)(l_acc + (sw * 2 + (g & 1)) * 192 + 8 * j + 4 * ((lane >> 4) & 1));
    unsigned src = (unsigned)(size_t)(lfloat*)(l_acc + (lane >> 4) * 384 + 64 * sw + 8 * j + 4 * (g & 1));
    unsigned fla = fbase + FC + sw, one = 1u, flr = fbase + FC;
    int pvo = (lane & 16) ? OOB : (g & 1) * 128 + j * 16 + (lane >> 5) * 8;
    int pso = WS_OP + h * OP_STRIDE + (b * 4 + s) * 768 + sw * 256;
    unsigned tagv = *l_tag;
    asm volatile("" : "+v"(cq), "+v"(dst), "+v"(src), "+v"(fla), "+v"(flr), "+v"(one), "+v"(pvo), "+s"(pso), "+v"(tagv));
    const bool wr = (lane >> 5) == (g & 1);
    SB();
    static_for<28, 32>([&](auto G) { consume(G); SB(); });
    if (wr) {
#pragma unroll
      for (int i = 0; i < 3; ++i) *(lf32x4*)(size_t)(dst + 256 * i) = acc[i];
      MEMFENCE();
      *(lbyte*)(size_t)fla = (unsigned char)one;
    }
    if (sw < 3) {
      f32x4 u0, u1;
      unsigned fv, fs;
      asm volatile("1:\n\tds_read_b32 %0, %4\n\tds_read_b128 %2, %5\n\tds_read_b128 %3, %5 offset:768\n\ts_waitcnt lgkmcnt(2)\n\tv_readfirstlane_b32 %1, %0\n\t"
                   "s_cmp_lg_u32 %1, 0x01010101\n\ts_waitcnt lgkmcnt(0)\n\ts_cbranch_scc1 1b"
                   : "=&v"(fv), "=&s"(fs), "=&v"(u0), "=&v"(u1) : "v"(flr), "v"(src) : "scc", "memory");
      float o[4];
#pragma unroll
      for (int e = 0; e < 4; ++e) o[e] = cq * (u0[e] + u1[e]);
      const auto s0 = __builtin_amdgcn_permlane32_swap(__float_as_uint(o[0]), __float_as_uint(o[2]), false, false);
      const auto s1 = __builtin_amdgcn_permlane32_swap(__float_as_uint(o[1]), __float_as_uint(o[3]), false, false);
      const float p0 = x16_sum(__uint_as_float(s0[0]) + __uint_as_float(s0[1])), p1 = x16_sum(__uint_as_float(s1[0]) + __uint_as_float(s1[1]));
      __builtin_amdgcn_raw_buffer_store_b64(u32x2{cvtpk(p0, p1), tagv}, rws, pvo, pso, AUX_SC1);
    }
    MEMFENCE();
    *(lbyte*)(size_t)(fla + (FD - FC)) = (unsigned char)one;
    return;
  }

  __builtin_amdgcn_s_setprio(2);
  unsigned tag, ep;
  asm volatile("s_load_dword %0, %1, 0x0" : "=&s"(ep) : "s"(ws));
#define TAG_READY() asm volatile("s_waitcnt lgkmcnt(0)\n\ts_add_u32 %0, %1, 1\n\ts_addc_u32 %0, %0, 0" : "=&s"(tag) : "s"(ep) : "scc", "memory")
  const int w = wave;
  const bool second = uq & 1, single = second && w == 2;
  const unsigned c0 = uq >> 1, uc = single ? (c0 >= 5 ? 17 : 16) : c0, ur = single ? (c0 >= 5 ? c0 - 5 : c0) : 3 * second + w;
  const bool has_front = (w < 3) & (ur < 5);
  const int st_col = (g & 1) ? OOB : (lane >> 5) * 128 + j * 16;
  const rsrc_t rx = mk_rsrc(x, 2560);
  const unsigned sLo = (e4 & 1) ? 0x01000c0cu : 0x0c0c0100u, sHi = (e4 & 1) ? 0x03020c0cu : 0x0c0c0302u;
  unsigned sel0 = e4 < 2 ? sLo : 0x0c0c0c0cu, sel1 = e4 < 2 ? 0x0c0c0c0cu : sLo, sel2 = e4 < 2 ? sHi : 0x0c0c0c0cu, sel3 = e4 < 2 ? 0x0c0c0c0cu : sHi;
  if (w == 3) {
    const int xo = (lane >> 5) * 1280 + (lane & 31) * 16;
    const bool x3 = (lane & 16) == 0;
    asm volatile("s_waitcnt vmcnt(0)" : "+v"(fw[0]), "+v"(fw[1]), "+v"(fw[2]) : "v"(fw[15]));
    u32x4 xv[3] = {fw[0], fw[1], x3 ? fw[2] : u32x4{0u, 0u, 0u, 0u}};
    float v[24];
#pragma unroll
    for (int e = 0; e < 12; ++e) { v[2 * e] = bflo(xv[e >> 2][e & 3]); v[2 * e + 1] = bfhi(xv[e >> 2][e & 3]); }
    float sum = 0.f;
#pragma unroll
    for (int e = 0; e < 24; ++e) sum += v[e];
    sum = x16_sum(row_sum(sum));
    const float mean = sum * (1.f / 640.f);
    float sq = 0.f, sq3 = 0.f;
#pragma unroll
    for (int e = 0; e < 16; ++e) { const float d = v[e] - mean; sq = fmaf(d, d, sq); }
#pragma unroll
    for (int e = 16; e < 24; ++e) { const float d = v[e] - mean; sq3 = fmaf(d, d, sq3); }
    sq = x16_sum(row_sum(sq + (x3 ? sq3 : 0.f)));
    const float mb = rbf(mean), den = rbf(__builtin_amdgcn_sqrtf(rbf(rbf(sq * (1.f / 640.f)) + 1e-5f)));
    const float inv = __builtin_amdgcn_rcpf(den);
#pragma unroll
    for (int e = 0; e < 12; ++e) xv[e >> 2][e & 3] = cvtpk(rbf(v[2 * e] - mb) * inv, rbf(v[2 * e + 1] - mb) * inv);
    *(u32x4*)(l_xn + xo) = xv[0];
    *(u32x4*)(l_xn + xo + 512) = xv[1];
    if (x3) *(u32x4*)(l_xn + xo + 1024) = xv[2];
    MEMFENCE();
    l_f[FX] = 1;
  }
  if (has_front) {
    wait_lds<1>(fbase + FX, 1u);
    const u32x4 xr[2] = {*(const u32x4*)(l_xn + bl * 1280 + 256 * ur + g * 32), *(const u32x4*)(l_xn + bl * 1280 + 256 * ur + g * 32 + 16)};
    TAG_READY();
    f32x4 acc0 = {0.f, 0.f, 0.f, 0.f}, acc1 = {0.f, 0.f, 0.f, 0.f};
    asm volatile("s_waitcnt vmcnt(4)" : "+v"(fw[0]), "+v"(fw[1]), "+v"(fw[2]), "+v"(fw[3]), "+v"(fw[4]), "+v"(fw[5]), "+v"(fw[6]), "+v"(fw[7]),
                 "+v"(fw[8]), "+v"(fw[9]), "+v"(fw[10]), "+v"(fw[11]));
#pragma unroll
    for (int n = 0; n < 16; ++n) {
      if (n == 12) { SB(); asm volatile("s_waitcnt vmcnt(0)" : "+v"(fw[12]), "+v"(fw[13]), "+v"(fw[14]), "+v"(fw[15])); }
      const unsigned xw = xr[n >> 3][(n >> 1) & 3];
      const unsigned a0 = __builtin_amdgcn_perm(0u, xw, (n & 1) ? sel2 : sel0), a1 = __builtin_amdgcn_perm(0u, xw, (n & 1) ? sel3 : sel1);
      acc0 = mfma16(a0, a1, fw[n][0], fw[n][1], acc0);
      acc1 = mfma16(a0, a1, fw[n][2], fw[n][3], acc1);
    }
    f32x4 r = proj_epilogue(acc0, acc1);
    f32x4* l_u = (f32x4*)l_acc;
    if (w == 0) {
      wait_lds<1>(fbase + FH, second ? 0x0100u : 0x010100u);
      r += l_u[lane];
      if (!second) r += l_u[64 + lane];
    } else if (!single) {
      l_u[(w - 1) * 64 + lane] = r;
      MEMFENCE();
      l_f[FH + w] = 1;
    }
    if (w == 0 || single)
      __builtin_amdgcn_raw_buffer_store_b128(granules(r, tag), rws, ((lane >> 4) & 1) * 256 + st_col, WS_FP + ((xq * 18 + uc) * 5 + (single ? ur : second)) * 512, AUX_SC1);
    asm volatile("s_waitcnt vmcnt(0)" ::: "memory");
  } else {
    asm volatile("s_waitcnt vmcnt(0)" ::"v"(fw[0]), "v"(fw[1]), "v"(fw[2]), "v"(fw[3]), "v"(fw[4]), "v"(fw[5]), "v"(fw[6]), "v"(fw[7]),
                 "v"(fw[8]), "v"(fw[9]), "v"(fw[10]), "v"(fw[11]), "v"(fw[12]), "v"(fw[13]), "v"(fw[14]), "v"(fw[15]));
    TAG_READY();
  }
  const int slot = w * 8 + j8;
  const bool has_back = slot < 30;
  const int t3 = slot / 10, cc = slot - 10 * t3;
  const int rid = xq * 20 + uq;
  const int rb = rid / 80, rc = (rid - 80 * rb) >> 3, ro = rid & 7;
  const bool reducer = (w == 0) && (bid < 160);
  unsigned ha[2][2] = {{0u, 0u}, {0u, 0u}};
  unsigned tend = tstart + T_PLAN;
  auto pace_round = [&](unsigned t_sample, unsigned t_read, int so, int dead, int gain16, int tdead, int trend, int keep16) {
    wait_until(t_sample);
    u32x4 e, f;
    do { MEMFENCE(); e = *(const u32x4*)l_est; f = *(const u32x4*)(l_est + 4); } while (!__builtin_amdgcn_readfirstlane(e[0] & e[2] & f[0] & f[2] & 1u));
    const u32x4 pv = {tstart + (unsigned)((int)((e[0] - tstart) + (e[2] - tstart) + (f[0] - tstart) + (f[2] - tstart)) >> 2), tag, e[1] + e[3] + f[1] + f[3], tag};
    __builtin_amdgcn_raw_buffer_store_b128(pv, rws, (lane == 0 && j8 == (((uq >> 3) * 5) & 7)) ? ((uq >> 3) * 8 + xq) * 16 : OOB, so, AUX_SC1);
    wait_until(t_read);
    MEMFENCE();
    const u32x4 tv = ld_sc1(rws, (lane & 31) * 16, so);
    if (__all((tv[1] == tag) & (tv[3] == tag))) {
      const float d = x16_sum(row_sum((float)(int)(tv[0] - tstart))) * (1.f / 32.f);
      const float l1 = x16_sum(row_sum((float)(tv[2] & 0xffffu))) * (1.f / 128.f), l0 = x16_sum(row_sum((float)(tv[2] >> 16))) * (1.f / 128.f);
      const float dr = l1 - (float)LAT0, sign = l1 - l0;
      int di = (int)(d + fmaxf(dr, 0.f) + fmaxf(dr - (float)dead, 0.f) * (float)gain16 * (1.f / 16.f) + fmaxf(sign - (float)tdead, 0.f) * (float)trend);
      const int dold = (int)(tend - tstart);
      if (keep16 && di < dold) di += ((dold - di) * keep16) >> 4;
      di = di < 0 ? 0 : (di > 2500 ? 2500 : di);
      tend = (tstart + __builtin_amdgcn_readfirstlane(di)) | 1u;
      *l_tend = tend;
    }
  };
  if (w == 0) {
    wait_until(tstart + Q_AT);
    u32x2 t[3];
    const int vo = (lane >> 5) * 512 + (lane & 31) * 8, so = WS_FP + (xq * 18 + (h & 3) * 3) * 2560 + b * 256;
    for (unsigned spin = 0;; ++spin) {
      MEMFENCE();
      t[0] = ld2_sc1(rws, vo, so); t[1] = ld2_sc1(rws, vo, so + 2560); t[2] = ld2_sc1(rws, vo, so + 5120);
      if (__all((t[0][1] == tag) & (t[1][1] == tag) & (t[2][1] == tag)) || spin > SPIN_CAP) break;
    }
#pragma unroll
    for (int i = 0; i < 3; ++i) l_q[32 * i + ((lane >> 1) & 7) * 4 + ((lane >> 4) & 1) * 2 + (lane & 1)] = cvtpk(x32_sum(bflo(t[i][0])), x32_sum(bfhi(t[i][0])));
    *l_tag = tag;
    MEMFENCE();
  }
  l_f[FG + w] = 1;
  if (w == 3) {
    pace_round(tstart + SMP0, tstart + SMP0 + WAIT0, WS_TS, DEAD0, GAIN0, TDEAD0, TREND0, 0);
  }
  if (has_back) {
    wait_until(tstart + GU_AT);
    u32x2 t[2];
    const int f16 = (h & 3) * 3 + t3;
    const int so = WS_FP + (xq * 18 + 12 + (f16 >> 2)) * 2560 + (f16 & 3) * 32;
    const int vo = bl * 256 + ((g >> 1) & 1) * 128 + (g >> 2) * 16 + (g & 1) * 8;
    const bool more = (f16 >= 4) & (e4 < 3);
    for (unsigned spin = 0;; ++spin) {
      MEMFENCE();
      t[0] = ld2_sc1(rws, vo + (e4 >> 1) * 7680 + (e4 & 1) * 512, so);
      t[1] = ld2_sc1(rws, more ? vo + 7680 + (2 + e4) * 512 : OOB, so);
      if (__all((t[0][1] == tag) & ((t[1][1] == tag) | !more)) || spin > SPIN_CAP) break;
    }
#pragma unroll
    for (int z = 0; z < 2; ++z) {
      const float a = z ? bfhi(t[0][0]) : bflo(t[0][0]), c = z ? bfhi(t[1][0]) : bflo(t[1][0]);
      const float gg = rbf(quad_sum(e4 < 2 ? a : 0.f));
      const float uu = rbf(quad_sum((e4 < 2 ? 0.f : a) + c));
      const float zz = 0.7978845608028654f * (gg + 0.044715f * gg * gg * gg);
      const float ge = rbf(gg * __builtin_amdgcn_rcpf(1.f + __builtin_amdgcn_exp2f(-2.8853900817779268f * zz)));
      const float hv = ge * uu;
      const unsigned d = cvtpk(hv, hv);
      ha[z][0] = __builtin_amdgcn_perm(0u, d, sel0); ha[z][1] = __builtin_amdgcn_perm(0u, d, sel1);
    }
  }
  const int oc = 16 * (ro & 3) + 8 * (lane & 1) + 4 * (ro >> 2) + 2 * (lane >> 5) + ((lane >> 4) & 1);
  const rsrc_t rout = mk_rsrc(out, 2560);
  int out_vo = (lane & 14) == 0 ? oc * 2 + rb * 1280 + rc * 128 : OOB;
  float xres = 0.f;
  if (reducer) {
    xres = bflo(__builtin_amdgcn_raw_buffer_load_b16(rx, oc * 2, rb * 1280 + rc * 128, 0));
    __builtin_amdgcn_raw_buffer_store_b16((unsigned short)0, rout, out_vo, 0, AUX_SC1);
  }
  if (w == 1) {
    wait_lds<4>(fbase + FA, ALL4);
    const int so = WS_ML + (h * 2 + b) * 256;
    if (lane < 4) { const f32x2 ml = *(const f32x2*)(l_ml + 2 * lane); st_pair(rws, lane * 16, so + s * 64, ml[0], ml[1], tag); }
    asm volatile("s_waitcnt vmcnt(0)" ::: "memory");
    __builtin_amdgcn_s_sleep(32);
    u32x4 t;
    for (unsigned spin = 0;; ++spin) {
      MEMFENCE();
      t = ld_sc1(rws, (lane & 15) * 16, so);
      if (__all((t[1] == tag) & (t[3] == tag)) || spin > SPIN_CAP) break;
    }
    const float mv = __uint_as_float(t[0]);
    const float M = row_max(mv);
    const float f = __builtin_amdgcn_exp2f(mv - M);
    const float L = row_sum(f * __uint_as_float(t[2]));
    if ((lane >> 2) == s) { l_c[lane & 3] = f / L; MEMFENCE(); l_f[FE + (lane & 3)] = 1; }
  }
  if (w == 3) {
    pace_round(tend - SMP1, tend - SMP1 + WAIT1, WS_TS + 512, DEAD1, GAIN1, TDEAD1, TREND1, KEEP1);
  }
  if (has_back) {
    int op_so = WS_OP + h * OP_STRIDE + t3 * 256, op_vo = (bl * 4 + e4) * 768 + 16 * g;
    int d_vo = ((48 * h + 16 * t3 + 2 * g) * 640 + 64 * cc) * 2 + 16 * j, o_vo = ((192 * h + 64 * t3 + 8 * g) * 640 + 64 * cc) * 2 + 16 * j;
    int yvo = ((lane >> 4) & 1) * 256 + st_col, yso = WS_YP + ((h * 3 + t3) * 10 + cc) * 512;
    int r_vo = (lane >> 1) * 5120 + (lane & 1) * 16 + WS_YP + rc * 512 + rb * 256 + ro * 32;
    unsigned tagv = tag, fd = fbase + FD;
    asm volatile("" : "+v"(sel0), "+v"(sel1), "+v"(sel2), "+v"(sel3), "+s"(op_so), "+v"(op_vo), "+v"(d_vo), "+v"(o_vo), "+v"(yvo), "+s"(yso), "+v"(r_vo), "+v"(out_vo), "+v"(tagv), "+v"(xres), "+v"(fd));
    const rsrc_t rd = mk_rsrc(wd, 1536 * 1280), ro_ = mk_rsrc(wo, 6144 * 1280);
    wait_lds<1>(fbase + FB, ALL4);
    {
      unsigned tf = *l_tend;
      asm volatile("" : "+v"(tf));
      tf = __builtin_amdgcn_readfirstlane(tf);
      const unsigned tn = (unsigned)__builtin_amdgcn_s_memrealtime();
      int wt = tf ? (int)(tf + BACK_AT - tn) : 0;
      asm("s_max_i32 %0, %0, %1\n\ts_min_i32 %0, %0, 0xc8" : "+s"(wt) : "s"(BACK_DELAY) : "scc");
      wait_until(tn + wt);
    }
    u32x4 wb[10], t0, t1;
    unsigned fdv = 0;
#pragma unroll
    for (int n = 0; n < 10; ++n) {
      if (n == POLLPOS) {
        const rsrc_t rp = mk_rsrc(ws, __builtin_amdgcn_readfirstlane(fdv) == ALL4 ? WS_END : 0);
        t0 = ld_sc1(rp, op_vo, op_so); t1 = ld_sc1(rp, op_vo + 128, op_so);
        SB();
      }
      if (n < 2) wb[8 + n] = ld_nt(rd, d_vo + n * 1280, 0); else wb[n - 2] = ld_nt(ro_, o_vo, (n - 2) * 1280);
      if (n == POLLPOS - 1) { MEMFENCE(); fdv = *(luint*)(size_t)fd; }
      SB();
      if (n < 9) __builtin_amdgcn_s_sleep(BPACE);
      SB();
    }
    u32x4 bq[10];
#define PAIR(p) do { bq[2 * (p)] = u32x4{wb[2 * (p)][0], wb[2 * (p)][1], wb[2 * (p) + 1][0], wb[2 * (p) + 1][1]}; \
                     bq[2 * (p) + 1] = u32x4{wb[2 * (p)][2], wb[2 * (p)][3], wb[2 * (p) + 1][2], wb[2 * (p) + 1][3]}; } while (0)
    PAIR(4); PAIR(0); PAIR(1); PAIR(2);
    const u32x4 hA = {ha[0][0], ha[0][1], ha[1][0], ha[1][1]};
    f32x4 acc0 = mfma32(hA, bq[8], f32x4{0.f, 0.f, 0.f, 0.f}), acc1 = mfma32(hA, bq[9], f32x4{0.f, 0.f, 0.f, 0.f});
    asm volatile("" : "+v"(bq[0]), "+v"(bq[1]), "+v"(bq[2]), "+v"(bq[3]), "+v"(bq[4]), "+v"(bq[5]), "+v"(acc0), "+v"(acc1));
    SB();
    unsigned np1 = 1;
    if (__builtin_expect(__builtin_amdgcn_ballot_w64((((t0[1] ^ tagv) | (t0[3] ^ tagv)) | ((t1[1] ^ tagv) | (t1[3] ^ tagv))) != 0u) != 0, 0)) {
      do {
        ++np1;
        MEMFENCE();
        if (__builtin_amdgcn_readfirstlane(*(luint*)(size_t)fd) != ALL4) { wait_lds<1>(fd, ALL4); delay_ticks(LATE_DELAY); }
        t0 = ld_sc1(rws, op_vo, op_so); t1 = ld_sc1(rws, op_vo + 128, op_so);
      } while (!__all((t0[1] == tag) & (t0[3] == tag) & (t1[1] == tag) & (t1[3] == tag)) && np1 < SPIN_CAP);
    }
    unsigned pk[4];
    {
      float u0, u1, u2, u3, u4, u5, u6, u7;
#define DPPADD(r, P) "v_add_f32_dpp " r ", " r ", " r " quad_perm:" P " row_mask:0xf bank_mask:0xf\n\t"
#define DPP8(P) DPPADD("%4", P) DPPADD("%5", P) DPPADD("%6", P) DPPADD("%7", P) DPPADD("%8", P) DPPADD("%9", P) DPPADD("%10", P) DPPADD("%11", P)
      asm volatile("v_lshlrev_b32 %4, 16, %12\n\tv_and_b32 %5, 0xffff0000, %12\n\tv_lshlrev_b32 %6, 16, %13\n\tv_and_b32 %7, 0xffff0000, %13\n\t"
                   "v_lshlrev_b32 %8, 16, %14\n\tv_and_b32 %9, 0xffff0000, %14\n\tv_lshlrev_b32 %10, 16, %15\n\tv_and_b32 %11, 0xffff0000, %15\n\t"
                   DPP8("[1,0,3,2]") DPP8("[2,3,0,1]")
                   "v_cvt_pk_bf16_f32 %0, %4, %5\n\tv_cvt_pk_bf16_f32 %1, %6, %7\n\tv_cvt_pk_bf16_f32 %2, %8, %9\n\tv_cvt_pk_bf16_f32 %3, %10, %11"
                   : "=&v"(pk[0]), "=&v"(pk[1]), "=&v"(pk[2]), "=&v"(pk[3]), "=&v"(u0), "=&v"(u1), "=&v"(u2), "=&v"(u3), "=&v"(u4), "=&v"(u5), "=&v"(u6), "=&v"(u7)
                   : "v"(t0[0]), "v"(t0[2]), "v"(t1[0]), "v"(t1[2]));
    }
    u32x4 aq[4];
#pragma unroll
    for (int p = 0; p < 4; ++p)
      aq[p] = u32x4{__builtin_amdgcn_perm(0u, pk[p], sel0), __builtin_amdgcn_perm(0u, pk[p], sel1), __builtin_amdgcn_perm(0u, pk[p], sel2), __builtin_amdgcn_perm(0u, pk[p], sel3)};
#pragma unroll
    for (int p = 0; p < 3; ++p) { acc0 = mfma32(aq[p], bq[2 * p], acc0); acc1 = mfma32(aq[p], bq[2 * p + 1], acc1); }
    asm volatile("" : "+v"(aq[3]), "+v"(acc0), "+v"(acc1));
    SB();
    acc0 = mfma16(aq[3][0], aq[3][1], wb[6][0], wb[6][1], acc0); acc1 = mfma16(aq[3][0], aq[3][1], wb[6][2], wb[6][3], acc1);
    acc0 = mfma16(aq[3][2], aq[3][3], wb[7][0], wb[7][1], acc0); acc1 = mfma16(aq[3][2], aq[3][3], wb[7][2], wb[7][3], acc1);
    __builtin_amdgcn_raw_buffer_store_b128(granules(proj_epilogue(acc0, acc1), tagv), rws, yvo, yso, AUX_SC1);
    if (reducer) {
      delay_ticks(FIN_DELAY);
      float y0, y1, y2, y3;
      unsigned np2 = 0;
      for (;;) {
        MEMFENCE();
        const u32x4 a = ld_sc1(rws, r_vo, 0), c = ld_sc1(rws, r_vo, 32 * 5120), d = ld_sc1(rws, r_vo, 64 * 5120);
        unsigned bad = ((a[1] ^ tagv) | (a[3] ^ tagv)) | ((c[1] ^ tagv) | (c[3] ^ tagv));
        y0 = bflo(a[0]) + bflo(c[0]); y1 = bfhi(a[0]) + bfhi(c[0]); y2 = bflo(a[2]) + bflo(c[2]); y3 = bfhi(a[2]) + bfhi(c[2]);
        asm volatile("" : "+v"(bad), "+v"(y0), "+v"(y1), "+v"(y2), "+v"(y3));
        bad |= (d[1] ^ tagv) | (d[3] ^ tagv);
        y0 += bflo(d[0]); y1 += bfhi(d[0]); y2 += bflo(d[2]); y3 += bfhi(d[2]);
        if (__builtin_expect(__builtin_amdgcn_ballot_w64(bad != 0u) == 0, 1)) break;
        if (++np2 > SPIN_CAP) break;
      }
      const auto s0 = __builtin_amdgcn_permlane32_swap(__float_as_uint(y0), __float_as_uint(y2), false, false);
      const auto s1 = __builtin_amdgcn_permlane32_swap(__float_as_uint(y1), __float_as_uint(y3), false, false);
      const float z0 = __uint_as_float(s0[0]) + __uint_as_float(s0[1]), z1 = __uint_as_float(s1[0]) + __uint_as_float(s1[1]);
      const auto s2 = __builtin_amdgcn_permlane16_swap(__float_as_uint(z0), __float_as_uint(z1), false, false);
      float y = __uint_as_float(s2[0]) + __uint_as_float(s2[1]);
      y += dpp<ROR8>(y); y += dpp<ROR4>(y); y += dpp<XOR2>(y);
      __builtin_amdgcn_raw_buffer_store_b16((unsigned short)cvtpk(xres + y, 0.f), rout, out_vo, 0, AUX_SC1);
      if (bid == 0) __builtin_amdgcn_raw_buffer_store_b32(tagv, rws, lane == 0 ? WS_EPOCH : OOB, 0, AUX_SC1);
    }
  }
}

extern "C" void kernel_launch(void* const* d_in, const int* in_sizes, int n_in, void* d_out, int out_size, void* d_ws, size_t ws_size,
                              hipStream_t stream) {
  fused_decode<<<256, 512, 0, stream>>>((const u16*)d_in[0], (const u16*)d_in[1], (const u16*)d_in[2], (const u16*)d_in[3], (const u16*)d_in[4],
                                        (const u16*)d_in[6], (const u16*)d_in[7], (const u16*)d_in[8], (u16*)d_out, (unsigned char*)d_ws);
}
```
